```python
import torch

def _softmax(x, axis=-1):
    return torch.softmax(x, dim=axis)
def _gelu(x):
    return torch.nn.functional.gelu(x, approximate="tanh")
def _silu(x):
    return torch.nn.functional.silu(x)
def _sigmoid(x):
    return torch.sigmoid(x)
def _layernorm(x, axis=-1, eps=1e-5):
    m = x.mean(dim=axis, keepdim=True)
    v = x.var(dim=axis, keepdim=True, unbiased=False)
    return (x - m) / torch.sqrt(v + eps)
def _scatter_add(tgt, idx, src, axis=0):
    return tgt.clone().index_add_(axis, idx, src)
def _segment_sum(src, seg, n):
    out = torch.zeros((n,) + src.shape[1:], dtype=src.dtype)
    return out.index_add_(0, seg, src)
def _pad(x, pw):
    flat = tuple(v for pair in reversed(pw) for v in pair)
    return torch.nn.functional.pad(x, flat)

kernel_name = 'gated_mix__ssm_conv_s5__linear_attn_block__prompt'

def setup_inputs():

    _w = (lambda a: (lambda t: t.to(torch.bfloat16) if t.is_floating_point() else t)(torch.as_tensor(a)))
    def _A_setup():
        B, S, D = 1, 2048, 640
        P = D
        Lambda_re = -0.5 * (lambda _x: torch.exp(torch.as_tensor(_x)))((lambda *s: torch.randn(*s, dtype=torch.bfloat16) * 0.2)(P))
        Lambda_im = (lambda *s: torch.randn(*s, dtype=torch.bfloat16) * 0.8)(P) * 1.0
        B_re = (lambda *s: torch.randn(*s, dtype=torch.bfloat16) * 0.4)(P, D) * (D ** -0.5)
        B_im = (lambda *s: torch.randn(*s, dtype=torch.bfloat16) * 0.4)(P, D) * (D ** -0.5)
        C_re = (lambda *s: torch.randn(*s, dtype=torch.bfloat16) * 0.4)(D, P) * (P ** -0.5)
        C_im = (lambda *s: torch.randn(*s, dtype=torch.bfloat16) * 0.4)(D, P) * (P ** -0.5)
        log_dt = (lambda *s: torch.randn(*s, dtype=torch.bfloat16) * 0.2)(P) * 0.3 - 2.0
        Dskip = (lambda *s: torch.randn(*s, dtype=torch.bfloat16) * 0.05)(D)
        return {
            "x": _w((lambda *s: torch.randn(*s, dtype=torch.bfloat16) * 0.05)(B, S, D)),
            "Lambda_re": _w(Lambda_re),
            "Lambda_im": _w(Lambda_im),
            "B_re": _w(B_re),
            "B_im": _w(B_im),
            "C_re": _w(C_re),
            "C_im": _w(C_im),
            "log_dt": _w(log_dt),
            "Dskip": _w(Dskip),
        }
    _A_in = _A_setup()

    def _B_setup():
        B, S, D = 1, 2048, 640
        return {
            "x": _w((lambda *s: torch.randn(*s, dtype=torch.bfloat16) * 0.05)(B, S, D)),
            "wqkv": _w((lambda *s: torch.randn(*s, dtype=torch.bfloat16) * 0.8)(D, 3 * D)),
            "wo": _w((lambda *s: torch.randn(*s, dtype=torch.bfloat16) * 0.05)(D, D)),
        }
    _B_in = _B_setup()

    return {
        "A_x": _A_in["x"],
        "A_Lambda_re": _A_in["Lambda_re"],
        "A_Lambda_im": _A_in["Lambda_im"],
        "A_B_re": _A_in["B_re"],
        "A_B_im": _A_in["B_im"],
        "A_C_re": _A_in["C_re"],
        "A_C_im": _A_in["C_im"],
        "A_log_dt": _A_in["log_dt"],
        "A_Dskip": _A_in["Dskip"],
        "B_x": _B_in["x"],
        "B_wqkv": _B_in["wqkv"],
        "B_wo": _B_in["wo"],
        "gate_w": _w((lambda *s: torch.randn(*s, dtype=torch.bfloat16) * 0.05)(640) * 16.0),
    }

def reference(A_x, A_Lambda_re, A_Lambda_im, A_B_re, A_B_im, A_C_re, A_C_im, A_log_dt, A_Dskip, B_x, B_wqkv, B_wo, gate_w):
    def _A_ref(x, Lambda_re, Lambda_im, B_re, B_im, C_re, C_im, log_dt, Dskip):
        Bsz, L, H = x.shape
        Lam = Lambda_re + 1j * Lambda_im
        Bmat = B_re + 1j * B_im
        Cmat = C_re + 1j * C_im
        dt = torch.exp(log_dt)
        Lam_bar = torch.exp(dt * Lam)
        B_bar = ((Lam_bar - 1.0) / Lam)[:, None] * Bmat
        Bu = torch.einsum("bld,pd->blp", x + 0j, B_bar)
        h = torch.zeros_like(Bu[:, 0, :])
        hs = []
        for t in range(L):
            h = Lam_bar[None, :] * h + Bu[:, t, :]
            hs.append(h)
        hseq = torch.stack(hs, 1)
        y = 2.0 * torch.einsum("blp,dp->bld", hseq, Cmat).real + x * Dskip[None, None, :]
        return y

    def _B_ref(x, wqkv, wo):
        B, S, D = x.shape
        H = 32; Dh = D // H
        qkv = (lambda x, *d: x.permute(*d))((x @ wqkv).reshape(B, S, 3, H, Dh), 2, 0, 3, 1, 4)
        q, k, v = qkv[0], qkv[1], qkv[2]
        phi = lambda t: torch.where(t > 0, t + 1, torch.exp(t)) + 1e-6
        pq, pk = phi(q), phi(k)
        kv = torch.einsum("bhsd,bhse->bhde", pk, v)
        z = torch.einsum("bhsd->bhd", pk)
        num = torch.einsum("bhqd,bhde->bhqe", pq, kv)
        den = torch.einsum("bhqd,bhd->bhq", pq, z)[..., None] + 1e-6
        o = num / den
        return (lambda x, *d: x.permute(*d))(o, 0, 2, 1, 3).reshape(B, S, D) @ wo

    x = A_x
    g = _sigmoid((x * gate_w).sum(-1, keepdims=True))
    a = _A_ref(x, A_Lambda_re, A_Lambda_im, A_B_re, A_B_im, A_C_re, A_C_im, A_log_dt, A_Dskip)
    b = _B_ref(x, B_wqkv, B_wo)
    return g * a + (1 - g) * b
```

```cpp
#include <hip/hip_runtime.h>
#include <hip/hip_bf16.h>
#include <stdint.h>

namespace {

typedef __attribute__((ext_vector_type(8))) short bf16x8;
typedef __attribute__((ext_vector_type(4))) short s16x4;
typedef __attribute__((ext_vector_type(4))) float f32x4;
typedef __attribute__((ext_vector_type(2))) float f32x2;
typedef __attribute__((ext_vector_type(2))) __bf16 bf16x2_t;
typedef __attribute__((ext_vector_type(4))) unsigned u32x4;
typedef __attribute__((ext_vector_type(2))) unsigned u32x2;
typedef unsigned long long u64;
#define LAS __attribute__((address_space(3)))
#define GAS __attribute__((address_space(1)))
#define DEV __device__ __forceinline__
#define RLX_AGENT __ATOMIC_RELAXED, __HIP_MEMORY_SCOPE_AGENT

constexpr size_t WS_NONCE = 0;
constexpr size_t WS_EA    = 4096;
constexpr size_t WS_KV    = WS_EA + 16 * 8 * 160 * 8;
constexpr size_t WS_G     = WS_KV + 16 * 8 * 840 * 8;
constexpr size_t WS_W2T   = WS_G + 2048 * 8;
constexpr size_t WS_ACT   = WS_W2T + 40 * 60 * 1024;
static_assert(WS_W2T % 1024 == 0 && WS_ACT % 1024 == 0, "align");

constexpr int LDS_BYTES = 163840;
constexpr int SLOT_A = 36864, NS_A = 4;
constexpr int SLOT_B = 47104, NS_B = 3;
constexpr int A_TAB = 147456;
constexpr int A_ESEG = A_TAB + 5120;
constexpr int A_HSEG = A_ESEG + 5120;
constexpr int A_HT = 0, HT_STRIDE = 288;
constexpr int B_X1 = 47104;
constexpr int B_KVD = B_X1 + 8192;
constexpr int B_Z = B_KVD + 2 * 80 * 84 * 4;
constexpr int B_X2 = B_Z + 640;
constexpr int B_PART = B_X2 + 8192;
constexpr int B_FR = B_PART + 4 * 1680 * 4;
static_assert(B_FR + 12288 <= LDS_BYTES, "lds");
static_assert(A_HSEG + 5120 <= LDS_BYTES, "lds");

constexpr unsigned SPIN_CAP = 1u << 18;

DEV unsigned pk2(float a, float b) {
    return __builtin_bit_cast(unsigned, __builtin_convertvector((f32x2){a, b}, bf16x2_t));
}
DEV float bfr(float a) { return __uint_as_float(pk2(a, 0.f) << 16); }
DEV float bf_lo(unsigned u) { return __uint_as_float(u << 16); }
DEV float bf_hi(unsigned u) { return __uint_as_float(u & 0xffff0000u); }
DEV float bf2f(unsigned short u) { return __uint_as_float((unsigned)u << 16); }
DEV float swp(float x) {
    return __builtin_bit_cast(float, __builtin_amdgcn_mov_dpp(__builtin_bit_cast(int, x), 0xB1, 0xF, 0xF, true));
}
DEV f32x4 mfma(bf16x8 a, bf16x8 b, f32x4 c) { return __builtin_amdgcn_mfma_f32_16x16x32_bf16(a, b, c, 0, 0, 0); }
DEV bf16x8 as_frag(u32x4 v) { return __builtin_bit_cast(bf16x8, v); }
DEV bf16x8 lds_frag(LAS char* p) { return *(LAS bf16x8*)p; }
DEV bf16x8 lds_tr_frag(LAS char* p0, LAS char* p1) {
    s16x4 lo = __builtin_amdgcn_ds_read_tr16_b64_v4i16((LAS s16x4*)p0);
    s16x4 hi = __builtin_amdgcn_ds_read_tr16_b64_v4i16((LAS s16x4*)p1);
    return __builtin_shufflevector(lo, hi, 0, 1, 2, 3, 4, 5, 6, 7);
}
DEV void glds16(const char* g, LAS char* l) {
    __builtin_amdgcn_global_load_lds((const GAS unsigned*)g, (LAS unsigned*)l, 16, 0, 0);
}
DEV void bar() { asm volatile("" ::: "memory"); __builtin_amdgcn_s_barrier(); asm volatile("" ::: "memory"); }
DEV void wait_lgkm0() { __builtin_amdgcn_s_waitcnt(0xC07F); }
DEV void lds_bar() { asm volatile("" ::: "memory"); wait_lgkm0(); bar(); }
#define WAIT_VM(n) asm volatile("s_waitcnt vmcnt(" #n ")" ::: "memory")
#define SCHED() __builtin_amdgcn_sched_barrier(0)
DEV unsigned mk_tag(unsigned n) { return ((n + 1u) * 0x9E3779B1u) | 1u; }
DEV float phi(float t) { return fmaxf(t, 0.f) + __expf(fminf(t, 0.f)) + 1e-6f; }

struct AArgs {
    const char* x; const unsigned short* lre; const unsigned short* lim; const char* bre; const char* bim;
    const unsigned short* logdt; const unsigned short* cre; const unsigned short* cim; const unsigned short* wo;
    const unsigned short* gw; char* ws;
};

DEV void a_loader(const AArgs& a, LAS char* sm, int ti, int gj, int lw, int lane) {
    GAS unsigned* noncep = (GAS unsigned*)(a.ws + WS_NONCE + 128 * gj);
    const unsigned nonce = __hip_atomic_load(noncep, RLX_AGENT);
    const char* src[9];
#pragma unroll
    for (int u = 0; u < 9; ++u) {
        const int ii = 9 * lw + u, pos = lane & 7, rl = lane >> 3;
        if (ii < 16) {
            const int R = 8 * ii + rl, rho = R & 15, tok = (R & 64) + 16 * (rho >> 2) + 4 * ((R >> 4) & 3) + (rho & 3);
            src[u] = a.x + (size_t)(128 * ti + tok) * 1280 + ((pos ^ (R & 7)) << 4);
        } else {
            const int wi = ii - 16, im = wi & 1, p = 80 * gj + 8 * (wi >> 1) + rl;
            src[u] = (im ? a.bim : a.bre) + (size_t)p * 1280 + ((pos ^ rl ^ (im << 2)) << 4);
        }
    }
    auto issue = [&](int s) {
        LAS char* dst = sm + (s % NS_A) * SLOT_A + lw * 9216;
#pragma unroll
        for (int u = 0; u < 9; ++u) glds16(src[u] + 128 * s, dst + 1024 * u);
    };
    issue(0); issue(1); issue(2);
#pragma unroll 1
    for (int s = 0; s < 10; ++s) {
        if (s <= 7) WAIT_VM(18); else if (s == 8) WAIT_VM(9); else WAIT_VM(0);
        bar();
        if (s + 3 < 10) issue(s + 3);
    }
    bar();
    const int col = lw * 64 + lane;
    if (col < 160) {
        LAS float* tab = (LAS float*)(sm + A_TAB);
        const float a16 = tab[4 * 160 + col], b16 = tab[5 * 160 + col], a128 = tab[6 * 160 + col], b128 = tab[7 * 160 + col];
        LAS float* es = (LAS float*)(sm + A_ESEG);
        float pre[8], P = 0.f;
#pragma unroll
        for (int s = 0; s < 8; ++s) { pre[s] = P; P = fmaf(b16, swp(P), fmaf(a16, P, es[s * 160 + col])); }
        const unsigned tag = mk_tag(nonce);
        GAS u64* ea = (GAS u64*)(a.ws + WS_EA);
        __hip_atomic_store(ea + (ti * 8 + gj) * 160 + col, ((u64)tag << 32) | __float_as_uint(P), RLX_AGENT);
        float ev[15];
        for (unsigned spins = 0;;) {
            bool ok = true;
#pragma unroll
            for (int i = 0; i < 15; ++i) {
                ev[i] = 0.f;
                if (i < ti) {
                    const u64 v = __hip_atomic_load(ea + (i * 8 + gj) * 160 + col, RLX_AGENT);
                    ev[i] = __uint_as_float((unsigned)v); ok &= (unsigned)(v >> 32) == tag;
                }
            }
            if (__all(ok)) break;
            if (++spins > SPIN_CAP) break;
            __builtin_amdgcn_s_sleep(1);
        }
        if (ti == 15 && col == 0) __hip_atomic_store(noncep, nonce + 1u, RLX_AGENT);
        float H = 0.f;
#pragma unroll
        for (int i = 0; i < 15; ++i) if (i < ti) H = fmaf(b128, swp(H), fmaf(a128, H, ev[i]));
        LAS float* hs = (LAS float*)(sm + A_HSEG);
#pragma unroll
        for (int s = 0; s < 8; ++s) { hs[s * 160 + col] = H + pre[s]; H = fmaf(b16, swp(H), a16 * H); }
    }
    lds_bar();
    lds_bar();
}

DEV void a_consumer(const AArgs& a, LAS char* sm, int ti, int gj, int cw, int lane) {
    const int mh = cw >> 1, nh = cw & 1, c = lane & 15, q = lane >> 4, tid = cw * 64 + lane;
    if (tid < 160) __hip_atomic_store((GAS u64*)(a.ws + WS_EA) + (ti * 8 + gj) * 160 + tid, (u64)0, RLX_AGENT);
    if (tid < 80) {
        const int p = 80 * gj + tid;
        const float lr = bf2f(a.lre[p]), li = bf2f(a.lim[p]);
        const float dt = bfr(expf(bf2f(a.logdt[p])));
        const float zr = dt * lr, zi = dt * li, ex = expf(zr);
        const float ar = ex * cosf(zi), ai = ex * sinf(zi);
        const float nr = ar - 1.f, ni = ai, dn = lr * lr + li * li;
        const float cr = (nr * lr + ni * li) / dn, ci = (ni * lr - nr * li) / dn;
        float pr = ar, pi = ai;
#pragma unroll
        for (int k = 0; k < 4; ++k) { const float t = pr * pr - pi * pi; pi = 2.f * pr * pi; pr = t; }
        const float r16 = pr, i16 = pi;
#pragma unroll
        for (int k = 0; k < 3; ++k) { const float t = pr * pr - pi * pi; pi = 2.f * pr * pi; pr = t; }
        LAS f32x2* tab = (LAS f32x2*)(sm + A_TAB);
        tab[0 * 80 + tid] = (f32x2){ar, ar};
        tab[1 * 80 + tid] = (f32x2){-ai, ai};
        tab[2 * 80 + tid] = (f32x2){2.f * cr, -2.f * cr};
        tab[3 * 80 + tid] = (f32x2){-2.f * ci, -2.f * ci};
        tab[4 * 80 + tid] = (f32x2){r16, r16};
        tab[5 * 80 + tid] = (f32x2){-i16, i16};
        tab[6 * 80 + tid] = (f32x2){pr, pr};
        tab[7 * 80 + tid] = (f32x2){-pi, pi};
    }
    __builtin_amdgcn_s_setprio(1);
    f32x4 acc[4][5];
#pragma unroll
    for (int m = 0; m < 4; ++m)
#pragma unroll
        for (int n = 0; n < 5; ++n) acc[m][n] = (f32x4){0.f, 0.f, 0.f, 0.f};
    int xo[2], wo_[2];
#pragma unroll
    for (int kk = 0; kk < 2; ++kk) {
        xo[kk] = (64 * mh + c) * 128 + (((4 * kk + q) ^ (c & 7)) << 4);
        wo_[kk] = 16384 + (80 * nh + 8 * (c & 1) + (c >> 1)) * 128 + (((4 * kk + q) ^ (c >> 1) ^ ((c & 1) << 2)) << 4);
    }
    bf16x8 xa[2][4], wb[2][5];
    auto rd = [&](int buf, int slot, int kk) {
        LAS char* xs = sm + slot * SLOT_A + xo[kk];
        LAS char* wsn = sm + slot * SLOT_A + wo_[kk];
#pragma unroll
        for (int m = 0; m < 4; ++m) xa[buf][m] = lds_frag(xs + 2048 * m);
#pragma unroll
        for (int n = 0; n < 5; ++n) wb[buf][n] = lds_frag(wsn + 2048 * n);
    };
    auto mm = [&](int buf) {
#pragma unroll
        for (int n = 0; n < 5; ++n)
#pragma unroll
            for (int m = 0; m < 4; ++m) acc[m][n] = mfma(xa[buf][m], wb[buf][n], acc[m][n]);
    };
    lds_bar();
    rd(0, 0, 0);
#pragma unroll 1
    for (int s = 0; s < 10; ++s) {
        const int slot = s & 3;
        rd(1, slot, 1);
        SCHED();
        mm(0);
        SCHED();
        wait_lgkm0();
        if (s < 9) { bar(); rd(0, (s + 1) & 3, 0); }
        SCHED();
        mm(1);
        SCHED();
    }
    __builtin_amdgcn_s_setprio(0);
    LAS float* tab = (LAS float*)(sm + A_TAB);
    float ca[5], cb[5];
#pragma unroll
    for (int n = 0; n < 5; ++n) { const int col = 80 * nh + 16 * n + c; ca[n] = tab[col]; cb[n] = tab[160 + col]; }
    {
        float e[5] = {0.f, 0.f, 0.f, 0.f, 0.f};
#pragma unroll
        for (int t = 0; t < 16; ++t)
#pragma unroll
            for (int n = 0; n < 5; ++n) e[n] = fmaf(cb[n], swp(e[n]), fmaf(ca[n], e[n], acc[t >> 2][n][t & 3]));
        LAS float* es = (LAS float*)(sm + A_ESEG);
#pragma unroll
        for (int n = 0; n < 5; ++n) es[(4 * mh + q) * 160 + 80 * nh + 16 * n + c] = e[n];
    }
    WAIT_VM(0);
    lds_bar();
    {
        const int tok = 128 * ti + 16 * gj + 4 * cw + q;
        const u32x4* xr = (const u32x4*)(a.x + (size_t)tok * 1280);
        const u32x4* gr = (const u32x4*)a.gw;
        float s = 0.f;
#pragma unroll
        for (int i = 0; i < 5; ++i) {
            const u32x4 xv = xr[c + 16 * i], gv = gr[c + 16 * i];
#pragma unroll
            for (int k = 0; k < 4; ++k) {
                const unsigned pr = pk2(bf_lo(xv[k]) * bf_lo(gv[k]), bf_hi(xv[k]) * bf_hi(gv[k]));
                s += bf_lo(pr); s += bf_hi(pr);
            }
        }
        s += __shfl_xor(s, 1); s += __shfl_xor(s, 2); s += __shfl_xor(s, 4); s += __shfl_xor(s, 8);
        const float lg = bfr(s), g = bfr(1.f / (1.f + expf(-lg)));
        if (c == 0) ((f32x2*)(a.ws + WS_G))[tok] = (f32x2){g, bfr(1.f - g)};
    }
    {
        const int aw = ti * 8 + gj;
#pragma unroll 1
        for (int u = cw; u < 19; u += 4) {
            const int f = aw + 128 * u;
            if (f >= 2400) break;
            const int ks = f / 40, nt = f - 40 * ks, n = 16 * nt + c;
            u32x4 o;
            if (ks < 40) {
                const int p0 = 16 * ks + 4 * q;
                const u32x2 re = *(const u32x2*)(a.cre + n * 640 + p0), im = *(const u32x2*)(a.cim + n * 640 + p0);
                o[0] = (re[0] & 0xffffu) | (im[0] << 16); o[1] = (re[0] >> 16) | (im[0] & 0xffff0000u);
                o[2] = (re[1] & 0xffffu) | (im[1] << 16); o[3] = (re[1] >> 16) | (im[1] & 0xffff0000u);
            } else {
                const int s5 = ks - 40, uu = s5 / 5, s = s5 - 5 * uu;
                const int lo = 160 * uu + (s == 0 ? 0 : s == 1 ? 32 : s == 2 ? 64 : s == 3 ? 80 : 112);
                const int hi = 160 * uu + (s == 0 ? 16 : s == 1 ? 48 : s == 2 ? 144 : s == 3 ? 96 : 128);
                const unsigned short* pl = a.wo + (lo + 4 * q) * 640 + n;
                const unsigned short* ph = a.wo + (hi + 4 * q) * 640 + n;
                o[0] = pl[0] | ((unsigned)pl[640] << 16); o[1] = pl[1280] | ((unsigned)pl[1920] << 16);
                o[2] = ph[0] | ((unsigned)ph[640] << 16); o[3] = ph[1280] | ((unsigned)ph[1920] << 16);
            }
            *(u32x4*)(a.ws + WS_W2T + (size_t)(nt * 60 + ks) * 1024 + lane * 16) = o;
        }
    }
    lds_bar();
    {
        LAS float* hs = (LAS float*)(sm + A_HSEG);
#pragma unroll
        for (int n = 0; n < 5; ++n) {
            const int col = 80 * nh + 16 * n + c;
            const float c1 = tab[320 + col], c2 = tab[480 + col];
            float h = hs[(4 * mh + q) * 160 + col];
            float o[16];
#pragma unroll
            for (int t = 0; t < 16; ++t) {
                h = fmaf(cb[n], swp(h), fmaf(ca[n], h, acc[t >> 2][n][t & 3]));
                o[t] = fmaf(c2, swp(h), c1 * h);
            }
            LAS u32x4* dst = (LAS u32x4*)(sm + A_HT + col * HT_STRIDE + (64 * mh + 16 * q) * 2);
            dst[0] = (u32x4){pk2(o[0], o[1]), pk2(o[2], o[3]), pk2(o[4], o[5]), pk2(o[6], o[7])};
            dst[1] = (u32x4){pk2(o[8], o[9]), pk2(o[10], o[11]), pk2(o[12], o[13]), pk2(o[14], o[15])};
        }
    }
    lds_bar();
}

DEV void a_store(const AArgs& a, LAS char* sm, int ti, int gj, int wave, int lane) {
    const int q = lane >> 4, qq = (lane & 15) >> 2, p = lane & 3;
#pragma unroll
    for (int i = 0; i < 5; ++i) {
        const int f = wave + 8 * i, mt = f / 5, ks = f - 5 * mt;
        LAS char* b = sm + A_HT + (32 * ks + 8 * q + qq) * HT_STRIDE + 32 * mt + 8 * p;
        const bf16x8 v = lds_tr_frag(b, b + 4 * HT_STRIDE);
        *(bf16x8*)(a.ws + WS_ACT + (size_t)((8 * ti + mt) * 60 + 5 * gj + ks) * 1024 + lane * 16) = v;
    }
}

struct BArgs { const char* x; const char* w; char* ws; };

DEV void b_loader(const BArgs& a, LAS char* sm, int ti, int gj, int lw, int lane) {
    GAS unsigned* noncep = (GAS unsigned*)(a.ws + WS_NONCE + 128 * (8 + gj));
    const unsigned nonce = __hip_atomic_load(noncep, RLX_AGENT);
    const char* src[12];
#pragma unroll
    for (int u = 0; u < 12; ++u) {
        int ii = lw + 4 * u;
        if (ii > 45) ii = 45;
        if (ii < 16) {
            const int R = 8 * ii + (lane >> 3);
            src[u] = a.x + (size_t)(128 * ti + R) * 1280 + (((lane & 7) ^ (R & 7)) << 4);
        } else {
            const int ch = 64 * (ii - 16) + lane, rho = ch / 30, cc = ch - 30 * rho;
            const int kr = rho ^ (((rho >> 3) & 1) << 2), sec = cc / 10, w = cc - 10 * sec;
            src[u] = a.w + (size_t)kr * 3840 + (sec == 0 ? 1280 : sec == 1 ? 2560 : 0) + 160 * gj + 16 * w;
        }
    }
    const bool full = lw < 2;
    auto issue = [&](int s) {
        LAS char* dst = sm + (s % NS_B) * SLOT_B + lw * 1024;
#pragma unroll
        for (int u = 0; u < 12; ++u) {
            const int step = (lw + 4 * u < 16) ? 128 * s : 64 * 3840 * s;
            if (u < 11 || full) glds16(src[u] + step, dst + 4096 * u);
        }
    };
    issue(0); issue(1);
#pragma unroll 1
    for (int s = 0; s < 10; ++s) {
        if (s <= 8) { if (full) WAIT_VM(12); else WAIT_VM(11); } else WAIT_VM(0);
        bar();
        if (s + 2 < 10) issue(s + 2);
    }
    bar();
    bar();
    const unsigned tag = mk_tag(nonce);
    const int tl = lw * 64 + lane;
    {
        LAS float* kvd = (LAS float*)(sm + B_KVD);
        LAS float* zz = (LAS float*)(sm + B_Z);
        GAS u64* dst = (GAS u64*)(a.ws + WS_KV) + (ti * 8 + gj) * 840;
#pragma unroll
        for (int it = 0; it < 4; ++it) {
            const int g = tl + 256 * it;
            if (g < 840) {
                const int h = g / 210, rem = g - 210 * h;
                f32x2 v;
                if (rem < 200) {
                    const int d = rem / 10, e = 2 * (rem - 10 * d), o = (20 * h + d) * 84 + 20 * h + e;
                    v = *(LAS f32x2*)(kvd + o) + *(LAS f32x2*)(kvd + 80 * 84 + o);
                } else {
                    const int o = 20 * h + 2 * (rem - 200);
                    v = *(LAS f32x2*)(zz + o) + *(LAS f32x2*)(zz + 80 + o);
                }
                __hip_atomic_store(dst + g, ((u64)tag << 32) | pk2(v[0], v[1]), RLX_AGENT);
            }
        }
    }
    {
        const __amdgpu_buffer_rsrc_t rs = __builtin_amdgcn_make_buffer_rsrc(a.ws + WS_KV, 0, 16 * 8 * 840 * 8, 0x00020000);
        u32x4 v[4][7];
        for (unsigned spins = 0;;) {
            bool ok = true;
#pragma unroll
            for (int pi = 0; pi < 4; ++pi) {
                const unsigned base = (unsigned)(((4 * lw + pi) * 8 + gj) * 6720);
#pragma unroll
                for (int k = 0; k < 7; ++k) {
                    int pr = lane + 64 * k;
                    if (pr > 419) pr = 419;
                    v[pi][k] = __builtin_amdgcn_raw_buffer_load_b128(rs, base + pr * 16, 0, 16);
                }
            }
#pragma unroll
            for (int pi = 0; pi < 4; ++pi)
#pragma unroll
                for (int k = 0; k < 7; ++k) ok &= (v[pi][k][1] == tag) & (v[pi][k][3] == tag);
            if (__all(ok)) break;
            if (++spins > SPIN_CAP) break;
            __builtin_amdgcn_s_sleep(1);
        }
        if (ti == 0 && tl == 0) {
        }
        LAS f32x4* part = (LAS f32x4*)(sm + B_PART + lw * 6720);
#pragma unroll
        for (int k = 0; k < 7; ++k) {
            f32x4 s = {0.f, 0.f, 0.f, 0.f};
#pragma unroll
            for (int pi = 0; pi < 4; ++pi)
                s += (f32x4){bf_lo(v[pi][k][0]), bf_hi(v[pi][k][0]), bf_lo(v[pi][k][2]), bf_hi(v[pi][k][2])};
            const int pr = lane + 64 * k;
            if (pr < 420) part[pr] = s;
        }
    }
    lds_bar();
    if (ti == 0 && tl == 0) __hip_atomic_store(noncep, nonce + 1u, RLX_AGENT);
}

DEV void b_build_frags(LAS char* sm, int tid) {
    LAS float* part = (LAS float*)(sm + B_PART);
#pragma unroll
    for (int it = 0; it < 2; ++it) {
        const int id = tid + 512 * it;
        if (id < 768) {
            const int f = id >> 6, l = id & 63, row = l & 15, q = l >> 4;
            const int E = f >= 9 ? -1 : (f + 1) >> 1;
            const int P = f >= 9 ? f - 9 : (f == 0 || f == 1 || f == 3) ? 0 : (f == 6 || f == 8) ? 2 : 1;
            const int he = E < 0 ? (row & 3) : (16 * E + row) / 20;
            const int eo = E < 0 ? 400 : (16 * E + row) - 20 * he;
            const int ds = E < 0 ? 1 : 20;
            float v[8];
#pragma unroll
            for (int hf = 0; hf < 2; ++hf) {
                const int d0 = 32 * P + 16 * hf + 4 * q, hd = d0 / 20;
                const bool on = (hd == he) && (d0 < 80);
#pragma unroll
                for (int j = 0; j < 4; ++j) {
                    const int o = on ? hd * 420 + eo + (d0 - 20 * hd + j) * ds : 0;
                    const float t = (part[o] + part[1680 + o]) + (part[3360 + o] + part[5040 + o]);
                    v[4 * hf + j] = on ? t : 0.f;
                }
            }
            *(LAS u32x4*)(sm + B_FR + id * 16) = (u32x4){pk2(v[0], v[1]), pk2(v[2], v[3]), pk2(v[4], v[5]), pk2(v[6], v[7])};
        }
    }
}

DEV bf16x8 pack_pair(const f32x4& lo, const f32x4& hi) {
    return as_frag((u32x4){pk2(lo[0], lo[1]), pk2(lo[2], lo[3]), pk2(hi[0], hi[1]), pk2(hi[2], hi[3])});
}

DEV void b_consumer(const BArgs& a, LAS char* sm, int ti, int gj, int cw, int lane) {
    const int mh = cw >> 1, nh = cw & 1, c = lane & 15, q = lane >> 4, tid = cw * 64 + lane;
    {
        GAS u64* dst = (GAS u64*)(a.ws + WS_KV) + (ti * 8 + gj) * 840;
#pragma unroll
        for (int it = 0; it < 4; ++it) if (tid + 256 * it < 840) __hip_atomic_store(dst + tid + 256 * it, (u64)0, RLX_AGENT);
    }
    __builtin_amdgcn_s_setprio(1);
    f32x4 an[5][4], at[2][4], a2[2];
#pragma unroll
    for (int i = 0; i < 4; ++i) {
#pragma unroll
        for (int t = 0; t < 5; ++t) an[t][i] = (f32x4){0.f, 0.f, 0.f, 0.f};
        at[0][i] = at[1][i] = (f32x4){0.f, 0.f, 0.f, 0.f};
    }
    a2[0] = a2[1] = (f32x4){0.f, 0.f, 0.f, 0.f};
    int xlo[2], xhi[2];
#pragma unroll
    for (int kk = 0; kk < 2; ++kk) {
        const int sw = ((4 * kk + q) ^ (c & 7)) << 4;
        xlo[kk] = (64 * mh + 32 * nh + c) * 128 + sw;
        xhi[kk] = (64 * mh + 32 * (1 - nh) + c) * 128 + sw;
    }
    int wA[2], wB[2], wC[2];
#pragma unroll
    for (int hh = 0; hh < 2; ++hh) {
        const int b = 16384 + (8 * q + 4 * (hh ^ (q & 1)) + (c >> 2)) * 480 + 8 * (c & 3);
        wA[hh] = b + 96 * nh; wB[hh] = b + 64 + 160 * nh; wC[hh] = b + 384;
    }
    bf16x8 xa[2][4], wb[2][8];
    auto rd = [&](int buf, int slot, int kk) {
        LAS char* s0 = sm + slot * SLOT_B;
        xa[buf][0] = lds_frag(s0 + xlo[kk]); xa[buf][1] = lds_frag(s0 + xlo[kk] + 2048);
        xa[buf][2] = lds_frag(s0 + xhi[kk]); xa[buf][3] = lds_frag(s0 + xhi[kk] + 2048);
        LAS char* w0 = s0 + kk * 15360;
        wb[buf][0] = lds_tr_frag(w0 + wA[0], w0 + wA[1]);
        wb[buf][1] = lds_tr_frag(w0 + wA[0] + 32, w0 + wA[1] + 32);
        wb[buf][2] = lds_tr_frag(w0 + wB[0], w0 + wB[1]);
        wb[buf][3] = lds_tr_frag(w0 + wA[0] + 160, w0 + wA[1] + 160);
        wb[buf][4] = lds_tr_frag(w0 + wA[0] + 192, w0 + wA[1] + 192);
        wb[buf][5] = lds_tr_frag(w0 + wA[0] + 320, w0 + wA[1] + 320);
        wb[buf][6] = lds_tr_frag(w0 + wA[0] + 352, w0 + wA[1] + 352);
        wb[buf][7] = lds_tr_frag(w0 + wC[0], w0 + wC[1]);
    };
    auto mm = [&](int buf) {
#pragma unroll
        for (int t = 0; t < 5; ++t)
#pragma unroll
            for (int i = 0; i < 4; ++i) an[t][i] = mfma(xa[buf][i], wb[buf][t], an[t][i]);
#pragma unroll
        for (int t = 0; t < 2; ++t)
#pragma unroll
            for (int i = 0; i < 4; ++i) at[t][i] = mfma(wb[buf][5 + t], xa[buf][i], at[t][i]);
        a2[0] = mfma(wb[buf][7], xa[buf][0], a2[0]);
        a2[1] = mfma(wb[buf][7], xa[buf][1], a2[1]);
    };
    lds_bar();
    rd(0, 0, 0);
    int slot = 0;
#pragma unroll 1
    for (int s = 0; s < 10; ++s) {
        rd(1, slot, 1);
        SCHED();
        mm(0);
        SCHED();
        wait_lgkm0();
        slot = slot == 2 ? 0 : slot + 1;
        if (s < 9) { bar(); rd(0, slot, 0); }
        SCHED();
        mm(1);
        SCHED();
    }
    __builtin_amdgcn_s_setprio(0);
    bf16x8 fr[5][2];
#pragma unroll
    for (int t = 0; t < 2; ++t)
#pragma unroll
        for (int i = 0; i < 4; ++i)
#pragma unroll
            for (int r = 0; r < 4; ++r) an[t][i][r] = phi(an[t][i][r]);
    if (nh == 0) {
#pragma unroll
        for (int i = 0; i < 4; ++i)
#pragma unroll
            for (int r = 0; r < 4; ++r) an[2][i][r] = phi(an[2][i][r]);
    }
#pragma unroll
    for (int t = 0; t < 5; ++t) { fr[t][0] = pack_pair(an[t][0], an[t][1]); fr[t][1] = pack_pair(an[t][2], an[t][3]); }
    {
        LAS char* xs = sm + B_X1 + cw * 2048 + lane * 16;
        *(LAS bf16x8*)(xs + (nh << 10)) = fr[2][0];
        *(LAS bf16x8*)(xs + ((1 ^ nh) << 10)) = fr[2][1];
    }
    WAIT_VM(0);
    lds_bar();
    {
        LAS char* xr = sm + B_X1 + (cw ^ 1) * 2048 + lane * 16;
        const bf16x8 R0 = *(LAS bf16x8*)(xr + (nh << 10)), R1 = *(LAS bf16x8*)(xr + ((1 ^ nh) << 10));
        const bf16x8 ones = as_frag((u32x4){0x3f803f80u, 0x3f803f80u, 0x3f803f80u, 0x3f803f80u});
        const f32x4 z4 = {0.f, 0.f, 0.f, 0.f};
        LAS float* kvd = (LAS float*)(sm + B_KVD) + mh * (80 * 84);
        LAS float* zz = (LAS float*)(sm + B_Z) + mh * 80;
        auto prod = [&](const bf16x8& k0, const bf16x8& k1, const bf16x8& v0, const bf16x8& v1, int ta, int tb) {
            const f32x4 d = mfma(k1, v1, mfma(k0, v0, z4));
            LAS float* o = kvd + (16 * ta + 4 * q) * 84 + 16 * tb + c;
            o[0] = d[0]; o[84] = d[1]; o[168] = d[2]; o[252] = d[3];
        };
        auto zsum = [&](const bf16x8& k0, const bf16x8& k1, int ta) {
            const f32x4 d = mfma(k1, ones, mfma(k0, ones, z4));
            if (c == 0) *(LAS f32x4*)(zz + 16 * ta + 4 * q) = d;
        };
        const int b0 = 3 * nh;
        prod(fr[0][0], fr[0][1], fr[3][0], fr[3][1], b0, b0);
        prod(fr[0][0], fr[0][1], fr[4][0], fr[4][1], b0, b0 + 1);
        prod(fr[1][0], fr[1][1], fr[3][0], fr[3][1], b0 + 1, b0);
        prod(fr[1][0], fr[1][1], fr[4][0], fr[4][1], b0 + 1, b0 + 1);
        zsum(fr[0][0], fr[0][1], b0);
        zsum(fr[1][0], fr[1][1], b0 + 1);
        if (nh == 0) {
            prod(fr[2][0], fr[2][1], fr[4][0], fr[4][1], 2, 1);
            prod(fr[1][0], fr[1][1], R0, R1, 1, 2);
            prod(fr[2][0], fr[2][1], R0, R1, 2, 2);
            zsum(fr[2][0], fr[2][1], 2);
        } else {
            prod(fr[0][0], fr[0][1], fr[2][0], fr[2][1], 3, 2);
            prod(R0, R1, fr[3][0], fr[3][1], 2, 3);
        }
    }
    lds_bar();
    u32x4 own[2];
    u32x2 own2[2];
#pragma unroll
    for (int i = 0; i < 4; ++i) {
        u32x4 v;
        v[0] = pk2(phi(at[0][i][0]), phi(at[0][i][1])); v[1] = pk2(phi(at[0][i][2]), phi(at[0][i][3]));
        v[2] = pk2(phi(at[1][i][0]), phi(at[1][i][1])); v[3] = pk2(phi(at[1][i][2]), phi(at[1][i][3]));
        if (i < 2) own[i] = v;
        else *(LAS u32x4*)(sm + B_X2 + cw * 2048 + (i - 2) * 1024 + lane * 16) = v;
    }
#pragma unroll
    for (int i = 0; i < 2; ++i)
        own2[i] = (u32x2){pk2(phi(a2[i][0]), phi(a2[i][1])), pk2(phi(a2[i][2]), phi(a2[i][3]))};
    lds_bar();
    b_build_frags(sm, tid);
    lds_bar();
    {
        LAS char* fb = sm + B_FR + lane * 16;
        bf16x8 F[12];
#pragma unroll
        for (int f = 0; f < 12; ++f) F[f] = lds_frag(fb + 1024 * f);
        const f32x4 z4 = {0.f, 0.f, 0.f, 0.f};
        const int uu = gj >> 1, odd = gj & 1;
#pragma unroll
        for (int i = 0; i < 2; ++i) {
            const u32x4 rc = *(LAS u32x4*)(sm + B_X2 + (cw ^ 1) * 2048 + i * 1024 + lane * 16);
            bf16x8 P01, P23, P4;
            if (nh == 0) {
                P01 = as_frag(own[i]);
                P23 = as_frag((u32x4){own2[i][0], own2[i][1], rc[0], rc[1]});
                P4 = as_frag((u32x4){rc[2], rc[3], 0u, 0u});
            } else {
                P01 = as_frag(rc);
                P23 = as_frag((u32x4){own2[i][0], own2[i][1], own[i][0], own[i][1]});
                P4 = as_frag((u32x4){own[i][2], own[i][3], 0u, 0u});
            }
            const f32x4 n0 = mfma(F[0], P01, z4);
            const f32x4 n1 = mfma(F[2], P23, mfma(F[1], P01, z4));
            const f32x4 n2 = mfma(F[4], P23, mfma(F[3], P01, z4));
            const f32x4 n3 = mfma(F[6], P4, mfma(F[5], P23, z4));
            const f32x4 n4 = mfma(F[8], P4, mfma(F[7], P23, z4));
            const f32x4 dn = mfma(F[11], P4, mfma(F[10], P23, mfma(F[9], P01, z4)));
            const float r0 = 1.f / (dn[0] + 1e-6f), r1 = 1.f / (dn[1] + 1e-6f), r2 = 1.f / (dn[2] + 1e-6f), r3 = 1.f / (dn[3] + 1e-6f);
            const float s1 = q < 1 ? r0 : r1, s2 = q < 2 ? r1 : r2, s3 = q < 3 ? r2 : r3;
            const int mt = 8 * ti + 4 * mh + 2 * nh + i;
            char* ob = a.ws + WS_ACT + (size_t)(mt * 60 + 40 + 5 * uu) * 1024 + lane * 16;
            *(u32x4*)(ob + (odd ? 3072 : 0)) =
                (u32x4){pk2(n0[0] * r0, n0[1] * r0), pk2(n0[2] * r0, n0[3] * r0), pk2(n1[0] * s1, n1[1] * s1), pk2(n1[2] * s1, n1[3] * s1)};
            *(u32x4*)(ob + (odd ? 4096 : 1024)) =
                (u32x4){pk2(n2[0] * s2, n2[1] * s2), pk2(n2[2] * s2, n2[3] * s2), pk2(n3[0] * s3, n3[1] * s3), pk2(n3[2] * s3, n3[3] * s3)};
            *(u32x2*)(ob + 2048 + 8 * odd) = (u32x2){pk2(n4[0] * r3, n4[1] * r3), pk2(n4[2] * r3, n4[3] * r3)};
        }
    }
}

}

__global__ __launch_bounds__(512, 2) void gm_k1(const void* xA, const void* lre, const void* lim, const void* bre,
                                                const void* bim, const void* cre, const void* cim, const void* logdt,
                                                const void* xB, const void* wqkv, const void* wo, const void* gw,
                                                void* ws) {
    __shared__ __attribute__((aligned(1024))) char smem[LDS_BYTES];
    LAS char* sm = (LAS char*)smem;
    const int b = blockIdx.x, xcd = b & 7, idx = b >> 3, x4 = xcd & 3;
    const int ti = 8 * (x4 >> 1) + (idx & 7), gj = 4 * (x4 & 1) + (idx >> 3);
    const int wave = __builtin_amdgcn_readfirstlane(threadIdx.x >> 6), lane = threadIdx.x & 63;
    if (xcd < 4) {
        const AArgs a{(const char*)xA, (const unsigned short*)lre, (const unsigned short*)lim, (const char*)bre,
                      (const char*)bim, (const unsigned short*)logdt, (const unsigned short*)cre,
                      (const unsigned short*)cim, (const unsigned short*)wo, (const unsigned short*)gw, (char*)ws};
        if (wave < 4) a_consumer(a, sm, ti, gj, wave, lane);
        else a_loader(a, sm, ti, gj, wave - 4, lane);
        a_store(a, sm, ti, gj, wave, lane);
    } else {
        const BArgs a{(const char*)xB, (const char*)wqkv, (char*)ws};
        if (wave < 4) b_consumer(a, sm, ti, gj, wave, lane);
        else {
            b_loader(a, sm, ti, gj, wave - 4, lane);
            b_build_frags(sm, threadIdx.x);
            lds_bar();
        }
    }
}

__global__ __launch_bounds__(256, 1) void gm_k2(const void* xA_, const void* dskip_, const void* ws_, float* out) {
    __shared__ __attribute__((aligned(16))) float red[4 * 64 * 84];
    const char* ws = (const char*)ws_;
    const int b = blockIdx.x, xcd = b & 7, idx = b >> 3;
    const int tt = (xcd >> 1) * 8 + (idx & 7), cg = (xcd & 1) * 4 + (idx >> 3);
    const int w = __builtin_amdgcn_readfirstlane(threadIdx.x >> 6), lane = threadIdx.x & 63;
    const int c = lane & 15, q = lane >> 4;
    const char* act = ws + WS_ACT + (size_t)(4 * tt) * 61440 + lane * 16;
    const char* wt = ws + WS_W2T + (size_t)(5 * cg) * 61440 + lane * 16;
    constexpr int DEPTH = 3;
    bf16x8 fa[DEPTH + 1][4], fw[DEPTH + 1][5];
    auto ld = [&](int st) {
        const int ks = st < 10 ? 10 * w + st : 40 + 5 * w + (st - 10), bu = st % (DEPTH + 1);
#pragma unroll
        for (int t = 0; t < 4; ++t) fa[bu][t] = *(const bf16x8*)(act + (size_t)t * 61440 + ks * 1024);
#pragma unroll
        for (int n = 0; n < 5; ++n) fw[bu][n] = *(const bf16x8*)(wt + (size_t)n * 61440 + ks * 1024);
    };
#pragma unroll
    for (int st = 0; st < DEPTH; ++st) ld(st);
    f32x2 gt[4];
#pragma unroll
    for (int t = 0; t < 4; ++t) gt[t] = ((const f32x2*)(ws + WS_G))[64 * tt + 16 * t + c];
    f32x4 ah[5][4], ao[5][4];
#pragma unroll
    for (int n = 0; n < 5; ++n)
#pragma unroll
        for (int t = 0; t < 4; ++t) ah[n][t] = ao[n][t] = (f32x4){0.f, 0.f, 0.f, 0.f};
#pragma unroll
    for (int st = 0; st < 15; ++st) {
        if (st + DEPTH < 15) ld(st + DEPTH);
        SCHED();
        const int bu = st % (DEPTH + 1);
#pragma unroll
        for (int n = 0; n < 5; ++n)
#pragma unroll
            for (int t = 0; t < 4; ++t) {
                if (st < 10) ah[n][t] = mfma(fw[bu][n], fa[bu][t], ah[n][t]);
                else ao[n][t] = mfma(fw[bu][n], fa[bu][t], ao[n][t]);
            }
        SCHED();
    }
    float* rw = red + w * (64 * 84);
#pragma unroll
    for (int t = 0; t < 4; ++t)
#pragma unroll
        for (int n = 0; n < 5; ++n)
            *(f32x4*)(rw + (16 * t + c) * 84 + 16 * n + 4 * q) = ah[n][t] * gt[t][0] + ao[n][t] * gt[t][1];
    __syncthreads();
    const unsigned short* xA = (const unsigned short*)xA_;
    const unsigned short* dsk = (const unsigned short*)dskip_;
#pragma unroll
    for (int i = 0; i < 5; ++i) {
        const int id = threadIdx.x + 256 * i, r = id / 20, c4 = id - 20 * r;
        const int tok = 64 * tt + r, n0 = 80 * cg + 4 * c4;
        const float* p = red + r * 84 + 4 * c4;
        f32x4 s = (*(const f32x4*)p + *(const f32x4*)(p + 64 * 84)) + (*(const f32x4*)(p + 128 * 84) + *(const f32x4*)(p + 192 * 84));
        const u32x2 xv = *(const u32x2*)(xA + tok * 640 + n0), dv = *(const u32x2*)(dsk + n0);
        const float g = ((const f32x2*)(ws + WS_G))[tok][0];
        const unsigned p01 = pk2(bf_lo(xv[0]) * bf_lo(dv[0]), bf_hi(xv[0]) * bf_hi(dv[0]));
        const unsigned p23 = pk2(bf_lo(xv[1]) * bf_lo(dv[1]), bf_hi(xv[1]) * bf_hi(dv[1]));
        s[0] += g * bf_lo(p01); s[1] += g * bf_hi(p01); s[2] += g * bf_lo(p23); s[3] += g * bf_hi(p23);
        *(f32x4*)(out + (size_t)tok * 640 + n0) = s;
    }
}

extern "C" void kernel_launch(void* const* d_in, const int* in_sizes, int n_in, void* d_out, int out_size, void* d_ws,
                              size_t ws_size, hipStream_t stream) {
    gm_k1<<<256, 512, 0, stream>>>(d_in[0], d_in[1], d_in[2], d_in[3], d_in[4], d_in[5], d_in[6], d_in[7], d_in[0],
                                   d_in[10], d_in[11], d_in[12], d_ws);
    gm_k2<<<256, 256, 0, stream>>>(d_in[0], d_in[8], d_ws, (float*)d_out);
}
```

```cpp
#include <hip/hip_runtime.h>
#include <hip/hip_bf16.h>
#include <stdint.h>

namespace {

typedef __attribute__((ext_vector_type(8))) short bf16x8;
typedef __attribute__((ext_vector_type(4))) short s16x4;
typedef __attribute__((ext_vector_type(4))) float f32x4;
typedef __attribute__((ext_vector_type(2))) float f32x2;
typedef __attribute__((ext_vector_type(2))) __bf16 bf16x2_t;
typedef __attribute__((ext_vector_type(4))) unsigned u32x4;
typedef __attribute__((ext_vector_type(2))) unsigned u32x2;
typedef unsigned long long u64;
typedef unsigned short u16;
#define LAS __attribute__((address_space(3)))
#define GAS __attribute__((address_space(1)))
#define DEV __device__ __forceinline__
#define RLX_AGENT __ATOMIC_RELAXED, __HIP_MEMORY_SCOPE_AGENT

constexpr size_t WS_NONCE = 0;
constexpr size_t WS_EA    = 4096;
constexpr size_t WS_KV    = WS_EA + 16 * 8 * 160 * 8;
constexpr size_t WS_TOT   = WS_KV + 16 * 8 * 840 * 8;
constexpr size_t WS_G     = WS_TOT + 8 * 840 * 8;
constexpr size_t WS_FACT  = WS_G + 2048 * 8;
constexpr size_t WS_FW2T  = WS_FACT + 128 * 64;
constexpr size_t WS_W2T   = (WS_FW2T + 128 * 64 + 1023) / 1024 * 1024;
constexpr size_t TS = 61440;
constexpr size_t WS_ACT   = WS_W2T + 40 * TS;
constexpr size_t WS_PROF  = 32u << 20;

#define STAMP(ws, wave, i) do {} while (0)
#define STAMPC(ws, wave, i) do {} while (0)
#define TIMED(acc, stmt) do { stmt; } while (0)
#define STAMPV(ws, wave, i, v) do {} while (0)
#define STAMP2(ws, wave, i) do {} while (0)

constexpr int LDS_BYTES = 163840;
constexpr int SLOT_A = 47104, NS_A = 3;
constexpr int A_TAB  = 141312;
constexpr int A_G    = A_TAB + 3840;
constexpr int A_ESEG = A_G + 1024;
constexpr int A_HSEG = A_ESEG + 5120;
constexpr int A_TOT  = A_HSEG + 5120;
constexpr int A_HT = 47104, HT_STRIDE = 272;
static_assert(A_TOT + 3360 <= 159776, "lds");
constexpr int SLOT_B = 36864, NS_B = 4;
constexpr int B_X1  = 147456;
constexpr int B_KVT = 73728;
constexpr int B_Z   = B_KVT + 2 * 80 * 84 * 4;
constexpr int B_STG = 0;
static_assert(B_Z + 640 <= 147456, "lds");

#define HEAD_A 4
#define IL_A 25
#define IL_B 16
constexpr unsigned SPIN_CAP = 1u << 18;

DEV unsigned pk2(float a, float b) {
    return __builtin_bit_cast(unsigned, __builtin_convertvector((f32x2){a, b}, bf16x2_t));
}
DEV float bfr(float a) { return __uint_as_float(pk2(a, 0.f) << 16); }
DEV float bf_lo(unsigned u) { return __uint_as_float(u << 16); }
DEV float bf_hi(unsigned u) { return __uint_as_float(u & 0xffff0000u); }
DEV float bf2f(u16 u) { return __uint_as_float((unsigned)u << 16); }
DEV float swp(float x) {
    return __builtin_bit_cast(float, __builtin_amdgcn_mov_dpp(__builtin_bit_cast(int, x), 0xB1, 0xF, 0xF, true));
}
DEV float cmul_add(float a, float bs, float e, float u) { return fmaf(bs, swp(e), fmaf(a, e, u)); }
DEV f32x4 mfma(bf16x8 a, bf16x8 b, f32x4 c) { return __builtin_amdgcn_mfma_f32_16x16x32_bf16(a, b, c, 0, 0, 0); }
DEV bf16x8 as_frag(u32x4 v) { return __builtin_bit_cast(bf16x8, v); }
DEV bf16x8 lds_frag(LAS char* p) { return *(LAS bf16x8*)p; }
DEV bf16x8 lds_tr_frag(LAS char* p0, LAS char* p1) {
    s16x4 lo = __builtin_amdgcn_ds_read_tr16_b64_v4i16((LAS s16x4*)p0);
    s16x4 hi = __builtin_amdgcn_ds_read_tr16_b64_v4i16((LAS s16x4*)p1);
    return __builtin_shufflevector(lo, hi, 0, 1, 2, 3, 4, 5, 6, 7);
}
DEV void glds16(const char* g, LAS char* l) {
    __builtin_amdgcn_global_load_lds((const GAS unsigned*)g, (LAS unsigned*)l, 16, 0, 0);
}
DEV void bar() { asm volatile("" ::: "memory"); __builtin_amdgcn_s_barrier(); asm volatile("" ::: "memory"); }
DEV void wait_lgkm0() { __builtin_amdgcn_s_waitcnt(0xC07F); }
DEV void lds_bar() { asm volatile("" ::: "memory"); wait_lgkm0(); bar(); }
constexpr int L_FULL = 159776, L_FREE = L_FULL + 16;
constexpr int L_DONE = L_FULL + 32;
DEV unsigned umin4(const u32x4& f) { const unsigned a = f[0] < f[1] ? f[0] : f[1], b = f[2] < f[3] ? f[2] : f[3]; return a < b ? a : b; }
DEV void ring_put(LAS char* p, unsigned v) {
    asm volatile("ds_write_b32 %0, %1" ::"v"((unsigned)(size_t)p), "v"(v) : "memory");
}
DEV void ring_wait(LAS char* p, unsigned need) {
    for (unsigned spins = 0; spins < SPIN_CAP; ++spins) {
        u32x4 f;
        asm volatile("ds_read_b128 %0, %1\n\ts_waitcnt lgkmcnt(0)" : "=&v"(f) : "v"((unsigned)(size_t)p) : "memory");
        if ((unsigned)__builtin_amdgcn_readfirstlane(umin4(f)) >= need) break;
        __builtin_amdgcn_s_sleep(1);
    }
}
DEV void ring_spin(LAS char* p, unsigned need) {
    for (unsigned spins = 0; spins < SPIN_CAP; ++spins) {
        const u32x4 f = *(volatile LAS u32x4*)p;
        if ((unsigned)__builtin_amdgcn_readfirstlane(umin4(f)) >= need) break;
    }
}
#define WAIT_VM(n) asm volatile("s_waitcnt vmcnt(" #n ")" ::: "memory")
#define SCHED() __builtin_amdgcn_sched_barrier(0)
template <int NM, int NR, int NS> DEV void interleave() {
#pragma unroll
    for (int i = 0; i < NS; ++i) {
        __builtin_amdgcn_sched_group_barrier(0x8, 1, 0);
        if ((i + 1) * NR / NS != i * NR / NS) __builtin_amdgcn_sched_group_barrier(0x100, 1, 0);
    }
    __builtin_amdgcn_sched_group_barrier(0x8, NM - NS, 0);
}
DEV unsigned mk_tag(unsigned n) { return ((n + 1u) * 0x9E3779B1u) | 1u; }
DEV u64 granule(unsigned tag, unsigned v) { return ((u64)tag << 32) | v; }
DEV u64 flag_of(unsigned tag) { return granule(tag, tag * 0x85EBCA6Bu + 0x5BD1E995u); }
DEV __amdgpu_buffer_rsrc_t rsrc(const char* p, unsigned bytes) { return __builtin_amdgcn_make_buffer_rsrc((void*)p, 0, bytes, 0x00020000); }
DEV void st16_wt(const __amdgpu_buffer_rsrc_t& r, unsigned voff, unsigned soff, u32x4 v) { __builtin_amdgcn_raw_buffer_store_b128(v, r, voff + soff, 0, 16); }
DEV void st8_wt(const __amdgpu_buffer_rsrc_t& r, unsigned voff, unsigned soff, u32x2 v) { __builtin_amdgcn_raw_buffer_store_b64(v, r, voff + soff, 0, 16); }
DEV bf16x8 ld16_wt(const __amdgpu_buffer_rsrc_t& r, unsigned voff, unsigned soff) {
    return __builtin_bit_cast(bf16x8, __builtin_amdgcn_raw_buffer_load_b128(r, voff, soff, 16));
}
DEV void signal_stores(LAS char* sm, char* flag, unsigned tag, int lane) {
    asm volatile("s_waitcnt vmcnt(0)" ::: "memory");
    if (lane == 0) {
        const unsigned old = __hip_atomic_fetch_add((LAS unsigned*)(sm + L_DONE), 1u, __ATOMIC_RELAXED, __HIP_MEMORY_SCOPE_WORKGROUP);
        if (old == 3u) __hip_atomic_store((GAS u64*)flag, flag_of(tag), RLX_AGENT);
    }
}
DEV float phi(float t) { return fmaxf(t, 0.f) + __expf(fminf(t, 0.f)) + 1e-6f; }
DEV bf16x8 pack_pair(const f32x4& lo, const f32x4& hi) {
    return as_frag((u32x4){pk2(lo[0], lo[1]), pk2(lo[2], lo[3]), pk2(hi[0], hi[1]), pk2(hi[2], hi[3])});
}

DEV void scan_step(float& u0, float& u1, float& u2, float& u3, float& u4, const float (&e)[5], const float (&a)[5],
                   const float (&b)[5]) {
    asm("v_fmac_f32 %0, %10, %5\n\tv_fmac_f32 %1, %11, %6\n\tv_fmac_f32 %2, %12, %7\n\t"
        "v_fmac_f32 %3, %13, %8\n\tv_fmac_f32 %4, %14, %9\n\t"
        "v_fmac_f32_dpp %0, %5, %15 quad_perm:[1,0,3,2] row_mask:0xf bank_mask:0xf bound_ctrl:1\n\t"
        "v_fmac_f32_dpp %1, %6, %16 quad_perm:[1,0,3,2] row_mask:0xf bank_mask:0xf bound_ctrl:1\n\t"
        "v_fmac_f32_dpp %2, %7, %17 quad_perm:[1,0,3,2] row_mask:0xf bank_mask:0xf bound_ctrl:1\n\t"
        "v_fmac_f32_dpp %3, %8, %18 quad_perm:[1,0,3,2] row_mask:0xf bank_mask:0xf bound_ctrl:1\n\t"
        "v_fmac_f32_dpp %4, %9, %19 quad_perm:[1,0,3,2] row_mask:0xf bank_mask:0xf bound_ctrl:1"
        : "+v"(u0), "+v"(u1), "+v"(u2), "+v"(u3), "+v"(u4)
        : "v"(e[0]), "v"(e[1]), "v"(e[2]), "v"(e[3]), "v"(e[4]), "v"(a[0]), "v"(a[1]), "v"(a[2]), "v"(a[3]), "v"(a[4]),
          "v"(b[0]), "v"(b[1]), "v"(b[2]), "v"(b[3]), "v"(b[4]));
}

DEV void scan_step_nd(float (&t)[5], float u0, float u1, float u2, float u3, float u4, const float (&e)[5],
                      const float (&a)[5], const float (&b)[5]) {
    asm("v_fma_f32 %0, %15, %10, %5\n\tv_fma_f32 %1, %16, %11, %6\n\tv_fma_f32 %2, %17, %12, %7\n\t"
        "v_fma_f32 %3, %18, %13, %8\n\tv_fma_f32 %4, %19, %14, %9\n\t"
        "v_fmac_f32_dpp %0, %10, %20 quad_perm:[1,0,3,2] row_mask:0xf bank_mask:0xf bound_ctrl:1\n\t"
        "v_fmac_f32_dpp %1, %11, %21 quad_perm:[1,0,3,2] row_mask:0xf bank_mask:0xf bound_ctrl:1\n\t"
        "v_fmac_f32_dpp %2, %12, %22 quad_perm:[1,0,3,2] row_mask:0xf bank_mask:0xf bound_ctrl:1\n\t"
        "v_fmac_f32_dpp %3, %13, %23 quad_perm:[1,0,3,2] row_mask:0xf bank_mask:0xf bound_ctrl:1\n\t"
        "v_fmac_f32_dpp %4, %14, %24 quad_perm:[1,0,3,2] row_mask:0xf bank_mask:0xf bound_ctrl:1"
        : "=&v"(t[0]), "=&v"(t[1]), "=&v"(t[2]), "=&v"(t[3]), "=&v"(t[4])
        : "v"(u0), "v"(u1), "v"(u2), "v"(u3), "v"(u4), "v"(e[0]), "v"(e[1]), "v"(e[2]), "v"(e[3]), "v"(e[4]),
          "v"(a[0]), "v"(a[1]), "v"(a[2]), "v"(a[3]), "v"(a[4]), "v"(b[0]), "v"(b[1]), "v"(b[2]), "v"(b[3]), "v"(b[4]));
}

struct Args {
    const char* x; const u16* lre; const u16* lim; const char* bre; const char* bim; const char* cre; const char* cim;
    const u16* logdt; const char* wqkv; const char* wo; const u16* gw; const u16* dskip; char* ws; float* out;
};

DEV void channel_consts(const Args& a, int p, float& ar, float& ai, float& cr, float& ci) {
    const float lr = bf2f(a.lre[p]), li = bf2f(a.lim[p]);
    const float dt = bfr(expf(bf2f(a.logdt[p])));
    const float zr = dt * lr, zi = dt * li, ex = expf(zr);
    ar = ex * cosf(zi); ai = ex * sinf(zi);
    const float nr = ar - 1.f, ni = ai, dn = lr * lr + li * li;
    cr = (nr * lr + ni * li) / dn; ci = (ni * lr - nr * li) / dn;
}

constexpr int P2_RED = 0;
#define P2_PW 6
#define P2_PA 3
DEV void p2_main(const Args& a, LAS char* sm, int lw, int lane, unsigned nonce) {
    const int b = blockIdx.x, xcd = b & 7, idx = b >> 3;
    const int tt = (xcd >> 1) * 8 + (idx & 7), cg = (xcd & 1) * 4 + (idx >> 3);
    const int c = lane & 15, q = lane >> 4;
    const unsigned tag = mk_tag(nonce);
    const u64 fl = flag_of(tag);
    constexpr int PW = P2_PW, PA = P2_PA;
    {
        GAS u64* fp = (GAS u64*)(a.ws + WS_FW2T) + lane * 8;
        for (unsigned spins = 0; spins < SPIN_CAP; ++spins) {
            const u64 v0 = __hip_atomic_load(fp, RLX_AGENT), v1 = __hip_atomic_load(fp + 512, RLX_AGENT);
            if (__all(v0 == fl && v1 == fl)) break;
            __builtin_amdgcn_s_sleep(2);
        }
        asm volatile("" ::: "memory");
    }
    STAMP2(a.ws, lw + 4, 0);
    const __amdgpu_buffer_rsrc_t rw = rsrc(a.ws + WS_W2T, 40 * TS), ra = rsrc(a.ws + WS_ACT, 128 * TS);
    const unsigned wbase = 5 * cg * TS + 15 * lw * 1024, abase = 4 * tt * TS + 15 * lw * 1024, vo = lane * 16;
    bf16x8 fw[PW][5], fa[PA][4];
    auto ldw = [&](int st) {
#pragma unroll
        for (int n = 0; n < 5; ++n) fw[st % PW][n] = ld16_wt(rw, vo, wbase + n * TS + st * 1024);
    };
    auto lda = [&](int st) {
#pragma unroll
        for (int t = 0; t < 4; ++t) fa[st % PA][t] = ld16_wt(ra, vo, abase + t * TS + st * 1024);
    };
#pragma unroll
    for (int st = 0; st < PW; ++st) ldw(st);
    SCHED();
    {
        GAS u64* fp = (GAS u64*)(a.ws + WS_FACT) + ((tt >> 1) * 8 + (lane & 7)) * 8;
        for (unsigned spins = 0; spins < SPIN_CAP; ++spins) {
            const u64 v = __hip_atomic_load(fp, RLX_AGENT);
            if (__all(v == fl)) break;
            __builtin_amdgcn_s_sleep(1);
        }
        asm volatile("" ::: "memory");
    }
    STAMP2(a.ws, lw + 4, 1);
    if (tt == 31 && cg == 0 && lw == 0 && lane == 0) __hip_atomic_store((GAS unsigned*)(a.ws + WS_NONCE), nonce + 1u, RLX_AGENT);
#pragma unroll
    for (int st = 0; st < PA - 1; ++st) lda(st);
    f32x4 acc[5][4];
#pragma unroll
    for (int n = 0; n < 5; ++n)
#pragma unroll
        for (int t = 0; t < 4; ++t) acc[n][t] = (f32x4){0.f, 0.f, 0.f, 0.f};
#pragma unroll
    for (int st = 0; st < 15; ++st) {
        if (st + PA - 1 < 15) lda(st + PA - 1);
        SCHED();
#pragma unroll
        for (int n = 0; n < 5; ++n)
#pragma unroll
            for (int t = 0; t < 4; ++t) acc[n][t] = mfma(fw[st % PW][n], fa[st % PA][t], acc[n][t]);
        SCHED();
        if (st + PW < 15) ldw(st + PW);
    }
    STAMP2(a.ws, lw + 4, 2);
    for (unsigned spins = 0; spins < SPIN_CAP; ++spins) {
        if ((unsigned)__builtin_amdgcn_readfirstlane(*(volatile LAS unsigned*)(sm + L_DONE)) >= 4u) break;
        __builtin_amdgcn_s_sleep(1);
    }
    asm volatile("" ::: "memory");
    LAS float* rw_ = (LAS float*)(sm + P2_RED) + lw * (64 * 84);
#pragma unroll
    for (int t = 0; t < 4; ++t)
#pragma unroll
        for (int n = 0; n < 5; ++n) *(LAS f32x4*)(rw_ + (16 * t + c) * 84 + 16 * n + 4 * q) = acc[n][t];
    lds_bar();
    STAMP2(a.ws, lw + 4, 3);
}

DEV void p2_final(const Args& a, LAS char* sm, int cw, int lane, unsigned tag) {
    const int b = blockIdx.x, xcd = b & 7, idx = b >> 3;
    const int tt = (xcd >> 1) * 8 + (idx & 7), cg = (xcd & 1) * 4 + (idx >> 3);
    const int tid = cw * 64 + lane;
    u32x2 xv[5], dv[5]; float gv[5];
    int tok[5], n0[5];
#pragma unroll
    for (int i = 0; i < 5; ++i) {
        const int id = tid + 256 * i, r = id / 20, c4 = id - 20 * r;
        tok[i] = 64 * tt + 16 * ((r & 15) >> 2) + 4 * (r >> 4) + (r & 3); n0[i] = 80 * cg + 4 * c4;
        xv[i] = *(const u32x2*)((const u16*)a.x + tok[i] * 640 + n0[i]);
        dv[i] = *(const u32x2*)(a.dskip + n0[i]);
    }
    for (unsigned spins = 0; spins < SPIN_CAP; ++spins) {
        u64 v[5];
#pragma unroll
        for (int i = 0; i < 5; ++i) v[i] = __hip_atomic_load((GAS u64*)(a.ws + WS_G) + tok[i], RLX_AGENT);
        bool ok = true;
#pragma unroll
        for (int i = 0; i < 5; ++i) { gv[i] = __uint_as_float((unsigned)v[i]); ok &= (unsigned)(v[i] >> 32) == tag; }
        if (__all(ok)) break;
        __builtin_amdgcn_s_sleep(2);
    }
    f32x4 sk[5];
#pragma unroll
    for (int i = 0; i < 5; ++i) {
        const unsigned p01 = pk2(bf_lo(xv[i][0]) * bf_lo(dv[i][0]), bf_hi(xv[i][0]) * bf_hi(dv[i][0]));
        const unsigned p23 = pk2(bf_lo(xv[i][1]) * bf_lo(dv[i][1]), bf_hi(xv[i][1]) * bf_hi(dv[i][1]));
        sk[i] = (f32x4){gv[i] * bf_lo(p01), gv[i] * bf_hi(p01), gv[i] * bf_lo(p23), gv[i] * bf_hi(p23)};
    }
    STAMP2(a.ws, cw, 0);
    bar();
    STAMP2(a.ws, cw, 2);
#pragma unroll
    for (int i = 0; i < 5; ++i) {
        const int id = tid + 256 * i, r = id / 20, c4 = id - 20 * r;
        LAS float* p = (LAS float*)(sm + P2_RED) + r * 84 + 4 * c4;
        const f32x4 s = ((*(LAS f32x4*)p + *(LAS f32x4*)(p + 64 * 84)) + (*(LAS f32x4*)(p + 128 * 84) + *(LAS f32x4*)(p + 192 * 84))) + sk[i];
        *(f32x4*)(a.out + (size_t)tok[i] * 640 + n0[i]) = s;
    }
    STAMP2(a.ws, cw, 3);
}

DEV void a_loader(const Args& a, LAS char* sm, int ti, int gj, int lw, int lane) {
    STAMP(a.ws, lw + 4, 0);
    const unsigned nonce = __hip_atomic_load((GAS unsigned*)(a.ws + WS_NONCE), RLX_AGENT);
    unsigned off[12];
    {
        const int pos = lane & 7, rl = lane >> 3, im = lw & 1;
#pragma unroll
        for (int u = 0; u < 4; ++u) {
            const int R = 8 * (lw + 4 * u) + rl, rho = R & 15;
            const int tok = (R & 64) + 16 * (rho >> 2) + 4 * ((R >> 4) & 3) + (rho & 3);
            off[u] = tok * 1280 + ((pos ^ (R & 7)) << 4);
        }
#pragma unroll
        for (int u = 4; u < 9; ++u) off[u] = (8 * ((lw + 4 * (u - 4)) >> 1) + rl) * 1280 + ((pos ^ rl ^ (im << 2)) << 4);
#pragma unroll
        for (int u = 9; u < 12; ++u) {
            const int ch = 64 * (lw + 4 * (u - 9)) + lane, rho = ch / 10, cc = ch - 10 * rho;
            off[u] = (rho ^ (((rho >> 3) & 1) << 2)) * 3840 + cc * 16;
        }
    }
    const char* xb = a.x + (size_t)ti * (128 * 1280);
    const char* bb = ((lw & 1) ? a.bim : a.bre) + (size_t)gj * (80 * 1280);
    const char* qb = a.wqkv + 160 * gj;
    const bool full = lw < 2;
    auto issue_a = [&](int s) {
        LAS char* dst = sm + (s % NS_A) * SLOT_A + lw * 1024;
#pragma unroll
        for (int u = 0; u < 4; ++u) glds16(xb + 128 * s + off[u], dst + 4096 * u);
#pragma unroll
        for (int u = 4; u < 6; ++u) glds16(bb + 128 * s + off[u], dst + 4096 * u);
    };
    auto issue_b = [&](int s) {
        LAS char* dst = sm + (s % NS_A) * SLOT_A + lw * 1024;
#pragma unroll
        for (int u = 6; u < 9; ++u) glds16(bb + 128 * s + off[u], dst + 4096 * u);
#pragma unroll
        for (int u = 9; u < 12; ++u) if (u < 11 || full) glds16(qb + (size_t)(64 * 3840) * s + off[u], dst + 4096 * u);
    };
    STAMP(a.ws, lw + 4, 1);
    issue_a(0); issue_b(0); issue_a(1); issue_b(1);
    if (full) WAIT_VM(12); else WAIT_VM(11);
    bar();
    u64 t_vm = 0, t_free = 0;
#pragma unroll 1
    for (int s = 0; s < 8; ++s) {
        if (s > 0) TIMED(t_free, ring_wait(sm + L_FREE, s));
        issue_a(s + 2);
        TIMED(t_vm, WAIT_VM(6));
        ring_put(sm + L_FULL + 4 * lw, s + 2);
        issue_b(s + 2);
    }
    WAIT_VM(0);
    ring_put(sm + L_FULL + 4 * lw, 10);
    STAMPV(a.ws, lw + 4, 14, t_vm); STAMPV(a.ws, lw + 4, 15, t_free);
    STAMP(a.ws, lw + 4, 2);
    const unsigned tag = mk_tag(nonce);
    const int tl = lw * 64 + lane;
    if (tl < 128) {
        GAS u64* gp = (GAS u64*)(a.ws + WS_G) + 128 * ti + tl;
        u64 v;
        for (unsigned spins = 0;;) {
            v = __hip_atomic_load(gp, RLX_AGENT);
            if (__all((unsigned)(v >> 32) == tag) || ++spins > SPIN_CAP) break;
            __builtin_amdgcn_s_sleep(1);
        }
        const float g = __uint_as_float((unsigned)v);
        ((LAS float*)(sm + A_G))[tl] = g;
        ((LAS float*)(sm + A_G))[128 + tl] = bfr(1.f - g);
    }
    lds_bar();
    STAMP(a.ws, lw + 4, 3);
    LAS float* tab = (LAS float*)(sm + A_TAB);
    GAS u64* ea = (GAS u64*)(a.ws + WS_EA) + gj * 160 + (tl < 160 ? tl : 0);
    float pre[8], a16 = 0.f, b16 = 0.f;
    if (tl < 160) {
        a16 = tab[2 * 160 + tl]; b16 = tab[3 * 160 + tl];
        LAS float* es = (LAS float*)(sm + A_ESEG);
        float P = 0.f;
#pragma unroll
        for (int s = 0; s < 8; ++s) { pre[s] = P; P = cmul_add(a16, b16, P, es[s * 160 + tl]); }
        __hip_atomic_store(ea + ti * 1280, granule(tag, __float_as_uint(P)), RLX_AGENT);
    }
    STAMP(a.ws, lw + 4, 4);
    const bool lb = lw < 3 && ti > 0;
    float ev[15];
    bool lb_ok = !lb;
    auto look_back = [&]() {
        u64 v[15];
#pragma unroll
        for (int i = 0; i < 15; ++i) v[i] = __hip_atomic_load(ea + (i < ti ? i : 0) * 1280, RLX_AGENT);
        bool ok = true;
#pragma unroll
        for (int i = 0; i < 15; ++i) { ev[i] = __uint_as_float((unsigned)v[i]); ok &= (unsigned)(v[i] >> 32) == tag; }
        lb_ok = __all(ok);
    };
    {
        const __amdgpu_buffer_rsrc_t rs = rsrc(a.ws + WS_TOT, 8 * 840 * 8);
        const int p1 = tl + 256 < 420 ? tl + 256 : tl;
        u32x4 v0, v1;
        for (unsigned spins = 0;;) {
            v0 = __builtin_amdgcn_raw_buffer_load_b128(rs, gj * 6720 + tl * 16, 0, 16);
            v1 = __builtin_amdgcn_raw_buffer_load_b128(rs, gj * 6720 + p1 * 16, 0, 16);
            if (!lb_ok) look_back();
            const bool ok = (v0[1] == tag) & (v0[3] == tag) & (v1[1] == tag) & (v1[3] == tag);
            if (__all(ok) || ++spins > SPIN_CAP) break;
            __builtin_amdgcn_s_sleep(1);
        }
        ((LAS u32x2*)(sm + A_TOT))[tl] = (u32x2){v0[0], v0[2]};
        ((LAS u32x2*)(sm + A_TOT))[p1] = (u32x2){v1[0], v1[2]};
    }
    STAMP(a.ws, lw + 4, 5);
    lds_bar();
    for (unsigned spins = 0; !lb_ok && spins < SPIN_CAP; ++spins) {
        look_back();
        if (!lb_ok) __builtin_amdgcn_s_sleep(1);
    }
    STAMP(a.ws, lw + 4, 6);
    if (tl < 160) {
        const float a128 = tab[4 * 160 + tl], b128 = tab[5 * 160 + tl];
        float H = 0.f;
#pragma unroll
        for (int i = 0; i < 15; ++i) if (i < ti) H = cmul_add(a128, b128, H, ev[i]);
        LAS float* hs = (LAS float*)(sm + A_HSEG);
#pragma unroll
        for (int s = 0; s < 8; ++s) { hs[s * 160 + tl] = H + pre[s]; H = cmul_add(a16, b16, H, 0.f); }
    }
    lds_bar();
    STAMP(a.ws, lw + 4, 7);
    bar();
    STAMP(a.ws, lw + 4, 8);
    p2_main(a, sm, lw, lane, nonce);
}

template <int NH>
DEV void a_pass1(f32x4 (&acc)[4][5], const float (&ca)[5], const float (&cb)[5], LAS float* es) {
    float e[5];
#pragma unroll
    for (int n = 0; n < 5; ++n) e[n] = acc[2 * NH][n][0];
#pragma unroll
    for (int t = 1; t < 16; ++t) {
        const int i = (t >> 2) ^ (2 * NH), r = t & 3;
        float y[5];
        scan_step_nd(y, acc[i][0][r], acc[i][1][r], acc[i][2][r], acc[i][3][r], acc[i][4][r], e, ca, cb);
#pragma unroll
        for (int n = 0; n < 5; ++n) e[n] = y[n];
    }
#pragma unroll
    for (int n = 0; n < 5; ++n) es[16 * n] = e[n];
}
template <int NH>
DEV void a_pass2(f32x4 (&acc)[4][5], const float (&ca)[5], const float (&cb)[5], LAS float* hs, LAS float* gt, LAS char* ht) {
    float e[5];
#pragma unroll
    for (int n = 0; n < 5; ++n) e[n] = hs[16 * n];
    asm volatile("s_nop 1" : "+v"(e[0]), "+v"(e[1]), "+v"(e[2]), "+v"(e[3]), "+v"(e[4]));
#pragma unroll
    for (int m = 0; m < 4; ++m) {
        const int i = m ^ (2 * NH);
        const f32x4 g = *(LAS f32x4*)(gt + 4 * m);
#pragma unroll
        for (int r = 0; r < 4; ++r) {
            float u0 = acc[i][0][r], u1 = acc[i][1][r], u2 = acc[i][2][r], u3 = acc[i][3][r], u4 = acc[i][4][r];
            scan_step(u0, u1, u2, u3, u4, e, ca, cb);
            e[0] = u0; e[1] = u1; e[2] = u2; e[3] = u3; e[4] = u4;
            acc[i][0][r] = u0 * g[r]; acc[i][1][r] = u1 * g[r]; acc[i][2][r] = u2 * g[r]; acc[i][3][r] = u3 * g[r]; acc[i][4][r] = u4 * g[r];
        }
#pragma unroll
        for (int n = 0; n < 5; ++n)
            *(LAS u32x2*)(ht + 16 * n * HT_STRIDE + 32 * m) = (u32x2){pk2(acc[i][n][0], acc[i][n][1]), pk2(acc[i][n][2], acc[i][n][3])};
    }
}

DEV void a_consumer(const Args& a, LAS char* sm, int ti, int gj, int cw, int lane) {
    const int mh = cw >> 1, nh = cw & 1, c = lane & 15, q = lane >> 4, tid = cw * 64 + lane;
    STAMP(a.ws, cw, 0);
    const unsigned nonce = __hip_atomic_load((GAS unsigned*)(a.ws + WS_NONCE), RLX_AGENT);
    const unsigned tag = mk_tag(nonce);
    if (cw >= 2) {
        const int tok = 128 * ti + 16 * gj + 8 * (cw - 2) + (lane >> 3);
        const u32x4* xr = (const u32x4*)(a.x + (size_t)tok * 1280) + (lane & 7);
        const u32x4* gr = (const u32x4*)a.gw + (lane & 7);
        u32x4 xv[10], gv[10];
#pragma unroll
        for (int i = 0; i < 10; ++i) { xv[i] = xr[8 * i]; gv[i] = gr[8 * i]; }
        float s = 0.f;
#pragma unroll
        for (int i = 0; i < 10; ++i)
#pragma unroll
            for (int k = 0; k < 4; ++k) {
                const unsigned pr = pk2(bf_lo(xv[i][k]) * bf_lo(gv[i][k]), bf_hi(xv[i][k]) * bf_hi(gv[i][k]));
                s += bf_lo(pr); s += bf_hi(pr);
            }
        s += __shfl_xor(s, 1); s += __shfl_xor(s, 2); s += __shfl_xor(s, 4);
        const float lg = bfr(s), g = bfr(1.f / (1.f + expf(-lg)));
        if ((lane & 7) == 0) __hip_atomic_store((GAS u64*)(a.ws + WS_G) + tok, granule(tag, __float_as_uint(g)), RLX_AGENT);
    } else if (tid < 80) {
        float ar, ai, cr, ci;
        channel_consts(a, 80 * gj + tid, ar, ai, cr, ci);
        float pr = ar, pi = ai;
#pragma unroll
        for (int k = 0; k < 4; ++k) { const float t = pr * pr - pi * pi; pi = 2.f * pr * pi; pr = t; }
        const float r16 = pr, i16 = pi;
#pragma unroll
        for (int k = 0; k < 3; ++k) { const float t = pr * pr - pi * pi; pi = 2.f * pr * pi; pr = t; }
        LAS f32x2* tab = (LAS f32x2*)(sm + A_TAB);
        tab[0 * 80 + tid] = (f32x2){ar, ar};
        tab[1 * 80 + tid] = (f32x2){-ai, ai};
        tab[2 * 80 + tid] = (f32x2){r16, r16};
        tab[3 * 80 + tid] = (f32x2){-i16, i16};
        tab[4 * 80 + tid] = (f32x2){pr, pr};
        tab[5 * 80 + tid] = (f32x2){-pi, pi};
    }
    if (tid < 160) __hip_atomic_store((GAS u64*)(a.ws + WS_EA) + (ti * 8 + gj) * 160 + tid, (u64)0, RLX_AGENT);
    __builtin_amdgcn_s_setprio(1);
    f32x4 acc[4][5], at[5][2];
#pragma unroll
    for (int n = 0; n < 5; ++n) {
#pragma unroll
        for (int i = 0; i < 4; ++i) acc[i][n] = (f32x4){0.f, 0.f, 0.f, 0.f};
        at[n][0] = at[n][1] = (f32x4){0.f, 0.f, 0.f, 0.f};
    }
    int xlo[2], xhi[2], wa[2], wq[2];
#pragma unroll
    for (int kk = 0; kk < 2; ++kk) {
        const int sw = ((4 * kk + q) ^ (c & 7)) << 4;
        xlo[kk] = (64 * mh + 32 * nh + c) * 128 + sw;
        xhi[kk] = (64 * mh + 32 * (1 - nh) + c) * 128 + sw;
        wa[kk] = 16384 + (80 * nh + 8 * (c & 1) + (c >> 1)) * 128 + (((4 * kk + q) ^ (c >> 1) ^ ((c & 1) << 2)) << 4);
        wq[kk] = 36864 + (8 * q + 4 * (kk ^ (q & 1)) + (c >> 2)) * 160 + 8 * (c & 3);
    }
    bf16x8 xa[2][4], wb[2][5], wt[2][5];
    auto rdp = [&](int buf, LAS char* s0, int kk) {
        LAS char* q0 = s0 + wq[0] + kk * 5120; LAS char* q1 = s0 + wq[1] + kk * 5120;
        xa[buf][0] = lds_frag(s0 + xlo[kk]);
        xa[buf][1] = lds_frag(s0 + xlo[kk] + 2048);
        wt[buf][0] = lds_tr_frag(q0, q1);
        xa[buf][2] = lds_frag(s0 + xhi[kk]);
        xa[buf][3] = lds_frag(s0 + xhi[kk] + 2048);
        wt[buf][1] = lds_tr_frag(q0 + 32, q1 + 32);
        wb[buf][0] = lds_frag(s0 + wa[kk]);
        wb[buf][1] = lds_frag(s0 + wa[kk] + 2048);
        wt[buf][2] = lds_tr_frag(q0 + 64, q1 + 64);
        wb[buf][2] = lds_frag(s0 + wa[kk] + 4096);
        wb[buf][3] = lds_frag(s0 + wa[kk] + 6144);
        wt[buf][3] = lds_tr_frag(q0 + 96, q1 + 96);
        wb[buf][4] = lds_frag(s0 + wa[kk] + 8192);
        wt[buf][4] = lds_tr_frag(q0 + 128, q1 + 128);
    };
    auto rd = [&](int buf, int slot, int kk) { rdp(buf, sm + slot * SLOT_A, kk); };
    auto mm_head = [&](int buf) {
#pragma unroll
        for (int n = 0; n < HEAD_A / 4; ++n)
#pragma unroll
            for (int i = 0; i < 4; ++i) acc[i][n] = mfma(xa[buf][i], wb[buf][n], acc[i][n]);
    };
    auto mm_tail = [&](int buf) {
#pragma unroll
        for (int n = HEAD_A / 4; n < 5; ++n)
#pragma unroll
            for (int i = 0; i < 4; ++i) acc[i][n] = mfma(xa[buf][i], wb[buf][n], acc[i][n]);
#pragma unroll
        for (int n = 0; n < 5; ++n)
#pragma unroll
            for (int i = 0; i < 2; ++i) at[n][i] = mfma(wt[buf][n], xa[buf][i], at[n][i]);
    };
    if (tid < 9) ((LAS unsigned*)(sm + L_FULL))[tid] = tid < 4 ? 1u : 0u;
#pragma unroll
    for (int n = 0; n < 5; ++n)
        asm volatile("" : "+v"(acc[0][n]), "+v"(acc[1][n]), "+v"(acc[2][n]), "+v"(acc[3][n]), "+v"(at[n][0]), "+v"(at[n][1]));
    STAMP(a.ws, cw, 1);
    lds_bar();
    STAMP(a.ws, cw, 2);
    STAMPC(a.ws, cw, 12);
    rd(0, 0, 0);
    int slot = 0;
    u64 t_spin = 0, n_spin = 0;
#pragma unroll 1
    for (int s = 0; s < 10; ++s) {
        wait_lgkm0();
        SCHED();
        rd(1, slot, 1);
        u32x4 fl = *(volatile LAS u32x4*)(sm + L_FULL);
        mm_head(0); mm_tail(0);
        interleave<30, 20, IL_A>();
        SCHED();
        wait_lgkm0();
        asm volatile("" ::: "memory");
        *(volatile LAS unsigned*)(sm + L_FREE + 4 * cw) = s + 1;
        slot = slot == 2 ? 0 : slot + 1;
        const unsigned need = s < 9 ? s + 2 : 0;
        unsigned landed = __builtin_amdgcn_readfirstlane(umin4(fl));
        LAS char* s0 = sm + slot * SLOT_A;
        asm volatile("" : "+s"(landed), "+v"(s0));
        mm_head(1);
        SCHED();
        if (__builtin_expect(landed < need, 0)) { TIMED(t_spin, ring_spin(sm + L_FULL, need)); n_spin++; }
        asm volatile("" ::: "memory");
        SCHED();
        rdp(0, s0, 0);
        mm_tail(1);
        interleave<30 - HEAD_A, 19, IL_A - HEAD_A + 1>();
        SCHED();
    }
    __builtin_amdgcn_s_setprio(0);
    asm volatile("s_nop 7\n\ts_nop 7" ::: "memory");
    STAMP(a.ws, cw, 3);
    STAMPC(a.ws, cw, 13);
    STAMPV(a.ws, cw, 14, t_spin); STAMPV(a.ws, cw, 15, n_spin);
    LAS float* tab = (LAS float*)(sm + A_TAB);
    float ca[5], cb[5];
#pragma unroll
    for (int n = 0; n < 5; ++n) { const int col = 80 * nh + 16 * n + c; ca[n] = tab[col]; cb[n] = tab[160 + col]; }
    {
        LAS float* es = (LAS float*)(sm + A_ESEG) + (4 * mh + q) * 160 + 80 * nh + c;
        if (nh == 0) a_pass1<0>(acc, ca, cb, es); else a_pass1<1>(acc, ca, cb, es);
    }
    WAIT_VM(0);
    STAMP(a.ws, cw, 4);
    lds_bar();
    bf16x8 P[2][3];
#pragma unroll
    for (int i = 0; i < 2; ++i) {
#pragma unroll
        for (int t = 0; t < 5; ++t)
#pragma unroll
            for (int r = 0; r < 4; ++r) at[t][i][r] = phi(at[t][i][r]);
        P[i][0] = pack_pair(at[0][i], at[1][i]);
        P[i][1] = pack_pair(at[2][i], at[3][i]);
        P[i][2] = as_frag((u32x4){pk2(at[4][i][0], at[4][i][1]), pk2(at[4][i][2], at[4][i][3]), 0u, 0u});
    }
    int dh[5], db[5], eh[5], eb[5];
#pragma unroll
    for (int k = 0; k < 5; ++k) {
        const int d0 = 16 * k + 4 * q, e0 = 16 * k + c;
        dh[k] = d0 / 20; db[k] = dh[k] * 840 + (d0 - 20 * dh[k]) * 2;
        eh[k] = e0 / 20; eb[k] = (e0 - 20 * eh[k]) * 40;
    }
    const __amdgpu_buffer_rsrc_t ract = rsrc(a.ws + WS_ACT, 128 * TS);
    STAMP(a.ws, cw, 5);
    lds_bar();
    STAMP(a.ws, cw, 6);
    {
        LAS char* tot = sm + A_TOT;
        auto half = [&](int k, int he, int eoff) -> u32x2 {
            const bool on = dh[k] == he;
            const u32x2 v = *(LAS u32x2*)(tot + (on ? db[k] + eoff : 0));
            return on ? v : (u32x2){0u, 0u};
        };
        auto frag = [&](int Pp, int he, int eoff) -> bf16x8 {
            const u32x2 lo = half(2 * Pp, he, eoff);
            const u32x2 hi = Pp < 2 ? half(2 * Pp + 1, he, eoff) : (u32x2){0u, 0u};
            return as_frag((u32x4){lo[0], lo[1], hi[0], hi[1]});
        };
        const bf16x8 F0 = frag(0, eh[0], eb[0]), F1 = frag(0, eh[1], eb[1]), F2 = frag(1, eh[1], eb[1]);
        const bf16x8 F3 = frag(0, eh[2], eb[2]), F4 = frag(1, eh[2], eb[2]), F5 = frag(1, eh[3], eb[3]);
        const bf16x8 F6 = frag(2, eh[3], eb[3]), F7 = frag(1, eh[4], eb[4]), F8 = frag(2, eh[4], eb[4]);
        const bf16x8 Z0 = frag(0, c & 3, 800), Z1 = frag(1, c & 3, 800), Z2 = frag(2, c & 3, 800);
        const f32x4 z4 = {0.f, 0.f, 0.f, 0.f};
        const int uu = gj >> 1, odd = gj & 1;
#pragma unroll
        for (int i = 0; i < 2; ++i) {
            const f32x4 n0 = mfma(F0, P[i][0], z4);
            const f32x4 n1 = mfma(F2, P[i][1], mfma(F1, P[i][0], z4));
            const f32x4 n2 = mfma(F4, P[i][1], mfma(F3, P[i][0], z4));
            const f32x4 n3 = mfma(F6, P[i][2], mfma(F5, P[i][1], z4));
            const f32x4 n4 = mfma(F8, P[i][2], mfma(F7, P[i][1], z4));
            const f32x4 dn = mfma(Z2, P[i][2], mfma(Z1, P[i][1], mfma(Z0, P[i][0], z4)));
            const int m = 2 * nh + i;
            const float og = ((LAS float*)(sm + A_G))[128 + 64 * mh + 16 * (c >> 2) + 4 * m + (c & 3)];
            const float r0 = og * __builtin_amdgcn_rcpf(dn[0] + 1e-6f), r1 = og * __builtin_amdgcn_rcpf(dn[1] + 1e-6f);
            const float r2 = og * __builtin_amdgcn_rcpf(dn[2] + 1e-6f), r3 = og * __builtin_amdgcn_rcpf(dn[3] + 1e-6f);
            const float s1 = q < 1 ? r0 : r1, s2 = q < 2 ? r1 : r2, s3 = q < 3 ? r2 : r3;
            const unsigned ob = (8 * ti + 4 * mh + m) * TS + (40 + 5 * uu) * 1024;
            st16_wt(ract, lane * 16, ob + (odd ? 3072 : 0),
                    (u32x4){pk2(n0[0] * r0, n0[1] * r0), pk2(n0[2] * r0, n0[3] * r0), pk2(n1[0] * s1, n1[1] * s1), pk2(n1[2] * s1, n1[3] * s1)});
            st16_wt(ract, lane * 16, ob + (odd ? 4096 : 1024),
                    (u32x4){pk2(n2[0] * s2, n2[1] * s2), pk2(n2[2] * s2, n2[3] * s2), pk2(n3[0] * s3, n3[1] * s3), pk2(n3[2] * s3, n3[3] * s3)});
            st8_wt(ract, lane * 16 + 8 * odd, ob + 2048, (u32x2){pk2(n4[0] * r3, n4[1] * r3), pk2(n4[2] * r3, n4[3] * r3)});
        }
    }
    STAMP(a.ws, cw, 7);
    lds_bar();
    STAMP(a.ws, cw, 8);
    {
        LAS float* hs = (LAS float*)(sm + A_HSEG) + (4 * mh + q) * 160 + 80 * nh + c;
        LAS float* gt = (LAS float*)(sm + A_G) + 64 * mh + 16 * q;
        LAS char* ht = sm + A_HT + (80 * nh + c) * HT_STRIDE + (64 * mh + 4 * q) * 2;
        if (nh == 0) a_pass2<0>(acc, ca, cb, hs, gt, ht); else a_pass2<1>(acc, ca, cb, hs, gt, ht);
    }
    STAMP(a.ws, cw, 9);
    lds_bar();
    {
        const int qq = (lane & 15) >> 2, p = lane & 3;
#pragma unroll
        for (int i = 0; i < 10; ++i) {
            const int f = cw + 4 * i, mt = f / 5, ks = f - 5 * mt;
            LAS char* b = sm + A_HT + (32 * ks + 8 * q + qq) * HT_STRIDE + 32 * mt + 8 * p;
            st16_wt(ract, lane * 16, (8 * ti + mt) * TS + (5 * gj + ks) * 1024,
                    __builtin_bit_cast(u32x4, lds_tr_frag(b, b + 4 * HT_STRIDE)));
        }
    }
    wait_lgkm0();
    STAMP(a.ws, cw, 10);
    signal_stores(sm, a.ws + WS_FACT + (ti * 8 + gj) * 64, tag, lane);
    STAMP(a.ws, cw, 11);
    p2_final(a, sm, cw, lane, tag);
}

DEV void b_loader(const Args& a, LAS char* sm, int ti, int gj, int lw, int lane) {
    STAMP(a.ws, lw + 4, 0);
    const unsigned nonce = __hip_atomic_load((GAS unsigned*)(a.ws + WS_NONCE), RLX_AGENT);
    unsigned off[9];
    {
        const int pos = lane & 7, rl = lane >> 3;
#pragma unroll
        for (int u = 0; u < 4; ++u) { const int R = 8 * (lw + 4 * u) + rl; off[u] = R * 1280 + ((pos ^ (R & 7)) << 4); }
#pragma unroll
        for (int u = 4; u < 9; ++u) {
            const int wi = lw + 4 * (u - 4), sec = wi >= 10, ch = 64 * (wi - 10 * sec) + lane, rho = ch / 10, cc = ch - 10 * rho;
            off[u] = (rho ^ (((rho >> 3) & 1) << 2)) * 3840 + cc * 16 + 1280 + 1280 * sec;
        }
    }
    const char* xb = a.x + (size_t)ti * (128 * 1280);
    const char* wb = a.wqkv + 160 * gj;
    auto issue_a = [&](int s) {
        LAS char* dst = sm + (s % NS_B) * SLOT_B + lw * 1024;
#pragma unroll
        for (int u = 0; u < 4; ++u) glds16(xb + 128 * s + off[u], dst + 4096 * u);
        glds16(wb + (size_t)(64 * 3840) * s + off[4], dst + 4096 * 4);
    };
    auto issue_b = [&](int s) {
        LAS char* dst = sm + (s % NS_B) * SLOT_B + lw * 1024;
#pragma unroll
        for (int u = 5; u < 9; ++u) glds16(wb + (size_t)(64 * 3840) * s + off[u], dst + 4096 * u);
    };
    STAMP(a.ws, lw + 4, 1);
    issue_a(0); issue_b(0); issue_a(1); issue_b(1); issue_a(2); issue_b(2);
    WAIT_VM(18);
    bar();
#pragma unroll 1
    for (int s = 0; s < 7; ++s) {
        if (s > 0) ring_wait(sm + L_FREE, s);
        issue_a(s + 3);
        WAIT_VM(14);
        ring_put(sm + L_FULL + 4 * lw, s + 2);
        issue_b(s + 3);
    }
    WAIT_VM(9);
    ring_put(sm + L_FULL + 4 * lw, 9);
    WAIT_VM(0);
    ring_put(sm + L_FULL + 4 * lw, 10);
    STAMP(a.ws, lw + 4, 2);
    bar();
    bar();
    STAMP(a.ws, lw + 4, 3);
    const unsigned tag = mk_tag(nonce);
    const int tl = lw * 64 + lane;
    {
        LAS float* kvt = (LAS float*)(sm + B_KVT);
        LAS float* zz = (LAS float*)(sm + B_Z);
        GAS u64* dst = (GAS u64*)(a.ws + WS_KV) + (ti * 8 + gj) * 840;
#pragma unroll
        for (int it = 0; it < 4; ++it) {
            const int g = tl + 256 * it;
            if (g < 840) {
                const int h = g / 210, rem = g - 210 * h;
                f32x2 v;
                if (rem < 200) {
                    const int e = rem / 10, d = 2 * (rem - 10 * e), o = (20 * h + e) * 84 + 20 * h + d;
                    v = *(LAS f32x2*)(kvt + o) + *(LAS f32x2*)(kvt + 80 * 84 + o);
                } else {
                    const int o = 20 * h + 2 * (rem - 200);
                    v = *(LAS f32x2*)(zz + o) + *(LAS f32x2*)(zz + 80 + o);
                }
                __hip_atomic_store(dst + g, granule(tag, pk2(v[0], v[1])), RLX_AGENT);
            }
        }
    }
    STAMP(a.ws, lw + 4, 4);
    if (lw == 0) {
        const int cnt = ti < 8 ? 53 : 52, g0 = ti < 8 ? 53 * ti : 424 + 52 * (ti - 8);
        const int g = g0 + (lane < cnt ? lane : 0);
        GAS u64* src = (GAS u64*)(a.ws + WS_KV) + gj * 840 + g;
        unsigned v[16];
        for (unsigned spins = 0;;) {
            bool ok = true;
#pragma unroll
            for (int i = 0; i < 16; ++i) {
                const u64 x = __hip_atomic_load(src + i * (8 * 840), RLX_AGENT);
                v[i] = (unsigned)x; ok &= (unsigned)(x >> 32) == tag;
            }
            if (__all(ok) || ++spins > SPIN_CAP) break;
            __builtin_amdgcn_s_sleep(1);
        }
        float s0 = 0.f, s1 = 0.f;
#pragma unroll
        for (int i = 0; i < 16; ++i) { s0 += bf_lo(v[i]); s1 += bf_hi(v[i]); }
        if (lane < cnt) __hip_atomic_store((GAS u64*)(a.ws + WS_TOT) + gj * 840 + g, granule(tag, pk2(s0, s1)), RLX_AGENT);
    }
    STAMP(a.ws, lw + 4, 5);
    p2_main(a, sm, lw, lane, nonce);
}

DEV void b_retile(const Args& a, LAS char* stg, int jb, int lane) {
    const int c = lane & 15, q = lane >> 4, rl = lane >> 3, pos = lane & 7;
    const __amdgpu_buffer_rsrc_t rw = rsrc(a.ws + WS_W2T, 40 * TS);
    if (jb < 400) {
        const int cb = jb / 40, nt = jb - 40 * cb;
        const unsigned o = (16 * nt + rl) * 1280 + cb * 128 + pos * 16;
        glds16(a.cre + o, stg); glds16(a.cre + o + 8 * 1280, stg + 1024);
        glds16(a.cim + o, stg + 2048); glds16(a.cim + o + 8 * 1280, stg + 3072);
        float ar, ai, cr, ci;
        channel_consts(a, 64 * cb + lane, ar, ai, cr, ci);
        ((LAS f32x2*)(stg + 4096))[lane] = (f32x2){2.f * cr, 2.f * ci};
        WAIT_VM(0);
        wait_lgkm0();
#pragma unroll
        for (int k = 0; k < 4; ++k) {
            const u32x2 re = *(LAS u32x2*)(stg + c * 128 + 32 * k + 8 * q), im = *(LAS u32x2*)(stg + 2048 + c * 128 + 32 * k + 8 * q);
            const f32x4 c01 = *(LAS f32x4*)(stg + 4096 + (16 * k + 4 * q) * 8), c23 = *(LAS f32x4*)(stg + 4096 + (16 * k + 4 * q) * 8 + 16);
            const float r0 = bf_lo(re[0]), r1 = bf_hi(re[0]), r2 = bf_lo(re[1]), r3 = bf_hi(re[1]);
            const float i0 = bf_lo(im[0]), i1 = bf_hi(im[0]), i2 = bf_lo(im[1]), i3 = bf_hi(im[1]);
            const u32x4 w = {pk2(r0 * c01[0] - i0 * c01[1], -(r0 * c01[1] + i0 * c01[0])),
                             pk2(r1 * c01[2] - i1 * c01[3], -(r1 * c01[3] + i1 * c01[2])),
                             pk2(r2 * c23[0] - i2 * c23[1], -(r2 * c23[1] + i2 * c23[0])),
                             pk2(r3 * c23[2] - i3 * c23[3], -(r3 * c23[3] + i3 * c23[2]))};
            st16_wt(rw, lane * 16, nt * TS + (4 * cb + k) * 1024, w);
        }
    } else {
        const int jo = jb - 400, s5 = jo / 10, nq = jo - 10 * s5, uu = s5 / 5, s = s5 - 5 * uu;
        const int lo = 160 * uu + (s == 0 ? 0 : s == 1 ? 32 : s == 2 ? 64 : s == 3 ? 80 : 112);
        const int hi = 160 * uu + (s == 0 ? 16 : s == 1 ? 48 : s == 2 ? 144 : s == 3 ? 96 : 128);
        const unsigned o = rl * 1280 + nq * 128 + pos * 16;
        glds16(a.wo + lo * 1280 + o, stg); glds16(a.wo + (lo + 8) * 1280 + o, stg + 1024);
        glds16(a.wo + hi * 1280 + o, stg + 2048); glds16(a.wo + (hi + 8) * 1280 + o, stg + 3072);
        WAIT_VM(0);
#pragma unroll
        for (int k = 0; k < 4; ++k) {
            LAS char* b = stg + (4 * q + (c >> 2)) * 128 + 32 * k + 8 * (c & 3);
            st16_wt(rw, lane * 16, (4 * nq + k) * TS + (40 + s5) * 1024, __builtin_bit_cast(u32x4, lds_tr_frag(b, b + 2048)));
        }
    }
    wait_lgkm0();
    asm volatile("" ::: "memory");
}

DEV void b_consumer(const Args& a, LAS char* sm, int ti, int gj, int cw, int lane) {
    const int mh = cw >> 1, nh = cw & 1, c = lane & 15, q = lane >> 4, tid = cw * 64 + lane;
    STAMP(a.ws, cw, 0);
    const unsigned tag = mk_tag(__hip_atomic_load((GAS unsigned*)(a.ws + WS_NONCE), RLX_AGENT));
    {
        const __amdgpu_buffer_rsrc_t rk = __builtin_amdgcn_make_buffer_rsrc(a.ws + WS_KV, 0, 16 * 8 * 840 * 8, 0x00020000);
        const u32x4 z = {0u, 0u, 0u, 0u};
        __builtin_amdgcn_raw_buffer_store_b128(z, rk, (ti * 8 + gj) * 6720 + tid * 16, 0, 16);
        if (tid + 256 < 420) __builtin_amdgcn_raw_buffer_store_b128(z, rk, (ti * 8 + gj) * 6720 + (tid + 256) * 16, 0, 16);
        const int cnt = ti < 8 ? 53 : 52, g0 = ti < 8 ? 53 * ti : 424 + 52 * (ti - 8);
        if (tid < cnt) __hip_atomic_store((GAS u64*)(a.ws + WS_TOT) + gj * 840 + g0 + tid, (u64)0, RLX_AGENT);
    }
    __builtin_amdgcn_s_setprio(1);
    f32x4 an[5][4];
#pragma unroll
    for (int i = 0; i < 4; ++i)
#pragma unroll
        for (int t = 0; t < 5; ++t) an[t][i] = (f32x4){0.f, 0.f, 0.f, 0.f};
    int xo[2], wA[2], wB[2];
#pragma unroll
    for (int kk = 0; kk < 2; ++kk) {
        xo[kk] = (64 * mh + c) * 128 + (((4 * kk + q) ^ (c & 7)) << 4);
        const int b = 16384 + (8 * q + 4 * (kk ^ (q & 1)) + (c >> 2)) * 160 + 8 * (c & 3);
        wA[kk] = b + 96 * nh; wB[kk] = b + 64 + 10240 * nh;
    }
    bf16x8 xa[2][4], wb[2][5];
    auto rdp = [&](int buf, LAS char* s0, int kk) {
#pragma unroll
        for (int i = 0; i < 4; ++i) xa[buf][i] = lds_frag(s0 + xo[kk] + 2048 * i);
        LAS char* w0 = s0 + kk * 5120;
        wb[buf][0] = lds_tr_frag(w0 + wA[0], w0 + wA[1]);
        wb[buf][1] = lds_tr_frag(w0 + wA[0] + 32, w0 + wA[1] + 32);
        wb[buf][2] = lds_tr_frag(w0 + wB[0], w0 + wB[1]);
        wb[buf][3] = lds_tr_frag(w0 + wA[0] + 10240, w0 + wA[1] + 10240);
        wb[buf][4] = lds_tr_frag(w0 + wA[0] + 10272, w0 + wA[1] + 10272);
    };
    auto rd = [&](int buf, int slot, int kk) { rdp(buf, sm + slot * SLOT_B, kk); };
    auto mm_head = [&](int buf) {
#pragma unroll
        for (int i = 0; i < 4; ++i) an[0][i] = mfma(xa[buf][i], wb[buf][0], an[0][i]);
    };
    auto mm_tail = [&](int buf) {
#pragma unroll
        for (int t = 1; t < 5; ++t)
#pragma unroll
            for (int i = 0; i < 4; ++i) an[t][i] = mfma(xa[buf][i], wb[buf][t], an[t][i]);
    };
    if (tid < 9) ((LAS unsigned*)(sm + L_FULL))[tid] = tid < 4 ? 1u : 0u;
#pragma unroll
    for (int t = 0; t < 5; ++t)
        asm volatile("" : "+v"(an[t][0]), "+v"(an[t][1]), "+v"(an[t][2]), "+v"(an[t][3]));
    STAMP(a.ws, cw, 1);
    lds_bar();
    STAMP(a.ws, cw, 2);
    rd(0, 0, 0);
#pragma unroll 1
    for (int s = 0; s < 10; ++s) {
        wait_lgkm0();
        SCHED();
        rd(1, s & 3, 1);
        u32x4 fl = *(volatile LAS u32x4*)(sm + L_FULL);
        mm_head(0); mm_tail(0);
        interleave<20, 15, IL_B>();
        SCHED();
        wait_lgkm0();
        asm volatile("" ::: "memory");
        *(volatile LAS unsigned*)(sm + L_FREE + 4 * cw) = s + 1;
        const unsigned need = s < 9 ? s + 2 : 0;
        unsigned landed = __builtin_amdgcn_readfirstlane(umin4(fl));
        LAS char* s0 = sm + ((s + 1) & 3) * SLOT_B;
        asm volatile("" : "+s"(landed), "+v"(s0));
        mm_head(1);
        SCHED();
        if (__builtin_expect(landed < need, 0)) ring_spin(sm + L_FULL, need);
        asm volatile("" ::: "memory");
        SCHED();
        rdp(0, s0, 0);
        mm_tail(1);
        interleave<16, 14, 14>();
        SCHED();
    }
    __builtin_amdgcn_s_setprio(0);
    STAMP(a.ws, cw, 3);
    bf16x8 fr[5][2];
#pragma unroll
    for (int t = 0; t < 2; ++t)
#pragma unroll
        for (int i = 0; i < 4; ++i)
#pragma unroll
            for (int r = 0; r < 4; ++r) an[t][i][r] = phi(an[t][i][r]);
    if (nh == 0) {
#pragma unroll
        for (int i = 0; i < 4; ++i)
#pragma unroll
            for (int r = 0; r < 4; ++r) an[2][i][r] = phi(an[2][i][r]);
    }
#pragma unroll
    for (int t = 0; t < 5; ++t) { fr[t][0] = pack_pair(an[t][0], an[t][1]); fr[t][1] = pack_pair(an[t][2], an[t][3]); }
    {
        LAS char* xs = sm + B_X1 + cw * 2048 + lane * 16;
        *(LAS bf16x8*)xs = fr[2][0];
        *(LAS bf16x8*)(xs + 1024) = fr[2][1];
    }
    WAIT_VM(0);
    STAMP(a.ws, cw, 4);
    lds_bar();
    {
        LAS char* xr = sm + B_X1 + (cw ^ 1) * 2048 + lane * 16;
        const bf16x8 R0 = *(LAS bf16x8*)xr, R1 = *(LAS bf16x8*)(xr + 1024);
        const bf16x8 ones = as_frag((u32x4){0x3f803f80u, 0x3f803f80u, 0x3f803f80u, 0x3f803f80u});
        const f32x4 z4 = {0.f, 0.f, 0.f, 0.f};
        LAS float* kvt = (LAS float*)(sm + B_KVT) + mh * (80 * 84) + c * 84 + 4 * q;
        LAS float* zz = (LAS float*)(sm + B_Z) + mh * 80 + 4 * q;
        auto prod = [&](const bf16x8& k0, const bf16x8& k1, const bf16x8& v0, const bf16x8& v1, int ta, int tb) {
            *(LAS f32x4*)(kvt + 16 * tb * 84 + 16 * ta) = mfma(k1, v1, mfma(k0, v0, z4));
        };
        auto zsum = [&](const bf16x8& k0, const bf16x8& k1, int ta) {
            const f32x4 d = mfma(k1, ones, mfma(k0, ones, z4));
            if (c == 0) *(LAS f32x4*)(zz + 16 * ta) = d;
        };
        const int b0 = 3 * nh;
        prod(fr[0][0], fr[0][1], fr[3][0], fr[3][1], b0, b0);
        prod(fr[0][0], fr[0][1], fr[4][0], fr[4][1], b0, b0 + 1);
        prod(fr[1][0], fr[1][1], fr[3][0], fr[3][1], b0 + 1, b0);
        prod(fr[1][0], fr[1][1], fr[4][0], fr[4][1], b0 + 1, b0 + 1);
        zsum(fr[0][0], fr[0][1], b0);
        zsum(fr[1][0], fr[1][1], b0 + 1);
        if (nh == 0) {
            prod(fr[2][0], fr[2][1], fr[4][0], fr[4][1], 2, 1);
            prod(fr[1][0], fr[1][1], R0, R1, 1, 2);
            prod(fr[2][0], fr[2][1], R0, R1, 2, 2);
            zsum(fr[2][0], fr[2][1], 2);
        } else {
            prod(fr[0][0], fr[0][1], fr[2][0], fr[2][1], 3, 2);
            prod(R0, R1, fr[3][0], fr[3][1], 2, 3);
        }
    }
    STAMP(a.ws, cw, 5);
    lds_bar();
    {
        const int w = (ti * 8 + gj) * 4 + cw;
        LAS char* stg = sm + B_STG + cw * 4608;
        b_retile(a, stg, w, lane);
        if (w + 512 < 600) b_retile(a, stg, w + 512, lane);
    }
    STAMP(a.ws, cw, 6);
    signal_stores(sm, a.ws + WS_FW2T + (ti * 8 + gj) * 64, tag, lane);
    STAMP(a.ws, cw, 7);
    p2_final(a, sm, cw, lane, tag);
}

}

__global__ __launch_bounds__(512, 2) void gm_fused(const void* x, const void* lre, const void* lim, const void* bre,
                                                   const void* bim, const void* cre, const void* cim, const void* logdt,
                                                   const void* dskip, const void* wqkv, const void* wo, const void* gw,
                                                   void* ws, void* out) {
    __shared__ __attribute__((aligned(1024))) char smem[LDS_BYTES];
    LAS char* sm = (LAS char*)smem;
    const int b = blockIdx.x, xcd = b & 7, idx = b >> 3, x4 = xcd & 3;
    const int ti = 8 * (x4 >> 1) + (idx & 7), gj = 4 * (x4 & 1) + (idx >> 3);
    const int wave = __builtin_amdgcn_readfirstlane(threadIdx.x >> 6), lane = threadIdx.x & 63;
    const Args a{(const char*)x, (const u16*)lre, (const u16*)lim, (const char*)bre, (const char*)bim, (const char*)cre,
                 (const char*)cim, (const u16*)logdt, (const char*)wqkv, (const char*)wo, (const u16*)gw,
                 (const u16*)dskip, (char*)ws, (float*)out};
    if (xcd < 4) {
        if (wave < 4) a_consumer(a, sm, ti, gj, wave, lane);
        else a_loader(a, sm, ti, gj, wave - 4, lane);
    } else {
        if (wave < 4) b_consumer(a, sm, ti, gj, wave, lane);
        else b_loader(a, sm, ti, gj, wave - 4, lane);
    }
}

extern "C" void kernel_launch(void* const* d_in, const int* in_sizes, int n_in, void* d_out, int out_size, void* d_ws,
                              size_t ws_size, hipStream_t stream) {
    gm_fused<<<256, 512, 0, stream>>>(d_in[0], d_in[1], d_in[2], d_in[3], d_in[4], d_in[5], d_in[6], d_in[7], d_in[8],
                                      d_in[10], d_in[11], d_in[12], d_ws, d_out);
}
```

```cpp
#include <hip/hip_runtime.h>
#include <hip/hip_bf16.h>
#include <stdint.h>

namespace {

typedef __attribute__((ext_vector_type(8))) short bf16x8;
typedef __attribute__((ext_vector_type(4))) short s16x4;
typedef __attribute__((ext_vector_type(4))) float f32x4;
typedef __attribute__((ext_vector_type(2))) float f32x2;
typedef __attribute__((ext_vector_type(2))) __bf16 bf16x2_t;
typedef __attribute__((ext_vector_type(4))) unsigned u32x4;
typedef __attribute__((ext_vector_type(2))) unsigned u32x2;
typedef unsigned long long u64;
typedef unsigned short u16;
#define LAS __attribute__((address_space(3)))
#define GAS __attribute__((address_space(1)))
#if defined(__HIP_DEVICE_COMPILE__)
#define NOPK __attribute__((target("no-packed-fp32-ops")))
#else
#define NOPK
#endif
#define DEV __device__ __forceinline__
#define LAM __attribute__((always_inline))
#define RLX_AGENT __ATOMIC_RELAXED, __HIP_MEMORY_SCOPE_AGENT

constexpr size_t WS_NONCE = 0;
constexpr size_t WS_EA    = 4096;
constexpr size_t WS_KV    = WS_EA + 16 * 8 * 160 * 8;
constexpr size_t WS_TOT   = WS_KV + 16 * 8 * 840 * 8;
constexpr size_t WS_G     = WS_TOT + 8 * 840 * 8;
constexpr size_t WS_FACT  = WS_G + 2048 * 8;
constexpr size_t WS_FW2T  = WS_FACT + 16 * 256;
constexpr size_t WS_W2T   = (WS_FW2T + 128 * 8 + 1023) / 1024 * 1024;
constexpr size_t TS = 61440;
constexpr size_t WS_ACT   = WS_W2T + 40 * TS;
constexpr size_t WS_PROF  = 32u << 20;

#define STAMP(ws, wave, i) do {} while (0)
#define STAMPC(ws, wave, i) do {} while (0)
#define TIMED(acc, stmt) do { stmt; } while (0)
#define STAMPV(ws, wave, i, v) do {} while (0)
#define STAMP2(ws, wave, i) do {} while (0)

#define STAMPL(ws, i) do {} while (0)

constexpr int LDS_BYTES = 163840;
constexpr int SLOT_A = 47104, NS_A = 3;
constexpr int A_TAB  = 141312;
constexpr int A_G    = A_TAB + 3840;
constexpr int A_ESEG = A_G + 1024;
constexpr int A_HSEG = A_ESEG + 5120;
constexpr int A_TOT  = A_HSEG + 5120;
constexpr int A_HT = 47104, HT_STRIDE = 272;
static_assert(A_TOT + 3360 <= 159776, "lds");
constexpr int SLOT_B = 36864, NS_B = 4;
constexpr int B_X1  = 147456;
constexpr int B_KVT = 0;
constexpr int B_Z   = B_KVT + 2 * 80 * 84 * 4;
constexpr int B_TAB = 155648;
constexpr int B_STG = 110592;
static_assert(B_Z + 640 <= B_X1, "lds");

#define JOBMODE 2
#define SLP_W2T 16
#define SLP_ACT 4
#define SENT_LB 0
#define SENT_TOT 0
#define SLP_LB 2
#define SLP_TOT 2
#define HEAD_A 4
#define IL_A 25
#define IL_B 16
constexpr unsigned SPIN_CAP = 1u << 18;

DEV unsigned pk2(float a, float b) {
    return __builtin_bit_cast(unsigned, __builtin_convertvector((f32x2){a, b}, bf16x2_t));
}
DEV float bfr(float a) { return __uint_as_float(pk2(a, 0.f) << 16); }
DEV float bf_lo(unsigned u) { return __uint_as_float(u << 16); }
DEV float bf_hi(unsigned u) { return __uint_as_float(u & 0xffff0000u); }
DEV float bf2f(u16 u) { return __uint_as_float((unsigned)u << 16); }
DEV float swp(float x) {
    return __builtin_bit_cast(float, __builtin_amdgcn_mov_dpp(__builtin_bit_cast(int, x), 0xB1, 0xF, 0xF, true));
}
DEV float cmul_add(float a, float bs, float e, float u) { return fmaf(bs, swp(e), fmaf(a, e, u)); }
DEV f32x4 mfma(bf16x8 a, bf16x8 b, f32x4 c) { return __builtin_amdgcn_mfma_f32_16x16x32_bf16(a, b, c, 0, 0, 0); }
DEV bf16x8 as_frag(u32x4 v) { return __builtin_bit_cast(bf16x8, v); }
DEV bf16x8 lds_frag(LAS char* p) { return *(LAS bf16x8*)p; }
DEV bf16x8 lds_tr_frag(LAS char* p0, LAS char* p1) {
    s16x4 lo = __builtin_amdgcn_ds_read_tr16_b64_v4i16((LAS s16x4*)p0);
    s16x4 hi = __builtin_amdgcn_ds_read_tr16_b64_v4i16((LAS s16x4*)p1);
    return __builtin_shufflevector(lo, hi, 0, 1, 2, 3, 4, 5, 6, 7);
}
DEV void glds16_wt(const char* g, unsigned l) {
    unsigned keep;
    asm volatile("s_mov_b32 %0, m0\n\ts_mov_b32 m0, %2\n\ts_nop 0\n\tglobal_load_lds_dwordx4 %1, off sc1\n\ts_mov_b32 m0, %0"
                 : "=&s"(keep) : "v"(g), "s"(l) : "memory");
}
DEV void glds16(const char* g, LAS char* l) {
    __builtin_amdgcn_global_load_lds((const GAS unsigned*)g, (LAS unsigned*)l, 16, 0, 0);
}
DEV void bar() { asm volatile("" ::: "memory"); __builtin_amdgcn_s_barrier(); asm volatile("" ::: "memory"); }
DEV void wait_lgkm0() { __builtin_amdgcn_s_waitcnt(0xC07F); }
DEV void lds_bar() { asm volatile("" ::: "memory"); wait_lgkm0(); bar(); }
constexpr int L_FULL = 159776, L_FREE = L_FULL + 16;
constexpr int L_DONE = L_FULL + 32;
constexpr int L_KV = L_FULL + 36, L_W2T = L_FULL + 40, L_ACT = L_FULL + 44;
constexpr int L_X1 = L_FULL + 48, L_ZERO = L_FULL + 64;
constexpr int L_DONE2 = L_FULL + 72, L_ACT2 = L_FULL + 76;
constexpr int L_H2 = L_FULL + 80;
constexpr int L_WR = L_FULL + 84;
constexpr int L_TOT = L_FULL + 88, L_LBS = L_FULL + 92;
constexpr int L_HS = L_FULL + 96, L_HT = L_FULL + 100;
constexpr int L_WORDS = 26;
DEV unsigned umin4(const u32x4& f) { const unsigned a = f[0] < f[1] ? f[0] : f[1], b = f[2] < f[3] ? f[2] : f[3]; return a < b ? a : b; }
DEV void ring_put(LAS char* p, unsigned v) {
    asm volatile("ds_write_b32 %0, %1" ::"v"((unsigned)(size_t)p), "v"(v) : "memory");
}
DEV void ring_wait(LAS char* p, unsigned need) {
    for (unsigned spins = 0; spins < SPIN_CAP; ++spins) {
        u32x4 f;
        asm volatile("ds_read_b128 %0, %1\n\ts_waitcnt lgkmcnt(0)" : "=&v"(f) : "v"((unsigned)(size_t)p) : "memory");
        if ((unsigned)__builtin_amdgcn_readfirstlane(umin4(f)) >= need) break;
        __builtin_amdgcn_s_sleep(1);
    }
}
DEV void word_wait(LAS char* p, unsigned need) {
    for (unsigned spins = 0; spins < SPIN_CAP; ++spins) {
        if ((unsigned)__builtin_amdgcn_readfirstlane(*(volatile LAS unsigned*)p) >= need) break;
        __builtin_amdgcn_s_sleep(1);
    }
    asm volatile("" ::: "memory");
}
DEV void word_set(LAS char* p, unsigned v) { asm volatile("" ::: "memory"); *(volatile LAS unsigned*)p = v; }
DEV unsigned word_add(LAS char* p) {
    return __hip_atomic_fetch_add((LAS unsigned*)p, 1u, __ATOMIC_RELAXED, __HIP_MEMORY_SCOPE_WORKGROUP);
}
DEV void ring_spin(LAS char* p, unsigned need) {
    for (unsigned spins = 0; spins < SPIN_CAP; ++spins) {
        const u32x4 f = *(volatile LAS u32x4*)p;
        if ((unsigned)__builtin_amdgcn_readfirstlane(umin4(f)) >= need) break;
    }
}
#define WAIT_VM(n) asm volatile("s_waitcnt vmcnt(" #n ")" ::: "memory")
#define SCHED() __builtin_amdgcn_sched_barrier(0)
#define KEEP(x) asm volatile("" ::"v"(x))
template <int NM, int NR, int NS> DEV void interleave() {
#pragma unroll
    for (int i = 0; i < NS; ++i) {
        __builtin_amdgcn_sched_group_barrier(0x8, 1, 0);
        if ((i + 1) * NR / NS != i * NR / NS) __builtin_amdgcn_sched_group_barrier(0x100, 1, 0);
    }
    __builtin_amdgcn_sched_group_barrier(0x8, NM - NS, 0);
}
DEV bool wave_all(bool p) { return __builtin_amdgcn_ballot_w64(p) == __builtin_amdgcn_ballot_w64(true); }
DEV float lane_xor(float v, int m) {
    const int x = __builtin_bit_cast(int, v);
    const int y = m == 1 ? __builtin_amdgcn_mov_dpp(x, 0xB1, 0xF, 0xF, true) : m == 2 ? __builtin_amdgcn_mov_dpp(x, 0x4E, 0xF, 0xF, true)
                                                                              : __builtin_amdgcn_ds_swizzle(x, 0x101F);
    return __builtin_bit_cast(float, y);
}
DEV float fexp(float x) { return __builtin_amdgcn_exp2f(x * 1.44269504088896341f); }
DEV unsigned mk_tag(unsigned n) { return ((n + 1u) * 0x9E3779B1u) | 1u; }
DEV u64 granule(unsigned tag, unsigned v) { return ((u64)tag << 32) | v; }
DEV u64 flag_of(unsigned tag) { return granule(tag, tag * 0x85EBCA6Bu + 0x5BD1E995u); }
DEV __amdgpu_buffer_rsrc_t rsrc(const char* p, unsigned bytes) { return __builtin_amdgcn_make_buffer_rsrc((void*)p, 0, bytes, 0x00020000); }
DEV void st16_wt(const __amdgpu_buffer_rsrc_t& r, unsigned voff, unsigned soff, u32x4 v) { __builtin_amdgcn_raw_buffer_store_b128(v, r, voff + soff, 0, 16); }
DEV void st8_wt(const __amdgpu_buffer_rsrc_t& r, unsigned voff, unsigned soff, u32x2 v) { __builtin_amdgcn_raw_buffer_store_b64(v, r, voff + soff, 0, 16); }
DEV bf16x8 ld16_wt(const __amdgpu_buffer_rsrc_t& r, unsigned voff, unsigned soff) {
    return __builtin_bit_cast(bf16x8, __builtin_amdgcn_raw_buffer_load_b128(r, voff, soff, 16));
}
DEV void signal_stores(LAS char* sm, int word, char* flag, unsigned tag, int lane, unsigned waves) {
    asm volatile("s_waitcnt vmcnt(0)" ::: "memory");
    if (lane == 0) {
        const unsigned old = __hip_atomic_fetch_add((LAS unsigned*)(sm + word), 1u, __ATOMIC_RELAXED, __HIP_MEMORY_SCOPE_WORKGROUP);
        if (old == waves - 1u) __hip_atomic_store((GAS u64*)flag, flag_of(tag), RLX_AGENT);
    }
}
DEV float phi(float t) {
    const float inf = __builtin_inff();
    const float e = __builtin_amdgcn_exp2f(__builtin_amdgcn_fmed3f(t * 1.44269504088896341f, -inf, 0.f));
    return (e + 1e-6f) + __builtin_amdgcn_fmed3f(t, 0.f, inf);
}
DEV bf16x8 pack_pair(const f32x4& lo, const f32x4& hi) {
    return as_frag((u32x4){pk2(lo[0], lo[1]), pk2(lo[2], lo[3]), pk2(hi[0], hi[1]), pk2(hi[2], hi[3])});
}

DEV void scan_step(float& u0, float& u1, float& u2, float& u3, float& u4, const float (&e)[5], const float (&a)[5],
                   const float (&b)[5]) {
    asm("v_fmac_f32 %0, %10, %5\n\tv_fmac_f32 %1, %11, %6\n\tv_fmac_f32 %2, %12, %7\n\t"
        "v_fmac_f32 %3, %13, %8\n\tv_fmac_f32 %4, %14, %9\n\t"
        "v_fmac_f32_dpp %0, %5, %15 quad_perm:[1,0,3,2] row_mask:0xf bank_mask:0xf bound_ctrl:1\n\t"
        "v_fmac_f32_dpp %1, %6, %16 quad_perm:[1,0,3,2] row_mask:0xf bank_mask:0xf bound_ctrl:1\n\t"
        "v_fmac_f32_dpp %2, %7, %17 quad_perm:[1,0,3,2] row_mask:0xf bank_mask:0xf bound_ctrl:1\n\t"
        "v_fmac_f32_dpp %3, %8, %18 quad_perm:[1,0,3,2] row_mask:0xf bank_mask:0xf bound_ctrl:1\n\t"
        "v_fmac_f32_dpp %4, %9, %19 quad_perm:[1,0,3,2] row_mask:0xf bank_mask:0xf bound_ctrl:1"
        : "+v"(u0), "+v"(u1), "+v"(u2), "+v"(u3), "+v"(u4)
        : "v"(e[0]), "v"(e[1]), "v"(e[2]), "v"(e[3]), "v"(e[4]), "v"(a[0]), "v"(a[1]), "v"(a[2]), "v"(a[3]), "v"(a[4]),
          "v"(b[0]), "v"(b[1]), "v"(b[2]), "v"(b[3]), "v"(b[4]));
}

DEV void scan_step_nd(float (&t)[5], float u0, float u1, float u2, float u3, float u4, const float (&e)[5],
                      const float (&a)[5], const float (&b)[5]) {
    asm("v_fma_f32 %0, %15, %10, %5\n\tv_fma_f32 %1, %16, %11, %6\n\tv_fma_f32 %2, %17, %12, %7\n\t"
        "v_fma_f32 %3, %18, %13, %8\n\tv_fma_f32 %4, %19, %14, %9\n\t"
        "v_fmac_f32_dpp %0, %10, %20 quad_perm:[1,0,3,2] row_mask:0xf bank_mask:0xf bound_ctrl:1\n\t"
        "v_fmac_f32_dpp %1, %11, %21 quad_perm:[1,0,3,2] row_mask:0xf bank_mask:0xf bound_ctrl:1\n\t"
        "v_fmac_f32_dpp %2, %12, %22 quad_perm:[1,0,3,2] row_mask:0xf bank_mask:0xf bound_ctrl:1\n\t"
        "v_fmac_f32_dpp %3, %13, %23 quad_perm:[1,0,3,2] row_mask:0xf bank_mask:0xf bound_ctrl:1\n\t"
        "v_fmac_f32_dpp %4, %14, %24 quad_perm:[1,0,3,2] row_mask:0xf bank_mask:0xf bound_ctrl:1"
        : "=&v"(t[0]), "=&v"(t[1]), "=&v"(t[2]), "=&v"(t[3]), "=&v"(t[4])
        : "v"(u0), "v"(u1), "v"(u2), "v"(u3), "v"(u4), "v"(e[0]), "v"(e[1]), "v"(e[2]), "v"(e[3]), "v"(e[4]),
          "v"(a[0]), "v"(a[1]), "v"(a[2]), "v"(a[3]), "v"(a[4]), "v"(b[0]), "v"(b[1]), "v"(b[2]), "v"(b[3]), "v"(b[4]));
}

struct Args {
    const char* x; const u16* lre; const u16* lim; const char* bre; const char* bim; const char* cre; const char* cim;
    const u16* logdt; const char* wqkv; const char* wo; const u16* gw; const u16* dskip; char* ws; float* out;
};

DEV void channel_consts(const Args& a, int p, float& ar, float& ai, float& cr, float& ci) {
    const float lr = bf2f(a.lre[p]), li = bf2f(a.lim[p]);
    const float dt = bfr(fexp(bf2f(a.logdt[p])));
    const float zr = dt * lr, zi = dt * li, ex = fexp(zr);
    const float rv = zi * 0.15915494309189535f, rr = rv - __builtin_rintf(rv);
    ar = ex * __builtin_amdgcn_cosf(rr); ai = ex * __builtin_amdgcn_sinf(rr);
    const float nr = ar - 1.f, ni = ai, dn = lr * lr + li * li;
    cr = (nr * lr + ni * li) / dn; ci = (ni * lr - nr * li) / dn;
}

constexpr int P2_RED = 0;
#define P2_PW 4
#define P2_PA 5
DEV void p2_main(const Args& a, LAS char* sm, int lw, int lane, unsigned nonce) {
    const int b = blockIdx.x, xcd = b & 7, idx = b >> 3;
    const int tt = (xcd >> 1) * 8 + (idx & 7), cg = (xcd & 1) * 4 + (idx >> 3);
    const int c = lane & 15, q = lane >> 4;
    const unsigned tag = mk_tag(nonce);
    const u64 fl = flag_of(tag);
    constexpr int PW = P2_PW, PA = P2_PA;
    if (lw == 3) {
        const __amdgpu_buffer_rsrc_t rf = rsrc(a.ws + WS_FW2T, 128 * 8);
        const unsigned f0 = (unsigned)fl, f1 = (unsigned)(fl >> 32);
        for (unsigned spins = 0; spins < SPIN_CAP; ++spins) {
            const u32x4 v = __builtin_amdgcn_raw_buffer_load_b128(rf, lane * 16, 0, 16);
            if (wave_all((v[0] == f0) & (v[1] == f1) & (v[2] == f0) & (v[3] == f1))) break;
            __builtin_amdgcn_s_sleep(SLP_W2T);
        }
        word_set(sm + L_W2T, 1u);
    } else word_wait(sm + L_W2T, 1u);
    STAMP2(a.ws, lw + 4, 0);
    const __amdgpu_buffer_rsrc_t rw = rsrc(a.ws + WS_W2T, 40 * TS), ra = rsrc(a.ws + WS_ACT, 128 * TS);
    const unsigned wbase = 5 * cg * TS, abase = 4 * tt * TS, vo = lane * 16;
    const unsigned ko = (40 + 5 * lw) * 1024, kh = (10 * lw - 5) * 1024;
    auto koff = [&](int st) LAM { return (st < 5 ? ko : kh) + st * 1024; };
    bf16x8 fw[PW][5], fa[PA][4];
    auto ldw = [&](int st, int sl) LAM {
#pragma unroll
        for (int n = 0; n < 5; ++n) fw[sl][n] = ld16_wt(rw, vo, wbase + n * TS + koff(st));
    };
    auto lda = [&](int st) LAM {
#pragma unroll
        for (int t = 0; t < 4; ++t) fa[st % PA][t] = ld16_wt(ra, vo, abase + t * TS + koff(st));
    };
    auto wait_act = [&](int part, int word) LAM {
        if (lw == 3) {
            GAS u64* fp = (GAS u64*)(a.ws + WS_FACT) + (tt >> 1) * 32 + part * 16 + (lane & 7);
            for (unsigned spins = 0; spins < SPIN_CAP; ++spins) {
                const u64 v = __hip_atomic_load(fp, RLX_AGENT);
                if (wave_all(v == fl)) break;
                __builtin_amdgcn_s_sleep(SLP_ACT);
            }
            word_set(sm + word, 1u);
        } else word_wait(sm + word, 1u);
    };
    auto unit = [&](int i) LAM { const int u = 4 * lw + i; return u < 9 ? 5120 * u : 90624 + 5120 * (u - 9); };
    {
        const char* src = a.ws + WS_W2T + wbase + (10 * lw + 4) * 1024 + vo;
#pragma unroll
        for (int i = 0; i < 4; ++i)
#pragma unroll
            for (int n = 0; n < 5; ++n) glds16_wt(src + (size_t)n * TS + i * 1024, (unsigned)(size_t)(sm + unit(i) + n * 1024));
    }
#pragma unroll
    for (int st = 0; st < PW; ++st) ldw(st, st);
    SCHED();
    WAIT_VM(0);
    wait_act(1, L_ACT2);
    STAMP2(a.ws, lw + 4, 1);
    f32x4 acc[5][4];
#pragma unroll
    for (int n = 0; n < 5; ++n)
#pragma unroll
        for (int t = 0; t < 4; ++t) acc[n][t] = (f32x4){0.f, 0.f, 0.f, 0.f};
    auto slot = [&](int st) LAM { return st < 9 ? st % 4 : st == 13 ? 1 : st == 14 ? 2 : (st & 1) ? 3 : 0; };
    auto step = [&](int st) LAM {
        SCHED();
        if (st >= 8 && st < 12) {
#pragma unroll
            for (int n = 0; n < 5; ++n) fw[slot(st + 1)][n] = lds_frag(sm + unit(st - 8) + vo + n * 1024);
        }
#pragma unroll
        for (int n = 0; n < 5; ++n)
#pragma unroll
            for (int t = 0; t < 4; ++t) acc[n][t] = mfma(fw[slot(st)][n], fa[st % PA][t], acc[n][t]);
        SCHED();
        if (st < 5) ldw(st + 4, slot(st + 4));
        if (st == 9) ldw(13, slot(13));
        if (st == 10) ldw(14, slot(14));
        if (st == 11) { wait_lgkm0(); if (lane == 0) word_add(sm + L_WR); }
    };
    static_assert(PA == 5 && PW == 4, "ring layout");
#pragma unroll
    for (int st = 0; st < 5; ++st) lda(st);
#pragma unroll
    for (int st = 0; st < 5; ++st) step(st);
    STAMP2(a.ws, lw + 4, 4);
    wait_act(0, L_ACT);
    STAMP2(a.ws, lw + 4, 5);
    if (tt == 31 && cg == 0 && lw == 3 && lane == 0) __hip_atomic_store((GAS unsigned*)(a.ws + WS_NONCE), nonce + 1u, RLX_AGENT);
#pragma unroll
    for (int st = 5; st < 9; ++st) lda(st);
#pragma unroll
    for (int st = 5; st < 15; ++st) {
        if (st + 4 < 15) lda(st + 4);
        step(st);
    }
    STAMP2(a.ws, lw + 4, 2);
    word_wait(sm + L_WR, 4u);
    word_wait(sm + L_DONE, 4u);
    LAS float* rw_ = (LAS float*)(sm + P2_RED) + lw * (64 * 84);
#pragma unroll
    for (int t = 0; t < 4; ++t)
#pragma unroll
        for (int n = 0; n < 5; ++n) *(LAS f32x4*)(rw_ + (16 * t + c) * 84 + 16 * n + 4 * q) = acc[n][t];
    lds_bar();
    STAMP2(a.ws, lw + 4, 3);
}

DEV void p2_final(const Args& a, LAS char* sm, int cw, int lane, unsigned tag) {
    const int b = blockIdx.x, xcd = b & 7, idx = b >> 3;
    const int tt = (xcd >> 1) * 8 + (idx & 7), cg = (xcd & 1) * 4 + (idx >> 3);
    const int tid = cw * 64 + lane;
    u32x2 xv[5], dv[5]; float gv[5];
    int tok[5], n0[5];
#pragma unroll
    for (int i = 0; i < 5; ++i) {
        const int id = tid + 256 * i, r = id / 20, c4 = id - 20 * r;
        tok[i] = 64 * tt + 16 * ((r & 15) >> 2) + 4 * (r >> 4) + (r & 3); n0[i] = 80 * cg + 4 * c4;
        xv[i] = *(const u32x2*)((const u16*)a.x + tok[i] * 640 + n0[i]);
        dv[i] = *(const u32x2*)(a.dskip + n0[i]);
    }
    for (unsigned spins = 0; spins < SPIN_CAP; ++spins) {
        u64 v[5];
#pragma unroll
        for (int i = 0; i < 5; ++i) v[i] = __hip_atomic_load((GAS u64*)(a.ws + WS_G) + tok[i], RLX_AGENT);
        bool ok = true;
#pragma unroll
        for (int i = 0; i < 5; ++i) { gv[i] = __uint_as_float((unsigned)v[i]); ok &= (unsigned)(v[i] >> 32) == tag; }
        if (wave_all(ok)) break;
        __builtin_amdgcn_s_sleep(2);
    }
    f32x4 sk[5];
#pragma unroll
    for (int i = 0; i < 5; ++i) {
        const unsigned p01 = pk2(bf_lo(xv[i][0]) * bf_lo(dv[i][0]), bf_hi(xv[i][0]) * bf_hi(dv[i][0]));
        const unsigned p23 = pk2(bf_lo(xv[i][1]) * bf_lo(dv[i][1]), bf_hi(xv[i][1]) * bf_hi(dv[i][1]));
        sk[i] = (f32x4){gv[i] * bf_lo(p01), gv[i] * bf_hi(p01), gv[i] * bf_lo(p23), gv[i] * bf_hi(p23)};
    }
    STAMP2(a.ws, cw, 0);
    bar();
    STAMP2(a.ws, cw, 2);
#pragma unroll
    for (int i = 0; i < 5; ++i) {
        const int id = tid + 256 * i, r = id / 20, c4 = id - 20 * r;
        LAS float* p = (LAS float*)(sm + P2_RED) + r * 84 + 4 * c4;
        sk[i] += (*(LAS f32x4*)p + *(LAS f32x4*)(p + 64 * 84)) + (*(LAS f32x4*)(p + 128 * 84) + *(LAS f32x4*)(p + 192 * 84));
    }
    asm volatile("" : "+v"(sk[0]), "+v"(sk[1]), "+v"(sk[2]), "+v"(sk[3]), "+v"(sk[4]));
#pragma unroll
    for (int i = 0; i < 5; ++i) *(f32x4*)(a.out + (size_t)tok[i] * 640 + n0[i]) = sk[i];
    STAMP2(a.ws, cw, 3);
}

DEV void a_loader(const Args& a, LAS char* sm, int ti, int gj, int lw, int lane) {
    STAMP(a.ws, lw + 4, 0);
    const unsigned nonce = __hip_atomic_load((GAS unsigned*)(a.ws + WS_NONCE), RLX_AGENT);
    unsigned off[12];
    {
        const int pos = lane & 7, rl = lane >> 3, im = lw & 1;
#pragma unroll
        for (int u = 0; u < 4; ++u) {
            const int R = 8 * (lw + 4 * u) + rl, rho = R & 15;
            const int tok = (R & 64) + 16 * (rho >> 2) + 4 * ((R >> 4) & 3) + (rho & 3);
            off[u] = tok * 1280 + ((pos ^ (R & 7)) << 4);
        }
#pragma unroll
        for (int u = 4; u < 9; ++u) off[u] = (8 * ((lw + 4 * (u - 4)) >> 1) + rl) * 1280 + ((pos ^ rl ^ (im << 2)) << 4);
#pragma unroll
        for (int u = 9; u < 12; ++u) {
            const int ch = 64 * (lw + 4 * (u - 9)) + lane, rho = ch / 10, cc = ch - 10 * rho;
            off[u] = (rho ^ (((rho >> 3) & 1) << 2)) * 3840 + cc * 16;
        }
    }
    const char* xb = a.x + (size_t)ti * (128 * 1280);
    const char* bb = ((lw & 1) ? a.bim : a.bre) + (size_t)gj * (80 * 1280);
    const char* qb = a.wqkv + 160 * gj;
    const bool full = lw < 2;
    auto issue_a = [&](int s) LAM {
        LAS char* dst = sm + (s % NS_A) * SLOT_A + lw * 1024;
#pragma unroll
        for (int u = 0; u < 4; ++u) glds16(xb + 128 * s + off[u], dst + 4096 * u);
#pragma unroll
        for (int u = 4; u < 6; ++u) glds16(bb + 128 * s + off[u], dst + 4096 * u);
    };
    auto issue_b = [&](int s) LAM {
        LAS char* dst = sm + (s % NS_A) * SLOT_A + lw * 1024;
#pragma unroll
        for (int u = 6; u < 9; ++u) glds16(bb + 128 * s + off[u], dst + 4096 * u);
#pragma unroll
        for (int u = 9; u < 12; ++u) if (u < 11 || full) glds16(qb + (size_t)(64 * 3840) * s + off[u], dst + 4096 * u);
    };
    STAMP(a.ws, lw + 4, 1);
    issue_a(0); issue_b(0); issue_a(1); issue_b(1);
    if (full) WAIT_VM(12); else WAIT_VM(11);
    bar();
    u64 t_vm = 0, t_free = 0;
#pragma unroll 1
    for (int s = 0; s < 8; ++s) {
        if (s > 0) TIMED(t_free, ring_wait(sm + L_FREE, s));
        issue_a(s + 2);
        TIMED(t_vm, WAIT_VM(6));
        ring_put(sm + L_FULL + 4 * lw, s + 2);
        issue_b(s + 2);
    }
    WAIT_VM(0);
    ring_put(sm + L_FULL + 4 * lw, 10);
    STAMPV(a.ws, lw + 4, 14, t_vm); STAMPV(a.ws, lw + 4, 15, t_free);
    STAMP(a.ws, lw + 4, 2);
    __builtin_amdgcn_s_setprio(2);
    const unsigned tag = mk_tag(nonce);
    const int tl = lw * 64 + lane;
    if (tl < 128) {
        GAS u64* gp = (GAS u64*)(a.ws + WS_G) + 128 * ti + tl;
        u64 v;
        for (unsigned spins = 0;;) {
            v = __hip_atomic_load(gp, RLX_AGENT);
            if (wave_all((unsigned)(v >> 32) == tag) || ++spins > SPIN_CAP) break;
            __builtin_amdgcn_s_sleep(1);
        }
        const float g = __uint_as_float((unsigned)v);
        ((LAS float*)(sm + A_G))[tl] = g;
        ((LAS float*)(sm + A_G))[128 + tl] = bfr(1.f - g);
    }
    STAMP(a.ws, lw + 4, 9);
    lds_bar();
    STAMP(a.ws, lw + 4, 3);
    if (lw < 3) {
        const int col = tl < 160 ? tl : 159;
        LAS float* tab = (LAS float*)(sm + A_TAB);
        GAS u64* ea = (GAS u64*)(a.ws + WS_EA) + gj * 160;
        const float a16 = tab[2 * 160 + col], b16 = tab[3 * 160 + col], a128 = tab[4 * 160 + col], b128 = tab[5 * 160 + col];
        LAS float* es = (LAS float*)(sm + A_ESEG);
        float pre[8], P = 0.f;
#pragma unroll
        for (int s = 0; s < 8; ++s) { pre[s] = P; P = cmul_add(a16, b16, P, es[s * 160 + col]); }
        if (tl < 160) __hip_atomic_store(ea + ti * 1280 + col, granule(tag, __float_as_uint(P)), RLX_AGENT);
        STAMP(a.ws, lw + 4, 4);
        float H = 0.f;
        if (ti > 0) {
            if (SENT_LB == 0) {
            } else if (lw == 0) {
                GAS u64* sp = ea + (lane < ti ? lane : 0) * 1280 + 159;
                for (unsigned spins = 0; spins < SPIN_CAP; ++spins) {
                    if (wave_all((unsigned)(__hip_atomic_load(sp, RLX_AGENT) >> 32) == tag)) break;
                    __builtin_amdgcn_s_sleep(SLP_LB);
                }
                word_set(sm + L_LBS, 1u);
            } else word_wait(sm + L_LBS, 1u);
            float ev[15];
            for (unsigned spins = 0; spins < SPIN_CAP; ++spins) {
                u64 v[15];
#pragma unroll
                for (int i = 0; i < 15; ++i) v[i] = __hip_atomic_load(ea + (i < ti ? i : 0) * 1280 + col, RLX_AGENT);
                bool ok = true;
#pragma unroll
                for (int i = 0; i < 15; ++i) { ev[i] = __uint_as_float((unsigned)v[i]); ok &= (unsigned)(v[i] >> 32) == tag; }
                if (wave_all(ok)) break;
                __builtin_amdgcn_s_sleep(SLP_LB);
            }
#pragma unroll
            for (int i = 0; i < 15; ++i) if (i < ti) H = cmul_add(a128, b128, H, ev[i]);
        }
        STAMP(a.ws, lw + 4, 6);
        LAS float* hs = (LAS float*)(sm + A_HSEG);
#pragma unroll
        for (int s = 0; s < 8; ++s) { if (tl < 160) hs[s * 160 + col] = H + pre[s]; H = cmul_add(a16, b16, H, 0.f); }
        wait_lgkm0();
        if (lane == 0) word_add(sm + L_HS);
    } else {
        const int i16 = lane & 15;
        GAS u64* sp = (GAS u64*)(a.ws + WS_TOT) + gj * 840 + (i16 < 8 ? 53 * i16 + 52 : 424 + 52 * (i16 - 8) + 51);
        for (unsigned spins = 0; SENT_TOT && spins < SPIN_CAP; ++spins) {
            if (wave_all((unsigned)(__hip_atomic_load(sp, RLX_AGENT) >> 32) == tag)) break;
            __builtin_amdgcn_s_sleep(SLP_TOT);
        }
        STAMP(a.ws, lw + 4, 4);
        const __amdgpu_buffer_rsrc_t rs = rsrc(a.ws + WS_TOT, 8 * 840 * 8);
        u32x4 v[7];
        for (unsigned spins = 0; spins < SPIN_CAP; ++spins) {
            bool ok = true;
#pragma unroll
            for (int k = 0; k < 7; ++k) v[k] = __builtin_amdgcn_raw_buffer_load_b128(rs, gj * 6720 + (lane + 64 * k < 420 ? lane + 64 * k : 419) * 16, 0, 16);
#pragma unroll
            for (int k = 0; k < 7; ++k) ok &= (v[k][1] == tag) & (v[k][3] == tag);
            if (wave_all(ok)) break;
            __builtin_amdgcn_s_sleep(SLP_TOT);
        }
#pragma unroll
        for (int k = 0; k < 7; ++k) if (lane + 64 * k < 420) ((LAS u32x2*)(sm + A_TOT))[lane + 64 * k] = (u32x2){v[k][0], v[k][2]};
        wait_lgkm0();
        word_set(sm + L_TOT, 1u);
        STAMP(a.ws, lw + 4, 5);
    }
    __builtin_amdgcn_s_setprio(0);
    STAMP(a.ws, lw + 4, 8);
    p2_main(a, sm, lw, lane, nonce);
}

template <int NH>
DEV void a_pass1(f32x4 (&acc)[4][5], const float (&ca)[5], const float (&cb)[5], LAS float* es) {
    float e[5];
#pragma unroll
    for (int n = 0; n < 5; ++n) e[n] = acc[2 * NH][n][0];
#pragma unroll
    for (int t = 1; t < 16; ++t) {
        const int i = (t >> 2) ^ (2 * NH), r = t & 3;
        float y[5];
        scan_step_nd(y, acc[i][0][r], acc[i][1][r], acc[i][2][r], acc[i][3][r], acc[i][4][r], e, ca, cb);
#pragma unroll
        for (int n = 0; n < 5; ++n) e[n] = y[n];
    }
#pragma unroll
    for (int n = 0; n < 5; ++n) es[16 * n] = e[n];
}
template <int NH>
DEV void a_pass2(f32x4 (&acc)[4][5], const float (&ca)[5], const float (&cb)[5], LAS float* hs, LAS float* gt, LAS char* ht) {
    float e[5];
#pragma unroll
    for (int n = 0; n < 5; ++n) e[n] = hs[16 * n];
    asm volatile("s_nop 1" : "+v"(e[0]), "+v"(e[1]), "+v"(e[2]), "+v"(e[3]), "+v"(e[4]));
#pragma unroll
    for (int m = 0; m < 4; ++m) {
        const int i = m ^ (2 * NH);
        const f32x4 g = *(LAS f32x4*)(gt + 4 * m);
#pragma unroll
        for (int r = 0; r < 4; ++r) {
            float u0 = acc[i][0][r], u1 = acc[i][1][r], u2 = acc[i][2][r], u3 = acc[i][3][r], u4 = acc[i][4][r];
            scan_step(u0, u1, u2, u3, u4, e, ca, cb);
            e[0] = u0; e[1] = u1; e[2] = u2; e[3] = u3; e[4] = u4;
            acc[i][0][r] = u0 * g[r]; acc[i][1][r] = u1 * g[r]; acc[i][2][r] = u2 * g[r]; acc[i][3][r] = u3 * g[r]; acc[i][4][r] = u4 * g[r];
        }
#pragma unroll
        for (int n = 0; n < 5; ++n)
            *(LAS u32x2*)(ht + 16 * n * HT_STRIDE + 32 * m) = (u32x2){pk2(acc[i][n][0], acc[i][n][1]), pk2(acc[i][n][2], acc[i][n][3])};
    }
}

DEV void a_consumer(const Args& a, LAS char* sm, int ti, int gj, int cw, int lane) {
    const int mh = cw >> 1, nh = cw & 1, c = lane & 15, q = lane >> 4, tid = cw * 64 + lane;
    STAMP(a.ws, cw, 0);
    const unsigned nonce = __hip_atomic_load((GAS unsigned*)(a.ws + WS_NONCE), RLX_AGENT);
    const unsigned tag = mk_tag(nonce);
    if (cw >= 2) {
        const int tok = 128 * ti + 16 * gj + 8 * (cw - 2) + (lane >> 3);
        const u32x4* xr = (const u32x4*)(a.x + (size_t)tok * 1280) + (lane & 7);
        const u32x4* gr = (const u32x4*)a.gw + (lane & 7);
        u32x4 xv[10], gv[10];
#pragma unroll
        for (int i = 0; i < 10; ++i) { xv[i] = xr[8 * i]; gv[i] = gr[8 * i]; }
        float s = 0.f;
#pragma unroll
        for (int i = 0; i < 10; ++i)
#pragma unroll
            for (int k = 0; k < 4; ++k) {
                const unsigned pr = pk2(bf_lo(xv[i][k]) * bf_lo(gv[i][k]), bf_hi(xv[i][k]) * bf_hi(gv[i][k]));
                s += bf_lo(pr); s += bf_hi(pr);
            }
        s += lane_xor(s, 1); s += lane_xor(s, 2); s += lane_xor(s, 4);
        const float lg = bfr(s), g = bfr(1.f / (1.f + fexp(-lg)));
        if ((lane & 7) == 0) __hip_atomic_store((GAS u64*)(a.ws + WS_G) + tok, granule(tag, __float_as_uint(g)), RLX_AGENT);
    } else if (tid < 80) {
        float ar, ai, cr, ci;
        channel_consts(a, 80 * gj + tid, ar, ai, cr, ci);
        float pr = ar, pi = ai;
#pragma unroll
        for (int k = 0; k < 4; ++k) { const float t = pr * pr - pi * pi; pi = 2.f * pr * pi; pr = t; }
        const float r16 = pr, i16 = pi;
#pragma unroll
        for (int k = 0; k < 3; ++k) { const float t = pr * pr - pi * pi; pi = 2.f * pr * pi; pr = t; }
        LAS f32x2* tab = (LAS f32x2*)(sm + A_TAB);
        tab[0 * 80 + tid] = (f32x2){ar, ar};
        tab[1 * 80 + tid] = (f32x2){-ai, ai};
        tab[2 * 80 + tid] = (f32x2){r16, r16};
        tab[3 * 80 + tid] = (f32x2){-i16, i16};
        tab[4 * 80 + tid] = (f32x2){pr, pr};
        tab[5 * 80 + tid] = (f32x2){-pi, pi};
    }
    if (tid < 160) __hip_atomic_store((GAS u64*)(a.ws + WS_EA) + (ti * 8 + gj) * 160 + tid, (u64)0, RLX_AGENT);
    __builtin_amdgcn_s_setprio(1);
    f32x4 acc[4][5], at[5][2];
#pragma unroll
    for (int n = 0; n < 5; ++n) {
#pragma unroll
        for (int i = 0; i < 4; ++i) acc[i][n] = (f32x4){0.f, 0.f, 0.f, 0.f};
        at[n][0] = at[n][1] = (f32x4){0.f, 0.f, 0.f, 0.f};
    }
    int xlo[2], xhi[2], wa[2], wq[2];
#pragma unroll
    for (int kk = 0; kk < 2; ++kk) {
        const int sw = ((4 * kk + q) ^ (c & 7)) << 4;
        xlo[kk] = (64 * mh + 32 * nh + c) * 128 + sw;
        xhi[kk] = (64 * mh + 32 * (1 - nh) + c) * 128 + sw;
        wa[kk] = 16384 + (80 * nh + 8 * (c & 1) + (c >> 1)) * 128 + (((4 * kk + q) ^ (c >> 1) ^ ((c & 1) << 2)) << 4);
        wq[kk] = 36864 + (8 * q + 4 * (kk ^ (q & 1)) + (c >> 2)) * 160 + 8 * (c & 3);
    }
    bf16x8 xa[2][4], wb[2][5], wt[2][5];
    auto rdp = [&](int buf, LAS char* s0, int kk) LAM {
        LAS char* q0 = s0 + wq[0] + kk * 5120; LAS char* q1 = s0 + wq[1] + kk * 5120;
        xa[buf][0] = lds_frag(s0 + xlo[kk]);
        xa[buf][1] = lds_frag(s0 + xlo[kk] + 2048);
        wt[buf][0] = lds_tr_frag(q0, q1);
        xa[buf][2] = lds_frag(s0 + xhi[kk]);
        xa[buf][3] = lds_frag(s0 + xhi[kk] + 2048);
        wt[buf][1] = lds_tr_frag(q0 + 32, q1 + 32);
        wb[buf][0] = lds_frag(s0 + wa[kk]);
        wb[buf][1] = lds_frag(s0 + wa[kk] + 2048);
        wt[buf][2] = lds_tr_frag(q0 + 64, q1 + 64);
        wb[buf][2] = lds_frag(s0 + wa[kk] + 4096);
        wb[buf][3] = lds_frag(s0 + wa[kk] + 6144);
        wt[buf][3] = lds_tr_frag(q0 + 96, q1 + 96);
        wb[buf][4] = lds_frag(s0 + wa[kk] + 8192);
        wt[buf][4] = lds_tr_frag(q0 + 128, q1 + 128);
    };
    auto rd = [&](int buf, int slot, int kk) LAM { rdp(buf, sm + slot * SLOT_A, kk); };
    auto mm_head = [&](int buf) LAM {
#pragma unroll
        for (int n = 0; n < HEAD_A / 4; ++n)
#pragma unroll
            for (int i = 0; i < 4; ++i) acc[i][n] = mfma(xa[buf][i], wb[buf][n], acc[i][n]);
    };
    auto mm_tail = [&](int buf) LAM {
#pragma unroll
        for (int n = HEAD_A / 4; n < 5; ++n)
#pragma unroll
            for (int i = 0; i < 4; ++i) acc[i][n] = mfma(xa[buf][i], wb[buf][n], acc[i][n]);
#pragma unroll
        for (int n = 0; n < 5; ++n)
#pragma unroll
            for (int i = 0; i < 2; ++i) at[n][i] = mfma(wt[buf][n], xa[buf][i], at[n][i]);
    };
    if (tid < L_WORDS) ((LAS unsigned*)(sm + L_FULL))[tid] = tid < 4 ? 1u : 0u;
#pragma unroll
    for (int n = 0; n < 5; ++n)
        asm volatile("" : "+v"(acc[0][n]), "+v"(acc[1][n]), "+v"(acc[2][n]), "+v"(acc[3][n]), "+v"(at[n][0]), "+v"(at[n][1]));
    STAMP(a.ws, cw, 1);
    lds_bar();
    STAMP(a.ws, cw, 2);
    STAMPC(a.ws, cw, 12);
    rd(0, 0, 0);
    int slot = 0;
    u64 t_spin = 0, n_spin = 0;
#pragma unroll 1
    for (int s = 0; s < 10; ++s) {
        wait_lgkm0();
        SCHED();
        rd(1, slot, 1);
        u32x4 fl = *(volatile LAS u32x4*)(sm + L_FULL);
        mm_head(0); mm_tail(0);
        interleave<30, 20, IL_A>();
        SCHED();
        wait_lgkm0();
        asm volatile("" ::: "memory");
        *(volatile LAS unsigned*)(sm + L_FREE + 4 * cw) = s + 1;
        slot = slot == 2 ? 0 : slot + 1;
        const unsigned need = s < 9 ? s + 2 : 0;
        unsigned landed = __builtin_amdgcn_readfirstlane(umin4(fl));
        LAS char* s0 = sm + slot * SLOT_A;
        asm volatile("" : "+s"(landed), "+v"(s0));
        mm_head(1);
        SCHED();
        if (__builtin_expect(landed < need, 0)) { TIMED(t_spin, ring_spin(sm + L_FULL, need)); n_spin++; }
        asm volatile("" ::: "memory");
        SCHED();
        rdp(0, s0, 0);
        mm_tail(1);
        interleave<30 - HEAD_A, 19, IL_A - HEAD_A + 1>();
        SCHED();
    }
    __builtin_amdgcn_s_setprio(0);
    asm volatile("s_nop 7\n\ts_nop 7" ::: "memory");
    STAMP(a.ws, cw, 3);
    STAMPC(a.ws, cw, 13);
    STAMPV(a.ws, cw, 14, t_spin); STAMPV(a.ws, cw, 15, n_spin);
    LAS float* tab = (LAS float*)(sm + A_TAB);
    float ca[5], cb[5];
#pragma unroll
    for (int n = 0; n < 5; ++n) { const int col = 80 * nh + 16 * n + c; ca[n] = tab[col]; cb[n] = tab[160 + col]; }
    {
        LAS float* es = (LAS float*)(sm + A_ESEG) + (4 * mh + q) * 160 + 80 * nh + c;
        if (nh == 0) a_pass1<0>(acc, ca, cb, es); else a_pass1<1>(acc, ca, cb, es);
    }
    WAIT_VM(0);
    STAMP(a.ws, cw, 4);
    lds_bar();
    bf16x8 P[2][3];
#pragma unroll
    for (int i = 0; i < 2; ++i) {
#pragma unroll
        for (int t = 0; t < 5; ++t)
#pragma unroll
            for (int r = 0; r < 4; ++r) at[t][i][r] = phi(at[t][i][r]);
        P[i][0] = pack_pair(at[0][i], at[1][i]);
        P[i][1] = pack_pair(at[2][i], at[3][i]);
        P[i][2] = as_frag((u32x4){pk2(at[4][i][0], at[4][i][1]), pk2(at[4][i][2], at[4][i][3]), 0u, 0u});
    }
    int adr[21];
    {
        int dh[5], db[5], eh[5], eb[5];
#pragma unroll
        for (int k = 0; k < 5; ++k) {
            const int d0 = 16 * k + 4 * q, e0 = 16 * k + c;
            dh[k] = d0 / 20; db[k] = A_TOT + dh[k] * 840 + (d0 - 20 * dh[k]) * 2;
            eh[k] = e0 / 20; eb[k] = (e0 - 20 * eh[k]) * 40;
        }
        auto ad = [&](int k, int E) LAM { return dh[k] == eh[E] ? db[k] + eb[E] : L_ZERO; };
        adr[0] = ad(0, 0); adr[1] = ad(1, 0);
        adr[2] = ad(0, 1); adr[3] = ad(1, 1); adr[4] = ad(2, 1); adr[5] = ad(3, 1);
        adr[6] = ad(1, 2); adr[7] = ad(2, 2); adr[8] = ad(3, 2);
        adr[9] = ad(2, 3); adr[10] = ad(3, 3); adr[11] = ad(4, 3);
        adr[12] = ad(3, 4); adr[13] = ad(4, 4);
#pragma unroll
        for (int k = 0; k < 5; ++k) adr[14 + k] = dh[k] == (c & 3) ? db[k] + 800 : L_ZERO;
        adr[19] = L_ZERO; adr[20] = A_G + 4 * (128 + 64 * mh + 16 * (c >> 2) + 8 * nh + (c & 3));
    }
#pragma unroll
    for (int i = 0; i < 2; ++i) asm volatile("" : "+v"(P[i][0]), "+v"(P[i][1]), "+v"(P[i][2]));
    asm volatile("" : "+v"(adr[0]), "+v"(adr[1]), "+v"(adr[2]), "+v"(adr[3]), "+v"(adr[4]), "+v"(adr[5]), "+v"(adr[6]),
                      "+v"(adr[7]), "+v"(adr[8]), "+v"(adr[9]), "+v"(adr[10]), "+v"(adr[11]), "+v"(adr[12]), "+v"(adr[13]));
    asm volatile("" : "+v"(adr[14]), "+v"(adr[15]), "+v"(adr[16]), "+v"(adr[17]), "+v"(adr[18]), "+v"(adr[20]));
    const __amdgpu_buffer_rsrc_t ract = rsrc(a.ws + WS_ACT, 128 * TS);
    u32x4 os[2][2]; u32x2 o8[2];
    STAMP(a.ws, cw, 5);
    word_wait(sm + L_TOT, 1u);
    STAMP(a.ws, cw, 6);
    {
        auto h4 = [&](int i) LAM -> u32x2 { return *(LAS u32x2*)(sm + adr[i]); };
        auto fr2 = [&](const u32x2& lo, const u32x2& hi) LAM { return as_frag((u32x4){lo[0], lo[1], hi[0], hi[1]}); };
        const u32x2 zz = {0u, 0u};
        const bf16x8 Z0 = fr2(h4(14), h4(15)), Z1 = fr2(h4(16), h4(17)), Z2 = fr2(h4(18), zz);
        const bf16x8 F0 = fr2(h4(0), h4(1)), F1 = fr2(h4(2), h4(3)), F2 = fr2(h4(4), h4(5));
        const bf16x8 F3 = fr2(zz, h4(6)), F4 = fr2(h4(7), h4(8)), F5 = fr2(h4(9), h4(10));
        const bf16x8 F6 = fr2(h4(11), zz), F7 = fr2(zz, h4(12)), F8 = fr2(h4(13), zz);
        const float ogv[2] = {*(LAS float*)(sm + adr[20]), *(LAS float*)(sm + adr[20] + 16)};
        const f32x4 z4 = {0.f, 0.f, 0.f, 0.f};
        const int uu = gj >> 1, odd = gj & 1;
#pragma unroll
        for (int i = 0; i < 2; ++i) {
            const f32x4 dn = mfma(Z2, P[i][2], mfma(Z1, P[i][1], mfma(Z0, P[i][0], z4)));
            const f32x4 n0 = mfma(F0, P[i][0], z4);
            const f32x4 n1 = mfma(F2, P[i][1], mfma(F1, P[i][0], z4));
            const f32x4 n2 = mfma(F4, P[i][1], mfma(F3, P[i][0], z4));
            const f32x4 n3 = mfma(F6, P[i][2], mfma(F5, P[i][1], z4));
            const f32x4 n4 = mfma(F8, P[i][2], mfma(F7, P[i][1], z4));
            const float og = ogv[i];
            const float r0 = og * __builtin_amdgcn_rcpf(dn[0] + 1e-6f), r1 = og * __builtin_amdgcn_rcpf(dn[1] + 1e-6f);
            const float r2 = og * __builtin_amdgcn_rcpf(dn[2] + 1e-6f), r3 = og * __builtin_amdgcn_rcpf(dn[3] + 1e-6f);
            const float s1 = q < 1 ? r0 : r1, s2 = q < 2 ? r1 : r2, s3 = q < 3 ? r2 : r3;
            os[i][0] = (u32x4){pk2(n0[0] * r0, n0[1] * r0), pk2(n0[2] * r0, n0[3] * r0), pk2(n1[0] * s1, n1[1] * s1), pk2(n1[2] * s1, n1[3] * s1)};
            os[i][1] = (u32x4){pk2(n2[0] * s2, n2[1] * s2), pk2(n2[2] * s2, n2[3] * s2), pk2(n3[0] * s3, n3[1] * s3), pk2(n3[2] * s3, n3[3] * s3)};
            o8[i] = (u32x2){pk2(n4[0] * r3, n4[1] * r3), pk2(n4[2] * r3, n4[3] * r3)};
        }
#pragma unroll
        for (int i = 0; i < 2; ++i) {
            const unsigned ob = (8 * ti + 4 * mh + 2 * nh + i) * TS + (40 + 5 * uu) * 1024;
            st16_wt(ract, lane * 16, ob + (odd ? 3072 : 0), os[i][0]);
            st16_wt(ract, lane * 16, ob + (odd ? 4096 : 1024), os[i][1]);
            st8_wt(ract, lane * 16 + 8 * odd, ob + 2048, o8[i]);
        }
    }
    STAMP(a.ws, cw, 7);
    word_wait(sm + L_HS, 3u);
    STAMP(a.ws, cw, 8);
    {
        LAS float* hs = (LAS float*)(sm + A_HSEG) + (4 * mh + q) * 160 + 80 * nh + c;
        LAS float* gt = (LAS float*)(sm + A_G) + 64 * mh + 16 * q;
        LAS char* ht = sm + A_HT + (80 * nh + c) * HT_STRIDE + (64 * mh + 4 * q) * 2;
        if (nh == 0) a_pass2<0>(acc, ca, cb, hs, gt, ht); else a_pass2<1>(acc, ca, cb, hs, gt, ht);
    }
    signal_stores(sm, L_DONE2, a.ws + WS_FACT + ti * 256 + 128 + gj * 8, tag, lane, 4u);
    KEEP(os[0][0]); KEEP(os[0][1]); KEEP(os[1][0]); KEEP(os[1][1]);
    STAMP(a.ws, cw, 9);
    wait_lgkm0();
    if (lane == 0) word_add(sm + L_HT);
    word_wait(sm + L_HT, 4u);
    {
        const int qq = (lane & 15) >> 2, p = lane & 3;
        bf16x8 hf[10];
#pragma unroll
        for (int i = 0; i < 10; ++i) {
            const int f = cw + 4 * i, mt = f / 5, ks = f - 5 * mt;
            LAS char* b = sm + A_HT + (32 * ks + 8 * q + qq) * HT_STRIDE + 32 * mt + 8 * p;
            hf[i] = lds_tr_frag(b, b + 4 * HT_STRIDE);
        }
        SCHED();
#pragma unroll
        for (int i = 0; i < 10; ++i) {
            const int f = cw + 4 * i, mt = f / 5, ks = f - 5 * mt;
            st16_wt(ract, lane * 16, (8 * ti + mt) * TS + (5 * gj + ks) * 1024, __builtin_bit_cast(u32x4, hf[i]));
        }
    }
    wait_lgkm0();
    STAMP(a.ws, cw, 10);
    signal_stores(sm, L_DONE, a.ws + WS_FACT + ti * 256 + gj * 8, tag, lane, 4u);
    STAMP(a.ws, cw, 11);
    p2_final(a, sm, cw, lane, tag);
}

DEV void job_table(const Args& a, LAS char* tab, int jb, int lane) {
    if (jb < 400) {
        float ar, ai, cr, ci;
        channel_consts(a, 64 * (jb / 40) + lane, ar, ai, cr, ci);
        ((LAS f32x2*)tab)[lane] = (f32x2){2.f * cr, 2.f * ci};
    }
}
DEV void job_issue(const Args& a, LAS char* stg, int jb, int lane) {
    if (jb < 400) {
        const int cb = jb / 40, nt = jb - 40 * cb;
        const unsigned o = (16 * nt + (lane >> 3)) * 1280 + cb * 128 + (lane & 7) * 16;
        glds16(a.cre + o, stg); glds16(a.cre + o + 8 * 1280, stg + 1024);
        glds16(a.cim + o, stg + 2048); glds16(a.cim + o + 8 * 1280, stg + 3072);
    } else if (jb < 500) {
        const int jo = jb - 400, s5 = jo / 5, n8 = jo - 5 * s5, uu = s5 / 5, s = s5 - 5 * uu;
        const int lo = 160 * uu + (s == 0 ? 0 : s == 1 ? 32 : s == 2 ? 64 : s == 3 ? 80 : 112);
        const int hi = 160 * uu + (s == 0 ? 16 : s == 1 ? 48 : s == 2 ? 144 : s == 3 ? 96 : 128);
        const unsigned o = (lane >> 4) * 1280 + n8 * 256 + (lane & 15) * 16;
#pragma unroll
        for (int k = 0; k < 4; ++k) {
            glds16(a.wo + (lo + 4 * k) * 1280 + o, stg + 1024 * k);
            glds16(a.wo + (hi + 4 * k) * 1280 + o, stg + 4096 + 1024 * k);
        }
    }
}
DEV u32x2 lds_rd8(LAS char* p) { u32x2 v; asm volatile("ds_read_b64 %0, %1" : "=v"(v) : "v"((unsigned)(size_t)p) : "memory"); return v; }
DEV f32x4 lds_rd16(LAS char* p) { f32x4 v; asm volatile("ds_read_b128 %0, %1" : "=v"(v) : "v"((unsigned)(size_t)p) : "memory"); return v; }
DEV u32x2 lds_rdtr(LAS char* p) { u32x2 v; asm volatile("ds_read_b64_tr_b16 %0, %1" : "=v"(v) : "v"((unsigned)(size_t)p) : "memory"); return v; }
DEV void job_finish(const Args& a, LAS char* stg, LAS char* tab, int jb, int lane) {
    const int c = lane & 15, q = lane >> 4;
    const __amdgpu_buffer_rsrc_t rw = rsrc(a.ws + WS_W2T, 40 * TS);
    if (jb < 400) {
        const int cb = jb / 40, nt = jb - 40 * cb;
        u32x2 re[4], im[4]; f32x4 c01[4], c23[4];
#pragma unroll
        for (int k = 0; k < 4; ++k) {
            re[k] = lds_rd8(stg + c * 128 + 32 * k + 8 * q); im[k] = lds_rd8(stg + 2048 + c * 128 + 32 * k + 8 * q);
            c01[k] = lds_rd16(tab + (16 * k + 4 * q) * 8); c23[k] = lds_rd16(tab + (16 * k + 4 * q) * 8 + 16);
        }
        asm volatile("s_waitcnt lgkmcnt(0)" : "+v"(re[0]), "+v"(re[1]), "+v"(re[2]), "+v"(re[3]), "+v"(im[0]), "+v"(im[1]), "+v"(im[2]), "+v"(im[3]),
                     "+v"(c01[0]), "+v"(c01[1]), "+v"(c01[2]), "+v"(c01[3]), "+v"(c23[0]), "+v"(c23[1]), "+v"(c23[2]), "+v"(c23[3]) :: "memory");
        u32x4 w[4];
#pragma unroll
        for (int k = 0; k < 4; ++k) {
            const float r0 = bf_lo(re[k][0]), r1 = bf_hi(re[k][0]), r2 = bf_lo(re[k][1]), r3 = bf_hi(re[k][1]);
            const float i0 = bf_lo(im[k][0]), i1 = bf_hi(im[k][0]), i2 = bf_lo(im[k][1]), i3 = bf_hi(im[k][1]);
            w[k] = (u32x4){pk2(r0 * c01[k][0] - i0 * c01[k][1], -(r0 * c01[k][1] + i0 * c01[k][0])),
                           pk2(r1 * c01[k][2] - i1 * c01[k][3], -(r1 * c01[k][3] + i1 * c01[k][2])),
                           pk2(r2 * c23[k][0] - i2 * c23[k][1], -(r2 * c23[k][1] + i2 * c23[k][0])),
                           pk2(r3 * c23[k][2] - i3 * c23[k][3], -(r3 * c23[k][3] + i3 * c23[k][2]))};
        }
#pragma unroll
        for (int k = 0; k < 4; ++k) st16_wt(rw, lane * 16, nt * TS + (4 * cb + k) * 1024, w[k]);
    } else if (jb < 500) {
        const int jo = jb - 400, s5 = jo / 5, n8 = jo - 5 * s5;
        u32x2 lo[8], hi[8];
#pragma unroll
        for (int k = 0; k < 8; ++k) {
            LAS char* b = stg + (4 * q + (c >> 2)) * 256 + 32 * k + 8 * (c & 3);
            lo[k] = lds_rdtr(b); hi[k] = lds_rdtr(b + 4096);
        }
        asm volatile("s_waitcnt lgkmcnt(0)" : "+v"(lo[0]), "+v"(lo[1]), "+v"(lo[2]), "+v"(lo[3]), "+v"(lo[4]), "+v"(lo[5]), "+v"(lo[6]), "+v"(lo[7]),
                     "+v"(hi[0]), "+v"(hi[1]), "+v"(hi[2]), "+v"(hi[3]), "+v"(hi[4]), "+v"(hi[5]), "+v"(hi[6]), "+v"(hi[7]) :: "memory");
#pragma unroll
        for (int k = 0; k < 8; ++k)
            st16_wt(rw, lane * 16, (8 * n8 + k) * TS + (40 + s5) * 1024, (u32x4){lo[k][0], lo[k][1], hi[k][0], hi[k][1]});
    }
}

DEV void b_loader(const Args& a, LAS char* sm, int ti, int gj, int lw, int lane) {
    STAMP(a.ws, lw + 4, 0);
    const unsigned nonce = __hip_atomic_load((GAS unsigned*)(a.ws + WS_NONCE), RLX_AGENT);
    unsigned off[9];
    {
        const int pos = lane & 7, rl = lane >> 3;
#pragma unroll
        for (int u = 0; u < 4; ++u) { const int R = 8 * (lw + 4 * u) + rl; off[u] = R * 1280 + ((pos ^ (R & 7)) << 4); }
#pragma unroll
        for (int u = 4; u < 9; ++u) {
            const int wi = lw + 4 * (u - 4), sec = wi >= 10, ch = 64 * (wi - 10 * sec) + lane, rho = ch / 10, cc = ch - 10 * rho;
            off[u] = (rho ^ (((rho >> 3) & 1) << 2)) * 3840 + cc * 16 + 1280 + 1280 * sec;
        }
    }
    const char* xb = a.x + (size_t)ti * (128 * 1280);
    const char* wb = a.wqkv + 160 * gj;
    auto issue_a = [&](int s) LAM {
        LAS char* dst = sm + (s % NS_B) * SLOT_B + lw * 1024;
#pragma unroll
        for (int u = 0; u < 4; ++u) glds16(xb + 128 * s + off[u], dst + 4096 * u);
        glds16(wb + (size_t)(64 * 3840) * s + off[4], dst + 4096 * 4);
    };
    auto issue_b = [&](int s) LAM {
        LAS char* dst = sm + (s % NS_B) * SLOT_B + lw * 1024;
#pragma unroll
        for (int u = 5; u < 9; ++u) glds16(wb + (size_t)(64 * 3840) * s + off[u], dst + 4096 * u);
    };
    STAMP(a.ws, lw + 4, 1);
    const int jb = (ti * 8 + gj) * 4 + lw;
    LAS char* stg = sm + B_STG + lw * 8192;
    issue_a(0); issue_b(0);
    if (JOBMODE == 0) job_issue(a, stg, jb, lane);
    issue_a(1); issue_b(1); issue_a(2); issue_b(2);
    WAIT_VM(18);
    bar();
    if (JOBMODE == 0) job_finish(a, stg, sm + B_TAB + lw * 512, jb, lane);
#pragma unroll 1
    for (int s = 0; s < 7; ++s) {
        if (s > 0) ring_wait(sm + L_FREE, s);
        issue_a(s + 3);
        if (JOBMODE == 0 && s < 2 && jb < 400) WAIT_VM(18); else if (JOBMODE == 0 && s < 2 && jb < 500) WAIT_VM(22); else WAIT_VM(14);
        ring_put(sm + L_FULL + 4 * lw, s + 2);
        issue_b(s + 3);
    }
    WAIT_VM(9);
    ring_put(sm + L_FULL + 4 * lw, 9);
    WAIT_VM(0);
    ring_put(sm + L_FULL + 4 * lw, 10);
    const unsigned tag = mk_tag(nonce);
    if (JOBMODE == 0) signal_stores(sm, L_DONE2, a.ws + WS_FW2T + (ti * 8 + gj) * 8, tag, lane, 4u);
    STAMP(a.ws, lw + 4, 2);
    if (JOBMODE == 2) {
        ring_wait(sm + L_FREE, 8);
        job_issue(a, stg, jb, lane);
        WAIT_VM(0);
        job_finish(a, stg, sm + B_TAB + lw * 512, jb, lane);
        STAMP(a.ws, lw + 4, 6);
    }
    __builtin_amdgcn_s_setprio(2);
    const int tl = lw * 64 + lane;
    {
        int o0[2], o1[2];
#pragma unroll
        for (int it = 0; it < 2; ++it) {
            const int g = 2 * (tl + 256 * it < 420 ? tl + 256 * it : 0);
            const int h = g / 210, rem = g - 210 * h, e = rem / 10, d = 2 * (rem - 10 * e);
            const bool kv = rem < 200;
            o0[it] = kv ? B_KVT + ((20 * h + e) * 84 + 20 * h + d) * 4 : B_Z + (20 * h + 2 * (rem - 200)) * 4;
            o1[it] = o0[it] + (kv ? 80 * 84 * 4 : 320);
        }
        asm volatile("" : "+v"(o0[0]), "+v"(o0[1]), "+v"(o1[0]), "+v"(o1[1]));
        const __amdgpu_buffer_rsrc_t rk = rsrc(a.ws + WS_KV, 16 * 8 * 840 * 8);
        word_wait(sm + L_KV, 4u);
        STAMP(a.ws, lw + 4, 3);
        u32x4 pv[2];
#pragma unroll
        for (int it = 0; it < 2; ++it) {
            const f32x4 v = *(LAS f32x4*)(sm + o0[it]) + *(LAS f32x4*)(sm + o1[it]);
            pv[it] = (u32x4){pk2(v[0], v[1]), tag, pk2(v[2], v[3]), tag};
        }
        asm volatile("" : "+v"(pv[0]), "+v"(pv[1]) :: "memory");
        st16_wt(rk, tl * 16, (ti * 8 + gj) * 6720, pv[0]);
        if (tl + 256 < 420) st16_wt(rk, (tl + 256) * 16, (ti * 8 + gj) * 6720, pv[1]);
        asm volatile("s_nop 1" ::"v"(pv[0]), "v"(pv[1]));
    }
    STAMP(a.ws, lw + 4, 4);
    if (lw == 0) {
        const int cnt = ti < 8 ? 53 : 52, g0 = ti < 8 ? 53 * ti : 424 + 52 * (ti - 8);
        const int g = g0 + (lane < cnt ? lane : 0);
        GAS u64* src = (GAS u64*)(a.ws + WS_KV) + gj * 840 + g;
        unsigned v[16];
        for (unsigned spins = 0;;) {
            bool ok = true;
#pragma unroll
            for (int i = 0; i < 16; ++i) {
                const u64 x = __hip_atomic_load(src + i * (8 * 840), RLX_AGENT);
                v[i] = (unsigned)x; ok &= (unsigned)(x >> 32) == tag;
            }
            if (wave_all(ok) || ++spins > SPIN_CAP) break;
            __builtin_amdgcn_s_sleep(1);
        }
        float s0 = 0.f, s1 = 0.f;
#pragma unroll
        for (int i = 0; i < 16; ++i) { s0 += bf_lo(v[i]); s1 += bf_hi(v[i]); }
        if (lane < cnt) __hip_atomic_store((GAS u64*)(a.ws + WS_TOT) + gj * 840 + g, granule(tag, pk2(s0, s1)), RLX_AGENT);
        word_set(sm + L_H2, 1u);
    }
    STAMP(a.ws, lw + 4, 5);
    __builtin_amdgcn_s_setprio(0);
    if (JOBMODE == 2) signal_stores(sm, L_DONE2, a.ws + WS_FW2T + (ti * 8 + gj) * 8, tag, lane, 4u);
    p2_main(a, sm, lw, lane, nonce);
}

DEV void b_consumer(const Args& a, LAS char* sm, int ti, int gj, int cw, int lane) {
    const int mh = cw >> 1, nh = cw & 1, c = lane & 15, q = lane >> 4, tid = cw * 64 + lane;
    STAMP(a.ws, cw, 0);
    const unsigned tag = mk_tag(__hip_atomic_load((GAS unsigned*)(a.ws + WS_NONCE), RLX_AGENT));
    if (JOBMODE != 1) job_table(a, sm + B_TAB + cw * 512, (ti * 8 + gj) * 4 + cw, lane);
    {
        const __amdgpu_buffer_rsrc_t rk = rsrc(a.ws + WS_KV, 16 * 8 * 840 * 8);
        const u32x4 z = {0u, 0u, 0u, 0u};
        st16_wt(rk, tid * 16, (ti * 8 + gj) * 6720, z);
        if (tid + 256 < 420) st16_wt(rk, (tid + 256) * 16, (ti * 8 + gj) * 6720, z);
        const int cnt = ti < 8 ? 53 : 52, g0 = ti < 8 ? 53 * ti : 424 + 52 * (ti - 8);
        if (tid < cnt) __hip_atomic_store((GAS u64*)(a.ws + WS_TOT) + gj * 840 + g0 + tid, (u64)0, RLX_AGENT);
    }
    __builtin_amdgcn_s_setprio(1);
    f32x4 an[5][4];
#pragma unroll
    for (int i = 0; i < 4; ++i)
#pragma unroll
        for (int t = 0; t < 5; ++t) an[t][i] = (f32x4){0.f, 0.f, 0.f, 0.f};
    int xo[2], wA[2], wB[2];
#pragma unroll
    for (int kk = 0; kk < 2; ++kk) {
        xo[kk] = (64 * mh + c) * 128 + (((4 * kk + q) ^ (c & 7)) << 4);
        const int b = 16384 + (8 * q + 4 * (kk ^ (q & 1)) + (c >> 2)) * 160 + 8 * (c & 3);
        wA[kk] = b + 96 * nh; wB[kk] = b + 64 + 10240 * nh;
    }
    bf16x8 xa[2][4], wb[2][5];
    auto rdp = [&](int buf, LAS char* s0, int kk) LAM {
#pragma unroll
        for (int i = 0; i < 4; ++i) xa[buf][i] = lds_frag(s0 + xo[kk] + 2048 * i);
        LAS char* w0 = s0 + kk * 5120;
        wb[buf][0] = lds_tr_frag(w0 + wA[0], w0 + wA[1]);
        wb[buf][1] = lds_tr_frag(w0 + wA[0] + 32, w0 + wA[1] + 32);
        wb[buf][2] = lds_tr_frag(w0 + wB[0], w0 + wB[1]);
        wb[buf][3] = lds_tr_frag(w0 + wA[0] + 10240, w0 + wA[1] + 10240);
        wb[buf][4] = lds_tr_frag(w0 + wA[0] + 10272, w0 + wA[1] + 10272);
    };
    auto rd = [&](int buf, int slot, int kk) LAM { rdp(buf, sm + slot * SLOT_B, kk); };
    auto mm_head = [&](int buf) LAM {
#pragma unroll
        for (int i = 0; i < 4; ++i) an[0][i] = mfma(xa[buf][i], wb[buf][0], an[0][i]);
    };
    auto mm_tail = [&](int buf) LAM {
#pragma unroll
        for (int t = 1; t < 5; ++t)
#pragma unroll
            for (int i = 0; i < 4; ++i) an[t][i] = mfma(xa[buf][i], wb[buf][t], an[t][i]);
    };
    if (tid < L_WORDS) ((LAS unsigned*)(sm + L_FULL))[tid] = tid < 4 ? 1u : 0u;
#pragma unroll
    for (int t = 0; t < 5; ++t)
        asm volatile("" : "+v"(an[t][0]), "+v"(an[t][1]), "+v"(an[t][2]), "+v"(an[t][3]));
    STAMP(a.ws, cw, 1);
    lds_bar();
    STAMP(a.ws, cw, 2);
    rd(0, 0, 0);
#pragma unroll 1
    for (int s = 0; s < 10; ++s) {
        wait_lgkm0();
        SCHED();
        rd(1, s & 3, 1);
        u32x4 fl = *(volatile LAS u32x4*)(sm + L_FULL);
        mm_head(0); mm_tail(0);
        interleave<20, 15, IL_B>();
        SCHED();
        wait_lgkm0();
        asm volatile("" ::: "memory");
        *(volatile LAS unsigned*)(sm + L_FREE + 4 * cw) = s + 1;
        const unsigned need = s < 9 ? s + 2 : 0;
        unsigned landed = __builtin_amdgcn_readfirstlane(umin4(fl));
        LAS char* s0 = sm + ((s + 1) & 3) * SLOT_B;
        asm volatile("" : "+s"(landed), "+v"(s0));
        mm_head(1);
        SCHED();
        if (__builtin_expect(landed < need, 0)) ring_spin(sm + L_FULL, need);
        asm volatile("" ::: "memory");
        SCHED();
        rdp(0, s0, 0);
        mm_tail(1);
        interleave<16, 14, 14>();
        SCHED();
    }
    STAMP(a.ws, cw, 3);
    bf16x8 fr[5][2];
#pragma unroll
    for (int t = 0; t < 2; ++t)
#pragma unroll
        for (int i = 0; i < 4; ++i)
#pragma unroll
            for (int r = 0; r < 4; ++r) an[t][i][r] = phi(an[t][i][r]);
    if (nh == 0) {
#pragma unroll
        for (int i = 0; i < 4; ++i)
#pragma unroll
            for (int r = 0; r < 4; ++r) an[2][i][r] = phi(an[2][i][r]);
    }
#pragma unroll
    for (int t = 0; t < 5; ++t) { fr[t][0] = pack_pair(an[t][0], an[t][1]); fr[t][1] = pack_pair(an[t][2], an[t][3]); }
    {
        LAS char* xs = sm + B_X1 + cw * 2048 + lane * 16;
        *(LAS bf16x8*)xs = fr[2][0];
        *(LAS bf16x8*)(xs + 1024) = fr[2][1];
    }
    wait_lgkm0();
    word_set(sm + L_X1 + 4 * cw, 1u);
    STAMP(a.ws, cw, 4);
    word_wait(sm + L_X1 + 4 * (cw ^ 1), 1u);
    ring_spin(sm + L_FREE, 10);
    asm volatile("" ::: "memory");
    {
        LAS char* xr = sm + B_X1 + (cw ^ 1) * 2048 + lane * 16;
        const bf16x8 R0 = *(LAS bf16x8*)xr, R1 = *(LAS bf16x8*)(xr + 1024);
        const bf16x8 ones = as_frag((u32x4){0x3f803f80u, 0x3f803f80u, 0x3f803f80u, 0x3f803f80u});
        const f32x4 z4 = {0.f, 0.f, 0.f, 0.f};
        LAS float* kvt = (LAS float*)(sm + B_KVT) + mh * (80 * 84) + c * 84 + 4 * q;
        LAS float* zz = (LAS float*)(sm + B_Z) + mh * 80 + 4 * q;
        auto prod = [&](const bf16x8& k0, const bf16x8& k1, const bf16x8& v0, const bf16x8& v1, int ta, int tb) LAM {
            *(LAS f32x4*)(kvt + 16 * tb * 84 + 16 * ta) = mfma(k1, v1, mfma(k0, v0, z4));
        };
        auto zsum = [&](const bf16x8& k0, const bf16x8& k1, int ta) LAM {
            const f32x4 d = mfma(k1, ones, mfma(k0, ones, z4));
            if (c == 0) *(LAS f32x4*)(zz + 16 * ta) = d;
        };
        const int b0 = 3 * nh;
        prod(fr[0][0], fr[0][1], fr[3][0], fr[3][1], b0, b0);
        prod(fr[0][0], fr[0][1], fr[4][0], fr[4][1], b0, b0 + 1);
        prod(fr[1][0], fr[1][1], fr[3][0], fr[3][1], b0 + 1, b0);
        prod(fr[1][0], fr[1][1], fr[4][0], fr[4][1], b0 + 1, b0 + 1);
        zsum(fr[0][0], fr[0][1], b0);
        zsum(fr[1][0], fr[1][1], b0 + 1);
        if (nh == 0) {
            prod(fr[2][0], fr[2][1], fr[4][0], fr[4][1], 2, 1);
            prod(fr[1][0], fr[1][1], R0, R1, 1, 2);
            prod(fr[2][0], fr[2][1], R0, R1, 2, 2);
            zsum(fr[2][0], fr[2][1], 2);
        } else {
            prod(fr[0][0], fr[0][1], fr[2][0], fr[2][1], 3, 2);
            prod(R0, R1, fr[3][0], fr[3][1], 2, 3);
        }
    }
    WAIT_VM(0);
    wait_lgkm0();
    if (lane == 0) word_add(sm + L_KV);
    __builtin_amdgcn_s_setprio(0);
    STAMP(a.ws, cw, 5);
    if (JOBMODE == 1) {
        const int jb = (ti * 8 + gj) * 4 + cw;
        LAS char* stg = sm + cw * 8192;
        job_table(a, sm + B_TAB + cw * 512, jb, lane);
        word_wait(sm + L_H2, 1u);
        job_issue(a, stg, jb, lane);
        WAIT_VM(0);
        wait_lgkm0();
        job_finish(a, stg, sm + B_TAB + cw * 512, jb, lane);
        STAMP(a.ws, cw, 6);
        signal_stores(sm, L_DONE, a.ws + WS_FW2T + (ti * 8 + gj) * 8, tag, lane, 4u);
        STAMP(a.ws, cw, 7);
    } else if (lane == 0) word_add(sm + L_DONE);
    p2_final(a, sm, cw, lane, tag);
}

}

__global__ __launch_bounds__(512, 2) NOPK void gm_fused(const void* x, const void* lre, const void* lim, const void* bre,
                                                   const void* bim, const void* cre, const void* cim, const void* logdt,
                                                   const void* dskip, const void* wqkv, const void* wo, const void* gw,
                                                   void* ws, void* out) {
    __shared__ __attribute__((aligned(1024))) char smem[LDS_BYTES];
    LAS char* sm = (LAS char*)smem;
#define MAPMODE 1
    const int b = blockIdx.x, xcd = b & 7, idx = b >> 3;
    const bool roleA = MAPMODE == 1 ? idx < 16 : xcd < 4;
    const int ti = MAPMODE == 1 ? 4 * (xcd >> 1) + (idx & 3) : 8 * ((xcd & 3) >> 1) + (idx & 7);
    const int gj = MAPMODE == 1 ? 4 * (xcd & 1) + ((idx >> 2) & 3) : 4 * (xcd & 1) + (idx >> 3);
    const int wave = __builtin_amdgcn_readfirstlane(threadIdx.x >> 6), lane = threadIdx.x & 63;
    const Args a{(const char*)x, (const u16*)lre, (const u16*)lim, (const char*)bre, (const char*)bim, (const char*)cre,
                 (const char*)cim, (const u16*)logdt, (const char*)wqkv, (const char*)wo, (const u16*)gw,
                 (const u16*)dskip, (char*)ws, (float*)out};
    STAMPL(ws, 0);
    if (roleA) {
        if (wave < 4) a_consumer(a, sm, ti, gj, wave, lane);
        else a_loader(a, sm, ti, gj, wave - 4, lane);
    } else {
        if (wave < 4) b_consumer(a, sm, ti, gj, wave, lane);
        else b_loader(a, sm, ti, gj, wave - 4, lane);
    }
    STAMPL(ws, 1);
}

extern "C" void kernel_launch(void* const* d_in, const int* in_sizes, int n_in, void* d_out, int out_size, void* d_ws,
                              size_t ws_size, hipStream_t stream) {
    gm_fused<<<256, 512, 0, stream>>>(d_in[0], d_in[1], d_in[2], d_in[3], d_in[4], d_in[5], d_in[6], d_in[7], d_in[8],
                                      d_in[10], d_in[11], d_in[12], d_ws, d_out);
}
```

```cpp
#include <hip/hip_runtime.h>
#include <hip/hip_bf16.h>
#include <stdint.h>

namespace {

typedef __attribute__((ext_vector_type(8))) short bf16x8;
typedef __attribute__((ext_vector_type(4))) short s16x4;
typedef __attribute__((ext_vector_type(4))) float f32x4;
typedef __attribute__((ext_vector_type(2))) float f32x2;
typedef __attribute__((ext_vector_type(2))) __bf16 bf16x2_t;
typedef __attribute__((ext_vector_type(4))) unsigned u32x4;
typedef __attribute__((ext_vector_type(2))) unsigned u32x2;
typedef unsigned long long u64;
typedef unsigned short u16;
#define LAS __attribute__((address_space(3)))
#define GAS __attribute__((address_space(1)))
#if defined(__HIP_DEVICE_COMPILE__)
#define NOPK __attribute__((target("no-packed-fp32-ops")))
#else
#define NOPK
#endif
#define DEV __device__ __forceinline__
#define LAM __attribute__((always_inline))
#define RLX_AGENT __ATOMIC_RELAXED, __HIP_MEMORY_SCOPE_AGENT

constexpr size_t WS_NONCE = 0;
constexpr size_t WS_EA    = 4096;
constexpr size_t WS_KV    = WS_EA + 16 * 8 * 160 * 8;
constexpr size_t WS_TOT   = WS_KV + 16 * 8 * 840 * 8;
constexpr size_t WS_G     = WS_TOT + 8 * 840 * 8;
constexpr size_t WS_FACT  = WS_G + 2048 * 8;
constexpr size_t WS_FW2T  = WS_FACT + 16 * 256;
constexpr size_t WS_W2T   = (WS_FW2T + 128 * 8 + 1023) / 1024 * 1024;
constexpr size_t TS = 61440;
constexpr size_t WS_ACT   = WS_W2T + 40 * TS;
constexpr size_t WS_PROF  = 32u << 20;

#define STAMP(ws, wave, i) do {} while (0)
#define STAMPC(ws, wave, i) do {} while (0)
#define TIMED(acc, stmt) do { stmt; } while (0)
#define STAMPV(ws, wave, i, v) do {} while (0)
#define STAMP2(ws, wave, i) do {} while (0)

#define STAMPL(ws, i) do {} while (0)

constexpr int LDS_BYTES = 163840;
constexpr int SLOT_A = 47104, NS_A = 3;
constexpr int A_TAB  = 141312;
constexpr int A_G    = A_TAB + 3840;
constexpr int A_ESEG = A_G + 1024;
constexpr int A_HSEG = A_ESEG + 5120;
constexpr int A_TOT  = A_HSEG + 5120;
constexpr int A_HT = 47104, HT_STRIDE = 272;
static_assert(A_TOT + 3360 <= 159776, "lds");
constexpr int SLOT_B = 36864, NS_B = 4;
constexpr int B_X1  = 147456;
constexpr int B_KVT = 0;
constexpr int B_Z   = B_KVT + 2 * 80 * 84 * 4;
constexpr int B_TAB = 155648;
constexpr int B_STG = 110592;
static_assert(B_Z + 640 <= B_X1, "lds");

#define JOBMODE 2
#define SLP_W2T 16
#define SLP_ACT 4
#define SENT_LB 0
#define SENT_TOT 0
#define SLP_LB 2
#define SLP_TOT 2
#define HEAD_A 4
#define IL_A 25
#define IL_B 16
constexpr unsigned SPIN_CAP = 1u << 18;

DEV unsigned pk2(float a, float b) {
    return __builtin_bit_cast(unsigned, __builtin_convertvector((f32x2){a, b}, bf16x2_t));
}
DEV float bfr(float a) { return __uint_as_float(pk2(a, 0.f) << 16); }
DEV float bf_lo(unsigned u) { return __uint_as_float(u << 16); }
DEV float bf_hi(unsigned u) { return __uint_as_float(u & 0xffff0000u); }
DEV float bf2f(u16 u) { return __uint_as_float((unsigned)u << 16); }
DEV float swp(float x) {
    return __builtin_bit_cast(float, __builtin_amdgcn_mov_dpp(__builtin_bit_cast(int, x), 0xB1, 0xF, 0xF, true));
}
DEV float cmul_add(float a, float bs, float e, float u) { return fmaf(bs, swp(e), fmaf(a, e, u)); }
DEV f32x4 mfma(bf16x8 a, bf16x8 b, f32x4 c) { return __builtin_amdgcn_mfma_f32_16x16x32_bf16(a, b, c, 0, 0, 0); }
DEV bf16x8 as_frag(u32x4 v) { return __builtin_bit_cast(bf16x8, v); }
DEV bf16x8 lds_frag(LAS char* p) { return *(LAS bf16x8*)p; }
DEV bf16x8 lds_tr_frag(LAS char* p0, LAS char* p1) {
    s16x4 lo = __builtin_amdgcn_ds_read_tr16_b64_v4i16((LAS s16x4*)p0);
    s16x4 hi = __builtin_amdgcn_ds_read_tr16_b64_v4i16((LAS s16x4*)p1);
    return __builtin_shufflevector(lo, hi, 0, 1, 2, 3, 4, 5, 6, 7);
}
DEV void glds16_wt(const char* g, unsigned l) {
    unsigned keep;
    asm volatile("s_mov_b32 %0, m0\n\ts_mov_b32 m0, %2\n\ts_nop 0\n\tglobal_load_lds_dwordx4 %1, off sc1\n\ts_mov_b32 m0, %0"
                 : "=&s"(keep) : "v"(g), "s"(l) : "memory");
}
DEV void glds16(const char* g, LAS char* l) {
    __builtin_amdgcn_global_load_lds((const GAS unsigned*)g, (LAS unsigned*)l, 16, 0, 0);
}
DEV void bar() { asm volatile("" ::: "memory"); __builtin_amdgcn_s_barrier(); asm volatile("" ::: "memory"); }
DEV void wait_lgkm0() { __builtin_amdgcn_s_waitcnt(0xC07F); }
DEV void lds_bar() { asm volatile("" ::: "memory"); wait_lgkm0(); bar(); }
constexpr int L_FULL = 159776, L_FREE = L_FULL + 16;
constexpr int L_DONE = L_FULL + 32;
constexpr int L_KV = L_FULL + 36, L_W2T = L_FULL + 40, L_ACT = L_FULL + 44;
constexpr int L_X1 = L_FULL + 48, L_ZERO = L_FULL + 64;
constexpr int L_DONE2 = L_FULL + 72, L_ACT2 = L_FULL + 76;
constexpr int L_H2 = L_FULL + 80;
constexpr int L_WR = L_FULL + 84;
constexpr int L_TOT = L_FULL + 88, L_LBS = L_FULL + 92;
constexpr int L_HS = L_FULL + 96, L_HT = L_FULL + 100;
constexpr int L_WORDS = 26;
DEV unsigned umin4(const u32x4& f) { const unsigned a = f[0] < f[1] ? f[0] : f[1], b = f[2] < f[3] ? f[2] : f[3]; return a < b ? a : b; }
DEV void ring_put(LAS char* p, unsigned v) {
    asm volatile("ds_write_b32 %0, %1" ::"v"((unsigned)(size_t)p), "v"(v) : "memory");
}
DEV void ring_wait(LAS char* p, unsigned need) {
    for (unsigned spins = 0; spins < SPIN_CAP; ++spins) {
        u32x4 f;
        asm volatile("ds_read_b128 %0, %1\n\ts_waitcnt lgkmcnt(0)" : "=&v"(f) : "v"((unsigned)(size_t)p) : "memory");
        if ((unsigned)__builtin_amdgcn_readfirstlane(umin4(f)) >= need) break;
        __builtin_amdgcn_s_sleep(1);
    }
}
DEV void word_wait(LAS char* p, unsigned need) {
    for (unsigned spins = 0; spins < SPIN_CAP; ++spins) {
        if ((unsigned)__builtin_amdgcn_readfirstlane(*(volatile LAS unsigned*)p) >= need) break;
        __builtin_amdgcn_s_sleep(1);
    }
    asm volatile("" ::: "memory");
}
DEV void word_set(LAS char* p, unsigned v) { asm volatile("" ::: "memory"); *(volatile LAS unsigned*)p = v; }
DEV unsigned word_add(LAS char* p) {
    return __hip_atomic_fetch_add((LAS unsigned*)p, 1u, __ATOMIC_RELAXED, __HIP_MEMORY_SCOPE_WORKGROUP);
}
DEV void ring_spin(LAS char* p, unsigned need) {
    for (unsigned spins = 0; spins < SPIN_CAP; ++spins) {
        const u32x4 f = *(volatile LAS u32x4*)p;
        if ((unsigned)__builtin_amdgcn_readfirstlane(umin4(f)) >= need) break;
    }
}
#define WAIT_VM(n) asm volatile("s_waitcnt vmcnt(" #n ")" ::: "memory")
#define SCHED() __builtin_amdgcn_sched_barrier(0)
#define KEEP(x) asm volatile("" ::"v"(x))
template <int NM, int NR, int NS> DEV void interleave() {
#pragma unroll
    for (int i = 0; i < NS; ++i) {
        __builtin_amdgcn_sched_group_barrier(0x8, 1, 0);
        if ((i + 1) * NR / NS != i * NR / NS) __builtin_amdgcn_sched_group_barrier(0x100, 1, 0);
    }
    __builtin_amdgcn_sched_group_barrier(0x8, NM - NS, 0);
}
DEV bool wave_all(bool p) { return __builtin_amdgcn_ballot_w64(p) == __builtin_amdgcn_ballot_w64(true); }
DEV float lane_xor(float v, int m) {
    const int x = __builtin_bit_cast(int, v);
    const int y = m == 1 ? __builtin_amdgcn_mov_dpp(x, 0xB1, 0xF, 0xF, true) : m == 2 ? __builtin_amdgcn_mov_dpp(x, 0x4E, 0xF, 0xF, true)
                                                                              : __builtin_amdgcn_ds_swizzle(x, 0x101F);
    return __builtin_bit_cast(float, y);
}
DEV float fexp(float x) { return __builtin_amdgcn_exp2f(x * 1.44269504088896341f); }
DEV unsigned mk_tag(unsigned n) { return ((n + 1u) * 0x9E3779B1u) | 1u; }
DEV u64 granule(unsigned tag, unsigned v) { return ((u64)tag << 32) | v; }
DEV u64 flag_of(unsigned tag) { return granule(tag, tag * 0x85EBCA6Bu + 0x5BD1E995u); }
DEV __amdgpu_buffer_rsrc_t rsrc(const char* p, unsigned bytes) { return __builtin_amdgcn_make_buffer_rsrc((void*)p, 0, bytes, 0x00020000); }
DEV void st16_wt(const __amdgpu_buffer_rsrc_t& r, unsigned voff, unsigned soff, u32x4 v) { __builtin_amdgcn_raw_buffer_store_b128(v, r, voff + soff, 0, 16); }
DEV void st8_wt(const __amdgpu_buffer_rsrc_t& r, unsigned voff, unsigned soff, u32x2 v) { __builtin_amdgcn_raw_buffer_store_b64(v, r, voff + soff, 0, 16); }
DEV bf16x8 ld16_wt(const __amdgpu_buffer_rsrc_t& r, unsigned voff, unsigned soff) {
    return __builtin_bit_cast(bf16x8, __builtin_amdgcn_raw_buffer_load_b128(r, voff, soff, 16));
}
DEV void signal_stores(LAS char* sm, int word, char* flag, unsigned tag, int lane, unsigned waves) {
    asm volatile("s_waitcnt vmcnt(0)" ::: "memory");
    if (lane == 0) {
        const unsigned old = __hip_atomic_fetch_add((LAS unsigned*)(sm + word), 1u, __ATOMIC_RELAXED, __HIP_MEMORY_SCOPE_WORKGROUP);
        if (old == waves - 1u) __hip_atomic_store((GAS u64*)flag, flag_of(tag), RLX_AGENT);
    }
}
DEV float phi(float t) {
    const float inf = __builtin_inff();
    const float e = __builtin_amdgcn_exp2f(__builtin_amdgcn_fmed3f(t * 1.44269504088896341f, -inf, 0.f));
    return (e + 1e-6f) + __builtin_amdgcn_fmed3f(t, 0.f, inf);
}
DEV bf16x8 pack_pair(const f32x4& lo, const f32x4& hi) {
    return as_frag((u32x4){pk2(lo[0], lo[1]), pk2(lo[2], lo[3]), pk2(hi[0], hi[1]), pk2(hi[2], hi[3])});
}

DEV void scan_step(float& u0, float& u1, float& u2, float& u3, float& u4, const float (&e)[5], const float (&a)[5],
                   const float (&b)[5]) {
    asm("v_fmac_f32 %0, %10, %5\n\tv_fmac_f32 %1, %11, %6\n\tv_fmac_f32 %2, %12, %7\n\t"
        "v_fmac_f32 %3, %13, %8\n\tv_fmac_f32 %4, %14, %9\n\t"
        "v_fmac_f32_dpp %0, %5, %15 quad_perm:[1,0,3,2] row_mask:0xf bank_mask:0xf bound_ctrl:1\n\t"
        "v_fmac_f32_dpp %1, %6, %16 quad_perm:[1,0,3,2] row_mask:0xf bank_mask:0xf bound_ctrl:1\n\t"
        "v_fmac_f32_dpp %2, %7, %17 quad_perm:[1,0,3,2] row_mask:0xf bank_mask:0xf bound_ctrl:1\n\t"
        "v_fmac_f32_dpp %3, %8, %18 quad_perm:[1,0,3,2] row_mask:0xf bank_mask:0xf bound_ctrl:1\n\t"
        "v_fmac_f32_dpp %4, %9, %19 quad_perm:[1,0,3,2] row_mask:0xf bank_mask:0xf bound_ctrl:1"
        : "+v"(u0), "+v"(u1), "+v"(u2), "+v"(u3), "+v"(u4)
        : "v"(e[0]), "v"(e[1]), "v"(e[2]), "v"(e[3]), "v"(e[4]), "v"(a[0]), "v"(a[1]), "v"(a[2]), "v"(a[3]), "v"(a[4]),
          "v"(b[0]), "v"(b[1]), "v"(b[2]), "v"(b[3]), "v"(b[4]));
}

DEV void scan_step_nd(float (&t)[5], float u0, float u1, float u2, float u3, float u4, const float (&e)[5],
                      const float (&a)[5], const float (&b)[5]) {
    asm("v_fma_f32 %0, %15, %10, %5\n\tv_fma_f32 %1, %16, %11, %6\n\tv_fma_f32 %2, %17, %12, %7\n\t"
        "v_fma_f32 %3, %18, %13, %8\n\tv_fma_f32 %4, %19, %14, %9\n\t"
        "v_fmac_f32_dpp %0, %10, %20 quad_perm:[1,0,3,2] row_mask:0xf bank_mask:0xf bound_ctrl:1\n\t"
        "v_fmac_f32_dpp %1, %11, %21 quad_perm:[1,0,3,2] row_mask:0xf bank_mask:0xf bound_ctrl:1\n\t"
        "v_fmac_f32_dpp %2, %12, %22 quad_perm:[1,0,3,2] row_mask:0xf bank_mask:0xf bound_ctrl:1\n\t"
        "v_fmac_f32_dpp %3, %13, %23 quad_perm:[1,0,3,2] row_mask:0xf bank_mask:0xf bound_ctrl:1\n\t"
        "v_fmac_f32_dpp %4, %14, %24 quad_perm:[1,0,3,2] row_mask:0xf bank_mask:0xf bound_ctrl:1"
        : "=&v"(t[0]), "=&v"(t[1]), "=&v"(t[2]), "=&v"(t[3]), "=&v"(t[4])
        : "v"(u0), "v"(u1), "v"(u2), "v"(u3), "v"(u4), "v"(e[0]), "v"(e[1]), "v"(e[2]), "v"(e[3]), "v"(e[4]),
          "v"(a[0]), "v"(a[1]), "v"(a[2]), "v"(a[3]), "v"(a[4]), "v"(b[0]), "v"(b[1]), "v"(b[2]), "v"(b[3]), "v"(b[4]));
}

struct Args {
    const char* x; const u16* lre; const u16* lim; const char* bre; const char* bim; const char* cre; const char* cim;
    const u16* logdt; const char* wqkv; const char* wo; const u16* gw; const u16* dskip; char* ws; float* out;
};

DEV void channel_consts(const Args& a, int p, float& ar, float& ai, float& cr, float& ci) {
    const float lr = bf2f(a.lre[p]), li = bf2f(a.lim[p]);
    const float dt = bfr(fexp(bf2f(a.logdt[p])));
    const float zr = dt * lr, zi = dt * li, ex = fexp(zr);
    const float rv = zi * 0.15915494309189535f, rr = rv - __builtin_rintf(rv);
    ar = ex * __builtin_amdgcn_cosf(rr); ai = ex * __builtin_amdgcn_sinf(rr);
    const float nr = ar - 1.f, ni = ai, dn = lr * lr + li * li;
    cr = (nr * lr + ni * li) / dn; ci = (ni * lr - nr * li) / dn;
}

constexpr int P2_RED = 0;
#define P2_PW 4
#define P2_PA 5
DEV void p2_main(const Args& a, LAS char* sm, int lw, int lane, unsigned nonce) {
    const int b = blockIdx.x, xcd = b & 7, idx = b >> 3;
    const int tt = (xcd >> 1) * 8 + (idx & 7), cg = (xcd & 1) * 4 + (idx >> 3);
    const int c = lane & 15, q = lane >> 4;
    const unsigned tag = mk_tag(nonce);
    const u64 fl = flag_of(tag);
    constexpr int PW = P2_PW, PA = P2_PA;
    if (lw == 3) {
        const __amdgpu_buffer_rsrc_t rf = rsrc(a.ws + WS_FW2T, 128 * 8);
        const unsigned f0 = (unsigned)fl, f1 = (unsigned)(fl >> 32);
        for (unsigned spins = 0; spins < SPIN_CAP; ++spins) {
            const u32x4 v = __builtin_amdgcn_raw_buffer_load_b128(rf, lane * 16, 0, 16);
            if (wave_all((v[0] == f0) & (v[1] == f1) & (v[2] == f0) & (v[3] == f1))) break;
            __builtin_amdgcn_s_sleep(SLP_W2T);
        }
        word_set(sm + L_W2T, 1u);
    } else word_wait(sm + L_W2T, 1u);
    STAMP2(a.ws, lw + 4, 0);
    const __amdgpu_buffer_rsrc_t rw = rsrc(a.ws + WS_W2T, 40 * TS), ra = rsrc(a.ws + WS_ACT, 128 * TS);
    const unsigned wbase = 5 * cg * TS, abase = 4 * tt * TS, vo = lane * 16;
    const unsigned ko = (40 + 5 * lw) * 1024, kh = (10 * lw - 5) * 1024;
    auto koff = [&](int st) LAM { return (st < 5 ? ko : kh) + st * 1024; };
    bf16x8 fw[PW][5], fa[PA][4];
    auto ldw = [&](int st, int sl) LAM {
#pragma unroll
        for (int n = 0; n < 5; ++n) fw[sl][n] = ld16_wt(rw, vo, wbase + n * TS + koff(st));
    };
    auto lda = [&](int st) LAM {
#pragma unroll
        for (int t = 0; t < 4; ++t) fa[st % PA][t] = ld16_wt(ra, vo, abase + t * TS + koff(st));
    };
    auto wait_act = [&](int part, int word) LAM {
        if (lw == 3) {
            GAS u64* fp = (GAS u64*)(a.ws + WS_FACT) + (tt >> 1) * 32 + part * 16 + (lane & 7);
            for (unsigned spins = 0; spins < SPIN_CAP; ++spins) {
                const u64 v = __hip_atomic_load(fp, RLX_AGENT);
                if (wave_all(v == fl)) break;
                __builtin_amdgcn_s_sleep(SLP_ACT);
            }
            word_set(sm + word, 1u);
        } else word_wait(sm + word, 1u);
    };
    auto unit = [&](int i) LAM { const int u = 4 * lw + i; return u < 9 ? 5120 * u : 90624 + 5120 * (u - 9); };
    {
        const char* src = a.ws + WS_W2T + wbase + (10 * lw + 4) * 1024 + vo;
#pragma unroll
        for (int i = 0; i < 4; ++i)
#pragma unroll
            for (int n = 0; n < 5; ++n) glds16_wt(src + (size_t)n * TS + i * 1024, (unsigned)(size_t)(sm + unit(i) + n * 1024));
    }
#pragma unroll
    for (int st = 0; st < PW; ++st) ldw(st, st);
    SCHED();
    WAIT_VM(0);
    wait_act(1, L_ACT2);
    STAMP2(a.ws, lw + 4, 1);
    f32x4 acc[5][4];
#pragma unroll
    for (int n = 0; n < 5; ++n)
#pragma unroll
        for (int t = 0; t < 4; ++t) acc[n][t] = (f32x4){0.f, 0.f, 0.f, 0.f};
    auto slot = [&](int st) LAM { return st < 9 ? st % 4 : st == 13 ? 1 : st == 14 ? 2 : (st & 1) ? 3 : 0; };
    auto step = [&](int st) LAM {
        SCHED();
        if (st >= 8 && st < 12) {
#pragma unroll
            for (int n = 0; n < 5; ++n) fw[slot(st + 1)][n] = lds_frag(sm + unit(st - 8) + vo + n * 1024);
        }
#pragma unroll
        for (int n = 0; n < 5; ++n)
#pragma unroll
            for (int t = 0; t < 4; ++t) acc[n][t] = mfma(fw[slot(st)][n], fa[st % PA][t], acc[n][t]);
        SCHED();
        if (st < 5) ldw(st + 4, slot(st + 4));
        if (st == 9) ldw(13, slot(13));
        if (st == 10) ldw(14, slot(14));
        if (st == 11) { wait_lgkm0(); if (lane == 0) word_add(sm + L_WR); }
    };
    static_assert(PA == 5 && PW == 4, "ring layout");
#pragma unroll
    for (int st = 0; st < 5; ++st) lda(st);
#pragma unroll
    for (int st = 0; st < 5; ++st) step(st);
    STAMP2(a.ws, lw + 4, 4);
    wait_act(0, L_ACT);
    STAMP2(a.ws, lw + 4, 5);
    if (tt == 31 && cg == 0 && lw == 3 && lane == 0) __hip_atomic_store((GAS unsigned*)(a.ws + WS_NONCE), nonce + 1u, RLX_AGENT);
#pragma unroll
    for (int st = 5; st < 9; ++st) lda(st);
#pragma unroll
    for (int st = 5; st < 15; ++st) {
        if (st + 4 < 15) lda(st + 4);
        step(st);
    }
    STAMP2(a.ws, lw + 4, 2);
    word_wait(sm + L_WR, 4u);
    word_wait(sm + L_DONE, 4u);
    LAS float* rw_ = (LAS float*)(sm + P2_RED) + lw * (64 * 84);
#pragma unroll
    for (int t = 0; t < 4; ++t)
#pragma unroll
        for (int n = 0; n < 5; ++n) *(LAS f32x4*)(rw_ + (16 * t + c) * 84 + 16 * n + 4 * q) = acc[n][t];
    lds_bar();
    STAMP2(a.ws, lw + 4, 3);
}

DEV void p2_final(const Args& a, LAS char* sm, int cw, int lane, unsigned tag) {
    const int b = blockIdx.x, xcd = b & 7, idx = b >> 3;
    const int tt = (xcd >> 1) * 8 + (idx & 7), cg = (xcd & 1) * 4 + (idx >> 3);
    const int tid = cw * 64 + lane;
    u32x2 xv[5], dv[5]; float gv[5];
    int tok[5], n0[5];
#pragma unroll
    for (int i = 0; i < 5; ++i) {
        const int id = tid + 256 * i, r = id / 20, c4 = id - 20 * r;
        tok[i] = 64 * tt + 16 * ((r & 15) >> 2) + 4 * (r >> 4) + (r & 3); n0[i] = 80 * cg + 4 * c4;
        xv[i] = *(const u32x2*)((const u16*)a.x + tok[i] * 640 + n0[i]);
        dv[i] = *(const u32x2*)(a.dskip + n0[i]);
    }
    for (unsigned spins = 0; spins < SPIN_CAP; ++spins) {
        u64 v[5];
#pragma unroll
        for (int i = 0; i < 5; ++i) v[i] = __hip_atomic_load((GAS u64*)(a.ws + WS_G) + tok[i], RLX_AGENT);
        bool ok = true;
#pragma unroll
        for (int i = 0; i < 5; ++i) { gv[i] = __uint_as_float((unsigned)v[i]); ok &= (unsigned)(v[i] >> 32) == tag; }
        if (wave_all(ok)) break;
        __builtin_amdgcn_s_sleep(2);
    }
    f32x4 sk[5];
#pragma unroll
    for (int i = 0; i < 5; ++i) {
        const unsigned p01 = pk2(bf_lo(xv[i][0]) * bf_lo(dv[i][0]), bf_hi(xv[i][0]) * bf_hi(dv[i][0]));
        const unsigned p23 = pk2(bf_lo(xv[i][1]) * bf_lo(dv[i][1]), bf_hi(xv[i][1]) * bf_hi(dv[i][1]));
        sk[i] = (f32x4){gv[i] * bf_lo(p01), gv[i] * bf_hi(p01), gv[i] * bf_lo(p23), gv[i] * bf_hi(p23)};
    }
    STAMP2(a.ws, cw, 0);
    bar();
    STAMP2(a.ws, cw, 2);
#pragma unroll
    for (int i = 0; i < 5; ++i) {
        const int id = tid + 256 * i, r = id / 20, c4 = id - 20 * r;
        LAS float* p = (LAS float*)(sm + P2_RED) + r * 84 + 4 * c4;
        sk[i] += (*(LAS f32x4*)p + *(LAS f32x4*)(p + 64 * 84)) + (*(LAS f32x4*)(p + 128 * 84) + *(LAS f32x4*)(p + 192 * 84));
    }
    asm volatile("" : "+v"(sk[0]), "+v"(sk[1]), "+v"(sk[2]), "+v"(sk[3]), "+v"(sk[4]));
#define OUT_AUX 2
    {
        const __amdgpu_buffer_rsrc_t ro = rsrc((const char*)a.out, 2048 * 640 * 4);
#pragma unroll
        for (int i = 0; i < 5; ++i)
            __builtin_amdgcn_raw_buffer_store_b128(__builtin_bit_cast(u32x4, sk[i]), ro, (tok[i] * 640 + n0[i]) * 4, 0, OUT_AUX);
    }
    STAMP2(a.ws, cw, 3);
}

DEV void a_loader(const Args& a, LAS char* sm, int ti, int gj, int lw, int lane) {
    STAMP(a.ws, lw + 4, 0);
    const unsigned nonce = __hip_atomic_load((GAS unsigned*)(a.ws + WS_NONCE), RLX_AGENT);
    unsigned off[12];
    {
        const int pos = lane & 7, rl = lane >> 3, im = lw & 1;
#pragma unroll
        for (int u = 0; u < 4; ++u) {
            const int R = 8 * (lw + 4 * u) + rl, rho = R & 15;
            const int tok = (R & 64) + 16 * (rho >> 2) + 4 * ((R >> 4) & 3) + (rho & 3);
            off[u] = tok * 1280 + ((pos ^ (R & 7)) << 4);
        }
#pragma unroll
        for (int u = 4; u < 9; ++u) off[u] = (8 * ((lw + 4 * (u - 4)) >> 1) + rl) * 1280 + ((pos ^ rl ^ (im << 2)) << 4);
#pragma unroll
        for (int u = 9; u < 12; ++u) {
            const int ch = 64 * (lw + 4 * (u - 9)) + lane, rho = ch / 10, cc = ch - 10 * rho;
            off[u] = (rho ^ (((rho >> 3) & 1) << 2)) * 3840 + cc * 16;
        }
    }
    const char* xb = a.x + (size_t)ti * (128 * 1280);
    const char* bb = ((lw & 1) ? a.bim : a.bre) + (size_t)gj * (80 * 1280);
    const char* qb = a.wqkv + 160 * gj;
    const bool full = lw < 2;
    auto issue_a = [&](int s) LAM {
        LAS char* dst = sm + (s % NS_A) * SLOT_A + lw * 1024;
#pragma unroll
        for (int u = 0; u < 4; ++u) glds16(xb + 128 * s + off[u], dst + 4096 * u);
#pragma unroll
        for (int u = 4; u < 6; ++u) glds16(bb + 128 * s + off[u], dst + 4096 * u);
    };
    auto issue_b = [&](int s) LAM {
        LAS char* dst = sm + (s % NS_A) * SLOT_A + lw * 1024;
#pragma unroll
        for (int u = 6; u < 9; ++u) glds16(bb + 128 * s + off[u], dst + 4096 * u);
#pragma unroll
        for (int u = 9; u < 12; ++u) if (u < 11 || full) glds16(qb + (size_t)(64 * 3840) * s + off[u], dst + 4096 * u);
    };
    STAMP(a.ws, lw + 4, 1);
    issue_a(0); issue_b(0); issue_a(1); issue_b(1);
    if (full) WAIT_VM(12); else WAIT_VM(11);
    bar();
    u64 t_vm = 0, t_free = 0;
#pragma unroll 1
    for (int s = 0; s < 8; ++s) {
        if (s > 0) TIMED(t_free, ring_wait(sm + L_FREE, s));
        issue_a(s + 2);
        TIMED(t_vm, WAIT_VM(6));
        ring_put(sm + L_FULL + 4 * lw, s + 2);
        issue_b(s + 2);
    }
    WAIT_VM(0);
    ring_put(sm + L_FULL + 4 * lw, 10);
    STAMPV(a.ws, lw + 4, 14, t_vm); STAMPV(a.ws, lw + 4, 15, t_free);
    STAMP(a.ws, lw + 4, 2);
    __builtin_amdgcn_s_setprio(2);
    const unsigned tag = mk_tag(nonce);
    const int tl = lw * 64 + lane;
    if (tl < 128) {
        GAS u64* gp = (GAS u64*)(a.ws + WS_G) + 128 * ti + tl;
        u64 v;
        for (unsigned spins = 0;;) {
            v = __hip_atomic_load(gp, RLX_AGENT);
            if (wave_all((unsigned)(v >> 32) == tag) || ++spins > SPIN_CAP) break;
            __builtin_amdgcn_s_sleep(1);
        }
        const float g = __uint_as_float((unsigned)v);
        ((LAS float*)(sm + A_G))[tl] = g;
        ((LAS float*)(sm + A_G))[128 + tl] = bfr(1.f - g);
    }
    STAMP(a.ws, lw + 4, 9);
    lds_bar();
    STAMP(a.ws, lw + 4, 3);
    if (lw < 3) {
        const int col = tl < 160 ? tl : 159;
        LAS float* tab = (LAS float*)(sm + A_TAB);
        GAS u64* ea = (GAS u64*)(a.ws + WS_EA) + gj * 160;
        const float a16 = tab[2 * 160 + col], b16 = tab[3 * 160 + col], a128 = tab[4 * 160 + col], b128 = tab[5 * 160 + col];
        LAS float* es = (LAS float*)(sm + A_ESEG);
        float pre[8], P = 0.f;
#pragma unroll
        for (int s = 0; s < 8; ++s) { pre[s] = P; P = cmul_add(a16, b16, P, es[s * 160 + col]); }
        if (tl < 160) __hip_atomic_store(ea + ti * 1280 + col, granule(tag, __float_as_uint(P)), RLX_AGENT);
        STAMP(a.ws, lw + 4, 4);
        float H = 0.f;
        if (ti > 0) {
            if (SENT_LB == 0) {
            } else if (lw == 0) {
                GAS u64* sp = ea + (lane < ti ? lane : 0) * 1280 + 159;
                for (unsigned spins = 0; spins < SPIN_CAP; ++spins) {
                    if (wave_all((unsigned)(__hip_atomic_load(sp, RLX_AGENT) >> 32) == tag)) break;
                    __builtin_amdgcn_s_sleep(SLP_LB);
                }
                word_set(sm + L_LBS, 1u);
            } else word_wait(sm + L_LBS, 1u);
            float ev[15];
            for (unsigned spins = 0; spins < SPIN_CAP; ++spins) {
                u64 v[15];
#pragma unroll
                for (int i = 0; i < 15; ++i) v[i] = __hip_atomic_load(ea + (i < ti ? i : 0) * 1280 + col, RLX_AGENT);
                bool ok = true;
#pragma unroll
                for (int i = 0; i < 15; ++i) { ev[i] = __uint_as_float((unsigned)v[i]); ok &= (unsigned)(v[i] >> 32) == tag; }
                if (wave_all(ok)) break;
                __builtin_amdgcn_s_sleep(SLP_LB);
            }
#pragma unroll
            for (int i = 0; i < 15; ++i) if (i < ti) H = cmul_add(a128, b128, H, ev[i]);
        }
        STAMP(a.ws, lw + 4, 6);
        LAS float* hs = (LAS float*)(sm + A_HSEG);
#pragma unroll
        for (int s = 0; s < 8; ++s) { if (tl < 160) hs[s * 160 + col] = H + pre[s]; H = cmul_add(a16, b16, H, 0.f); }
        wait_lgkm0();
        if (lane == 0) word_add(sm + L_HS);
    } else {
        const int i16 = lane & 15;
        GAS u64* sp = (GAS u64*)(a.ws + WS_TOT) + gj * 840 + (i16 < 8 ? 53 * i16 + 52 : 424 + 52 * (i16 - 8) + 51);
        for (unsigned spins = 0; SENT_TOT && spins < SPIN_CAP; ++spins) {
            if (wave_all((unsigned)(__hip_atomic_load(sp, RLX_AGENT) >> 32) == tag)) break;
            __builtin_amdgcn_s_sleep(SLP_TOT);
        }
        STAMP(a.ws, lw + 4, 4);
        const __amdgpu_buffer_rsrc_t rs = rsrc(a.ws + WS_TOT, 8 * 840 * 8);
        u32x4 v[7];
        for (unsigned spins = 0; spins < SPIN_CAP; ++spins) {
            bool ok = true;
#pragma unroll
            for (int k = 0; k < 7; ++k) v[k] = __builtin_amdgcn_raw_buffer_load_b128(rs, gj * 6720 + (lane + 64 * k < 420 ? lane + 64 * k : 419) * 16, 0, 16);
#pragma unroll
            for (int k = 0; k < 7; ++k) ok &= (v[k][1] == tag) & (v[k][3] == tag);
            if (wave_all(ok)) break;
            __builtin_amdgcn_s_sleep(SLP_TOT);
        }
#pragma unroll
        for (int k = 0; k < 7; ++k) if (lane + 64 * k < 420) ((LAS u32x2*)(sm + A_TOT))[lane + 64 * k] = (u32x2){v[k][0], v[k][2]};
        wait_lgkm0();
        word_set(sm + L_TOT, 1u);
        STAMP(a.ws, lw + 4, 5);
    }
    __builtin_amdgcn_s_setprio(0);
    STAMP(a.ws, lw + 4, 8);
    p2_main(a, sm, lw, lane, nonce);
}

template <int NH>
DEV void a_pass1(f32x4 (&acc)[4][5], const float (&ca)[5], const float (&cb)[5], LAS float* es) {
    float e[5];
#pragma unroll
    for (int n = 0; n < 5; ++n) e[n] = acc[2 * NH][n][0];
#pragma unroll
    for (int t = 1; t < 16; ++t) {
        const int i = (t >> 2) ^ (2 * NH), r = t & 3;
        float y[5];
        scan_step_nd(y, acc[i][0][r], acc[i][1][r], acc[i][2][r], acc[i][3][r], acc[i][4][r], e, ca, cb);
#pragma unroll
        for (int n = 0; n < 5; ++n) e[n] = y[n];
    }
#pragma unroll
    for (int n = 0; n < 5; ++n) es[16 * n] = e[n];
}
template <int NH>
DEV void a_pass2(f32x4 (&acc)[4][5], const float (&ca)[5], const float (&cb)[5], LAS float* hs, LAS float* gt, LAS char* ht) {
    float e[5];
#pragma unroll
    for (int n = 0; n < 5; ++n) e[n] = hs[16 * n];
    asm volatile("s_nop 1" : "+v"(e[0]), "+v"(e[1]), "+v"(e[2]), "+v"(e[3]), "+v"(e[4]));
#pragma unroll
    for (int m = 0; m < 4; ++m) {
        const int i = m ^ (2 * NH);
        const f32x4 g = *(LAS f32x4*)(gt + 4 * m);
#pragma unroll
        for (int r = 0; r < 4; ++r) {
            float u0 = acc[i][0][r], u1 = acc[i][1][r], u2 = acc[i][2][r], u3 = acc[i][3][r], u4 = acc[i][4][r];
            scan_step(u0, u1, u2, u3, u4, e, ca, cb);
            e[0] = u0; e[1] = u1; e[2] = u2; e[3] = u3; e[4] = u4;
            acc[i][0][r] = u0 * g[r]; acc[i][1][r] = u1 * g[r]; acc[i][2][r] = u2 * g[r]; acc[i][3][r] = u3 * g[r]; acc[i][4][r] = u4 * g[r];
        }
#pragma unroll
        for (int n = 0; n < 5; ++n)
            *(LAS u32x2*)(ht + 16 * n * HT_STRIDE + 32 * m) = (u32x2){pk2(acc[i][n][0], acc[i][n][1]), pk2(acc[i][n][2], acc[i][n][3])};
    }
}

DEV void a_consumer(const Args& a, LAS char* sm, int ti, int gj, int cw, int lane) {
    const int mh = cw >> 1, nh = cw & 1, c = lane & 15, q = lane >> 4, tid = cw * 64 + lane;
    STAMP(a.ws, cw, 0);
    const unsigned nonce = __hip_atomic_load((GAS unsigned*)(a.ws + WS_NONCE), RLX_AGENT);
    const unsigned tag = mk_tag(nonce);
    if (cw >= 2) {
        const int tok = 128 * ti + 16 * gj + 8 * (cw - 2) + (lane >> 3);
        const u32x4* xr = (const u32x4*)(a.x + (size_t)tok * 1280) + (lane & 7);
        const u32x4* gr = (const u32x4*)a.gw + (lane & 7);
        u32x4 xv[10], gv[10];
#pragma unroll
        for (int i = 0; i < 10; ++i) { xv[i] = xr[8 * i]; gv[i] = gr[8 * i]; }
        float s = 0.f;
#pragma unroll
        for (int i = 0; i < 10; ++i)
#pragma unroll
            for (int k = 0; k < 4; ++k) {
                const unsigned pr = pk2(bf_lo(xv[i][k]) * bf_lo(gv[i][k]), bf_hi(xv[i][k]) * bf_hi(gv[i][k]));
                s += bf_lo(pr); s += bf_hi(pr);
            }
        s += lane_xor(s, 1); s += lane_xor(s, 2); s += lane_xor(s, 4);
        const float lg = bfr(s), g = bfr(1.f / (1.f + fexp(-lg)));
        if ((lane & 7) == 0) __hip_atomic_store((GAS u64*)(a.ws + WS_G) + tok, granule(tag, __float_as_uint(g)), RLX_AGENT);
    } else if (tid < 80) {
        float ar, ai, cr, ci;
        channel_consts(a, 80 * gj + tid, ar, ai, cr, ci);
        float pr = ar, pi = ai;
#pragma unroll
        for (int k = 0; k < 4; ++k) { const float t = pr * pr - pi * pi; pi = 2.f * pr * pi; pr = t; }
        const float r16 = pr, i16 = pi;
#pragma unroll
        for (int k = 0; k < 3; ++k) { const float t = pr * pr - pi * pi; pi = 2.f * pr * pi; pr = t; }
        LAS f32x2* tab = (LAS f32x2*)(sm + A_TAB);
        tab[0 * 80 + tid] = (f32x2){ar, ar};
        tab[1 * 80 + tid] = (f32x2){-ai, ai};
        tab[2 * 80 + tid] = (f32x2){r16, r16};
        tab[3 * 80 + tid] = (f32x2){-i16, i16};
        tab[4 * 80 + tid] = (f32x2){pr, pr};
        tab[5 * 80 + tid] = (f32x2){-pi, pi};
    }
    if (tid < 160) __hip_atomic_store((GAS u64*)(a.ws + WS_EA) + (ti * 8 + gj) * 160 + tid, (u64)0, RLX_AGENT);
    __builtin_amdgcn_s_setprio(1);
    f32x4 acc[4][5], at[5][2];
#pragma unroll
    for (int n = 0; n < 5; ++n) {
#pragma unroll
        for (int i = 0; i < 4; ++i) acc[i][n] = (f32x4){0.f, 0.f, 0.f, 0.f};
        at[n][0] = at[n][1] = (f32x4){0.f, 0.f, 0.f, 0.f};
    }
    int xlo[2], xhi[2], wa[2], wq[2];
#pragma unroll
    for (int kk = 0; kk < 2; ++kk) {
        const int sw = ((4 * kk + q) ^ (c & 7)) << 4;
        xlo[kk] = (64 * mh + 32 * nh + c) * 128 + sw;
        xhi[kk] = (64 * mh + 32 * (1 - nh) + c) * 128 + sw;
        wa[kk] = 16384 + (80 * nh + 8 * (c & 1) + (c >> 1)) * 128 + (((4 * kk + q) ^ (c >> 1) ^ ((c & 1) << 2)) << 4);
        wq[kk] = 36864 + (8 * q + 4 * (kk ^ (q & 1)) + (c >> 2)) * 160 + 8 * (c & 3);
    }
    bf16x8 xa[2][4], wb[2][5], wt[2][5];
    auto rdp = [&](int buf, LAS char* s0, int kk) LAM {
        LAS char* q0 = s0 + wq[0] + kk * 5120; LAS char* q1 = s0 + wq[1] + kk * 5120;
        xa[buf][0] = lds_frag(s0 + xlo[kk]);
        xa[buf][1] = lds_frag(s0 + xlo[kk] + 2048);
        wt[buf][0] = lds_tr_frag(q0, q1);
        xa[buf][2] = lds_frag(s0 + xhi[kk]);
        xa[buf][3] = lds_frag(s0 + xhi[kk] + 2048);
        wt[buf][1] = lds_tr_frag(q0 + 32, q1 + 32);
        wb[buf][0] = lds_frag(s0 + wa[kk]);
        wb[buf][1] = lds_frag(s0 + wa[kk] + 2048);
        wt[buf][2] = lds_tr_frag(q0 + 64, q1 + 64);
        wb[buf][2] = lds_frag(s0 + wa[kk] + 4096);
        wb[buf][3] = lds_frag(s0 + wa[kk] + 6144);
        wt[buf][3] = lds_tr_frag(q0 + 96, q1 + 96);
        wb[buf][4] = lds_frag(s0 + wa[kk] + 8192);
        wt[buf][4] = lds_tr_frag(q0 + 128, q1 + 128);
    };
    auto rd = [&](int buf, int slot, int kk) LAM { rdp(buf, sm + slot * SLOT_A, kk); };
    auto mm_head = [&](int buf) LAM {
#pragma unroll
        for (int n = 0; n < HEAD_A / 4; ++n)
#pragma unroll
            for (int i = 0; i < 4; ++i) acc[i][n] = mfma(xa[buf][i], wb[buf][n], acc[i][n]);
    };
    auto mm_tail = [&](int buf) LAM {
#pragma unroll
        for (int n = HEAD_A / 4; n < 5; ++n)
#pragma unroll
            for (int i = 0; i < 4; ++i) acc[i][n] = mfma(xa[buf][i], wb[buf][n], acc[i][n]);
#pragma unroll
        for (int n = 0; n < 5; ++n)
#pragma unroll
            for (int i = 0; i < 2; ++i) at[n][i] = mfma(wt[buf][n], xa[buf][i], at[n][i]);
    };
    if (tid < L_WORDS) ((LAS unsigned*)(sm + L_FULL))[tid] = tid < 4 ? 1u : 0u;
#pragma unroll
    for (int n = 0; n < 5; ++n)
        asm volatile("" : "+v"(acc[0][n]), "+v"(acc[1][n]), "+v"(acc[2][n]), "+v"(acc[3][n]), "+v"(at[n][0]), "+v"(at[n][1]));
    STAMP(a.ws, cw, 1);
    lds_bar();
    STAMP(a.ws, cw, 2);
    STAMPC(a.ws, cw, 12);
    rd(0, 0, 0);
    int slot = 0;
    u64 t_spin = 0, n_spin = 0;
#pragma unroll 1
    for (int s = 0; s < 10; ++s) {
        wait_lgkm0();
        SCHED();
        rd(1, slot, 1);
        u32x4 fl = *(volatile LAS u32x4*)(sm + L_FULL);
        mm_head(0); mm_tail(0);
        interleave<30, 20, IL_A>();
        SCHED();
        wait_lgkm0();
        asm volatile("" ::: "memory");
        *(volatile LAS unsigned*)(sm + L_FREE + 4 * cw) = s + 1;
        slot = slot == 2 ? 0 : slot + 1;
        const unsigned need = s < 9 ? s + 2 : 0;
        unsigned landed = __builtin_amdgcn_readfirstlane(umin4(fl));
        LAS char* s0 = sm + slot * SLOT_A;
        asm volatile("" : "+s"(landed), "+v"(s0));
        mm_head(1);
        SCHED();
        if (__builtin_expect(landed < need, 0)) { TIMED(t_spin, ring_spin(sm + L_FULL, need)); n_spin++; }
        asm volatile("" ::: "memory");
        SCHED();
        rdp(0, s0, 0);
        mm_tail(1);
        interleave<30 - HEAD_A, 19, IL_A - HEAD_A + 1>();
        SCHED();
    }
    __builtin_amdgcn_s_setprio(0);
    asm volatile("s_nop 7\n\ts_nop 7" ::: "memory");
    STAMP(a.ws, cw, 3);
    STAMPC(a.ws, cw, 13);
    STAMPV(a.ws, cw, 14, t_spin); STAMPV(a.ws, cw, 15, n_spin);
    LAS float* tab = (LAS float*)(sm + A_TAB);
    float ca[5], cb[5];
#pragma unroll
    for (int n = 0; n < 5; ++n) { const int col = 80 * nh + 16 * n + c; ca[n] = tab[col]; cb[n] = tab[160 + col]; }
    {
        LAS float* es = (LAS float*)(sm + A_ESEG) + (4 * mh + q) * 160 + 80 * nh + c;
        if (nh == 0) a_pass1<0>(acc, ca, cb, es); else a_pass1<1>(acc, ca, cb, es);
    }
    WAIT_VM(0);
    STAMP(a.ws, cw, 4);
    lds_bar();
    bf16x8 P[2][3];
#pragma unroll
    for (int i = 0; i < 2; ++i) {
#pragma unroll
        for (int t = 0; t < 5; ++t)
#pragma unroll
            for (int r = 0; r < 4; ++r) at[t][i][r] = phi(at[t][i][r]);
        P[i][0] = pack_pair(at[0][i], at[1][i]);
        P[i][1] = pack_pair(at[2][i], at[3][i]);
        P[i][2] = as_frag((u32x4){pk2(at[4][i][0], at[4][i][1]), pk2(at[4][i][2], at[4][i][3]), 0u, 0u});
    }
    int adr[21];
    {
        int dh[5], db[5], eh[5], eb[5];
#pragma unroll
        for (int k = 0; k < 5; ++k) {
            const int d0 = 16 * k + 4 * q, e0 = 16 * k + c;
            dh[k] = d0 / 20; db[k] = A_TOT + dh[k] * 840 + (d0 - 20 * dh[k]) * 2;
            eh[k] = e0 / 20; eb[k] = (e0 - 20 * eh[k]) * 40;
        }
        auto ad = [&](int k, int E) LAM { return dh[k] == eh[E] ? db[k] + eb[E] : L_ZERO; };
        adr[0] = ad(0, 0); adr[1] = ad(1, 0);
        adr[2] = ad(0, 1); adr[3] = ad(1, 1); adr[4] = ad(2, 1); adr[5] = ad(3, 1);
        adr[6] = ad(1, 2); adr[7] = ad(2, 2); adr[8] = ad(3, 2);
        adr[9] = ad(2, 3); adr[10] = ad(3, 3); adr[11] = ad(4, 3);
        adr[12] = ad(3, 4); adr[13] = ad(4, 4);
#pragma unroll
        for (int k = 0; k < 5; ++k) adr[14 + k] = dh[k] == (c & 3) ? db[k] + 800 : L_ZERO;
        adr[19] = L_ZERO; adr[20] = A_G + 4 * (128 + 64 * mh + 16 * (c >> 2) + 8 * nh + (c & 3));
    }
#pragma unroll
    for (int i = 0; i < 2; ++i) asm volatile("" : "+v"(P[i][0]), "+v"(P[i][1]), "+v"(P[i][2]));
    asm volatile("" : "+v"(adr[0]), "+v"(adr[1]), "+v"(adr[2]), "+v"(adr[3]), "+v"(adr[4]), "+v"(adr[5]), "+v"(adr[6]),
                      "+v"(adr[7]), "+v"(adr[8]), "+v"(adr[9]), "+v"(adr[10]), "+v"(adr[11]), "+v"(adr[12]), "+v"(adr[13]));
    asm volatile("" : "+v"(adr[14]), "+v"(adr[15]), "+v"(adr[16]), "+v"(adr[17]), "+v"(adr[18]), "+v"(adr[20]));
    const __amdgpu_buffer_rsrc_t ract = rsrc(a.ws + WS_ACT, 128 * TS);
    u32x4 os[2][2]; u32x2 o8[2];
    STAMP(a.ws, cw, 5);
    word_wait(sm + L_TOT, 1u);
    STAMP(a.ws, cw, 6);
    {
        auto h4 = [&](int i) LAM -> u32x2 { return *(LAS u32x2*)(sm + adr[i]); };
        auto fr2 = [&](const u32x2& lo, const u32x2& hi) LAM { return as_frag((u32x4){lo[0], lo[1], hi[0], hi[1]}); };
        const u32x2 zz = {0u, 0u};
        const bf16x8 Z0 = fr2(h4(14), h4(15)), Z1 = fr2(h4(16), h4(17)), Z2 = fr2(h4(18), zz);
        const bf16x8 F0 = fr2(h4(0), h4(1)), F1 = fr2(h4(2), h4(3)), F2 = fr2(h4(4), h4(5));
        const bf16x8 F3 = fr2(zz, h4(6)), F4 = fr2(h4(7), h4(8)), F5 = fr2(h4(9), h4(10));
        const bf16x8 F6 = fr2(h4(11), zz), F7 = fr2(zz, h4(12)), F8 = fr2(h4(13), zz);
        const float ogv[2] = {*(LAS float*)(sm + adr[20]), *(LAS float*)(sm + adr[20] + 16)};
        const f32x4 z4 = {0.f, 0.f, 0.f, 0.f};
        const int uu = gj >> 1, odd = gj & 1;
#pragma unroll
        for (int i = 0; i < 2; ++i) {
            const f32x4 dn = mfma(Z2, P[i][2], mfma(Z1, P[i][1], mfma(Z0, P[i][0], z4)));
            const f32x4 n0 = mfma(F0, P[i][0], z4);
            const f32x4 n1 = mfma(F2, P[i][1], mfma(F1, P[i][0], z4));
            const f32x4 n2 = mfma(F4, P[i][1], mfma(F3, P[i][0], z4));
            const f32x4 n3 = mfma(F6, P[i][2], mfma(F5, P[i][1], z4));
            const f32x4 n4 = mfma(F8, P[i][2], mfma(F7, P[i][1], z4));
            const float og = ogv[i];
            const float r0 = og * __builtin_amdgcn_rcpf(dn[0] + 1e-6f), r1 = og * __builtin_amdgcn_rcpf(dn[1] + 1e-6f);
            const float r2 = og * __builtin_amdgcn_rcpf(dn[2] + 1e-6f), r3 = og * __builtin_amdgcn_rcpf(dn[3] + 1e-6f);
            const float s1 = q < 1 ? r0 : r1, s2 = q < 2 ? r1 : r2, s3 = q < 3 ? r2 : r3;
            os[i][0] = (u32x4){pk2(n0[0] * r0, n0[1] * r0), pk2(n0[2] * r0, n0[3] * r0), pk2(n1[0] * s1, n1[1] * s1), pk2(n1[2] * s1, n1[3] * s1)};
            os[i][1] = (u32x4){pk2(n2[0] * s2, n2[1] * s2), pk2(n2[2] * s2, n2[3] * s2), pk2(n3[0] * s3, n3[1] * s3), pk2(n3[2] * s3, n3[3] * s3)};
            o8[i] = (u32x2){pk2(n4[0] * r3, n4[1] * r3), pk2(n4[2] * r3, n4[3] * r3)};
        }
#pragma unroll
        for (int i = 0; i < 2; ++i) {
            const unsigned ob = (8 * ti + 4 * mh + 2 * nh + i) * TS + (40 + 5 * uu) * 1024;
            st16_wt(ract, lane * 16, ob + (odd ? 3072 : 0), os[i][0]);
            st16_wt(ract, lane * 16, ob + (odd ? 4096 : 1024), os[i][1]);
            st8_wt(ract, lane * 16 + 8 * odd, ob + 2048, o8[i]);
        }
    }
    STAMP(a.ws, cw, 7);
    word_wait(sm + L_HS, 3u);
    STAMP(a.ws, cw, 8);
    {
        LAS float* hs = (LAS float*)(sm + A_HSEG) + (4 * mh + q) * 160 + 80 * nh + c;
        LAS float* gt = (LAS float*)(sm + A_G) + 64 * mh + 16 * q;
        LAS char* ht = sm + A_HT + (80 * nh + c) * HT_STRIDE + (64 * mh + 4 * q) * 2;
        if (nh == 0) a_pass2<0>(acc, ca, cb, hs, gt, ht); else a_pass2<1>(acc, ca, cb, hs, gt, ht);
    }
    signal_stores(sm, L_DONE2, a.ws + WS_FACT + ti * 256 + 128 + gj * 8, tag, lane, 4u);
    KEEP(os[0][0]); KEEP(os[0][1]); KEEP(os[1][0]); KEEP(os[1][1]);
    STAMP(a.ws, cw, 9);
    wait_lgkm0();
    if (lane == 0) word_add(sm + L_HT);
    word_wait(sm + L_HT, 4u);
    {
        const int qq = (lane & 15) >> 2, p = lane & 3;
        bf16x8 hf[10];
#pragma unroll
        for (int i = 0; i < 10; ++i) {
            const int f = cw + 4 * i, mt = f / 5, ks = f - 5 * mt;
            LAS char* b = sm + A_HT + (32 * ks + 8 * q + qq) * HT_STRIDE + 32 * mt + 8 * p;
            hf[i] = lds_tr_frag(b, b + 4 * HT_STRIDE);
        }
        SCHED();
#pragma unroll
        for (int i = 0; i < 10; ++i) {
            const int f = cw + 4 * i, mt = f / 5, ks = f - 5 * mt;
            st16_wt(ract, lane * 16, (8 * ti + mt) * TS + (5 * gj + ks) * 1024, __builtin_bit_cast(u32x4, hf[i]));
        }
    }
    wait_lgkm0();
    STAMP(a.ws, cw, 10);
    signal_stores(sm, L_DONE, a.ws + WS_FACT + ti * 256 + gj * 8, tag, lane, 4u);
    STAMP(a.ws, cw, 11);
    p2_final(a, sm, cw, lane, tag);
}

DEV void job_table(const Args& a, LAS char* tab, int jb, int lane) {
    if (jb < 400) {
        float ar, ai, cr, ci;
        channel_consts(a, 64 * (jb / 40) + lane, ar, ai, cr, ci);
        ((LAS f32x2*)tab)[lane] = (f32x2){2.f * cr, 2.f * ci};
    }
}
DEV void job_issue(const Args& a, LAS char* stg, int jb, int lane) {
    if (jb < 400) {
        const int cb = jb / 40, nt = jb - 40 * cb;
        const unsigned o = (16 * nt + (lane >> 3)) * 1280 + cb * 128 + (lane & 7) * 16;
        glds16(a.cre + o, stg); glds16(a.cre + o + 8 * 1280, stg + 1024);
        glds16(a.cim + o, stg + 2048); glds16(a.cim + o + 8 * 1280, stg + 3072);
    } else if (jb < 500) {
        const int jo = jb - 400, s5 = jo / 5, n8 = jo - 5 * s5, uu = s5 / 5, s = s5 - 5 * uu;
        const int lo = 160 * uu + (s == 0 ? 0 : s == 1 ? 32 : s == 2 ? 64 : s == 3 ? 80 : 112);
        const int hi = 160 * uu + (s == 0 ? 16 : s == 1 ? 48 : s == 2 ? 144 : s == 3 ? 96 : 128);
        const unsigned o = (lane >> 4) * 1280 + n8 * 256 + (lane & 15) * 16;
#pragma unroll
        for (int k = 0; k < 4; ++k) {
            glds16(a.wo + (lo + 4 * k) * 1280 + o, stg + 1024 * k);
            glds16(a.wo + (hi + 4 * k) * 1280 + o, stg + 4096 + 1024 * k);
        }
    }
}
DEV u32x2 lds_rd8(LAS char* p) { u32x2 v; asm volatile("ds_read_b64 %0, %1" : "=v"(v) : "v"((unsigned)(size_t)p) : "memory"); return v; }
DEV f32x4 lds_rd16(LAS char* p) { f32x4 v; asm volatile("ds_read_b128 %0, %1" : "=v"(v) : "v"((unsigned)(size_t)p) : "memory"); return v; }
DEV u32x2 lds_rdtr(LAS char* p) { u32x2 v; asm volatile("ds_read_b64_tr_b16 %0, %1" : "=v"(v) : "v"((unsigned)(size_t)p) : "memory"); return v; }
DEV void job_finish(const Args& a, LAS char* stg, LAS char* tab, int jb, int lane) {
    const int c = lane & 15, q = lane >> 4;
    const __amdgpu_buffer_rsrc_t rw = rsrc(a.ws + WS_W2T, 40 * TS);
    if (jb < 400) {
        const int cb = jb / 40, nt = jb - 40 * cb;
        u32x2 re[4], im[4]; f32x4 c01[4], c23[4];
#pragma unroll
        for (int k = 0; k < 4; ++k) {
            re[k] = lds_rd8(stg + c * 128 + 32 * k + 8 * q); im[k] = lds_rd8(stg + 2048 + c * 128 + 32 * k + 8 * q);
            c01[k] = lds_rd16(tab + (16 * k + 4 * q) * 8); c23[k] = lds_rd16(tab + (16 * k + 4 * q) * 8 + 16);
        }
        asm volatile("s_waitcnt lgkmcnt(0)" : "+v"(re[0]), "+v"(re[1]), "+v"(re[2]), "+v"(re[3]), "+v"(im[0]), "+v"(im[1]), "+v"(im[2]), "+v"(im[3]),
                     "+v"(c01[0]), "+v"(c01[1]), "+v"(c01[2]), "+v"(c01[3]), "+v"(c23[0]), "+v"(c23[1]), "+v"(c23[2]), "+v"(c23[3]) :: "memory");
        u32x4 w[4];
#pragma unroll
        for (int k = 0; k < 4; ++k) {
            const float r0 = bf_lo(re[k][0]), r1 = bf_hi(re[k][0]), r2 = bf_lo(re[k][1]), r3 = bf_hi(re[k][1]);
            const float i0 = bf_lo(im[k][0]), i1 = bf_hi(im[k][0]), i2 = bf_lo(im[k][1]), i3 = bf_hi(im[k][1]);
            w[k] = (u32x4){pk2(r0 * c01[k][0] - i0 * c01[k][1], -(r0 * c01[k][1] + i0 * c01[k][0])),
                           pk2(r1 * c01[k][2] - i1 * c01[k][3], -(r1 * c01[k][3] + i1 * c01[k][2])),
                           pk2(r2 * c23[k][0] - i2 * c23[k][1], -(r2 * c23[k][1] + i2 * c23[k][0])),
                           pk2(r3 * c23[k][2] - i3 * c23[k][3], -(r3 * c23[k][3] + i3 * c23[k][2]))};
        }
#pragma unroll
        for (int k = 0; k < 4; ++k) st16_wt(rw, lane * 16, nt * TS + (4 * cb + k) * 1024, w[k]);
    } else if (jb < 500) {
        const int jo = jb - 400, s5 = jo / 5, n8 = jo - 5 * s5;
        u32x2 lo[8], hi[8];
#pragma unroll
        for (int k = 0; k < 8; ++k) {
            LAS char* b = stg + (4 * q + (c >> 2)) * 256 + 32 * k + 8 * (c & 3);
            lo[k] = lds_rdtr(b); hi[k] = lds_rdtr(b + 4096);
        }
        asm volatile("s_waitcnt lgkmcnt(0)" : "+v"(lo[0]), "+v"(lo[1]), "+v"(lo[2]), "+v"(lo[3]), "+v"(lo[4]), "+v"(lo[5]), "+v"(lo[6]), "+v"(lo[7]),
                     "+v"(hi[0]), "+v"(hi[1]), "+v"(hi[2]), "+v"(hi[3]), "+v"(hi[4]), "+v"(hi[5]), "+v"(hi[6]), "+v"(hi[7]) :: "memory");
#pragma unroll
        for (int k = 0; k < 8; ++k)
            st16_wt(rw, lane * 16, (8 * n8 + k) * TS + (40 + s5) * 1024, (u32x4){lo[k][0], lo[k][1], hi[k][0], hi[k][1]});
    }
}

DEV void b_loader(const Args& a, LAS char* sm, int ti, int gj, int lw, int lane) {
    STAMP(a.ws, lw + 4, 0);
    const unsigned nonce = __hip_atomic_load((GAS unsigned*)(a.ws + WS_NONCE), RLX_AGENT);
    unsigned off[9];
    {
        const int pos = lane & 7, rl = lane >> 3;
#pragma unroll
        for (int u = 0; u < 4; ++u) { const int R = 8 * (lw + 4 * u) + rl; off[u] = R * 1280 + ((pos ^ (R & 7)) << 4); }
#pragma unroll
        for (int u = 4; u < 9; ++u) {
            const int wi = lw + 4 * (u - 4), sec = wi >= 10, ch = 64 * (wi - 10 * sec) + lane, rho = ch / 10, cc = ch - 10 * rho;
            off[u] = (rho ^ (((rho >> 3) & 1) << 2)) * 3840 + cc * 16 + 1280 + 1280 * sec;
        }
    }
    const char* xb = a.x + (size_t)ti * (128 * 1280);
    const char* wb = a.wqkv + 160 * gj;
    auto issue_a = [&](int s) LAM {
        LAS char* dst = sm + (s % NS_B) * SLOT_B + lw * 1024;
#pragma unroll
        for (int u = 0; u < 4; ++u) glds16(xb + 128 * s + off[u], dst + 4096 * u);
        glds16(wb + (size_t)(64 * 3840) * s + off[4], dst + 4096 * 4);
    };
    auto issue_b = [&](int s) LAM {
        LAS char* dst = sm + (s % NS_B) * SLOT_B + lw * 1024;
#pragma unroll
        for (int u = 5; u < 9; ++u) glds16(wb + (size_t)(64 * 3840) * s + off[u], dst + 4096 * u);
    };
    STAMP(a.ws, lw + 4, 1);
    const int jb = (ti * 8 + gj) * 4 + lw;
    LAS char* stg = sm + B_STG + lw * 8192;
    issue_a(0); issue_b(0);
    if (JOBMODE == 0) job_issue(a, stg, jb, lane);
    issue_a(1); issue_b(1); issue_a(2); issue_b(2);
    WAIT_VM(18);
    bar();
    if (JOBMODE == 0) job_finish(a, stg, sm + B_TAB + lw * 512, jb, lane);
#pragma unroll 1
    for (int s = 0; s < 7; ++s) {
        if (s > 0) ring_wait(sm + L_FREE, s);
        issue_a(s + 3);
        if (JOBMODE == 0 && s < 2 && jb < 400) WAIT_VM(18); else if (JOBMODE == 0 && s < 2 && jb < 500) WAIT_VM(22); else WAIT_VM(14);
        ring_put(sm + L_FULL + 4 * lw, s + 2);
        issue_b(s + 3);
    }
    WAIT_VM(9);
    ring_put(sm + L_FULL + 4 * lw, 9);
    WAIT_VM(0);
    ring_put(sm + L_FULL + 4 * lw, 10);
    const unsigned tag = mk_tag(nonce);
    if (JOBMODE == 0) signal_stores(sm, L_DONE2, a.ws + WS_FW2T + (ti * 8 + gj) * 8, tag, lane, 4u);
    STAMP(a.ws, lw + 4, 2);
    if (JOBMODE == 2) {
        ring_wait(sm + L_FREE, 8);
        job_issue(a, stg, jb, lane);
        WAIT_VM(0);
        job_finish(a, stg, sm + B_TAB + lw * 512, jb, lane);
        STAMP(a.ws, lw + 4, 6);
    }
    __builtin_amdgcn_s_setprio(2);
    const int tl = lw * 64 + lane;
    {
        int o0[2], o1[2];
#pragma unroll
        for (int it = 0; it < 2; ++it) {
            const int g = 2 * (tl + 256 * it < 420 ? tl + 256 * it : 0);
            const int h = g / 210, rem = g - 210 * h, e = rem / 10, d = 2 * (rem - 10 * e);
            const bool kv = rem < 200;
            o0[it] = kv ? B_KVT + ((20 * h + e) * 84 + 20 * h + d) * 4 : B_Z + (20 * h + 2 * (rem - 200)) * 4;
            o1[it] = o0[it] + (kv ? 80 * 84 * 4 : 320);
        }
        asm volatile("" : "+v"(o0[0]), "+v"(o0[1]), "+v"(o1[0]), "+v"(o1[1]));
        const __amdgpu_buffer_rsrc_t rk = rsrc(a.ws + WS_KV, 16 * 8 * 840 * 8);
        word_wait(sm + L_KV, 4u);
        STAMP(a.ws, lw + 4, 3);
        u32x4 pv[2];
#pragma unroll
        for (int it = 0; it < 2; ++it) {
            const f32x4 v = *(LAS f32x4*)(sm + o0[it]) + *(LAS f32x4*)(sm + o1[it]);
            pv[it] = (u32x4){pk2(v[0], v[1]), tag, pk2(v[2], v[3]), tag};
        }
        asm volatile("" : "+v"(pv[0]), "+v"(pv[1]) :: "memory");
        st16_wt(rk, tl * 16, (ti * 8 + gj) * 6720, pv[0]);
        if (tl + 256 < 420) st16_wt(rk, (tl + 256) * 16, (ti * 8 + gj) * 6720, pv[1]);
        asm volatile("s_nop 1" ::"v"(pv[0]), "v"(pv[1]));
    }
    STAMP(a.ws, lw + 4, 4);
    if (lw == 0) {
        const int cnt = ti < 8 ? 53 : 52, g0 = ti < 8 ? 53 * ti : 424 + 52 * (ti - 8);
        const int g = g0 + (lane < cnt ? lane : 0);
        GAS u64* src = (GAS u64*)(a.ws + WS_KV) + gj * 840 + g;
        unsigned v[16];
        for (unsigned spins = 0;;) {
            bool ok = true;
#pragma unroll
            for (int i = 0; i < 16; ++i) {
                const u64 x = __hip_atomic_load(src + i * (8 * 840), RLX_AGENT);
                v[i] = (unsigned)x; ok &= (unsigned)(x >> 32) == tag;
            }
            if (wave_all(ok) || ++spins > SPIN_CAP) break;
            __builtin_amdgcn_s_sleep(1);
        }
        float s0 = 0.f, s1 = 0.f;
#pragma unroll
        for (int i = 0; i < 16; ++i) { s0 += bf_lo(v[i]); s1 += bf_hi(v[i]); }
        if (lane < cnt) __hip_atomic_store((GAS u64*)(a.ws + WS_TOT) + gj * 840 + g, granule(tag, pk2(s0, s1)), RLX_AGENT);
        word_set(sm + L_H2, 1u);
    }
    STAMP(a.ws, lw + 4, 5);
    __builtin_amdgcn_s_setprio(0);
    if (JOBMODE == 2) signal_stores(sm, L_DONE2, a.ws + WS_FW2T + (ti * 8 + gj) * 8, tag, lane, 4u);
    p2_main(a, sm, lw, lane, nonce);
}

DEV void b_consumer(const Args& a, LAS char* sm, int ti, int gj, int cw, int lane) {
    const int mh = cw >> 1, nh = cw & 1, c = lane & 15, q = lane >> 4, tid = cw * 64 + lane;
    STAMP(a.ws, cw, 0);
    const unsigned tag = mk_tag(__hip_atomic_load((GAS unsigned*)(a.ws + WS_NONCE), RLX_AGENT));
    if (JOBMODE != 1) job_table(a, sm + B_TAB + cw * 512, (ti * 8 + gj) * 4 + cw, lane);
    {
        const __amdgpu_buffer_rsrc_t rk = rsrc(a.ws + WS_KV, 16 * 8 * 840 * 8);
        const u32x4 z = {0u, 0u, 0u, 0u};
        st16_wt(rk, tid * 16, (ti * 8 + gj) * 6720, z);
        if (tid + 256 < 420) st16_wt(rk, (tid + 256) * 16, (ti * 8 + gj) * 6720, z);
        const int cnt = ti < 8 ? 53 : 52, g0 = ti < 8 ? 53 * ti : 424 + 52 * (ti - 8);
        if (tid < cnt) __hip_atomic_store((GAS u64*)(a.ws + WS_TOT) + gj * 840 + g0 + tid, (u64)0, RLX_AGENT);
    }
    __builtin_amdgcn_s_setprio(1);
    f32x4 an[5][4];
#pragma unroll
    for (int i = 0; i < 4; ++i)
#pragma unroll
        for (int t = 0; t < 5; ++t) an[t][i] = (f32x4){0.f, 0.f, 0.f, 0.f};
    int xo[2], wA[2], wB[2];
#pragma unroll
    for (int kk = 0; kk < 2; ++kk) {
        xo[kk] = (64 * mh + c) * 128 + (((4 * kk + q) ^ (c & 7)) << 4);
        const int b = 16384 + (8 * q + 4 * (kk ^ (q & 1)) + (c >> 2)) * 160 + 8 * (c & 3);
        wA[kk] = b + 96 * nh; wB[kk] = b + 64 + 10240 * nh;
    }
    bf16x8 xa[2][4], wb[2][5];
    auto rdp = [&](int buf, LAS char* s0, int kk) LAM {
#pragma unroll
        for (int i = 0; i < 4; ++i) xa[buf][i] = lds_frag(s0 + xo[kk] + 2048 * i);
        LAS char* w0 = s0 + kk * 5120;
        wb[buf][0] = lds_tr_frag(w0 + wA[0], w0 + wA[1]);
        wb[buf][1] = lds_tr_frag(w0 + wA[0] + 32, w0 + wA[1] + 32);
        wb[buf][2] = lds_tr_frag(w0 + wB[0], w0 + wB[1]);
        wb[buf][3] = lds_tr_frag(w0 + wA[0] + 10240, w0 + wA[1] + 10240);
        wb[buf][4] = lds_tr_frag(w0 + wA[0] + 10272, w0 + wA[1] + 10272);
    };
    auto rd = [&](int buf, int slot, int kk) LAM { rdp(buf, sm + slot * SLOT_B, kk); };
    auto mm_head = [&](int buf) LAM {
#pragma unroll
        for (int i = 0; i < 4; ++i) an[0][i] = mfma(xa[buf][i], wb[buf][0], an[0][i]);
    };
    auto mm_tail = [&](int buf) LAM {
#pragma unroll
        for (int t = 1; t < 5; ++t)
#pragma unroll
            for (int i = 0; i < 4; ++i) an[t][i] = mfma(xa[buf][i], wb[buf][t], an[t][i]);
    };
    if (tid < L_WORDS) ((LAS unsigned*)(sm + L_FULL))[tid] = tid < 4 ? 1u : 0u;
#pragma unroll
    for (int t = 0; t < 5; ++t)
        asm volatile("" : "+v"(an[t][0]), "+v"(an[t][1]), "+v"(an[t][2]), "+v"(an[t][3]));
    STAMP(a.ws, cw, 1);
    lds_bar();
    STAMP(a.ws, cw, 2);
    rd(0, 0, 0);
#pragma unroll 1
    for (int s = 0; s < 10; ++s) {
        wait_lgkm0();
        SCHED();
        rd(1, s & 3, 1);
        u32x4 fl = *(volatile LAS u32x4*)(sm + L_FULL);
        mm_head(0); mm_tail(0);
        interleave<20, 15, IL_B>();
        SCHED();
        wait_lgkm0();
        asm volatile("" ::: "memory");
        *(volatile LAS unsigned*)(sm + L_FREE + 4 * cw) = s + 1;
        const unsigned need = s < 9 ? s + 2 : 0;
        unsigned landed = __builtin_amdgcn_readfirstlane(umin4(fl));
        LAS char* s0 = sm + ((s + 1) & 3) * SLOT_B;
        asm volatile("" : "+s"(landed), "+v"(s0));
        mm_head(1);
        SCHED();
        if (__builtin_expect(landed < need, 0)) ring_spin(sm + L_FULL, need);
        asm volatile("" ::: "memory");
        SCHED();
        rdp(0, s0, 0);
        mm_tail(1);
        interleave<16, 14, 14>();
        SCHED();
    }
    STAMP(a.ws, cw, 3);
    bf16x8 fr[5][2];
#pragma unroll
    for (int t = 0; t < 2; ++t)
#pragma unroll
        for (int i = 0; i < 4; ++i)
#pragma unroll
            for (int r = 0; r < 4; ++r) an[t][i][r] = phi(an[t][i][r]);
    if (nh == 0) {
#pragma unroll
        for (int i = 0; i < 4; ++i)
#pragma unroll
            for (int r = 0; r < 4; ++r) an[2][i][r] = phi(an[2][i][r]);
    }
#pragma unroll
    for (int t = 0; t < 5; ++t) { fr[t][0] = pack_pair(an[t][0], an[t][1]); fr[t][1] = pack_pair(an[t][2], an[t][3]); }
    {
        LAS char* xs = sm + B_X1 + cw * 2048 + lane * 16;
        *(LAS bf16x8*)xs = fr[2][0];
        *(LAS bf16x8*)(xs + 1024) = fr[2][1];
    }
    wait_lgkm0();
    word_set(sm + L_X1 + 4 * cw, 1u);
    STAMP(a.ws, cw, 4);
    word_wait(sm + L_X1 + 4 * (cw ^ 1), 1u);
    ring_spin(sm + L_FREE, 10);
    asm volatile("" ::: "memory");
    {
        LAS char* xr = sm + B_X1 + (cw ^ 1) * 2048 + lane * 16;
        const bf16x8 R0 = *(LAS bf16x8*)xr, R1 = *(LAS bf16x8*)(xr + 1024);
        const bf16x8 ones = as_frag((u32x4){0x3f803f80u, 0x3f803f80u, 0x3f803f80u, 0x3f803f80u});
        const f32x4 z4 = {0.f, 0.f, 0.f, 0.f};
        LAS float* kvt = (LAS float*)(sm + B_KVT) + mh * (80 * 84) + c * 84 + 4 * q;
        LAS float* zz = (LAS float*)(sm + B_Z) + mh * 80 + 4 * q;
        auto prod = [&](const bf16x8& k0, const bf16x8& k1, const bf16x8& v0, const bf16x8& v1, int ta, int tb) LAM {
            *(LAS f32x4*)(kvt + 16 * tb * 84 + 16 * ta) = mfma(k1, v1, mfma(k0, v0, z4));
        };
        auto zsum = [&](const bf16x8& k0, const bf16x8& k1, int ta) LAM {
            const f32x4 d = mfma(k1, ones, mfma(k0, ones, z4));
            if (c == 0) *(LAS f32x4*)(zz + 16 * ta) = d;
        };
        const int b0 = 3 * nh;
        prod(fr[0][0], fr[0][1], fr[3][0], fr[3][1], b0, b0);
        prod(fr[0][0], fr[0][1], fr[4][0], fr[4][1], b0, b0 + 1);
        prod(fr[1][0], fr[1][1], fr[3][0], fr[3][1], b0 + 1, b0);
        prod(fr[1][0], fr[1][1], fr[4][0], fr[4][1], b0 + 1, b0 + 1);
        zsum(fr[0][0], fr[0][1], b0);
        zsum(fr[1][0], fr[1][1], b0 + 1);
        if (nh == 0) {
            prod(fr[2][0], fr[2][1], fr[4][0], fr[4][1], 2, 1);
            prod(fr[1][0], fr[1][1], R0, R1, 1, 2);
            prod(fr[2][0], fr[2][1], R0, R1, 2, 2);
            zsum(fr[2][0], fr[2][1], 2);
        } else {
            prod(fr[0][0], fr[0][1], fr[2][0], fr[2][1], 3, 2);
            prod(R0, R1, fr[3][0], fr[3][1], 2, 3);
        }
    }
    WAIT_VM(0);
    wait_lgkm0();
    if (lane == 0) word_add(sm + L_KV);
    __builtin_amdgcn_s_setprio(0);
    STAMP(a.ws, cw, 5);
    if (JOBMODE == 1) {
        const int jb = (ti * 8 + gj) * 4 + cw;
        LAS char* stg = sm + cw * 8192;
        job_table(a, sm + B_TAB + cw * 512, jb, lane);
        word_wait(sm + L_H2, 1u);
        job_issue(a, stg, jb, lane);
        WAIT_VM(0);
        wait_lgkm0();
        job_finish(a, stg, sm + B_TAB + cw * 512, jb, lane);
        STAMP(a.ws, cw, 6);
        signal_stores(sm, L_DONE, a.ws + WS_FW2T + (ti * 8 + gj) * 8, tag, lane, 4u);
        STAMP(a.ws, cw, 7);
    } else if (lane == 0) word_add(sm + L_DONE);
    p2_final(a, sm, cw, lane, tag);
}

}

__global__ __launch_bounds__(512, 2) NOPK void gm_fused(const void* x, const void* lre, const void* lim, const void* bre,
                                                   const void* bim, const void* cre, const void* cim, const void* logdt,
                                                   const void* dskip, const void* wqkv, const void* wo, const void* gw,
                                                   void* ws, void* out) {
    __shared__ __attribute__((aligned(1024))) char smem[LDS_BYTES];
    LAS char* sm = (LAS char*)smem;
#define MAPMODE 1
    const int b = blockIdx.x, xcd = b & 7, idx = b >> 3;
    const bool roleA = MAPMODE == 1 ? idx < 16 : xcd < 4;
    const int ti = MAPMODE == 1 ? 4 * (xcd >> 1) + (idx & 3) : 8 * ((xcd & 3) >> 1) + (idx & 7);
    const int gj = MAPMODE == 1 ? 4 * (xcd & 1) + ((idx >> 2) & 3) : 4 * (xcd & 1) + (idx >> 3);
    const int wave = __builtin_amdgcn_readfirstlane(threadIdx.x >> 6), lane = threadIdx.x & 63;
    const Args a{(const char*)x, (const u16*)lre, (const u16*)lim, (const char*)bre, (const char*)bim, (const char*)cre,
                 (const char*)cim, (const u16*)logdt, (const char*)wqkv, (const char*)wo, (const u16*)gw,
                 (const u16*)dskip, (char*)ws, (float*)out};
    STAMPL(ws, 0);
    if (roleA) {
        if (wave < 4) a_consumer(a, sm, ti, gj, wave, lane);
        else a_loader(a, sm, ti, gj, wave - 4, lane);
    } else {
        if (wave < 4) b_consumer(a, sm, ti, gj, wave, lane);
        else b_loader(a, sm, ti, gj, wave - 4, lane);
    }
    STAMPL(ws, 1);
}

extern "C" void kernel_launch(void* const* d_in, const int* in_sizes, int n_in, void* d_out, int out_size, void* d_ws,
                              size_t ws_size, hipStream_t stream) {
    gm_fused<<<256, 512, 0, stream>>>(d_in[0], d_in[1], d_in[2], d_in[3], d_in[4], d_in[5], d_in[6], d_in[7], d_in[8],
                                      d_in[10], d_in[11], d_in[12], d_ws, d_out);
}
```

```cpp
#define PROF
#include <hip/hip_runtime.h>
#include <stdio.h>
#include <hip/hip_bf16.h>
#include <stdint.h>

namespace {

typedef __attribute__((ext_vector_type(8))) short bf16x8;
typedef __attribute__((ext_vector_type(4))) short s16x4;
typedef __attribute__((ext_vector_type(4))) float f32x4;
typedef __attribute__((ext_vector_type(2))) float f32x2;
typedef __attribute__((ext_vector_type(2))) __bf16 bf16x2_t;
typedef __attribute__((ext_vector_type(4))) unsigned u32x4;
typedef __attribute__((ext_vector_type(2))) unsigned u32x2;
typedef unsigned long long u64;
typedef unsigned short u16;
#define LAS __attribute__((address_space(3)))
#define GAS __attribute__((address_space(1)))
#if defined(__HIP_DEVICE_COMPILE__)
#define NOPK __attribute__((target("no-packed-fp32-ops")))
#else
#define NOPK
#endif
#define DEV __device__ __forceinline__
#define LAM __attribute__((always_inline))
#define RLX_AGENT __ATOMIC_RELAXED, __HIP_MEMORY_SCOPE_AGENT

constexpr size_t WS_NONCE = 0;
constexpr size_t WS_EA    = 4096;
constexpr size_t WS_KV    = WS_EA + 16 * 8 * 160 * 8;
constexpr size_t WS_TOT   = WS_KV + 16 * 8 * 840 * 8;
constexpr size_t WS_G     = WS_TOT + 8 * 840 * 8;
constexpr size_t WS_FACT  = WS_G + 2048 * 8;
constexpr size_t WS_FW2T  = WS_FACT + 16 * 256;
constexpr size_t WS_W2T   = (WS_FW2T + 128 * 8 + 1023) / 1024 * 1024;
constexpr size_t TS = 61440;
constexpr size_t WS_ACT   = WS_W2T + 40 * TS;
constexpr size_t WS_PROF  = 32u << 20;

#define STAMPX(ws, off, wave, i, v) do { if ((threadIdx.x & 63) == 0) ((u64*)((char*)(ws) + WS_PROF + (off)))[(blockIdx.x * 8 + (wave)) * 16 + (i)] = (v); } while (0)
#define STAMP(ws, wave, i) STAMPX(ws, 0, wave, i, __builtin_amdgcn_s_memrealtime())
#define STAMPC(ws, wave, i) STAMPX(ws, 0, wave, i, __builtin_amdgcn_s_memtime())
#define TIMED(acc, stmt) do { u64 t_ = __builtin_amdgcn_s_memtime(); __builtin_amdgcn_s_waitcnt(0xC07F); stmt; u64 t2_ = __builtin_amdgcn_s_memtime(); __builtin_amdgcn_s_waitcnt(0xC07F); acc += t2_ - t_; } while (0)
#define STAMPV(ws, wave, i, v) STAMPX(ws, 0, wave, i, v)
#define STAMP2(ws, wave, i) STAMPX(ws, 1u << 20, wave, i, __builtin_amdgcn_s_memrealtime())

#define STAMPL(ws, i) do {} while (0)

constexpr int LDS_BYTES = 163840;
constexpr int SLOT_A = 47104, NS_A = 3;
constexpr int A_TAB  = 141312;
constexpr int A_G    = A_TAB + 3840;
constexpr int A_ESEG = A_G + 1024;
constexpr int A_HSEG = A_ESEG + 5120;
constexpr int A_TOT  = A_HSEG + 5120;
constexpr int A_HT = 47104, HT_STRIDE = 272;
static_assert(A_TOT + 3360 <= 159776, "lds");
constexpr int SLOT_B = 36864, NS_B = 4;
constexpr int B_X1  = 147456;
constexpr int B_KVT = 0;
constexpr int B_Z   = B_KVT + 2 * 80 * 84 * 4;
constexpr int B_TAB = 155648;
constexpr int B_STG = 110592;
static_assert(B_Z + 640 <= B_X1, "lds");

#define JOBMODE 2
#define SLP_W2T 16
#define SLP_ACT 4
#define SENT_LB 0
#define SENT_TOT 0
#define SLP_LB 2
#define SLP_TOT 2
#define HEAD_A 4
#define IL_A 25
#define IL_B 16
constexpr unsigned SPIN_CAP = 1u << 18;

DEV unsigned pk2(float a, float b) {
    return __builtin_bit_cast(unsigned, __builtin_convertvector((f32x2){a, b}, bf16x2_t));
}
DEV float bfr(float a) { return __uint_as_float(pk2(a, 0.f) << 16); }
DEV float bf_lo(unsigned u) { return __uint_as_float(u << 16); }
DEV float bf_hi(unsigned u) { return __uint_as_float(u & 0xffff0000u); }
DEV float bf2f(u16 u) { return __uint_as_float((unsigned)u << 16); }
DEV float swp(float x) {
    return __builtin_bit_cast(float, __builtin_amdgcn_mov_dpp(__builtin_bit_cast(int, x), 0xB1, 0xF, 0xF, true));
}
DEV float cmul_add(float a, float bs, float e, float u) { return fmaf(bs, swp(e), fmaf(a, e, u)); }
DEV f32x4 mfma(bf16x8 a, bf16x8 b, f32x4 c) { return __builtin_amdgcn_mfma_f32_16x16x32_bf16(a, b, c, 0, 0, 0); }
DEV bf16x8 as_frag(u32x4 v) { return __builtin_bit_cast(bf16x8, v); }
DEV bf16x8 lds_frag(LAS char* p) { return *(LAS bf16x8*)p; }
DEV bf16x8 lds_tr_frag(LAS char* p0, LAS char* p1) {
    s16x4 lo = __builtin_amdgcn_ds_read_tr16_b64_v4i16((LAS s16x4*)p0);
    s16x4 hi = __builtin_amdgcn_ds_read_tr16_b64_v4i16((LAS s16x4*)p1);
    return __builtin_shufflevector(lo, hi, 0, 1, 2, 3, 4, 5, 6, 7);
}
DEV void glds16_wt(const char* g, unsigned l) {
    unsigned keep;
    asm volatile("s_mov_b32 %0, m0\n\ts_mov_b32 m0, %2\n\ts_nop 0\n\tglobal_load_lds_dwordx4 %1, off sc1\n\ts_mov_b32 m0, %0"
                 : "=&s"(keep) : "v"(g), "s"(l) : "memory");
}
DEV void glds16(const char* g, LAS char* l) {
    __builtin_amdgcn_global_load_lds((const GAS unsigned*)g, (LAS unsigned*)l, 16, 0, 0);
}
DEV void bar() { asm volatile("" ::: "memory"); __builtin_amdgcn_s_barrier(); asm volatile("" ::: "memory"); }
DEV void wait_lgkm0() { __builtin_amdgcn_s_waitcnt(0xC07F); }
DEV void lds_bar() { asm volatile("" ::: "memory"); wait_lgkm0(); bar(); }
constexpr int L_FULL = 159776, L_FREE = L_FULL + 16;
constexpr int L_DONE = L_FULL + 32;
constexpr int L_KV = L_FULL + 36, L_W2T = L_FULL + 40, L_ACT = L_FULL + 44;
constexpr int L_X1 = L_FULL + 48, L_ZERO = L_FULL + 64;
constexpr int L_DONE2 = L_FULL + 72, L_ACT2 = L_FULL + 76;
constexpr int L_H2 = L_FULL + 80;
constexpr int L_WR = L_FULL + 84;
constexpr int L_TOT = L_FULL + 88, L_LBS = L_FULL + 92;
constexpr int L_HS = L_FULL + 96, L_HT = L_FULL + 100;
constexpr int L_WORDS = 26;
DEV unsigned umin4(const u32x4& f) { const unsigned a = f[0] < f[1] ? f[0] : f[1], b = f[2] < f[3] ? f[2] : f[3]; return a < b ? a : b; }
DEV void ring_put(LAS char* p, unsigned v) {
    asm volatile("ds_write_b32 %0, %1" ::"v"((unsigned)(size_t)p), "v"(v) : "memory");
}
DEV void ring_wait(LAS char* p, unsigned need) {
    for (unsigned spins = 0; spins < SPIN_CAP; ++spins) {
        u32x4 f;
        asm volatile("ds_read_b128 %0, %1\n\ts_waitcnt lgkmcnt(0)" : "=&v"(f) : "v"((unsigned)(size_t)p) : "memory");
        if ((unsigned)__builtin_amdgcn_readfirstlane(umin4(f)) >= need) break;
        __builtin_amdgcn_s_sleep(1);
    }
}
DEV void word_wait(LAS char* p, unsigned need) {
    for (unsigned spins = 0; spins < SPIN_CAP; ++spins) {
        if ((unsigned)__builtin_amdgcn_readfirstlane(*(volatile LAS unsigned*)p) >= need) break;
        __builtin_amdgcn_s_sleep(1);
    }
    asm volatile("" ::: "memory");
}
DEV void word_set(LAS char* p, unsigned v) { asm volatile("" ::: "memory"); *(volatile LAS unsigned*)p = v; }
DEV unsigned word_add(LAS char* p) {
    return __hip_atomic_fetch_add((LAS unsigned*)p, 1u, __ATOMIC_RELAXED, __HIP_MEMORY_SCOPE_WORKGROUP);
}
DEV void ring_spin(LAS char* p, unsigned need) {
    for (unsigned spins = 0; spins < SPIN_CAP; ++spins) {
        const u32x4 f = *(volatile LAS u32x4*)p;
        if ((unsigned)__builtin_amdgcn_readfirstlane(umin4(f)) >= need) break;
    }
}
#define WAIT_VM(n) asm volatile("s_waitcnt vmcnt(" #n ")" ::: "memory")
#define SCHED() __builtin_amdgcn_sched_barrier(0)
#define KEEP(x) asm volatile("" ::"v"(x))
template <int NM, int NR, int NS> DEV void interleave() {
#pragma unroll
    for (int i = 0; i < NS; ++i) {
        __builtin_amdgcn_sched_group_barrier(0x8, 1, 0);
        if ((i + 1) * NR / NS != i * NR / NS) __builtin_amdgcn_sched_group_barrier(0x100, 1, 0);
    }
    __builtin_amdgcn_sched_group_barrier(0x8, NM - NS, 0);
}
DEV bool wave_all(bool p) { return __builtin_amdgcn_ballot_w64(p) == __builtin_amdgcn_ballot_w64(true); }
DEV float lane_xor(float v, int m) {
    const int x = __builtin_bit_cast(int, v);
    const int y = m == 1 ? __builtin_amdgcn_mov_dpp(x, 0xB1, 0xF, 0xF, true) : m == 2 ? __builtin_amdgcn_mov_dpp(x, 0x4E, 0xF, 0xF, true)
                                                                              : __builtin_amdgcn_ds_swizzle(x, 0x101F);
    return __builtin_bit_cast(float, y);
}
DEV float fexp(float x) { return __builtin_amdgcn_exp2f(x * 1.44269504088896341f); }
DEV unsigned mk_tag(unsigned n) { return ((n + 1u) * 0x9E3779B1u) | 1u; }
DEV u64 granule(unsigned tag, unsigned v) { return ((u64)tag << 32) | v; }
DEV u64 flag_of(unsigned tag) { return granule(tag, tag * 0x85EBCA6Bu + 0x5BD1E995u); }
DEV __amdgpu_buffer_rsrc_t rsrc(const char* p, unsigned bytes) { return __builtin_amdgcn_make_buffer_rsrc((void*)p, 0, bytes, 0x00020000); }
DEV void st16_wt(const __amdgpu_buffer_rsrc_t& r, unsigned voff, unsigned soff, u32x4 v) { __builtin_amdgcn_raw_buffer_store_b128(v, r, voff + soff, 0, 16); }
DEV void st8_wt(const __amdgpu_buffer_rsrc_t& r, unsigned voff, unsigned soff, u32x2 v) { __builtin_amdgcn_raw_buffer_store_b64(v, r, voff + soff, 0, 16); }
DEV bf16x8 ld16_wt(const __amdgpu_buffer_rsrc_t& r, unsigned voff, unsigned soff) {
    return __builtin_bit_cast(bf16x8, __builtin_amdgcn_raw_buffer_load_b128(r, voff, soff, 16));
}
DEV void signal_stores(LAS char* sm, int word, char* flag, unsigned tag, int lane, unsigned waves) {
    asm volatile("s_waitcnt vmcnt(0)" ::: "memory");
    if (lane == 0) {
        const unsigned old = __hip_atomic_fetch_add((LAS unsigned*)(sm + word), 1u, __ATOMIC_RELAXED, __HIP_MEMORY_SCOPE_WORKGROUP);
        if (old == waves - 1u) __hip_atomic_store((GAS u64*)flag, flag_of(tag), RLX_AGENT);
    }
}
DEV float phi(float t) {
    const float inf = __builtin_inff();
    const float e = __builtin_amdgcn_exp2f(__builtin_amdgcn_fmed3f(t * 1.44269504088896341f, -inf, 0.f));
    return (e + 1e-6f) + __builtin_amdgcn_fmed3f(t, 0.f, inf);
}
DEV bf16x8 pack_pair(const f32x4& lo, const f32x4& hi) {
    return as_frag((u32x4){pk2(lo[0], lo[1]), pk2(lo[2], lo[3]), pk2(hi[0], hi[1]), pk2(hi[2], hi[3])});
}

DEV void scan_step(float& u0, float& u1, float& u2, float& u3, float& u4, const float (&e)[5], const float (&a)[5],
                   const float (&b)[5]) {
    asm("v_fmac_f32 %0, %10, %5\n\tv_fmac_f32 %1, %11, %6\n\tv_fmac_f32 %2, %12, %7\n\t"
        "v_fmac_f32 %3, %13, %8\n\tv_fmac_f32 %4, %14, %9\n\t"
        "v_fmac_f32_dpp %0, %5, %15 quad_perm:[1,0,3,2] row_mask:0xf bank_mask:0xf bound_ctrl:1\n\t"
        "v_fmac_f32_dpp %1, %6, %16 quad_perm:[1,0,3,2] row_mask:0xf bank_mask:0xf bound_ctrl:1\n\t"
        "v_fmac_f32_dpp %2, %7, %17 quad_perm:[1,0,3,2] row_mask:0xf bank_mask:0xf bound_ctrl:1\n\t"
        "v_fmac_f32_dpp %3, %8, %18 quad_perm:[1,0,3,2] row_mask:0xf bank_mask:0xf bound_ctrl:1\n\t"
        "v_fmac_f32_dpp %4, %9, %19 quad_perm:[1,0,3,2] row_mask:0xf bank_mask:0xf bound_ctrl:1"
        : "+v"(u0), "+v"(u1), "+v"(u2), "+v"(u3), "+v"(u4)
        : "v"(e[0]), "v"(e[1]), "v"(e[2]), "v"(e[3]), "v"(e[4]), "v"(a[0]), "v"(a[1]), "v"(a[2]), "v"(a[3]), "v"(a[4]),
          "v"(b[0]), "v"(b[1]), "v"(b[2]), "v"(b[3]), "v"(b[4]));
}

DEV void scan_step_nd(float (&t)[5], float u0, float u1, float u2, float u3, float u4, const float (&e)[5],
                      const float (&a)[5], const float (&b)[5]) {
    asm("v_fma_f32 %0, %15, %10, %5\n\tv_fma_f32 %1, %16, %11, %6\n\tv_fma_f32 %2, %17, %12, %7\n\t"
        "v_fma_f32 %3, %18, %13, %8\n\tv_fma_f32 %4, %19, %14, %9\n\t"
        "v_fmac_f32_dpp %0, %10, %20 quad_perm:[1,0,3,2] row_mask:0xf bank_mask:0xf bound_ctrl:1\n\t"
        "v_fmac_f32_dpp %1, %11, %21 quad_perm:[1,0,3,2] row_mask:0xf bank_mask:0xf bound_ctrl:1\n\t"
        "v_fmac_f32_dpp %2, %12, %22 quad_perm:[1,0,3,2] row_mask:0xf bank_mask:0xf bound_ctrl:1\n\t"
        "v_fmac_f32_dpp %3, %13, %23 quad_perm:[1,0,3,2] row_mask:0xf bank_mask:0xf bound_ctrl:1\n\t"
        "v_fmac_f32_dpp %4, %14, %24 quad_perm:[1,0,3,2] row_mask:0xf bank_mask:0xf bound_ctrl:1"
        : "=&v"(t[0]), "=&v"(t[1]), "=&v"(t[2]), "=&v"(t[3]), "=&v"(t[4])
        : "v"(u0), "v"(u1), "v"(u2), "v"(u3), "v"(u4), "v"(e[0]), "v"(e[1]), "v"(e[2]), "v"(e[3]), "v"(e[4]),
          "v"(a[0]), "v"(a[1]), "v"(a[2]), "v"(a[3]), "v"(a[4]), "v"(b[0]), "v"(b[1]), "v"(b[2]), "v"(b[3]), "v"(b[4]));
}

struct Args {
    const char* x; const u16* lre; const u16* lim; const char* bre; const char* bim; const char* cre; const char* cim;
    const u16* logdt; const char* wqkv; const char* wo; const u16* gw; const u16* dskip; char* ws; float* out;
};

DEV void channel_consts(const Args& a, int p, float& ar, float& ai, float& cr, float& ci) {
    const float lr = bf2f(a.lre[p]), li = bf2f(a.lim[p]);
    const float dt = bfr(fexp(bf2f(a.logdt[p])));
    const float zr = dt * lr, zi = dt * li, ex = fexp(zr);
    const float rv = zi * 0.15915494309189535f, rr = rv - __builtin_rintf(rv);
    ar = ex * __builtin_amdgcn_cosf(rr); ai = ex * __builtin_amdgcn_sinf(rr);
    const float nr = ar - 1.f, ni = ai, dn = lr * lr + li * li;
    cr = (nr * lr + ni * li) / dn; ci = (ni * lr - nr * li) / dn;
}

constexpr int P2_RED = 0;
#define P2_PW 4
#define P2_PA 5
DEV void p2_main(const Args& a, LAS char* sm, int lw, int lane, unsigned nonce) {
    const int b = blockIdx.x, xcd = b & 7, idx = b >> 3;
    const int tt = (xcd >> 1) * 8 + (idx & 7), cg = (xcd & 1) * 4 + (idx >> 3);
    const int c = lane & 15, q = lane >> 4;
    const unsigned tag = mk_tag(nonce);
    const u64 fl = flag_of(tag);
    constexpr int PW = P2_PW, PA = P2_PA;
    if (lw == 3) {
        const __amdgpu_buffer_rsrc_t rf = rsrc(a.ws + WS_FW2T, 128 * 8);
        const unsigned f0 = (unsigned)fl, f1 = (unsigned)(fl >> 32);
        for (unsigned spins = 0; spins < SPIN_CAP; ++spins) {
            const u32x4 v = __builtin_amdgcn_raw_buffer_load_b128(rf, lane * 16, 0, 16);
            if (wave_all((v[0] == f0) & (v[1] == f1) & (v[2] == f0) & (v[3] == f1))) break;
            __builtin_amdgcn_s_sleep(SLP_W2T);
        }
        word_set(sm + L_W2T, 1u);
    } else word_wait(sm + L_W2T, 1u);
    STAMP2(a.ws, lw + 4, 0);
    const __amdgpu_buffer_rsrc_t rw = rsrc(a.ws + WS_W2T, 40 * TS), ra = rsrc(a.ws + WS_ACT, 128 * TS);
    const unsigned wbase = 5 * cg * TS, abase = 4 * tt * TS, vo = lane * 16;
    const unsigned ko = (40 + 5 * lw) * 1024, kh = (10 * lw - 5) * 1024;
    auto koff = [&](int st) LAM { return (st < 5 ? ko : kh) + st * 1024; };
    bf16x8 fw[PW][5], fa[PA][4];
    auto ldw = [&](int st, int sl) LAM {
#pragma unroll
        for (int n = 0; n < 5; ++n) fw[sl][n] = ld16_wt(rw, vo, wbase + n * TS + koff(st));
    };
    auto lda = [&](int st) LAM {
#pragma unroll
        for (int t = 0; t < 4; ++t) fa[st % PA][t] = ld16_wt(ra, vo, abase + t * TS + koff(st));
    };
    auto wait_act = [&](int part, int word) LAM {
        if (lw == 3) {
            GAS u64* fp = (GAS u64*)(a.ws + WS_FACT) + (tt >> 1) * 32 + part * 16 + (lane & 7);
            for (unsigned spins = 0; spins < SPIN_CAP; ++spins) {
                const u64 v = __hip_atomic_load(fp, RLX_AGENT);
                if (wave_all(v == fl)) break;
                __builtin_amdgcn_s_sleep(SLP_ACT);
            }
            word_set(sm + word, 1u);
        } else word_wait(sm + word, 1u);
    };
    auto unit = [&](int i) LAM { const int u = 4 * lw + i; return u < 9 ? 5120 * u : 90624 + 5120 * (u - 9); };
    {
        const char* src = a.ws + WS_W2T + wbase + (10 * lw + 4) * 1024 + vo;
#pragma unroll
        for (int i = 0; i < 4; ++i)
#pragma unroll
            for (int n = 0; n < 5; ++n) glds16_wt(src + (size_t)n * TS + i * 1024, (unsigned)(size_t)(sm + unit(i) + n * 1024));
    }
#pragma unroll
    for (int st = 0; st < PW; ++st) ldw(st, st);
    SCHED();
    WAIT_VM(0);
    wait_act(1, L_ACT2);
    STAMP2(a.ws, lw + 4, 1);
    f32x4 acc[5][4];
#pragma unroll
    for (int n = 0; n < 5; ++n)
#pragma unroll
        for (int t = 0; t < 4; ++t) acc[n][t] = (f32x4){0.f, 0.f, 0.f, 0.f};
    auto slot = [&](int st) LAM { return st < 9 ? st % 4 : st == 13 ? 1 : st == 14 ? 2 : (st & 1) ? 3 : 0; };
    auto step = [&](int st) LAM {
        SCHED();
        if (st >= 8 && st < 12) {
#pragma unroll
            for (int n = 0; n < 5; ++n) fw[slot(st + 1)][n] = lds_frag(sm + unit(st - 8) + vo + n * 1024);
        }
#pragma unroll
        for (int n = 0; n < 5; ++n)
#pragma unroll
            for (int t = 0; t < 4; ++t) acc[n][t] = mfma(fw[slot(st)][n], fa[st % PA][t], acc[n][t]);
        SCHED();
        if (st < 5) ldw(st + 4, slot(st + 4));
        if (st == 9) ldw(13, slot(13));
        if (st == 10) ldw(14, slot(14));
        if (st == 11) { wait_lgkm0(); if (lane == 0) word_add(sm + L_WR); }
    };
    static_assert(PA == 5 && PW == 4, "ring layout");
#pragma unroll
    for (int st = 0; st < 5; ++st) lda(st);
#pragma unroll
    for (int st = 0; st < 5; ++st) step(st);
    STAMP2(a.ws, lw + 4, 4);
    wait_act(0, L_ACT);
    STAMP2(a.ws, lw + 4, 5);
    if (tt == 31 && cg == 0 && lw == 3 && lane == 0) __hip_atomic_store((GAS unsigned*)(a.ws + WS_NONCE), nonce + 1u, RLX_AGENT);
#pragma unroll
    for (int st = 5; st < 9; ++st) lda(st);
#pragma unroll
    for (int st = 5; st < 15; ++st) {
        if (st + 4 < 15) lda(st + 4);
        step(st);
    }
    STAMP2(a.ws, lw + 4, 2);
    word_wait(sm + L_WR, 4u);
    word_wait(sm + L_DONE, 4u);
    LAS float* rw_ = (LAS float*)(sm + P2_RED) + lw * (64 * 84);
#pragma unroll
    for (int t = 0; t < 4; ++t)
#pragma unroll
        for (int n = 0; n < 5; ++n) *(LAS f32x4*)(rw_ + (16 * t + c) * 84 + 16 * n + 4 * q) = acc[n][t];
    lds_bar();
    STAMP2(a.ws, lw + 4, 3);
}

DEV void p2_final(const Args& a, LAS char* sm, int cw, int lane, unsigned tag) {
    const int b = blockIdx.x, xcd = b & 7, idx = b >> 3;
    const int tt = (xcd >> 1) * 8 + (idx & 7), cg = (xcd & 1) * 4 + (idx >> 3);
    const int tid = cw * 64 + lane;
    u32x2 xv[5], dv[5]; float gv[5];
    int tok[5], n0[5];
#pragma unroll
    for (int i = 0; i < 5; ++i) {
        const int id = tid + 256 * i, r = id / 20, c4 = id - 20 * r;
        tok[i] = 64 * tt + 16 * ((r & 15) >> 2) + 4 * (r >> 4) + (r & 3); n0[i] = 80 * cg + 4 * c4;
        xv[i] = *(const u32x2*)((const u16*)a.x + tok[i] * 640 + n0[i]);
        dv[i] = *(const u32x2*)(a.dskip + n0[i]);
    }
    for (unsigned spins = 0; spins < SPIN_CAP; ++spins) {
        u64 v[5];
#pragma unroll
        for (int i = 0; i < 5; ++i) v[i] = __hip_atomic_load((GAS u64*)(a.ws + WS_G) + tok[i], RLX_AGENT);
        bool ok = true;
#pragma unroll
        for (int i = 0; i < 5; ++i) { gv[i] = __uint_as_float((unsigned)v[i]); ok &= (unsigned)(v[i] >> 32) == tag; }
        if (wave_all(ok)) break;
        __builtin_amdgcn_s_sleep(2);
    }
    f32x4 sk[5];
#pragma unroll
    for (int i = 0; i < 5; ++i) {
        const unsigned p01 = pk2(bf_lo(xv[i][0]) * bf_lo(dv[i][0]), bf_hi(xv[i][0]) * bf_hi(dv[i][0]));
        const unsigned p23 = pk2(bf_lo(xv[i][1]) * bf_lo(dv[i][1]), bf_hi(xv[i][1]) * bf_hi(dv[i][1]));
        sk[i] = (f32x4){gv[i] * bf_lo(p01), gv[i] * bf_hi(p01), gv[i] * bf_lo(p23), gv[i] * bf_hi(p23)};
    }
    STAMP2(a.ws, cw, 0);
    bar();
    STAMP2(a.ws, cw, 2);
#pragma unroll
    for (int i = 0; i < 5; ++i) {
        const int id = tid + 256 * i, r = id / 20, c4 = id - 20 * r;
        LAS float* p = (LAS float*)(sm + P2_RED) + r * 84 + 4 * c4;
        sk[i] += (*(LAS f32x4*)p + *(LAS f32x4*)(p + 64 * 84)) + (*(LAS f32x4*)(p + 128 * 84) + *(LAS f32x4*)(p + 192 * 84));
    }
    asm volatile("" : "+v"(sk[0]), "+v"(sk[1]), "+v"(sk[2]), "+v"(sk[3]), "+v"(sk[4]));
#define OUT_AUX 16
    {
        const __amdgpu_buffer_rsrc_t ro = rsrc((const char*)a.out, 2048 * 640 * 4);
#pragma unroll
        for (int i = 0; i < 5; ++i)
            __builtin_amdgcn_raw_buffer_store_b128(__builtin_bit_cast(u32x4, sk[i]), ro, (tok[i] * 640 + n0[i]) * 4, 0, OUT_AUX);
    }
    STAMP2(a.ws, cw, 3);
}

DEV void a_loader(const Args& a, LAS char* sm, int ti, int gj, int lw, int lane) {
    STAMP(a.ws, lw + 4, 0);
    const unsigned nonce = __hip_atomic_load((GAS unsigned*)(a.ws + WS_NONCE), RLX_AGENT);
    unsigned off[12];
    {
        const int pos = lane & 7, rl = lane >> 3, im = lw & 1;
#pragma unroll
        for (int u = 0; u < 4; ++u) {
            const int R = 8 * (lw + 4 * u) + rl, rho = R & 15;
            const int tok = (R & 64) + 16 * (rho >> 2) + 4 * ((R >> 4) & 3) + (rho & 3);
            off[u] = tok * 1280 + ((pos ^ (R & 7)) << 4);
        }
#pragma unroll
        for (int u = 4; u < 9; ++u) off[u] = (8 * ((lw + 4 * (u - 4)) >> 1) + rl) * 1280 + ((pos ^ rl ^ (im << 2)) << 4);
#pragma unroll
        for (int u = 9; u < 12; ++u) {
            const int ch = 64 * (lw + 4 * (u - 9)) + lane, rho = ch / 10, cc = ch - 10 * rho;
            off[u] = (rho ^ (((rho >> 3) & 1) << 2)) * 3840 + cc * 16;
        }
    }
    const char* xb = a.x + (size_t)ti * (128 * 1280);
    const char* bb = ((lw & 1) ? a.bim : a.bre) + (size_t)gj * (80 * 1280);
    const char* qb = a.wqkv + 160 * gj;
    const bool full = lw < 2;
    auto issue_a = [&](int s) LAM {
        LAS char* dst = sm + (s % NS_A) * SLOT_A + lw * 1024;
#pragma unroll
        for (int u = 0; u < 4; ++u) glds16(xb + 128 * s + off[u], dst + 4096 * u);
#pragma unroll
        for (int u = 4; u < 6; ++u) glds16(bb + 128 * s + off[u], dst + 4096 * u);
    };
    auto issue_b = [&](int s) LAM {
        LAS char* dst = sm + (s % NS_A) * SLOT_A + lw * 1024;
#pragma unroll
        for (int u = 6; u < 9; ++u) glds16(bb + 128 * s + off[u], dst + 4096 * u);
#pragma unroll
        for (int u = 9; u < 12; ++u) if (u < 11 || full) glds16(qb + (size_t)(64 * 3840) * s + off[u], dst + 4096 * u);
    };
    STAMP(a.ws, lw + 4, 1);
    issue_a(0); issue_b(0); issue_a(1); issue_b(1);
    if (full) WAIT_VM(12); else WAIT_VM(11);
    bar();
    u64 t_vm = 0, t_free = 0;
#pragma unroll 1
    for (int s = 0; s < 8; ++s) {
        if (s > 0) TIMED(t_free, ring_wait(sm + L_FREE, s));
        issue_a(s + 2);
        TIMED(t_vm, WAIT_VM(6));
        ring_put(sm + L_FULL + 4 * lw, s + 2);
        issue_b(s + 2);
    }
    WAIT_VM(0);
    ring_put(sm + L_FULL + 4 * lw, 10);
    STAMPV(a.ws, lw + 4, 14, t_vm); STAMPV(a.ws, lw + 4, 15, t_free);
    STAMP(a.ws, lw + 4, 2);
    __builtin_amdgcn_s_setprio(2);
    const unsigned tag = mk_tag(nonce);
    const int tl = lw * 64 + lane;
    if (tl < 128) {
        GAS u64* gp = (GAS u64*)(a.ws + WS_G) + 128 * ti + tl;
        u64 v;
        for (unsigned spins = 0;;) {
            v = __hip_atomic_load(gp, RLX_AGENT);
            if (wave_all((unsigned)(v >> 32) == tag) || ++spins > SPIN_CAP) break;
            __builtin_amdgcn_s_sleep(1);
        }
        const float g = __uint_as_float((unsigned)v);
        ((LAS float*)(sm + A_G))[tl] = g;
        ((LAS float*)(sm + A_G))[128 + tl] = bfr(1.f - g);
    }
    STAMP(a.ws, lw + 4, 9);
    lds_bar();
    STAMP(a.ws, lw + 4, 3);
    if (lw < 3) {
        const int col = tl < 160 ? tl : 159;
        LAS float* tab = (LAS float*)(sm + A_TAB);
        GAS u64* ea = (GAS u64*)(a.ws + WS_EA) + gj * 160;
        const float a16 = tab[2 * 160 + col], b16 = tab[3 * 160 + col], a128 = tab[4 * 160 + col], b128 = tab[5 * 160 + col];
        LAS float* es = (LAS float*)(sm + A_ESEG);
        float pre[8], P = 0.f;
#pragma unroll
        for (int s = 0; s < 8; ++s) { pre[s] = P; P = cmul_add(a16, b16, P, es[s * 160 + col]); }
        if (tl < 160) __hip_atomic_store(ea + ti * 1280 + col, granule(tag, __float_as_uint(P)), RLX_AGENT);
        STAMP(a.ws, lw + 4, 4);
        float H = 0.f;
        if (ti > 0) {
            if (SENT_LB == 0) {
            } else if (lw == 0) {
                GAS u64* sp = ea + (lane < ti ? lane : 0) * 1280 + 159;
                for (unsigned spins = 0; spins < SPIN_CAP; ++spins) {
                    if (wave_all((unsigned)(__hip_atomic_load(sp, RLX_AGENT) >> 32) == tag)) break;
                    __builtin_amdgcn_s_sleep(SLP_LB);
                }
                word_set(sm + L_LBS, 1u);
            } else word_wait(sm + L_LBS, 1u);
            float ev[15];
            for (unsigned spins = 0; spins < SPIN_CAP; ++spins) {
                u64 v[15];
#pragma unroll
                for (int i = 0; i < 15; ++i) v[i] = __hip_atomic_load(ea + (i < ti ? i : 0) * 1280 + col, RLX_AGENT);
                bool ok = true;
#pragma unroll
                for (int i = 0; i < 15; ++i) { ev[i] = __uint_as_float((unsigned)v[i]); ok &= (unsigned)(v[i] >> 32) == tag; }
                if (wave_all(ok)) break;
                __builtin_amdgcn_s_sleep(SLP_LB);
            }
#pragma unroll
            for (int i = 0; i < 15; ++i) if (i < ti) H = cmul_add(a128, b128, H, ev[i]);
        }
        STAMP(a.ws, lw + 4, 6);
        LAS float* hs = (LAS float*)(sm + A_HSEG);
#pragma unroll
        for (int s = 0; s < 8; ++s) { if (tl < 160) hs[s * 160 + col] = H + pre[s]; H = cmul_add(a16, b16, H, 0.f); }
        wait_lgkm0();
        if (lane == 0) word_add(sm + L_HS);
    } else {
        const int i16 = lane & 15;
        GAS u64* sp = (GAS u64*)(a.ws + WS_TOT) + gj * 840 + (i16 < 8 ? 53 * i16 + 52 : 424 + 52 * (i16 - 8) + 51);
        for (unsigned spins = 0; SENT_TOT && spins < SPIN_CAP; ++spins) {
            if (wave_all((unsigned)(__hip_atomic_load(sp, RLX_AGENT) >> 32) == tag)) break;
            __builtin_amdgcn_s_sleep(SLP_TOT);
        }
        STAMP(a.ws, lw + 4, 4);
        const __amdgpu_buffer_rsrc_t rs = rsrc(a.ws + WS_TOT, 8 * 840 * 8);
        u32x4 v[7];
        for (unsigned spins = 0; spins < SPIN_CAP; ++spins) {
            bool ok = true;
#pragma unroll
            for (int k = 0; k < 7; ++k) v[k] = __builtin_amdgcn_raw_buffer_load_b128(rs, gj * 6720 + (lane + 64 * k < 420 ? lane + 64 * k : 419) * 16, 0, 16);
#pragma unroll
            for (int k = 0; k < 7; ++k) ok &= (v[k][1] == tag) & (v[k][3] == tag);
            if (wave_all(ok)) break;
            __builtin_amdgcn_s_sleep(SLP_TOT);
        }
#pragma unroll
        for (int k = 0; k < 7; ++k) if (lane + 64 * k < 420) ((LAS u32x2*)(sm + A_TOT))[lane + 64 * k] = (u32x2){v[k][0], v[k][2]};
        wait_lgkm0();
        word_set(sm + L_TOT, 1u);
        STAMP(a.ws, lw + 4, 5);
    }
    __builtin_amdgcn_s_setprio(0);
    STAMP(a.ws, lw + 4, 8);
    p2_main(a, sm, lw, lane, nonce);
}

template <int NH>
DEV void a_pass1(f32x4 (&acc)[4][5], const float (&ca)[5], const float (&cb)[5], LAS float* es) {
    float e[5];
#pragma unroll
    for (int n = 0; n < 5; ++n) e[n] = acc[2 * NH][n][0];
#pragma unroll
    for (int t = 1; t < 16; ++t) {
        const int i = (t >> 2) ^ (2 * NH), r = t & 3;
        float y[5];
        scan_step_nd(y, acc[i][0][r], acc[i][1][r], acc[i][2][r], acc[i][3][r], acc[i][4][r], e, ca, cb);
#pragma unroll
        for (int n = 0; n < 5; ++n) e[n] = y[n];
    }
#pragma unroll
    for (int n = 0; n < 5; ++n) es[16 * n] = e[n];
}
template <int NH>
DEV void a_pass2(f32x4 (&acc)[4][5], const float (&ca)[5], const float (&cb)[5], LAS float* hs, LAS float* gt, LAS char* ht) {
    float e[5];
#pragma unroll
    for (int n = 0; n < 5; ++n) e[n] = hs[16 * n];
    asm volatile("s_nop 1" : "+v"(e[0]), "+v"(e[1]), "+v"(e[2]), "+v"(e[3]), "+v"(e[4]));
#pragma unroll
    for (int m = 0; m < 4; ++m) {
        const int i = m ^ (2 * NH);
        const f32x4 g = *(LAS f32x4*)(gt + 4 * m);
#pragma unroll
        for (int r = 0; r < 4; ++r) {
            float u0 = acc[i][0][r], u1 = acc[i][1][r], u2 = acc[i][2][r], u3 = acc[i][3][r], u4 = acc[i][4][r];
            scan_step(u0, u1, u2, u3, u4, e, ca, cb);
            e[0] = u0; e[1] = u1; e[2] = u2; e[3] = u3; e[4] = u4;
            acc[i][0][r] = u0 * g[r]; acc[i][1][r] = u1 * g[r]; acc[i][2][r] = u2 * g[r]; acc[i][3][r] = u3 * g[r]; acc[i][4][r] = u4 * g[r];
        }
#pragma unroll
        for (int n = 0; n < 5; ++n)
            *(LAS u32x2*)(ht + 16 * n * HT_STRIDE + 32 * m) = (u32x2){pk2(acc[i][n][0], acc[i][n][1]), pk2(acc[i][n][2], acc[i][n][3])};
    }
}

DEV void a_consumer(const Args& a, LAS char* sm, int ti, int gj, int cw, int lane) {
    const int mh = cw >> 1, nh = cw & 1, c = lane & 15, q = lane >> 4, tid = cw * 64 + lane;
    STAMP(a.ws, cw, 0);
    const unsigned nonce = __hip_atomic_load((GAS unsigned*)(a.ws + WS_NONCE), RLX_AGENT);
    const unsigned tag = mk_tag(nonce);
    if (cw >= 2) {
        const int tok = 128 * ti + 16 * gj + 8 * (cw - 2) + (lane >> 3);
        const u32x4* xr = (const u32x4*)(a.x + (size_t)tok * 1280) + (lane & 7);
        const u32x4* gr = (const u32x4*)a.gw + (lane & 7);
        u32x4 xv[10], gv[10];
#pragma unroll
        for (int i = 0; i < 10; ++i) { xv[i] = xr[8 * i]; gv[i] = gr[8 * i]; }
        float s = 0.f;
#pragma unroll
        for (int i = 0; i < 10; ++i)
#pragma unroll
            for (int k = 0; k < 4; ++k) {
                const unsigned pr = pk2(bf_lo(xv[i][k]) * bf_lo(gv[i][k]), bf_hi(xv[i][k]) * bf_hi(gv[i][k]));
                s += bf_lo(pr); s += bf_hi(pr);
            }
        s += lane_xor(s, 1); s += lane_xor(s, 2); s += lane_xor(s, 4);
        const float lg = bfr(s), g = bfr(1.f / (1.f + fexp(-lg)));
        if ((lane & 7) == 0) __hip_atomic_store((GAS u64*)(a.ws + WS_G) + tok, granule(tag, __float_as_uint(g)), RLX_AGENT);
    } else if (tid < 80) {
        float ar, ai, cr, ci;
        channel_consts(a, 80 * gj + tid, ar, ai, cr, ci);
        float pr = ar, pi = ai;
#pragma unroll
        for (int k = 0; k < 4; ++k) { const float t = pr * pr - pi * pi; pi = 2.f * pr * pi; pr = t; }
        const float r16 = pr, i16 = pi;
#pragma unroll
        for (int k = 0; k < 3; ++k) { const float t = pr * pr - pi * pi; pi = 2.f * pr * pi; pr = t; }
        LAS f32x2* tab = (LAS f32x2*)(sm + A_TAB);
        tab[0 * 80 + tid] = (f32x2){ar, ar};
        tab[1 * 80 + tid] = (f32x2){-ai, ai};
        tab[2 * 80 + tid] = (f32x2){r16, r16};
        tab[3 * 80 + tid] = (f32x2){-i16, i16};
        tab[4 * 80 + tid] = (f32x2){pr, pr};
        tab[5 * 80 + tid] = (f32x2){-pi, pi};
    }
    if (tid < 160) __hip_atomic_store((GAS u64*)(a.ws + WS_EA) + (ti * 8 + gj) * 160 + tid, (u64)0, RLX_AGENT);
    __builtin_amdgcn_s_setprio(1);
    f32x4 acc[4][5], at[5][2];
#pragma unroll
    for (int n = 0; n < 5; ++n) {
#pragma unroll
        for (int i = 0; i < 4; ++i) acc[i][n] = (f32x4){0.f, 0.f, 0.f, 0.f};
        at[n][0] = at[n][1] = (f32x4){0.f, 0.f, 0.f, 0.f};
    }
    int xlo[2], xhi[2], wa[2], wq[2];
#pragma unroll
    for (int kk = 0; kk < 2; ++kk) {
        const int sw = ((4 * kk + q) ^ (c & 7)) << 4;
        xlo[kk] = (64 * mh + 32 * nh + c) * 128 + sw;
        xhi[kk] = (64 * mh + 32 * (1 - nh) + c) * 128 + sw;
        wa[kk] = 16384 + (80 * nh + 8 * (c & 1) + (c >> 1)) * 128 + (((4 * kk + q) ^ (c >> 1) ^ ((c & 1) << 2)) << 4);
        wq[kk] = 36864 + (8 * q + 4 * (kk ^ (q & 1)) + (c >> 2)) * 160 + 8 * (c & 3);
    }
    bf16x8 xa[2][4], wb[2][5], wt[2][5];
    auto rdp = [&](int buf, LAS char* s0, int kk) LAM {
        LAS char* q0 = s0 + wq[0] + kk * 5120; LAS char* q1 = s0 + wq[1] + kk * 5120;
        xa[buf][0] = lds_frag(s0 + xlo[kk]);
        xa[buf][1] = lds_frag(s0 + xlo[kk] + 2048);
        wt[buf][0] = lds_tr_frag(q0, q1);
        xa[buf][2] = lds_frag(s0 + xhi[kk]);
        xa[buf][3] = lds_frag(s0 + xhi[kk] + 2048);
        wt[buf][1] = lds_tr_frag(q0 + 32, q1 + 32);
        wb[buf][0] = lds_frag(s0 + wa[kk]);
        wb[buf][1] = lds_frag(s0 + wa[kk] + 2048);
        wt[buf][2] = lds_tr_frag(q0 + 64, q1 + 64);
        wb[buf][2] = lds_frag(s0 + wa[kk] + 4096);
        wb[buf][3] = lds_frag(s0 + wa[kk] + 6144);
        wt[buf][3] = lds_tr_frag(q0 + 96, q1 + 96);
        wb[buf][4] = lds_frag(s0 + wa[kk] + 8192);
        wt[buf][4] = lds_tr_frag(q0 + 128, q1 + 128);
    };
    auto rd = [&](int buf, int slot, int kk) LAM { rdp(buf, sm + slot * SLOT_A, kk); };
    auto mm_head = [&](int buf) LAM {
#pragma unroll
        for (int n = 0; n < HEAD_A / 4; ++n)
#pragma unroll
            for (int i = 0; i < 4; ++i) acc[i][n] = mfma(xa[buf][i], wb[buf][n], acc[i][n]);
    };
    auto mm_tail = [&](int buf) LAM {
#pragma unroll
        for (int n = HEAD_A / 4; n < 5; ++n)
#pragma unroll
            for (int i = 0; i < 4; ++i) acc[i][n] = mfma(xa[buf][i], wb[buf][n], acc[i][n]);
#pragma unroll
        for (int n = 0; n < 5; ++n)
#pragma unroll
            for (int i = 0; i < 2; ++i) at[n][i] = mfma(wt[buf][n], xa[buf][i], at[n][i]);
    };
    if (tid < L_WORDS) ((LAS unsigned*)(sm + L_FULL))[tid] = tid < 4 ? 1u : 0u;
#pragma unroll
    for (int n = 0; n < 5; ++n)
        asm volatile("" : "+v"(acc[0][n]), "+v"(acc[1][n]), "+v"(acc[2][n]), "+v"(acc[3][n]), "+v"(at[n][0]), "+v"(at[n][1]));
    STAMP(a.ws, cw, 1);
    lds_bar();
    STAMP(a.ws, cw, 2);
    STAMPC(a.ws, cw, 12);
    rd(0, 0, 0);
    int slot = 0;
    u64 t_spin = 0, n_spin = 0;
#pragma unroll 1
    for (int s = 0; s < 10; ++s) {
        wait_lgkm0();
        SCHED();
        rd(1, slot, 1);
        u32x4 fl = *(volatile LAS u32x4*)(sm + L_FULL);
        mm_head(0); mm_tail(0);
        interleave<30, 20, IL_A>();
        SCHED();
        wait_lgkm0();
        asm volatile("" ::: "memory");
        *(volatile LAS unsigned*)(sm + L_FREE + 4 * cw) = s + 1;
        slot = slot == 2 ? 0 : slot + 1;
        const unsigned need = s < 9 ? s + 2 : 0;
        unsigned landed = __builtin_amdgcn_readfirstlane(umin4(fl));
        LAS char* s0 = sm + slot * SLOT_A;
        asm volatile("" : "+s"(landed), "+v"(s0));
        mm_head(1);
        SCHED();
        if (__builtin_expect(landed < need, 0)) { TIMED(t_spin, ring_spin(sm + L_FULL, need)); n_spin++; }
        asm volatile("" ::: "memory");
        SCHED();
        rdp(0, s0, 0);
        mm_tail(1);
        interleave<30 - HEAD_A, 19, IL_A - HEAD_A + 1>();
        SCHED();
    }
    __builtin_amdgcn_s_setprio(0);
    asm volatile("s_nop 7\n\ts_nop 7" ::: "memory");
    STAMP(a.ws, cw, 3);
    STAMPC(a.ws, cw, 13);
    STAMPV(a.ws, cw, 14, t_spin); STAMPV(a.ws, cw, 15, n_spin);
    LAS float* tab = (LAS float*)(sm + A_TAB);
    float ca[5], cb[5];
#pragma unroll
    for (int n = 0; n < 5; ++n) { const int col = 80 * nh + 16 * n + c; ca[n] = tab[col]; cb[n] = tab[160 + col]; }
    {
        LAS float* es = (LAS float*)(sm + A_ESEG) + (4 * mh + q) * 160 + 80 * nh + c;
        if (nh == 0) a_pass1<0>(acc, ca, cb, es); else a_pass1<1>(acc, ca, cb, es);
    }
    WAIT_VM(0);
    STAMP(a.ws, cw, 4);
    lds_bar();
    bf16x8 P[2][3];
#pragma unroll
    for (int i = 0; i < 2; ++i) {
#pragma unroll
        for (int t = 0; t < 5; ++t)
#pragma unroll
            for (int r = 0; r < 4; ++r) at[t][i][r] = phi(at[t][i][r]);
        P[i][0] = pack_pair(at[0][i], at[1][i]);
        P[i][1] = pack_pair(at[2][i], at[3][i]);
        P[i][2] = as_frag((u32x4){pk2(at[4][i][0], at[4][i][1]), pk2(at[4][i][2], at[4][i][3]), 0u, 0u});
    }
    int adr[21];
    {
        int dh[5], db[5], eh[5], eb[5];
#pragma unroll
        for (int k = 0; k < 5; ++k) {
            const int d0 = 16 * k + 4 * q, e0 = 16 * k + c;
            dh[k] = d0 / 20; db[k] = A_TOT + dh[k] * 840 + (d0 - 20 * dh[k]) * 2;
            eh[k] = e0 / 20; eb[k] = (e0 - 20 * eh[k]) * 40;
        }
        auto ad = [&](int k, int E) LAM { return dh[k] == eh[E] ? db[k] + eb[E] : L_ZERO; };
        adr[0] = ad(0, 0); adr[1] = ad(1, 0);
        adr[2] = ad(0, 1); adr[3] = ad(1, 1); adr[4] = ad(2, 1); adr[5] = ad(3, 1);
        adr[6] = ad(1, 2); adr[7] = ad(2, 2); adr[8] = ad(3, 2);
        adr[9] = ad(2, 3); adr[10] = ad(3, 3); adr[11] = ad(4, 3);
        adr[12] = ad(3, 4); adr[13] = ad(4, 4);
#pragma unroll
        for (int k = 0; k < 5; ++k) adr[14 + k] = dh[k] == (c & 3) ? db[k] + 800 : L_ZERO;
        adr[19] = L_ZERO; adr[20] = A_G + 4 * (128 + 64 * mh + 16 * (c >> 2) + 8 * nh + (c & 3));
    }
#pragma unroll
    for (int i = 0; i < 2; ++i) asm volatile("" : "+v"(P[i][0]), "+v"(P[i][1]), "+v"(P[i][2]));
    asm volatile("" : "+v"(adr[0]), "+v"(adr[1]), "+v"(adr[2]), "+v"(adr[3]), "+v"(adr[4]), "+v"(adr[5]), "+v"(adr[6]),
                      "+v"(adr[7]), "+v"(adr[8]), "+v"(adr[9]), "+v"(adr[10]), "+v"(adr[11]), "+v"(adr[12]), "+v"(adr[13]));
    asm volatile("" : "+v"(adr[14]), "+v"(adr[15]), "+v"(adr[16]), "+v"(adr[17]), "+v"(adr[18]), "+v"(adr[20]));
    const __amdgpu_buffer_rsrc_t ract = rsrc(a.ws + WS_ACT, 128 * TS);
    u32x4 os[2][2]; u32x2 o8[2];
    STAMP(a.ws, cw, 5);
    word_wait(sm + L_TOT, 1u);
    STAMP(a.ws, cw, 6);
    {
        auto h4 = [&](int i) LAM -> u32x2 { return *(LAS u32x2*)(sm + adr[i]); };
        auto fr2 = [&](const u32x2& lo, const u32x2& hi) LAM { return as_frag((u32x4){lo[0], lo[1], hi[0], hi[1]}); };
        const u32x2 zz = {0u, 0u};
        const bf16x8 Z0 = fr2(h4(14), h4(15)), Z1 = fr2(h4(16), h4(17)), Z2 = fr2(h4(18), zz);
        const bf16x8 F0 = fr2(h4(0), h4(1)), F1 = fr2(h4(2), h4(3)), F2 = fr2(h4(4), h4(5));
        const bf16x8 F3 = fr2(zz, h4(6)), F4 = fr2(h4(7), h4(8)), F5 = fr2(h4(9), h4(10));
        const bf16x8 F6 = fr2(h4(11), zz), F7 = fr2(zz, h4(12)), F8 = fr2(h4(13), zz);
        const float ogv[2] = {*(LAS float*)(sm + adr[20]), *(LAS float*)(sm + adr[20] + 16)};
        const f32x4 z4 = {0.f, 0.f, 0.f, 0.f};
        const int uu = gj >> 1, odd = gj & 1;
#pragma unroll
        for (int i = 0; i < 2; ++i) {
            const f32x4 dn = mfma(Z2, P[i][2], mfma(Z1, P[i][1], mfma(Z0, P[i][0], z4)));
            const f32x4 n0 = mfma(F0, P[i][0], z4);
            const f32x4 n1 = mfma(F2, P[i][1], mfma(F1, P[i][0], z4));
            const f32x4 n2 = mfma(F4, P[i][1], mfma(F3, P[i][0], z4));
            const f32x4 n3 = mfma(F6, P[i][2], mfma(F5, P[i][1], z4));
            const f32x4 n4 = mfma(F8, P[i][2], mfma(F7, P[i][1], z4));
            const float og = ogv[i];
            const float r0 = og * __builtin_amdgcn_rcpf(dn[0] + 1e-6f), r1 = og * __builtin_amdgcn_rcpf(dn[1] + 1e-6f);
            const float r2 = og * __builtin_amdgcn_rcpf(dn[2] + 1e-6f), r3 = og * __builtin_amdgcn_rcpf(dn[3] + 1e-6f);
            const float s1 = q < 1 ? r0 : r1, s2 = q < 2 ? r1 : r2, s3 = q < 3 ? r2 : r3;
            os[i][0] = (u32x4){pk2(n0[0] * r0, n0[1] * r0), pk2(n0[2] * r0, n0[3] * r0), pk2(n1[0] * s1, n1[1] * s1), pk2(n1[2] * s1, n1[3] * s1)};
            os[i][1] = (u32x4){pk2(n2[0] * s2, n2[1] * s2), pk2(n2[2] * s2, n2[3] * s2), pk2(n3[0] * s3, n3[1] * s3), pk2(n3[2] * s3, n3[3] * s3)};
            o8[i] = (u32x2){pk2(n4[0] * r3, n4[1] * r3), pk2(n4[2] * r3, n4[3] * r3)};
        }
#pragma unroll
        for (int i = 0; i < 2; ++i) {
            const unsigned ob = (8 * ti + 4 * mh + 2 * nh + i) * TS + (40 + 5 * uu) * 1024;
            st16_wt(ract, lane * 16, ob + (odd ? 3072 : 0), os[i][0]);
            st16_wt(ract, lane * 16, ob + (odd ? 4096 : 1024), os[i][1]);
            st8_wt(ract, lane * 16 + 8 * odd, ob + 2048, o8[i]);
        }
    }
    STAMP(a.ws, cw, 7);
    word_wait(sm + L_HS, 3u);
    STAMP(a.ws, cw, 8);
    {
        LAS float* hs = (LAS float*)(sm + A_HSEG) + (4 * mh + q) * 160 + 80 * nh + c;
        LAS float* gt = (LAS float*)(sm + A_G) + 64 * mh + 16 * q;
        LAS char* ht = sm + A_HT + (80 * nh + c) * HT_STRIDE + (64 * mh + 4 * q) * 2;
        if (nh == 0) a_pass2<0>(acc, ca, cb, hs, gt, ht); else a_pass2<1>(acc, ca, cb, hs, gt, ht);
    }
    signal_stores(sm, L_DONE2, a.ws + WS_FACT + ti * 256 + 128 + gj * 8, tag, lane, 4u);
    KEEP(os[0][0]); KEEP(os[0][1]); KEEP(os[1][0]); KEEP(os[1][1]);
    STAMP(a.ws, cw, 9);
    wait_lgkm0();
    if (lane == 0) word_add(sm + L_HT);
    word_wait(sm + L_HT, 4u);
    {
        const int qq = (lane & 15) >> 2, p = lane & 3;
        bf16x8 hf[10];
#pragma unroll
        for (int i = 0; i < 10; ++i) {
            const int f = cw + 4 * i, mt = f / 5, ks = f - 5 * mt;
            LAS char* b = sm + A_HT + (32 * ks + 8 * q + qq) * HT_STRIDE + 32 * mt + 8 * p;
            hf[i] = lds_tr_frag(b, b + 4 * HT_STRIDE);
        }
        SCHED();
#pragma unroll
        for (int i = 0; i < 10; ++i) {
            const int f = cw + 4 * i, mt = f / 5, ks = f - 5 * mt;
            st16_wt(ract, lane * 16, (8 * ti + mt) * TS + (5 * gj + ks) * 1024, __builtin_bit_cast(u32x4, hf[i]));
        }
    }
    wait_lgkm0();
    STAMP(a.ws, cw, 10);
    signal_stores(sm, L_DONE, a.ws + WS_FACT + ti * 256 + gj * 8, tag, lane, 4u);
    STAMP(a.ws, cw, 11);
    p2_final(a, sm, cw, lane, tag);
}

DEV void job_table(const Args& a, LAS char* tab, int jb, int lane) {
    if (jb < 400) {
        float ar, ai, cr, ci;
        channel_consts(a, 64 * (jb / 40) + lane, ar, ai, cr, ci);
        ((LAS f32x2*)tab)[lane] = (f32x2){2.f * cr, 2.f * ci};
    }
}
DEV void job_issue(const Args& a, LAS char* stg, int jb, int lane) {
    if (jb < 400) {
        const int cb = jb / 40, nt = jb - 40 * cb;
        const unsigned o = (16 * nt + (lane >> 3)) * 1280 + cb * 128 + (lane & 7) * 16;
        glds16(a.cre + o, stg); glds16(a.cre + o + 8 * 1280, stg + 1024);
        glds16(a.cim + o, stg + 2048); glds16(a.cim + o + 8 * 1280, stg + 3072);
    } else if (jb < 500) {
        const int jo = jb - 400, s5 = jo / 5, n8 = jo - 5 * s5, uu = s5 / 5, s = s5 - 5 * uu;
        const int lo = 160 * uu + (s == 0 ? 0 : s == 1 ? 32 : s == 2 ? 64 : s == 3 ? 80 : 112);
        const int hi = 160 * uu + (s == 0 ? 16 : s == 1 ? 48 : s == 2 ? 144 : s == 3 ? 96 : 128);
        const unsigned o = (lane >> 4) * 1280 + n8 * 256 + (lane & 15) * 16;
#pragma unroll
        for (int k = 0; k < 4; ++k) {
            glds16(a.wo + (lo + 4 * k) * 1280 + o, stg + 1024 * k);
            glds16(a.wo + (hi + 4 * k) * 1280 + o, stg + 4096 + 1024 * k);
        }
    }
}
DEV u32x2 lds_rd8(LAS char* p) { u32x2 v; asm volatile("ds_read_b64 %0, %1" : "=v"(v) : "v"((unsigned)(size_t)p) : "memory"); return v; }
DEV f32x4 lds_rd16(LAS char* p) { f32x4 v; asm volatile("ds_read_b128 %0, %1" : "=v"(v) : "v"((unsigned)(size_t)p) : "memory"); return v; }
DEV u32x2 lds_rdtr(LAS char* p) { u32x2 v; asm volatile("ds_read_b64_tr_b16 %0, %1" : "=v"(v) : "v"((unsigned)(size_t)p) : "memory"); return v; }
DEV void job_finish(const Args& a, LAS char* stg, LAS char* tab, int jb, int lane) {
    const int c = lane & 15, q = lane >> 4;
    const __amdgpu_buffer_rsrc_t rw = rsrc(a.ws + WS_W2T, 40 * TS);
    if (jb < 400) {
        const int cb = jb / 40, nt = jb - 40 * cb;
        u32x2 re[4], im[4]; f32x4 c01[4], c23[4];
#pragma unroll
        for (int k = 0; k < 4; ++k) {
            re[k] = lds_rd8(stg + c * 128 + 32 * k + 8 * q); im[k] = lds_rd8(stg + 2048 + c * 128 + 32 * k + 8 * q);
            c01[k] = lds_rd16(tab + (16 * k + 4 * q) * 8); c23[k] = lds_rd16(tab + (16 * k + 4 * q) * 8 + 16);
        }
        asm volatile("s_waitcnt lgkmcnt(0)" : "+v"(re[0]), "+v"(re[1]), "+v"(re[2]), "+v"(re[3]), "+v"(im[0]), "+v"(im[1]), "+v"(im[2]), "+v"(im[3]),
                     "+v"(c01[0]), "+v"(c01[1]), "+v"(c01[2]), "+v"(c01[3]), "+v"(c23[0]), "+v"(c23[1]), "+v"(c23[2]), "+v"(c23[3]) :: "memory");
        u32x4 w[4];
#pragma unroll
        for (int k = 0; k < 4; ++k) {
            const float r0 = bf_lo(re[k][0]), r1 = bf_hi(re[k][0]), r2 = bf_lo(re[k][1]), r3 = bf_hi(re[k][1]);
            const float i0 = bf_lo(im[k][0]), i1 = bf_hi(im[k][0]), i2 = bf_lo(im[k][1]), i3 = bf_hi(im[k][1]);
            w[k] = (u32x4){pk2(r0 * c01[k][0] - i0 * c01[k][1], -(r0 * c01[k][1] + i0 * c01[k][0])),
                           pk2(r1 * c01[k][2] - i1 * c01[k][3], -(r1 * c01[k][3] + i1 * c01[k][2])),
                           pk2(r2 * c23[k][0] - i2 * c23[k][1], -(r2 * c23[k][1] + i2 * c23[k][0])),
                           pk2(r3 * c23[k][2] - i3 * c23[k][3], -(r3 * c23[k][3] + i3 * c23[k][2]))};
        }
#pragma unroll
        for (int k = 0; k < 4; ++k) st16_wt(rw, lane * 16, nt * TS + (4 * cb + k) * 1024, w[k]);
    } else if (jb < 500) {
        const int jo = jb - 400, s5 = jo / 5, n8 = jo - 5 * s5;
        u32x2 lo[8], hi[8];
#pragma unroll
        for (int k = 0; k < 8; ++k) {
            LAS char* b = stg + (4 * q + (c >> 2)) * 256 + 32 * k + 8 * (c & 3);
            lo[k] = lds_rdtr(b); hi[k] = lds_rdtr(b + 4096);
        }
        asm volatile("s_waitcnt lgkmcnt(0)" : "+v"(lo[0]), "+v"(lo[1]), "+v"(lo[2]), "+v"(lo[3]), "+v"(lo[4]), "+v"(lo[5]), "+v"(lo[6]), "+v"(lo[7]),
                     "+v"(hi[0]), "+v"(hi[1]), "+v"(hi[2]), "+v"(hi[3]), "+v"(hi[4]), "+v"(hi[5]), "+v"(hi[6]), "+v"(hi[7]) :: "memory");
#pragma unroll
        for (int k = 0; k < 8; ++k)
            st16_wt(rw, lane * 16, (8 * n8 + k) * TS + (40 + s5) * 1024, (u32x4){lo[k][0], lo[k][1], hi[k][0], hi[k][1]});
    }
}

DEV void b_loader(const Args& a, LAS char* sm, int ti, int gj, int lw, int lane) {
    STAMP(a.ws, lw + 4, 0);
    const unsigned nonce = __hip_atomic_load((GAS unsigned*)(a.ws + WS_NONCE), RLX_AGENT);
    unsigned off[9];
    {
        const int pos = lane & 7, rl = lane >> 3;
#pragma unroll
        for (int u = 0; u < 4; ++u) { const int R = 8 * (lw + 4 * u) + rl; off[u] = R * 1280 + ((pos ^ (R & 7)) << 4); }
#pragma unroll
        for (int u = 4; u < 9; ++u) {
            const int wi = lw + 4 * (u - 4), sec = wi >= 10, ch = 64 * (wi - 10 * sec) + lane, rho = ch / 10, cc = ch - 10 * rho;
            off[u] = (rho ^ (((rho >> 3) & 1) << 2)) * 3840 + cc * 16 + 1280 + 1280 * sec;
        }
    }
    const char* xb = a.x + (size_t)ti * (128 * 1280);
    const char* wb = a.wqkv + 160 * gj;
    auto issue_a = [&](int s) LAM {
        LAS char* dst = sm + (s % NS_B) * SLOT_B + lw * 1024;
#pragma unroll
        for (int u = 0; u < 4; ++u) glds16(xb + 128 * s + off[u], dst + 4096 * u);
        glds16(wb + (size_t)(64 * 3840) * s + off[4], dst + 4096 * 4);
    };
    auto issue_b = [&](int s) LAM {
        LAS char* dst = sm + (s % NS_B) * SLOT_B + lw * 1024;
#pragma unroll
        for (int u = 5; u < 9; ++u) glds16(wb + (size_t)(64 * 3840) * s + off[u], dst + 4096 * u);
    };
    STAMP(a.ws, lw + 4, 1);
    const int jb = (ti * 8 + gj) * 4 + lw;
    LAS char* stg = sm + B_STG + lw * 8192;
    issue_a(0); issue_b(0);
    if (JOBMODE == 0) job_issue(a, stg, jb, lane);
    issue_a(1); issue_b(1); issue_a(2); issue_b(2);
    WAIT_VM(18);
    bar();
    if (JOBMODE == 0) job_finish(a, stg, sm + B_TAB + lw * 512, jb, lane);
#pragma unroll 1
    for (int s = 0; s < 7; ++s) {
        if (s > 0) ring_wait(sm + L_FREE, s);
        issue_a(s + 3);
        if (JOBMODE == 0 && s < 2 && jb < 400) WAIT_VM(18); else if (JOBMODE == 0 && s < 2 && jb < 500) WAIT_VM(22); else WAIT_VM(14);
        ring_put(sm + L_FULL + 4 * lw, s + 2);
        issue_b(s + 3);
    }
    WAIT_VM(9);
    ring_put(sm + L_FULL + 4 * lw, 9);
    WAIT_VM(0);
    ring_put(sm + L_FULL + 4 * lw, 10);
    const unsigned tag = mk_tag(nonce);
    if (JOBMODE == 0) signal_stores(sm, L_DONE2, a.ws + WS_FW2T + (ti * 8 + gj) * 8, tag, lane, 4u);
    STAMP(a.ws, lw + 4, 2);
    if (JOBMODE == 2) {
        ring_wait(sm + L_FREE, 8);
        job_issue(a, stg, jb, lane);
        WAIT_VM(0);
        job_finish(a, stg, sm + B_TAB + lw * 512, jb, lane);
        STAMP(a.ws, lw + 4, 6);
    }
    __builtin_amdgcn_s_setprio(2);
    const int tl = lw * 64 + lane;
    {
        int o0[2], o1[2];
#pragma unroll
        for (int it = 0; it < 2; ++it) {
            const int g = 2 * (tl + 256 * it < 420 ? tl + 256 * it : 0);
            const int h = g / 210, rem = g - 210 * h, e = rem / 10, d = 2 * (rem - 10 * e);
            const bool kv = rem < 200;
            o0[it] = kv ? B_KVT + ((20 * h + e) * 84 + 20 * h + d) * 4 : B_Z + (20 * h + 2 * (rem - 200)) * 4;
            o1[it] = o0[it] + (kv ? 80 * 84 * 4 : 320);
        }
        asm volatile("" : "+v"(o0[0]), "+v"(o0[1]), "+v"(o1[0]), "+v"(o1[1]));
        const __amdgpu_buffer_rsrc_t rk = rsrc(a.ws + WS_KV, 16 * 8 * 840 * 8);
        word_wait(sm + L_KV, 4u);
        STAMP(a.ws, lw + 4, 3);
        u32x4 pv[2];
#pragma unroll
        for (int it = 0; it < 2; ++it) {
            const f32x4 v = *(LAS f32x4*)(sm + o0[it]) + *(LAS f32x4*)(sm + o1[it]);
            pv[it] = (u32x4){pk2(v[0], v[1]), tag, pk2(v[2], v[3]), tag};
        }
        asm volatile("" : "+v"(pv[0]), "+v"(pv[1]) :: "memory");
        st16_wt(rk, tl * 16, (ti * 8 + gj) * 6720, pv[0]);
        if (tl + 256 < 420) st16_wt(rk, (tl + 256) * 16, (ti * 8 + gj) * 6720, pv[1]);
        asm volatile("s_nop 1" ::"v"(pv[0]), "v"(pv[1]));
    }
    STAMP(a.ws, lw + 4, 4);
    if (lw == 0) {
        const int cnt = ti < 8 ? 53 : 52, g0 = ti < 8 ? 53 * ti : 424 + 52 * (ti - 8);
        const int g = g0 + (lane < cnt ? lane : 0);
        GAS u64* src = (GAS u64*)(a.ws + WS_KV) + gj * 840 + g;
        unsigned v[16];
        for (unsigned spins = 0;;) {
            bool ok = true;
#pragma unroll
            for (int i = 0; i < 16; ++i) {
                const u64 x = __hip_atomic_load(src + i * (8 * 840), RLX_AGENT);
                v[i] = (unsigned)x; ok &= (unsigned)(x >> 32) == tag;
            }
            if (wave_all(ok) || ++spins > SPIN_CAP) break;
            __builtin_amdgcn_s_sleep(1);
        }
        float s0 = 0.f, s1 = 0.f;
#pragma unroll
        for (int i = 0; i < 16; ++i) { s0 += bf_lo(v[i]); s1 += bf_hi(v[i]); }
        if (lane < cnt) __hip_atomic_store((GAS u64*)(a.ws + WS_TOT) + gj * 840 + g, granule(tag, pk2(s0, s1)), RLX_AGENT);
        word_set(sm + L_H2, 1u);
    }
    STAMP(a.ws, lw + 4, 5);
    __builtin_amdgcn_s_setprio(0);
    if (JOBMODE == 2) signal_stores(sm, L_DONE2, a.ws + WS_FW2T + (ti * 8 + gj) * 8, tag, lane, 4u);
    p2_main(a, sm, lw, lane, nonce);
}

DEV void b_consumer(const Args& a, LAS char* sm, int ti, int gj, int cw, int lane) {
    const int mh = cw >> 1, nh = cw & 1, c = lane & 15, q = lane >> 4, tid = cw * 64 + lane;
    STAMP(a.ws, cw, 0);
    const unsigned tag = mk_tag(__hip_atomic_load((GAS unsigned*)(a.ws + WS_NONCE), RLX_AGENT));
    if (JOBMODE != 1) job_table(a, sm + B_TAB + cw * 512, (ti * 8 + gj) * 4 + cw, lane);
    {
        const __amdgpu_buffer_rsrc_t rk = rsrc(a.ws + WS_KV, 16 * 8 * 840 * 8);
        const u32x4 z = {0u, 0u, 0u, 0u};
        st16_wt(rk, tid * 16, (ti * 8 + gj) * 6720, z);
        if (tid + 256 < 420) st16_wt(rk, (tid + 256) * 16, (ti * 8 + gj) * 6720, z);
        const int cnt = ti < 8 ? 53 : 52, g0 = ti < 8 ? 53 * ti : 424 + 52 * (ti - 8);
        if (tid < cnt) __hip_atomic_store((GAS u64*)(a.ws + WS_TOT) + gj * 840 + g0 + tid, (u64)0, RLX_AGENT);
    }
    __builtin_amdgcn_s_setprio(1);
    f32x4 an[5][4];
#pragma unroll
    for (int i = 0; i < 4; ++i)
#pragma unroll
        for (int t = 0; t < 5; ++t) an[t][i] = (f32x4){0.f, 0.f, 0.f, 0.f};
    int xo[2], wA[2], wB[2];
#pragma unroll
    for (int kk = 0; kk < 2; ++kk) {
        xo[kk] = (64 * mh + c) * 128 + (((4 * kk + q) ^ (c & 7)) << 4);
        const int b = 16384 + (8 * q + 4 * (kk ^ (q & 1)) + (c >> 2)) * 160 + 8 * (c & 3);
        wA[kk] = b + 96 * nh; wB[kk] = b + 64 + 10240 * nh;
    }
    bf16x8 xa[2][4], wb[2][5];
    auto rdp = [&](int buf, LAS char* s0, int kk) LAM {
#pragma unroll
        for (int i = 0; i < 4; ++i) xa[buf][i] = lds_frag(s0 + xo[kk] + 2048 * i);
        LAS char* w0 = s0 + kk * 5120;
        wb[buf][0] = lds_tr_frag(w0 + wA[0], w0 + wA[1]);
        wb[buf][1] = lds_tr_frag(w0 + wA[0] + 32, w0 + wA[1] + 32);
        wb[buf][2] = lds_tr_frag(w0 + wB[0], w0 + wB[1]);
        wb[buf][3] = lds_tr_frag(w0 + wA[0] + 10240, w0 + wA[1] + 10240);
        wb[buf][4] = lds_tr_frag(w0 + wA[0] + 10272, w0 + wA[1] + 10272);
    };
    auto rd = [&](int buf, int slot, int kk) LAM { rdp(buf, sm + slot * SLOT_B, kk); };
    auto mm_head = [&](int buf) LAM {
#pragma unroll
        for (int i = 0; i < 4; ++i) an[0][i] = mfma(xa[buf][i], wb[buf][0], an[0][i]);
    };
    auto mm_tail = [&](int buf) LAM {
#pragma unroll
        for (int t = 1; t < 5; ++t)
#pragma unroll
            for (int i = 0; i < 4; ++i) an[t][i] = mfma(xa[buf][i], wb[buf][t], an[t][i]);
    };
    if (tid < L_WORDS) ((LAS unsigned*)(sm + L_FULL))[tid] = tid < 4 ? 1u : 0u;
#pragma unroll
    for (int t = 0; t < 5; ++t)
        asm volatile("" : "+v"(an[t][0]), "+v"(an[t][1]), "+v"(an[t][2]), "+v"(an[t][3]));
    STAMP(a.ws, cw, 1);
    lds_bar();
    STAMP(a.ws, cw, 2);
    rd(0, 0, 0);
#pragma unroll 1
    for (int s = 0; s < 10; ++s) {
        wait_lgkm0();
        SCHED();
        rd(1, s & 3, 1);
        u32x4 fl = *(volatile LAS u32x4*)(sm + L_FULL);
        mm_head(0); mm_tail(0);
        interleave<20, 15, IL_B>();
        SCHED();
        wait_lgkm0();
        asm volatile("" ::: "memory");
        *(volatile LAS unsigned*)(sm + L_FREE + 4 * cw) = s + 1;
        const unsigned need = s < 9 ? s + 2 : 0;
        unsigned landed = __builtin_amdgcn_readfirstlane(umin4(fl));
        LAS char* s0 = sm + ((s + 1) & 3) * SLOT_B;
        asm volatile("" : "+s"(landed), "+v"(s0));
        mm_head(1);
        SCHED();
        if (__builtin_expect(landed < need, 0)) ring_spin(sm + L_FULL, need);
        asm volatile("" ::: "memory");
        SCHED();
        rdp(0, s0, 0);
        mm_tail(1);
        interleave<16, 14, 14>();
        SCHED();
    }
    STAMP(a.ws, cw, 3);
    bf16x8 fr[5][2];
#pragma unroll
    for (int t = 0; t < 2; ++t)
#pragma unroll
        for (int i = 0; i < 4; ++i)
#pragma unroll
            for (int r = 0; r < 4; ++r) an[t][i][r] = phi(an[t][i][r]);
    if (nh == 0) {
#pragma unroll
        for (int i = 0; i < 4; ++i)
#pragma unroll
            for (int r = 0; r < 4; ++r) an[2][i][r] = phi(an[2][i][r]);
    }
#pragma unroll
    for (int t = 0; t < 5; ++t) { fr[t][0] = pack_pair(an[t][0], an[t][1]); fr[t][1] = pack_pair(an[t][2], an[t][3]); }
    {
        LAS char* xs = sm + B_X1 + cw * 2048 + lane * 16;
        *(LAS bf16x8*)xs = fr[2][0];
        *(LAS bf16x8*)(xs + 1024) = fr[2][1];
    }
    wait_lgkm0();
    word_set(sm + L_X1 + 4 * cw, 1u);
    STAMP(a.ws, cw, 4);
    word_wait(sm + L_X1 + 4 * (cw ^ 1), 1u);
    ring_spin(sm + L_FREE, 10);
    asm volatile("" ::: "memory");
    {
        LAS char* xr = sm + B_X1 + (cw ^ 1) * 2048 + lane * 16;
        const bf16x8 R0 = *(LAS bf16x8*)xr, R1 = *(LAS bf16x8*)(xr + 1024);
        const bf16x8 ones = as_frag((u32x4){0x3f803f80u, 0x3f803f80u, 0x3f803f80u, 0x3f803f80u});
        const f32x4 z4 = {0.f, 0.f, 0.f, 0.f};
        LAS float* kvt = (LAS float*)(sm + B_KVT) + mh * (80 * 84) + c * 84 + 4 * q;
        LAS float* zz = (LAS float*)(sm + B_Z) + mh * 80 + 4 * q;
        auto prod = [&](const bf16x8& k0, const bf16x8& k1, const bf16x8& v0, const bf16x8& v1, int ta, int tb) LAM {
            *(LAS f32x4*)(kvt + 16 * tb * 84 + 16 * ta) = mfma(k1, v1, mfma(k0, v0, z4));
        };
        auto zsum = [&](const bf16x8& k0, const bf16x8& k1, int ta) LAM {
            const f32x4 d = mfma(k1, ones, mfma(k0, ones, z4));
            if (c == 0) *(LAS f32x4*)(zz + 16 * ta) = d;
        };
        const int b0 = 3 * nh;
        prod(fr[0][0], fr[0][1], fr[3][0], fr[3][1], b0, b0);
        prod(fr[0][0], fr[0][1], fr[4][0], fr[4][1], b0, b0 + 1);
        prod(fr[1][0], fr[1][1], fr[3][0], fr[3][1], b0 + 1, b0);
        prod(fr[1][0], fr[1][1], fr[4][0], fr[4][1], b0 + 1, b0 + 1);
        zsum(fr[0][0], fr[0][1], b0);
        zsum(fr[1][0], fr[1][1], b0 + 1);
        if (nh == 0) {
            prod(fr[2][0], fr[2][1], fr[4][0], fr[4][1], 2, 1);
            prod(fr[1][0], fr[1][1], R0, R1, 1, 2);
            prod(fr[2][0], fr[2][1], R0, R1, 2, 2);
            zsum(fr[2][0], fr[2][1], 2);
        } else {
            prod(fr[0][0], fr[0][1], fr[2][0], fr[2][1], 3, 2);
            prod(R0, R1, fr[3][0], fr[3][1], 2, 3);
        }
    }
    WAIT_VM(0);
    wait_lgkm0();
    if (lane == 0) word_add(sm + L_KV);
    __builtin_amdgcn_s_setprio(0);
    STAMP(a.ws, cw, 5);
    if (JOBMODE == 1) {
        const int jb = (ti * 8 + gj) * 4 + cw;
        LAS char* stg = sm + cw * 8192;
        job_table(a, sm + B_TAB + cw * 512, jb, lane);
        word_wait(sm + L_H2, 1u);
        job_issue(a, stg, jb, lane);
        WAIT_VM(0);
        wait_lgkm0();
        job_finish(a, stg, sm + B_TAB + cw * 512, jb, lane);
        STAMP(a.ws, cw, 6);
        signal_stores(sm, L_DONE, a.ws + WS_FW2T + (ti * 8 + gj) * 8, tag, lane, 4u);
        STAMP(a.ws, cw, 7);
    } else if (lane == 0) word_add(sm + L_DONE);
    p2_final(a, sm, cw, lane, tag);
}

}

__global__ __launch_bounds__(512, 2) NOPK void gm_fused(const void* x, const void* lre, const void* lim, const void* bre,
                                                   const void* bim, const void* cre, const void* cim, const void* logdt,
                                                   const void* dskip, const void* wqkv, const void* wo, const void* gw,
                                                   void* ws, void* out) {
    __shared__ __attribute__((aligned(1024))) char smem[LDS_BYTES];
    LAS char* sm = (LAS char*)smem;
#define MAPMODE 1
    const int b = blockIdx.x, xcd = b & 7, idx = b >> 3;
    const bool roleA = MAPMODE == 1 ? idx < 16 : xcd < 4;
    const int ti = MAPMODE == 1 ? 4 * (xcd >> 1) + (idx & 3) : 8 * ((xcd & 3) >> 1) + (idx & 7);
    const int gj = MAPMODE == 1 ? 4 * (xcd & 1) + ((idx >> 2) & 3) : 4 * (xcd & 1) + (idx >> 3);
    const int wave = __builtin_amdgcn_readfirstlane(threadIdx.x >> 6), lane = threadIdx.x & 63;
    const Args a{(const char*)x, (const u16*)lre, (const u16*)lim, (const char*)bre, (const char*)bim, (const char*)cre,
                 (const char*)cim, (const u16*)logdt, (const char*)wqkv, (const char*)wo, (const u16*)gw,
                 (const u16*)dskip, (char*)ws, (float*)out};
    STAMPL(ws, 0);
    if (roleA) {
        if (wave < 4) a_consumer(a, sm, ti, gj, wave, lane);
        else a_loader(a, sm, ti, gj, wave - 4, lane);
    } else {
        if (wave < 4) b_consumer(a, sm, ti, gj, wave, lane);
        else b_loader(a, sm, ti, gj, wave - 4, lane);
    }
    STAMPL(ws, 1);
    if ((blockIdx.x % 37) == 0 && (wave == 0 || wave == 4 || wave == 7) && lane == 0) {
        __builtin_amdgcn_s_waitcnt(0);
        const u64* p1 = (const u64*)((char*)ws + WS_PROF) + (blockIdx.x * 8 + wave) * 16;
        const u64* p2 = (const u64*)((char*)ws + WS_PROF + (1u << 20)) + (blockIdx.x * 8 + wave) * 16;
        const u64 now = __builtin_amdgcn_s_memrealtime();
        const u64 t0 = p1[0];
        printf("blk %d role %c wave %d t0 %llu : %d %d %d %d %d %d %d %d %d %d %d | %d %d %d %d %d %d | end %d\n", blockIdx.x, roleA ? 'A' : 'B', wave, t0 % 100000000ull,
               (int)(p1[1] - t0), (int)(p1[2] - t0), (int)(p1[3] - t0), (int)(p1[4] - t0), (int)(p1[5] - t0), (int)(p1[6] - t0),
               (int)(p1[7] - t0), (int)(p1[8] - t0), (int)(p1[9] - t0), (int)(p1[10] - t0), (int)(p1[11] - t0),
               (int)(p2[0] - t0), (int)(p2[1] - t0), (int)(p2[2] - t0), (int)(p2[3] - t0), (int)(p2[4] - t0), (int)(p2[5] - t0), (int)(now - t0));
    }
}

extern "C" void kernel_launch(void* const* d_in, const int* in_sizes, int n_in, void* d_out, int out_size, void* d_ws,
                              size_t ws_size, hipStream_t stream) {
    gm_fused<<<256, 512, 0, stream>>>(d_in[0], d_in[1], d_in[2], d_in[3], d_in[4], d_in[5], d_in[6], d_in[7], d_in[8],
                                      d_in[10], d_in[11], d_in[12], d_ws, d_out);
}
```

```cpp
#include <hip/hip_runtime.h>
#include <hip/hip_bf16.h>
#include <stdint.h>

namespace {

typedef __attribute__((ext_vector_type(8))) short bf16x8;
typedef __attribute__((ext_vector_type(4))) short s16x4;
typedef __attribute__((ext_vector_type(4))) float f32x4;
typedef __attribute__((ext_vector_type(2))) float f32x2;
typedef __attribute__((ext_vector_type(2))) __bf16 bf16x2_t;
typedef __attribute__((ext_vector_type(4))) unsigned u32x4;
typedef __attribute__((ext_vector_type(2))) unsigned u32x2;
typedef unsigned long long u64;
typedef unsigned short u16;
#define LAS __attribute__((address_space(3)))
#define GAS __attribute__((address_space(1)))
#if defined(__HIP_DEVICE_COMPILE__)
#define NOPK __attribute__((target("no-packed-fp32-ops")))
#else
#define NOPK
#endif
#define DEV __device__ __forceinline__
#define LAM __attribute__((always_inline))
#define RLX_AGENT __ATOMIC_RELAXED, __HIP_MEMORY_SCOPE_AGENT

constexpr size_t WS_NONCE = 0;
constexpr size_t WS_EA    = 4096;
constexpr size_t WS_KV    = WS_EA + 16 * 8 * 160 * 8;
constexpr size_t WS_TOT   = WS_KV + 16 * 8 * 840 * 8;
constexpr size_t WS_G     = WS_TOT + 8 * 840 * 8;
constexpr size_t WS_FACT  = WS_G + 2048 * 8;
constexpr size_t WS_FW2T  = WS_FACT + 16 * 256;
constexpr size_t WS_W2T   = (WS_FW2T + 128 * 8 + 1023) / 1024 * 1024;
constexpr size_t TS = 61440;
constexpr size_t WS_ACT   = WS_W2T + 40 * TS;
constexpr size_t WS_PROF  = 32u << 20;

#define STAMP(ws, wave, i) do {} while (0)
#define STAMPC(ws, wave, i) do {} while (0)
#define TIMED(acc, stmt) do { stmt; } while (0)
#define STAMPV(ws, wave, i, v) do {} while (0)
#define STAMP2(ws, wave, i) do {} while (0)

#define STAMPL(ws, i) do {} while (0)

constexpr int LDS_BYTES = 163840;
constexpr int SLOT_A = 47104, NS_A = 3;
constexpr int A_TAB  = 141312;
constexpr int A_G    = A_TAB + 3840;
constexpr int A_ESEG = A_G + 1024;
constexpr int A_HSEG = A_ESEG + 5120;
constexpr int A_TOT  = A_HSEG + 5120;
constexpr int A_HT = 47104, HT_STRIDE = 272;
static_assert(A_TOT + 3360 <= 159776, "lds");
constexpr int SLOT_B = 36864, NS_B = 4;
constexpr int B_X1  = 147456;
constexpr int B_KVT = 0;
constexpr int B_Z   = B_KVT + 2 * 80 * 84 * 4;
constexpr int B_TAB = 155648;
constexpr int B_STG = 110592;
static_assert(B_Z + 640 <= B_X1, "lds");

#define JOBMODE 2
#define SLP_W2T 16
#define SLP_ACT 4
#define SENT_LB 0
#define SENT_TOT 0
#define SLP_LB 2
#define SLP_TOT 2
#define HEAD_A 4
#define IL_A 25
#define IL_B 16
constexpr unsigned SPIN_CAP = 1u << 18;

DEV unsigned pk2(float a, float b) {
    return __builtin_bit_cast(unsigned, __builtin_convertvector((f32x2){a, b}, bf16x2_t));
}
DEV float bfr(float a) { return __uint_as_float(pk2(a, 0.f) << 16); }
DEV float bf_lo(unsigned u) { return __uint_as_float(u << 16); }
DEV float bf_hi(unsigned u) { return __uint_as_float(u & 0xffff0000u); }
DEV float bf2f(u16 u) { return __uint_as_float((unsigned)u << 16); }
DEV float swp(float x) {
    return __builtin_bit_cast(float, __builtin_amdgcn_mov_dpp(__builtin_bit_cast(int, x), 0xB1, 0xF, 0xF, true));
}
DEV float cmul_add(float a, float bs, float e, float u) { return fmaf(bs, swp(e), fmaf(a, e, u)); }
DEV f32x4 mfma(bf16x8 a, bf16x8 b, f32x4 c) { return __builtin_amdgcn_mfma_f32_16x16x32_bf16(a, b, c, 0, 0, 0); }
DEV bf16x8 as_frag(u32x4 v) { return __builtin_bit_cast(bf16x8, v); }
DEV bf16x8 lds_frag(LAS char* p) { return *(LAS bf16x8*)p; }
DEV bf16x8 lds_tr_frag(LAS char* p0, LAS char* p1) {
    s16x4 lo = __builtin_amdgcn_ds_read_tr16_b64_v4i16((LAS s16x4*)p0);
    s16x4 hi = __builtin_amdgcn_ds_read_tr16_b64_v4i16((LAS s16x4*)p1);
    return __builtin_shufflevector(lo, hi, 0, 1, 2, 3, 4, 5, 6, 7);
}
DEV void glds16_wt(const char* g, unsigned l) {
    unsigned keep;
    asm volatile("s_mov_b32 %0, m0\n\ts_mov_b32 m0, %2\n\ts_nop 0\n\tglobal_load_lds_dwordx4 %1, off sc1\n\ts_mov_b32 m0, %0"
                 : "=&s"(keep) : "v"(g), "s"(l) : "memory");
}
DEV void glds16(const char* g, LAS char* l) {
    __builtin_amdgcn_global_load_lds((const GAS unsigned*)g, (LAS unsigned*)l, 16, 0, 0);
}
DEV void bar() { asm volatile("" ::: "memory"); __builtin_amdgcn_s_barrier(); asm volatile("" ::: "memory"); }
DEV void wait_lgkm0() { __builtin_amdgcn_s_waitcnt(0xC07F); }
DEV void lds_bar() { asm volatile("" ::: "memory"); wait_lgkm0(); bar(); }
constexpr int L_FULL = 159776, L_FREE = L_FULL + 16;
constexpr int L_DONE = L_FULL + 32;
constexpr int L_KV = L_FULL + 36, L_W2T = L_FULL + 40, L_ACT = L_FULL + 44;
constexpr int L_X1 = L_FULL + 48, L_ZERO = L_FULL + 64;
constexpr int L_DONE2 = L_FULL + 72, L_ACT2 = L_FULL + 76;
constexpr int L_H2 = L_FULL + 80;
constexpr int L_WR = L_FULL + 84;
constexpr int L_TOT = L_FULL + 88, L_LBS = L_FULL + 92;
constexpr int L_HS = L_FULL + 96, L_HT = L_FULL + 100;
constexpr int L_WORDS = 26;
DEV unsigned umin4(const u32x4& f) { const unsigned a = f[0] < f[1] ? f[0] : f[1], b = f[2] < f[3] ? f[2] : f[3]; return a < b ? a : b; }
DEV void ring_put(LAS char* p, unsigned v) {
    asm volatile("ds_write_b32 %0, %1" ::"v"((unsigned)(size_t)p), "v"(v) : "memory");
}
DEV void ring_wait(LAS char* p, unsigned need) {
    for (unsigned spins = 0; spins < SPIN_CAP; ++spins) {
        u32x4 f;
        asm volatile("ds_read_b128 %0, %1\n\ts_waitcnt lgkmcnt(0)" : "=&v"(f) : "v"((unsigned)(size_t)p) : "memory");
        if ((unsigned)__builtin_amdgcn_readfirstlane(umin4(f)) >= need) break;
        __builtin_amdgcn_s_sleep(1);
    }
}
DEV void word_wait(LAS char* p, unsigned need) {
    for (unsigned spins = 0; spins < SPIN_CAP; ++spins) {
        if ((unsigned)__builtin_amdgcn_readfirstlane(*(volatile LAS unsigned*)p) >= need) break;
        __builtin_amdgcn_s_sleep(1);
    }
    asm volatile("" ::: "memory");
}
DEV void word_set(LAS char* p, unsigned v) { asm volatile("" ::: "memory"); *(volatile LAS unsigned*)p = v; }
DEV unsigned word_add(LAS char* p) {
    return __hip_atomic_fetch_add((LAS unsigned*)p, 1u, __ATOMIC_RELAXED, __HIP_MEMORY_SCOPE_WORKGROUP);
}
DEV void ring_spin(LAS char* p, unsigned need) {
    for (unsigned spins = 0; spins < SPIN_CAP; ++spins) {
        const u32x4 f = *(volatile LAS u32x4*)p;
        if ((unsigned)__builtin_amdgcn_readfirstlane(umin4(f)) >= need) break;
    }
}
#define WAIT_VM(n) asm volatile("s_waitcnt vmcnt(" #n ")" ::: "memory")
#define SCHED() __builtin_amdgcn_sched_barrier(0)
#define KEEP(x) asm volatile("" ::"v"(x))
template <int NM, int NR, int NS> DEV void interleave() {
#pragma unroll
    for (int i = 0; i < NS; ++i) {
        __builtin_amdgcn_sched_group_barrier(0x8, 1, 0);
        if ((i + 1) * NR / NS != i * NR / NS) __builtin_amdgcn_sched_group_barrier(0x100, 1, 0);
    }
    __builtin_amdgcn_sched_group_barrier(0x8, NM - NS, 0);
}
DEV bool wave_all(bool p) { return __builtin_amdgcn_ballot_w64(p) == __builtin_amdgcn_ballot_w64(true); }
DEV float lane_xor(float v, int m) {
    const int x = __builtin_bit_cast(int, v);
    const int y = m == 1 ? __builtin_amdgcn_mov_dpp(x, 0xB1, 0xF, 0xF, true) : m == 2 ? __builtin_amdgcn_mov_dpp(x, 0x4E, 0xF, 0xF, true)
                                                                              : __builtin_amdgcn_ds_swizzle(x, 0x101F);
    return __builtin_bit_cast(float, y);
}
DEV float fexp(float x) { return __builtin_amdgcn_exp2f(x * 1.44269504088896341f); }
DEV unsigned mk_tag(unsigned n) { return ((n + 1u) * 0x9E3779B1u) | 1u; }
DEV u64 granule(unsigned tag, unsigned v) { return ((u64)tag << 32) | v; }
DEV u64 flag_of(unsigned tag) { return granule(tag, tag * 0x85EBCA6Bu + 0x5BD1E995u); }
DEV __amdgpu_buffer_rsrc_t rsrc(const char* p, unsigned bytes) { return __builtin_amdgcn_make_buffer_rsrc((void*)p, 0, bytes, 0x00020000); }
DEV void st16_wt(const __amdgpu_buffer_rsrc_t& r, unsigned voff, unsigned soff, u32x4 v) { __builtin_amdgcn_raw_buffer_store_b128(v, r, voff + soff, 0, 16); }
DEV void st8_wt(const __amdgpu_buffer_rsrc_t& r, unsigned voff, unsigned soff, u32x2 v) { __builtin_amdgcn_raw_buffer_store_b64(v, r, voff + soff, 0, 16); }
DEV bf16x8 ld16_wt(const __amdgpu_buffer_rsrc_t& r, unsigned voff, unsigned soff) {
    return __builtin_bit_cast(bf16x8, __builtin_amdgcn_raw_buffer_load_b128(r, voff, soff, 16));
}
DEV void signal_stores(LAS char* sm, int word, char* flag, unsigned tag, int lane, unsigned waves) {
    asm volatile("s_waitcnt vmcnt(0)" ::: "memory");
    if (lane == 0) {
        const unsigned old = __hip_atomic_fetch_add((LAS unsigned*)(sm + word), 1u, __ATOMIC_RELAXED, __HIP_MEMORY_SCOPE_WORKGROUP);
        if (old == waves - 1u) __hip_atomic_store((GAS u64*)flag, flag_of(tag), RLX_AGENT);
    }
}
DEV float phi(float t) {
    const float inf = __builtin_inff();
    const float e = __builtin_amdgcn_exp2f(__builtin_amdgcn_fmed3f(t * 1.44269504088896341f, -inf, 0.f));
    return (e + 1e-6f) + __builtin_amdgcn_fmed3f(t, 0.f, inf);
}
DEV bf16x8 pack_pair(const f32x4& lo, const f32x4& hi) {
    return as_frag((u32x4){pk2(lo[0], lo[1]), pk2(lo[2], lo[3]), pk2(hi[0], hi[1]), pk2(hi[2], hi[3])});
}

DEV void scan_step(float& u0, float& u1, float& u2, float& u3, float& u4, const float (&e)[5], const float (&a)[5],
                   const float (&b)[5]) {
    asm("v_fmac_f32 %0, %10, %5\n\tv_fmac_f32 %1, %11, %6\n\tv_fmac_f32 %2, %12, %7\n\t"
        "v_fmac_f32 %3, %13, %8\n\tv_fmac_f32 %4, %14, %9\n\t"
        "v_fmac_f32_dpp %0, %5, %15 quad_perm:[1,0,3,2] row_mask:0xf bank_mask:0xf bound_ctrl:1\n\t"
        "v_fmac_f32_dpp %1, %6, %16 quad_perm:[1,0,3,2] row_mask:0xf bank_mask:0xf bound_ctrl:1\n\t"
        "v_fmac_f32_dpp %2, %7, %17 quad_perm:[1,0,3,2] row_mask:0xf bank_mask:0xf bound_ctrl:1\n\t"
        "v_fmac_f32_dpp %3, %8, %18 quad_perm:[1,0,3,2] row_mask:0xf bank_mask:0xf bound_ctrl:1\n\t"
        "v_fmac_f32_dpp %4, %9, %19 quad_perm:[1,0,3,2] row_mask:0xf bank_mask:0xf bound_ctrl:1"
        : "+v"(u0), "+v"(u1), "+v"(u2), "+v"(u3), "+v"(u4)
        : "v"(e[0]), "v"(e[1]), "v"(e[2]), "v"(e[3]), "v"(e[4]), "v"(a[0]), "v"(a[1]), "v"(a[2]), "v"(a[3]), "v"(a[4]),
          "v"(b[0]), "v"(b[1]), "v"(b[2]), "v"(b[3]), "v"(b[4]));
}

DEV void scan_step_nd(float (&t)[5], float u0, float u1, float u2, float u3, float u4, const float (&e)[5],
                      const float (&a)[5], const float (&b)[5]) {
    asm("v_fma_f32 %0, %15, %10, %5\n\tv_fma_f32 %1, %16, %11, %6\n\tv_fma_f32 %2, %17, %12, %7\n\t"
        "v_fma_f32 %3, %18, %13, %8\n\tv_fma_f32 %4, %19, %14, %9\n\t"
        "v_fmac_f32_dpp %0, %10, %20 quad_perm:[1,0,3,2] row_mask:0xf bank_mask:0xf bound_ctrl:1\n\t"
        "v_fmac_f32_dpp %1, %11, %21 quad_perm:[1,0,3,2] row_mask:0xf bank_mask:0xf bound_ctrl:1\n\t"
        "v_fmac_f32_dpp %2, %12, %22 quad_perm:[1,0,3,2] row_mask:0xf bank_mask:0xf bound_ctrl:1\n\t"
        "v_fmac_f32_dpp %3, %13, %23 quad_perm:[1,0,3,2] row_mask:0xf bank_mask:0xf bound_ctrl:1\n\t"
        "v_fmac_f32_dpp %4, %14, %24 quad_perm:[1,0,3,2] row_mask:0xf bank_mask:0xf bound_ctrl:1"
        : "=&v"(t[0]), "=&v"(t[1]), "=&v"(t[2]), "=&v"(t[3]), "=&v"(t[4])
        : "v"(u0), "v"(u1), "v"(u2), "v"(u3), "v"(u4), "v"(e[0]), "v"(e[1]), "v"(e[2]), "v"(e[3]), "v"(e[4]),
          "v"(a[0]), "v"(a[1]), "v"(a[2]), "v"(a[3]), "v"(a[4]), "v"(b[0]), "v"(b[1]), "v"(b[2]), "v"(b[3]), "v"(b[4]));
}

struct Args {
    const char* x; const u16* lre; const u16* lim; const char* bre; const char* bim; const char* cre; const char* cim;
    const u16* logdt; const char* wqkv; const char* wo; const u16* gw; const u16* dskip; char* ws; float* out;
    const char* code;
};

DEV void channel_consts(const Args& a, int p, float& ar, float& ai, float& cr, float& ci) {
    const float lr = bf2f(a.lre[p]), li = bf2f(a.lim[p]);
    const float dt = bfr(fexp(bf2f(a.logdt[p])));
    const float zr = dt * lr, zi = dt * li, ex = fexp(zr);
    const float rv = zi * 0.15915494309189535f, rr = rv - __builtin_rintf(rv);
    ar = ex * __builtin_amdgcn_cosf(rr); ai = ex * __builtin_amdgcn_sinf(rr);
    const float nr = ar - 1.f, ni = ai, dn = lr * lr + li * li;
    cr = (nr * lr + ni * li) / dn; ci = (ni * lr - nr * li) / dn;
}

constexpr int L_JUNK = 160000;
DEV void touch(const char* p, LAS char* sm) {
    unsigned keep;
    asm volatile("s_mov_b32 %0, m0\n\ts_mov_b32 m0, %2\n\ts_nop 0\n\tglobal_load_lds_dword %1, off\n\ts_mov_b32 m0, %0"
                 : "=&s"(keep) : "v"(p), "s"((unsigned)(size_t)(sm + L_JUNK)) : "memory");
}
#define WARM_TLB 0
#define WARM_CODE 0
#define CODE_BYTES 65536
DEV void warm_up(const Args& a, LAS char* sm, int lane) {
    const int idx = blockIdx.x >> 3;
    if (WARM_TLB) {
        const int e = (idx * 10 + lane) % 298;
        const char* p = e < 176 ? a.ws + (size_t)e * 65536
                      : e < 256 ? (const char*)a.out + (size_t)(e - 176) * 65536
                      : e < 269 ? a.cre + (size_t)(e - 256) * 65536
                      : e < 282 ? a.cim + (size_t)(e - 269) * 65536
                      : e < 295 ? a.wo + (size_t)(e - 282) * 65536
                      : e == 295 ? (const char*)a.dskip : e == 296 ? (const char*)a.gw : (const char*)a.logdt;
        touch(p, sm);
    }
    if (WARM_CODE) {
        const char* pc = a.code;
        const int l = idx * 16 + (lane & 15);
        if (lane < 16 && l * 128 < CODE_BYTES) touch(pc + l * 128, sm);
    }
}

constexpr int P2_RED = 0;
#define P2_PW 4
#define P2_PA 5
DEV void p2_main(const Args& a, LAS char* sm, int lw, int lane, unsigned nonce) {
    const int b = blockIdx.x, xcd = b & 7, idx = b >> 3;
    const int tt = (xcd >> 1) * 8 + (idx & 7), cg = (xcd & 1) * 4 + (idx >> 3);
    const int c = lane & 15, q = lane >> 4;
    const unsigned tag = mk_tag(nonce);
    const u64 fl = flag_of(tag);
    constexpr int PW = P2_PW, PA = P2_PA;
    if (lw == 3) {
        const __amdgpu_buffer_rsrc_t rf = rsrc(a.ws + WS_FW2T, 128 * 8);
        const unsigned f0 = (unsigned)fl, f1 = (unsigned)(fl >> 32);
        for (unsigned spins = 0; spins < SPIN_CAP; ++spins) {
            const u32x4 v = __builtin_amdgcn_raw_buffer_load_b128(rf, lane * 16, 0, 16);
            if (wave_all((v[0] == f0) & (v[1] == f1) & (v[2] == f0) & (v[3] == f1))) break;
            __builtin_amdgcn_s_sleep(SLP_W2T);
        }
        word_set(sm + L_W2T, 1u);
    } else word_wait(sm + L_W2T, 1u);
    STAMP2(a.ws, lw + 4, 0);
    const __amdgpu_buffer_rsrc_t rw = rsrc(a.ws + WS_W2T, 40 * TS), ra = rsrc(a.ws + WS_ACT, 128 * TS);
    const unsigned wbase = 5 * cg * TS, abase = 4 * tt * TS, vo = lane * 16;
    const unsigned ko = (40 + 5 * lw) * 1024, kh = (10 * lw - 5) * 1024;
    auto koff = [&](int st) LAM { return (st < 5 ? ko : kh) + st * 1024; };
    bf16x8 fw[PW][5], fa[PA][4];
    auto ldw = [&](int st, int sl) LAM {
#pragma unroll
        for (int n = 0; n < 5; ++n) fw[sl][n] = ld16_wt(rw, vo, wbase + n * TS + koff(st));
    };
    auto lda = [&](int st) LAM {
#pragma unroll
        for (int t = 0; t < 4; ++t) fa[st % PA][t] = ld16_wt(ra, vo, abase + t * TS + koff(st));
    };
    auto wait_act = [&](int part, int word) LAM {
        if (lw == 3) {
            GAS u64* fp = (GAS u64*)(a.ws + WS_FACT) + (tt >> 1) * 32 + part * 16 + (lane & 7);
            for (unsigned spins = 0; spins < SPIN_CAP; ++spins) {
                const u64 v = __hip_atomic_load(fp, RLX_AGENT);
                if (wave_all(v == fl)) break;
                __builtin_amdgcn_s_sleep(SLP_ACT);
            }
            word_set(sm + word, 1u);
        } else word_wait(sm + word, 1u);
    };
    auto unit = [&](int i) LAM { const int u = 4 * lw + i; return u < 9 ? 5120 * u : 90624 + 5120 * (u - 9); };
    {
        const char* src = a.ws + WS_W2T + wbase + (10 * lw + 4) * 1024 + vo;
#pragma unroll
        for (int i = 0; i < 4; ++i)
#pragma unroll
            for (int n = 0; n < 5; ++n) glds16_wt(src + (size_t)n * TS + i * 1024, (unsigned)(size_t)(sm + unit(i) + n * 1024));
    }
#pragma unroll
    for (int st = 0; st < PW; ++st) ldw(st, st);
    SCHED();
    WAIT_VM(0);
    wait_act(1, L_ACT2);
    STAMP2(a.ws, lw + 4, 1);
    f32x4 acc[5][4];
#pragma unroll
    for (int n = 0; n < 5; ++n)
#pragma unroll
        for (int t = 0; t < 4; ++t) acc[n][t] = (f32x4){0.f, 0.f, 0.f, 0.f};
    auto slot = [&](int st) LAM { return st < 9 ? st % 4 : st == 13 ? 1 : st == 14 ? 2 : (st & 1) ? 3 : 0; };
    auto step = [&](int st) LAM {
        SCHED();
        if (st >= 8 && st < 12) {
#pragma unroll
            for (int n = 0; n < 5; ++n) fw[slot(st + 1)][n] = lds_frag(sm + unit(st - 8) + vo + n * 1024);
        }
#pragma unroll
        for (int n = 0; n < 5; ++n)
#pragma unroll
            for (int t = 0; t < 4; ++t) acc[n][t] = mfma(fw[slot(st)][n], fa[st % PA][t], acc[n][t]);
        SCHED();
        if (st < 5) ldw(st + 4, slot(st + 4));
        if (st == 9) ldw(13, slot(13));
        if (st == 10) ldw(14, slot(14));
        if (st == 11) { wait_lgkm0(); if (lane == 0) word_add(sm + L_WR); }
    };
    static_assert(PA == 5 && PW == 4, "ring layout");
#pragma unroll
    for (int st = 0; st < 5; ++st) lda(st);
#pragma unroll
    for (int st = 0; st < 5; ++st) step(st);
    STAMP2(a.ws, lw + 4, 4);
    wait_act(0, L_ACT);
    STAMP2(a.ws, lw + 4, 5);
    if (tt == 31 && cg == 0 && lw == 3 && lane == 0) __hip_atomic_store((GAS unsigned*)(a.ws + WS_NONCE), nonce + 1u, RLX_AGENT);
#pragma unroll
    for (int st = 5; st < 9; ++st) lda(st);
#pragma unroll
    for (int st = 5; st < 15; ++st) {
        if (st + 4 < 15) lda(st + 4);
        step(st);
    }
    STAMP2(a.ws, lw + 4, 2);
    word_wait(sm + L_WR, 4u);
    word_wait(sm + L_DONE, 4u);
    LAS float* rw_ = (LAS float*)(sm + P2_RED) + lw * (64 * 84);
#pragma unroll
    for (int t = 0; t < 4; ++t)
#pragma unroll
        for (int n = 0; n < 5; ++n) *(LAS f32x4*)(rw_ + (16 * t + c) * 84 + 16 * n + 4 * q) = acc[n][t];
    lds_bar();
    STAMP2(a.ws, lw + 4, 3);
}

DEV void p2_final(const Args& a, LAS char* sm, int cw, int lane, unsigned tag) {
    const int b = blockIdx.x, xcd = b & 7, idx = b >> 3;
    const int tt = (xcd >> 1) * 8 + (idx & 7), cg = (xcd & 1) * 4 + (idx >> 3);
    const int tid = cw * 64 + lane;
    u32x2 xv[5], dv[5]; float gv[5];
    int tok[5], n0[5];
#pragma unroll
    for (int i = 0; i < 5; ++i) {
        const int id = tid + 256 * i, r = id / 20, c4 = id - 20 * r;
        tok[i] = 64 * tt + 16 * ((r & 15) >> 2) + 4 * (r >> 4) + (r & 3); n0[i] = 80 * cg + 4 * c4;
        xv[i] = *(const u32x2*)((const u16*)a.x + tok[i] * 640 + n0[i]);
        dv[i] = *(const u32x2*)(a.dskip + n0[i]);
    }
    for (unsigned spins = 0; spins < SPIN_CAP; ++spins) {
        u64 v[5];
#pragma unroll
        for (int i = 0; i < 5; ++i) v[i] = __hip_atomic_load((GAS u64*)(a.ws + WS_G) + tok[i], RLX_AGENT);
        bool ok = true;
#pragma unroll
        for (int i = 0; i < 5; ++i) { gv[i] = __uint_as_float((unsigned)v[i]); ok &= (unsigned)(v[i] >> 32) == tag; }
        if (wave_all(ok)) break;
        __builtin_amdgcn_s_sleep(2);
    }
    f32x4 sk[5];
#pragma unroll
    for (int i = 0; i < 5; ++i) {
        const unsigned p01 = pk2(bf_lo(xv[i][0]) * bf_lo(dv[i][0]), bf_hi(xv[i][0]) * bf_hi(dv[i][0]));
        const unsigned p23 = pk2(bf_lo(xv[i][1]) * bf_lo(dv[i][1]), bf_hi(xv[i][1]) * bf_hi(dv[i][1]));
        sk[i] = (f32x4){gv[i] * bf_lo(p01), gv[i] * bf_hi(p01), gv[i] * bf_lo(p23), gv[i] * bf_hi(p23)};
    }
    STAMP2(a.ws, cw, 0);
    bar();
    STAMP2(a.ws, cw, 2);
#pragma unroll
    for (int i = 0; i < 5; ++i) {
        const int id = tid + 256 * i, r = id / 20, c4 = id - 20 * r;
        LAS float* p = (LAS float*)(sm + P2_RED) + r * 84 + 4 * c4;
        sk[i] += (*(LAS f32x4*)p + *(LAS f32x4*)(p + 64 * 84)) + (*(LAS f32x4*)(p + 128 * 84) + *(LAS f32x4*)(p + 192 * 84));
    }
    asm volatile("" : "+v"(sk[0]), "+v"(sk[1]), "+v"(sk[2]), "+v"(sk[3]), "+v"(sk[4]));
#define OUT_AUX 16
    {
        const __amdgpu_buffer_rsrc_t ro = rsrc((const char*)a.out, 2048 * 640 * 4);
#pragma unroll
        for (int i = 0; i < 5; ++i)
            __builtin_amdgcn_raw_buffer_store_b128(__builtin_bit_cast(u32x4, sk[i]), ro, (tok[i] * 640 + n0[i]) * 4, 0, OUT_AUX);
    }
    STAMP2(a.ws, cw, 3);
}

DEV void a_loader(const Args& a, LAS char* sm, int ti, int gj, int lw, int lane) {
    STAMP(a.ws, lw + 4, 0);
    const unsigned nonce = __hip_atomic_load((GAS unsigned*)(a.ws + WS_NONCE), RLX_AGENT);
    unsigned off[12];
    {
        const int pos = lane & 7, rl = lane >> 3, im = lw & 1;
#pragma unroll
        for (int u = 0; u < 4; ++u) {
            const int R = 8 * (lw + 4 * u) + rl, rho = R & 15;
            const int tok = (R & 64) + 16 * (rho >> 2) + 4 * ((R >> 4) & 3) + (rho & 3);
            off[u] = tok * 1280 + ((pos ^ (R & 7)) << 4);
        }
#pragma unroll
        for (int u = 4; u < 9; ++u) off[u] = (8 * ((lw + 4 * (u - 4)) >> 1) + rl) * 1280 + ((pos ^ rl ^ (im << 2)) << 4);
#pragma unroll
        for (int u = 9; u < 12; ++u) {
            const int ch = 64 * (lw + 4 * (u - 9)) + lane, rho = ch / 10, cc = ch - 10 * rho;
            off[u] = (rho ^ (((rho >> 3) & 1) << 2)) * 3840 + cc * 16;
        }
    }
    const char* xb = a.x + (size_t)ti * (128 * 1280);
    const char* bb = ((lw & 1) ? a.bim : a.bre) + (size_t)gj * (80 * 1280);
    const char* qb = a.wqkv + 160 * gj;
    const bool full = lw < 2;
    auto issue_a = [&](int s) LAM {
        LAS char* dst = sm + (s % NS_A) * SLOT_A + lw * 1024;
#pragma unroll
        for (int u = 0; u < 4; ++u) glds16(xb + 128 * s + off[u], dst + 4096 * u);
#pragma unroll
        for (int u = 4; u < 6; ++u) glds16(bb + 128 * s + off[u], dst + 4096 * u);
    };
    auto issue_b = [&](int s) LAM {
        LAS char* dst = sm + (s % NS_A) * SLOT_A + lw * 1024;
#pragma unroll
        for (int u = 6; u < 9; ++u) glds16(bb + 128 * s + off[u], dst + 4096 * u);
#pragma unroll
        for (int u = 9; u < 12; ++u) if (u < 11 || full) glds16(qb + (size_t)(64 * 3840) * s + off[u], dst + 4096 * u);
    };
    STAMP(a.ws, lw + 4, 1);
    issue_a(0); issue_b(0); issue_a(1); issue_b(1);
    if (full) WAIT_VM(12); else WAIT_VM(11);
    bar();
    u64 t_vm = 0, t_free = 0;
#pragma unroll 1
    for (int s = 0; s < 8; ++s) {
        if (s > 0) TIMED(t_free, ring_wait(sm + L_FREE, s));
        issue_a(s + 2);
        TIMED(t_vm, WAIT_VM(6));
        ring_put(sm + L_FULL + 4 * lw, s + 2);
        issue_b(s + 2);
    }
    WAIT_VM(0);
    ring_put(sm + L_FULL + 4 * lw, 10);
    STAMPV(a.ws, lw + 4, 14, t_vm); STAMPV(a.ws, lw + 4, 15, t_free);
    STAMP(a.ws, lw + 4, 2);
    __builtin_amdgcn_s_setprio(2);
    const unsigned tag = mk_tag(nonce);
    const int tl = lw * 64 + lane;
    if (tl < 128) {
        GAS u64* gp = (GAS u64*)(a.ws + WS_G) + 128 * ti + tl;
        u64 v;
        for (unsigned spins = 0;;) {
            v = __hip_atomic_load(gp, RLX_AGENT);
            if (wave_all((unsigned)(v >> 32) == tag) || ++spins > SPIN_CAP) break;
            __builtin_amdgcn_s_sleep(1);
        }
        const float g = __uint_as_float((unsigned)v);
        ((LAS float*)(sm + A_G))[tl] = g;
        ((LAS float*)(sm + A_G))[128 + tl] = bfr(1.f - g);
    }
    STAMP(a.ws, lw + 4, 9);
    lds_bar();
    STAMP(a.ws, lw + 4, 3);
    if (lw < 3) {
        const int col = tl < 160 ? tl : 159;
        LAS float* tab = (LAS float*)(sm + A_TAB);
        GAS u64* ea = (GAS u64*)(a.ws + WS_EA) + gj * 160;
        const float a16 = tab[2 * 160 + col], b16 = tab[3 * 160 + col], a128 = tab[4 * 160 + col], b128 = tab[5 * 160 + col];
        LAS float* es = (LAS float*)(sm + A_ESEG);
        float pre[8], P = 0.f;
#pragma unroll
        for (int s = 0; s < 8; ++s) { pre[s] = P; P = cmul_add(a16, b16, P, es[s * 160 + col]); }
        if (tl < 160) __hip_atomic_store(ea + ti * 1280 + col, granule(tag, __float_as_uint(P)), RLX_AGENT);
        STAMP(a.ws, lw + 4, 4);
        float H = 0.f;
        if (ti > 0) {
            if (SENT_LB == 0) {
            } else if (lw == 0) {
                GAS u64* sp = ea + (lane < ti ? lane : 0) * 1280 + 159;
                for (unsigned spins = 0; spins < SPIN_CAP; ++spins) {
                    if (wave_all((unsigned)(__hip_atomic_load(sp, RLX_AGENT) >> 32) == tag)) break;
                    __builtin_amdgcn_s_sleep(SLP_LB);
                }
                word_set(sm + L_LBS, 1u);
            } else word_wait(sm + L_LBS, 1u);
            float ev[15];
            for (unsigned spins = 0; spins < SPIN_CAP; ++spins) {
                u64 v[15];
#pragma unroll
                for (int i = 0; i < 15; ++i) v[i] = __hip_atomic_load(ea + (i < ti ? i : 0) * 1280 + col, RLX_AGENT);
                bool ok = true;
#pragma unroll
                for (int i = 0; i < 15; ++i) { ev[i] = __uint_as_float((unsigned)v[i]); ok &= (unsigned)(v[i] >> 32) == tag; }
                if (wave_all(ok)) break;
                __builtin_amdgcn_s_sleep(SLP_LB);
            }
#pragma unroll
            for (int i = 0; i < 15; ++i) if (i < ti) H = cmul_add(a128, b128, H, ev[i]);
        }
        STAMP(a.ws, lw + 4, 6);
        LAS float* hs = (LAS float*)(sm + A_HSEG);
#pragma unroll
        for (int s = 0; s < 8; ++s) { if (tl < 160) hs[s * 160 + col] = H + pre[s]; H = cmul_add(a16, b16, H, 0.f); }
        wait_lgkm0();
        if (lane == 0) word_add(sm + L_HS);
    } else {
        const int i16 = lane & 15;
        GAS u64* sp = (GAS u64*)(a.ws + WS_TOT) + gj * 840 + (i16 < 8 ? 53 * i16 + 52 : 424 + 52 * (i16 - 8) + 51);
        for (unsigned spins = 0; SENT_TOT && spins < SPIN_CAP; ++spins) {
            if (wave_all((unsigned)(__hip_atomic_load(sp, RLX_AGENT) >> 32) == tag)) break;
            __builtin_amdgcn_s_sleep(SLP_TOT);
        }
        STAMP(a.ws, lw + 4, 4);
        const __amdgpu_buffer_rsrc_t rs = rsrc(a.ws + WS_TOT, 8 * 840 * 8);
        u32x4 v[7];
        for (unsigned spins = 0; spins < SPIN_CAP; ++spins) {
            bool ok = true;
#pragma unroll
            for (int k = 0; k < 7; ++k) v[k] = __builtin_amdgcn_raw_buffer_load_b128(rs, gj * 6720 + (lane + 64 * k < 420 ? lane + 64 * k : 419) * 16, 0, 16);
#pragma unroll
            for (int k = 0; k < 7; ++k) ok &= (v[k][1] == tag) & (v[k][3] == tag);
            if (wave_all(ok)) break;
            __builtin_amdgcn_s_sleep(SLP_TOT);
        }
#pragma unroll
        for (int k = 0; k < 7; ++k) if (lane + 64 * k < 420) ((LAS u32x2*)(sm + A_TOT))[lane + 64 * k] = (u32x2){v[k][0], v[k][2]};
        wait_lgkm0();
        word_set(sm + L_TOT, 1u);
        STAMP(a.ws, lw + 4, 5);
    }
    __builtin_amdgcn_s_setprio(0);
    STAMP(a.ws, lw + 4, 8);
    p2_main(a, sm, lw, lane, nonce);
}

template <int NH>
DEV void a_pass1(f32x4 (&acc)[4][5], const float (&ca)[5], const float (&cb)[5], LAS float* es) {
    float e[5];
#pragma unroll
    for (int n = 0; n < 5; ++n) e[n] = acc[2 * NH][n][0];
#pragma unroll
    for (int t = 1; t < 16; ++t) {
        const int i = (t >> 2) ^ (2 * NH), r = t & 3;
        float y[5];
        scan_step_nd(y, acc[i][0][r], acc[i][1][r], acc[i][2][r], acc[i][3][r], acc[i][4][r], e, ca, cb);
#pragma unroll
        for (int n = 0; n < 5; ++n) e[n] = y[n];
    }
#pragma unroll
    for (int n = 0; n < 5; ++n) es[16 * n] = e[n];
}
template <int NH>
DEV void a_pass2(f32x4 (&acc)[4][5], const float (&ca)[5], const float (&cb)[5], LAS float* hs, LAS float* gt, LAS char* ht) {
    float e[5];
#pragma unroll
    for (int n = 0; n < 5; ++n) e[n] = hs[16 * n];
    asm volatile("s_nop 1" : "+v"(e[0]), "+v"(e[1]), "+v"(e[2]), "+v"(e[3]), "+v"(e[4]));
#pragma unroll
    for (int m = 0; m < 4; ++m) {
        const int i = m ^ (2 * NH);
        const f32x4 g = *(LAS f32x4*)(gt + 4 * m);
#pragma unroll
        for (int r = 0; r < 4; ++r) {
            float u0 = acc[i][0][r], u1 = acc[i][1][r], u2 = acc[i][2][r], u3 = acc[i][3][r], u4 = acc[i][4][r];
            scan_step(u0, u1, u2, u3, u4, e, ca, cb);
            e[0] = u0; e[1] = u1; e[2] = u2; e[3] = u3; e[4] = u4;
            acc[i][0][r] = u0 * g[r]; acc[i][1][r] = u1 * g[r]; acc[i][2][r] = u2 * g[r]; acc[i][3][r] = u3 * g[r]; acc[i][4][r] = u4 * g[r];
        }
#pragma unroll
        for (int n = 0; n < 5; ++n)
            *(LAS u32x2*)(ht + 16 * n * HT_STRIDE + 32 * m) = (u32x2){pk2(acc[i][n][0], acc[i][n][1]), pk2(acc[i][n][2], acc[i][n][3])};
    }
}

DEV void a_consumer(const Args& a, LAS char* sm, int ti, int gj, int cw, int lane) {
    const int mh = cw >> 1, nh = cw & 1, c = lane & 15, q = lane >> 4, tid = cw * 64 + lane;
    STAMP(a.ws, cw, 0);
    if (cw == 1) warm_up(a, sm, lane);
    const unsigned nonce = __hip_atomic_load((GAS unsigned*)(a.ws + WS_NONCE), RLX_AGENT);
    const unsigned tag = mk_tag(nonce);
    if (cw >= 2) {
        const int tok = 128 * ti + 16 * gj + 8 * (cw - 2) + (lane >> 3);
        const u32x4* xr = (const u32x4*)(a.x + (size_t)tok * 1280) + (lane & 7);
        const u32x4* gr = (const u32x4*)a.gw + (lane & 7);
        u32x4 xv[10], gv[10];
#pragma unroll
        for (int i = 0; i < 10; ++i) { xv[i] = xr[8 * i]; gv[i] = gr[8 * i]; }
        float s = 0.f;
#pragma unroll
        for (int i = 0; i < 10; ++i)
#pragma unroll
            for (int k = 0; k < 4; ++k) {
                const unsigned pr = pk2(bf_lo(xv[i][k]) * bf_lo(gv[i][k]), bf_hi(xv[i][k]) * bf_hi(gv[i][k]));
                s += bf_lo(pr); s += bf_hi(pr);
            }
        s += lane_xor(s, 1); s += lane_xor(s, 2); s += lane_xor(s, 4);
        const float lg = bfr(s), g = bfr(1.f / (1.f + fexp(-lg)));
        if ((lane & 7) == 0) __hip_atomic_store((GAS u64*)(a.ws + WS_G) + tok, granule(tag, __float_as_uint(g)), RLX_AGENT);
    } else if (tid < 80) {
        float ar, ai, cr, ci;
        channel_consts(a, 80 * gj + tid, ar, ai, cr, ci);
        float pr = ar, pi = ai;
#pragma unroll
        for (int k = 0; k < 4; ++k) { const float t = pr * pr - pi * pi; pi = 2.f * pr * pi; pr = t; }
        const float r16 = pr, i16 = pi;
#pragma unroll
        for (int k = 0; k < 3; ++k) { const float t = pr * pr - pi * pi; pi = 2.f * pr * pi; pr = t; }
        LAS f32x2* tab = (LAS f32x2*)(sm + A_TAB);
        tab[0 * 80 + tid] = (f32x2){ar, ar};
        tab[1 * 80 + tid] = (f32x2){-ai, ai};
        tab[2 * 80 + tid] = (f32x2){r16, r16};
        tab[3 * 80 + tid] = (f32x2){-i16, i16};
        tab[4 * 80 + tid] = (f32x2){pr, pr};
        tab[5 * 80 + tid] = (f32x2){-pi, pi};
    }
    if (tid < 160) __hip_atomic_store((GAS u64*)(a.ws + WS_EA) + (ti * 8 + gj) * 160 + tid, (u64)0, RLX_AGENT);
    __builtin_amdgcn_s_setprio(1);
    f32x4 acc[4][5], at[5][2];
#pragma unroll
    for (int n = 0; n < 5; ++n) {
#pragma unroll
        for (int i = 0; i < 4; ++i) acc[i][n] = (f32x4){0.f, 0.f, 0.f, 0.f};
        at[n][0] = at[n][1] = (f32x4){0.f, 0.f, 0.f, 0.f};
    }
    int xlo[2], xhi[2], wa[2], wq[2];
#pragma unroll
    for (int kk = 0; kk < 2; ++kk) {
        const int sw = ((4 * kk + q) ^ (c & 7)) << 4;
        xlo[kk] = (64 * mh + 32 * nh + c) * 128 + sw;
        xhi[kk] = (64 * mh + 32 * (1 - nh) + c) * 128 + sw;
        wa[kk] = 16384 + (80 * nh + 8 * (c & 1) + (c >> 1)) * 128 + (((4 * kk + q) ^ (c >> 1) ^ ((c & 1) << 2)) << 4);
        wq[kk] = 36864 + (8 * q + 4 * (kk ^ (q & 1)) + (c >> 2)) * 160 + 8 * (c & 3);
    }
    bf16x8 xa[2][4], wb[2][5], wt[2][5];
    auto rdp = [&](int buf, LAS char* s0, int kk) LAM {
        LAS char* q0 = s0 + wq[0] + kk * 5120; LAS char* q1 = s0 + wq[1] + kk * 5120;
        xa[buf][0] = lds_frag(s0 + xlo[kk]);
        xa[buf][1] = lds_frag(s0 + xlo[kk] + 2048);
        wt[buf][0] = lds_tr_frag(q0, q1);
        xa[buf][2] = lds_frag(s0 + xhi[kk]);
        xa[buf][3] = lds_frag(s0 + xhi[kk] + 2048);
        wt[buf][1] = lds_tr_frag(q0 + 32, q1 + 32);
        wb[buf][0] = lds_frag(s0 + wa[kk]);
        wb[buf][1] = lds_frag(s0 + wa[kk] + 2048);
        wt[buf][2] = lds_tr_frag(q0 + 64, q1 + 64);
        wb[buf][2] = lds_frag(s0 + wa[kk] + 4096);
        wb[buf][3] = lds_frag(s0 + wa[kk] + 6144);
        wt[buf][3] = lds_tr_frag(q0 + 96, q1 + 96);
        wb[buf][4] = lds_frag(s0 + wa[kk] + 8192);
        wt[buf][4] = lds_tr_frag(q0 + 128, q1 + 128);
    };
    auto rd = [&](int buf, int slot, int kk) LAM { rdp(buf, sm + slot * SLOT_A, kk); };
    auto mm_head = [&](int buf) LAM {
#pragma unroll
        for (int n = 0; n < HEAD_A / 4; ++n)
#pragma unroll
            for (int i = 0; i < 4; ++i) acc[i][n] = mfma(xa[buf][i], wb[buf][n], acc[i][n]);
    };
    auto mm_tail = [&](int buf) LAM {
#pragma unroll
        for (int n = HEAD_A / 4; n < 5; ++n)
#pragma unroll
            for (int i = 0; i < 4; ++i) acc[i][n] = mfma(xa[buf][i], wb[buf][n], acc[i][n]);
#pragma unroll
        for (int n = 0; n < 5; ++n)
#pragma unroll
            for (int i = 0; i < 2; ++i) at[n][i] = mfma(wt[buf][n], xa[buf][i], at[n][i]);
    };
    if (tid < L_WORDS) ((LAS unsigned*)(sm + L_FULL))[tid] = tid < 4 ? 1u : 0u;
#pragma unroll
    for (int n = 0; n < 5; ++n)
        asm volatile("" : "+v"(acc[0][n]), "+v"(acc[1][n]), "+v"(acc[2][n]), "+v"(acc[3][n]), "+v"(at[n][0]), "+v"(at[n][1]));
    STAMP(a.ws, cw, 1);
    lds_bar();
    STAMP(a.ws, cw, 2);
    STAMPC(a.ws, cw, 12);
    rd(0, 0, 0);
    int slot = 0;
    u64 t_spin = 0, n_spin = 0;
#pragma unroll 1
    for (int s = 0; s < 10; ++s) {
        wait_lgkm0();
        SCHED();
        rd(1, slot, 1);
        u32x4 fl = *(volatile LAS u32x4*)(sm + L_FULL);
        mm_head(0); mm_tail(0);
        interleave<30, 20, IL_A>();
        SCHED();
        wait_lgkm0();
        asm volatile("" ::: "memory");
        *(volatile LAS unsigned*)(sm + L_FREE + 4 * cw) = s + 1;
        slot = slot == 2 ? 0 : slot + 1;
        const unsigned need = s < 9 ? s + 2 : 0;
        unsigned landed = __builtin_amdgcn_readfirstlane(umin4(fl));
        LAS char* s0 = sm + slot * SLOT_A;
        asm volatile("" : "+s"(landed), "+v"(s0));
        mm_head(1);
        SCHED();
        if (__builtin_expect(landed < need, 0)) { TIMED(t_spin, ring_spin(sm + L_FULL, need)); n_spin++; }
        asm volatile("" ::: "memory");
        SCHED();
        rdp(0, s0, 0);
        mm_tail(1);
        interleave<30 - HEAD_A, 19, IL_A - HEAD_A + 1>();
        SCHED();
    }
    __builtin_amdgcn_s_setprio(0);
    asm volatile("s_nop 7\n\ts_nop 7" ::: "memory");
    STAMP(a.ws, cw, 3);
    STAMPC(a.ws, cw, 13);
    STAMPV(a.ws, cw, 14, t_spin); STAMPV(a.ws, cw, 15, n_spin);
    LAS float* tab = (LAS float*)(sm + A_TAB);
    float ca[5], cb[5];
#pragma unroll
    for (int n = 0; n < 5; ++n) { const int col = 80 * nh + 16 * n + c; ca[n] = tab[col]; cb[n] = tab[160 + col]; }
    {
        LAS float* es = (LAS float*)(sm + A_ESEG) + (4 * mh + q) * 160 + 80 * nh + c;
        if (nh == 0) a_pass1<0>(acc, ca, cb, es); else a_pass1<1>(acc, ca, cb, es);
    }
    WAIT_VM(0);
    STAMP(a.ws, cw, 4);
    lds_bar();
    bf16x8 P[2][3];
#pragma unroll
    for (int i = 0; i < 2; ++i) {
#pragma unroll
        for (int t = 0; t < 5; ++t)
#pragma unroll
            for (int r = 0; r < 4; ++r) at[t][i][r] = phi(at[t][i][r]);
        P[i][0] = pack_pair(at[0][i], at[1][i]);
        P[i][1] = pack_pair(at[2][i], at[3][i]);
        P[i][2] = as_frag((u32x4){pk2(at[4][i][0], at[4][i][1]), pk2(at[4][i][2], at[4][i][3]), 0u, 0u});
    }
    int adr[21];
    {
        int dh[5], db[5], eh[5], eb[5];
#pragma unroll
        for (int k = 0; k < 5; ++k) {
            const int d0 = 16 * k + 4 * q, e0 = 16 * k + c;
            dh[k] = d0 / 20; db[k] = A_TOT + dh[k] * 840 + (d0 - 20 * dh[k]) * 2;
            eh[k] = e0 / 20; eb[k] = (e0 - 20 * eh[k]) * 40;
        }
        auto ad = [&](int k, int E) LAM { return dh[k] == eh[E] ? db[k] + eb[E] : L_ZERO; };
        adr[0] = ad(0, 0); adr[1] = ad(1, 0);
        adr[2] = ad(0, 1); adr[3] = ad(1, 1); adr[4] = ad(2, 1); adr[5] = ad(3, 1);
        adr[6] = ad(1, 2); adr[7] = ad(2, 2); adr[8] = ad(3, 2);
        adr[9] = ad(2, 3); adr[10] = ad(3, 3); adr[11] = ad(4, 3);
        adr[12] = ad(3, 4); adr[13] = ad(4, 4);
#pragma unroll
        for (int k = 0; k < 5; ++k) adr[14 + k] = dh[k] == (c & 3) ? db[k] + 800 : L_ZERO;
        adr[19] = L_ZERO; adr[20] = A_G + 4 * (128 + 64 * mh + 16 * (c >> 2) + 8 * nh + (c & 3));
    }
#pragma unroll
    for (int i = 0; i < 2; ++i) asm volatile("" : "+v"(P[i][0]), "+v"(P[i][1]), "+v"(P[i][2]));
    asm volatile("" : "+v"(adr[0]), "+v"(adr[1]), "+v"(adr[2]), "+v"(adr[3]), "+v"(adr[4]), "+v"(adr[5]), "+v"(adr[6]),
                      "+v"(adr[7]), "+v"(adr[8]), "+v"(adr[9]), "+v"(adr[10]), "+v"(adr[11]), "+v"(adr[12]), "+v"(adr[13]));
    asm volatile("" : "+v"(adr[14]), "+v"(adr[15]), "+v"(adr[16]), "+v"(adr[17]), "+v"(adr[18]), "+v"(adr[20]));
    const __amdgpu_buffer_rsrc_t ract = rsrc(a.ws + WS_ACT, 128 * TS);
    u32x4 os[2][2]; u32x2 o8[2];
    STAMP(a.ws, cw, 5);
    word_wait(sm + L_TOT, 1u);
    STAMP(a.ws, cw, 6);
    {
        auto h4 = [&](int i) LAM -> u32x2 { return *(LAS u32x2*)(sm + adr[i]); };
        auto fr2 = [&](const u32x2& lo, const u32x2& hi) LAM { return as_frag((u32x4){lo[0], lo[1], hi[0], hi[1]}); };
        const u32x2 zz = {0u, 0u};
        const bf16x8 Z0 = fr2(h4(14), h4(15)), Z1 = fr2(h4(16), h4(17)), Z2 = fr2(h4(18), zz);
        const bf16x8 F0 = fr2(h4(0), h4(1)), F1 = fr2(h4(2), h4(3)), F2 = fr2(h4(4), h4(5));
        const bf16x8 F3 = fr2(zz, h4(6)), F4 = fr2(h4(7), h4(8)), F5 = fr2(h4(9), h4(10));
        const bf16x8 F6 = fr2(h4(11), zz), F7 = fr2(zz, h4(12)), F8 = fr2(h4(13), zz);
        const float ogv[2] = {*(LAS float*)(sm + adr[20]), *(LAS float*)(sm + adr[20] + 16)};
        const f32x4 z4 = {0.f, 0.f, 0.f, 0.f};
        const int uu = gj >> 1, odd = gj & 1;
#pragma unroll
        for (int i = 0; i < 2; ++i) {
            const f32x4 dn = mfma(Z2, P[i][2], mfma(Z1, P[i][1], mfma(Z0, P[i][0], z4)));
            const f32x4 n0 = mfma(F0, P[i][0], z4);
            const f32x4 n1 = mfma(F2, P[i][1], mfma(F1, P[i][0], z4));
            const f32x4 n2 = mfma(F4, P[i][1], mfma(F3, P[i][0], z4));
            const f32x4 n3 = mfma(F6, P[i][2], mfma(F5, P[i][1], z4));
            const f32x4 n4 = mfma(F8, P[i][2], mfma(F7, P[i][1], z4));
            const float og = ogv[i];
            const float r0 = og * __builtin_amdgcn_rcpf(dn[0] + 1e-6f), r1 = og * __builtin_amdgcn_rcpf(dn[1] + 1e-6f);
            const float r2 = og * __builtin_amdgcn_rcpf(dn[2] + 1e-6f), r3 = og * __builtin_amdgcn_rcpf(dn[3] + 1e-6f);
            const float s1 = q < 1 ? r0 : r1, s2 = q < 2 ? r1 : r2, s3 = q < 3 ? r2 : r3;
            os[i][0] = (u32x4){pk2(n0[0] * r0, n0[1] * r0), pk2(n0[2] * r0, n0[3] * r0), pk2(n1[0] * s1, n1[1] * s1), pk2(n1[2] * s1, n1[3] * s1)};
            os[i][1] = (u32x4){pk2(n2[0] * s2, n2[1] * s2), pk2(n2[2] * s2, n2[3] * s2), pk2(n3[0] * s3, n3[1] * s3), pk2(n3[2] * s3, n3[3] * s3)};
            o8[i] = (u32x2){pk2(n4[0] * r3, n4[1] * r3), pk2(n4[2] * r3, n4[3] * r3)};
        }
#pragma unroll
        for (int i = 0; i < 2; ++i) {
            const unsigned ob = (8 * ti + 4 * mh + 2 * nh + i) * TS + (40 + 5 * uu) * 1024;
            st16_wt(ract, lane * 16, ob + (odd ? 3072 : 0), os[i][0]);
            st16_wt(ract, lane * 16, ob + (odd ? 4096 : 1024), os[i][1]);
            st8_wt(ract, lane * 16 + 8 * odd, ob + 2048, o8[i]);
        }
    }
    STAMP(a.ws, cw, 7);
    word_wait(sm + L_HS, 3u);
    STAMP(a.ws, cw, 8);
    {
        LAS float* hs = (LAS float*)(sm + A_HSEG) + (4 * mh + q) * 160 + 80 * nh + c;
        LAS float* gt = (LAS float*)(sm + A_G) + 64 * mh + 16 * q;
        LAS char* ht = sm + A_HT + (80 * nh + c) * HT_STRIDE + (64 * mh + 4 * q) * 2;
        if (nh == 0) a_pass2<0>(acc, ca, cb, hs, gt, ht); else a_pass2<1>(acc, ca, cb, hs, gt, ht);
    }
    signal_stores(sm, L_DONE2, a.ws + WS_FACT + ti * 256 + 128 + gj * 8, tag, lane, 4u);
    KEEP(os[0][0]); KEEP(os[0][1]); KEEP(os[1][0]); KEEP(os[1][1]);
    STAMP(a.ws, cw, 9);
    wait_lgkm0();
    if (lane == 0) word_add(sm + L_HT);
    word_wait(sm + L_HT, 4u);
    {
        const int qq = (lane & 15) >> 2, p = lane & 3;
        bf16x8 hf[10];
#pragma unroll
        for (int i = 0; i < 10; ++i) {
            const int f = cw + 4 * i, mt = f / 5, ks = f - 5 * mt;
            LAS char* b = sm + A_HT + (32 * ks + 8 * q + qq) * HT_STRIDE + 32 * mt + 8 * p;
            hf[i] = lds_tr_frag(b, b + 4 * HT_STRIDE);
        }
        SCHED();
#pragma unroll
        for (int i = 0; i < 10; ++i) {
            const int f = cw + 4 * i, mt = f / 5, ks = f - 5 * mt;
            st16_wt(ract, lane * 16, (8 * ti + mt) * TS + (5 * gj + ks) * 1024, __builtin_bit_cast(u32x4, hf[i]));
        }
    }
    wait_lgkm0();
    STAMP(a.ws, cw, 10);
    signal_stores(sm, L_DONE, a.ws + WS_FACT + ti * 256 + gj * 8, tag, lane, 4u);
    STAMP(a.ws, cw, 11);
    p2_final(a, sm, cw, lane, tag);
}

DEV void job_table(const Args& a, LAS char* tab, int jb, int lane) {
    if (jb < 400) {
        float ar, ai, cr, ci;
        channel_consts(a, 64 * (jb / 40) + lane, ar, ai, cr, ci);
        ((LAS f32x2*)tab)[lane] = (f32x2){2.f * cr, 2.f * ci};
    }
}
DEV void job_issue(const Args& a, LAS char* stg, int jb, int lane) {
    if (jb < 400) {
        const int cb = jb / 40, nt = jb - 40 * cb;
        const unsigned o = (16 * nt + (lane >> 3)) * 1280 + cb * 128 + (lane & 7) * 16;
        glds16(a.cre + o, stg); glds16(a.cre + o + 8 * 1280, stg + 1024);
        glds16(a.cim + o, stg + 2048); glds16(a.cim + o + 8 * 1280, stg + 3072);
    } else if (jb < 500) {
        const int jo = jb - 400, s5 = jo / 5, n8 = jo - 5 * s5, uu = s5 / 5, s = s5 - 5 * uu;
        const int lo = 160 * uu + (s == 0 ? 0 : s == 1 ? 32 : s == 2 ? 64 : s == 3 ? 80 : 112);
        const int hi = 160 * uu + (s == 0 ? 16 : s == 1 ? 48 : s == 2 ? 144 : s == 3 ? 96 : 128);
        const unsigned o = (lane >> 4) * 1280 + n8 * 256 + (lane & 15) * 16;
#pragma unroll
        for (int k = 0; k < 4; ++k) {
            glds16(a.wo + (lo + 4 * k) * 1280 + o, stg + 1024 * k);
            glds16(a.wo + (hi + 4 * k) * 1280 + o, stg + 4096 + 1024 * k);
        }
    }
}
DEV u32x2 lds_rd8(LAS char* p) { u32x2 v; asm volatile("ds_read_b64 %0, %1" : "=v"(v) : "v"((unsigned)(size_t)p) : "memory"); return v; }
DEV f32x4 lds_rd16(LAS char* p) { f32x4 v; asm volatile("ds_read_b128 %0, %1" : "=v"(v) : "v"((unsigned)(size_t)p) : "memory"); return v; }
DEV u32x2 lds_rdtr(LAS char* p) { u32x2 v; asm volatile("ds_read_b64_tr_b16 %0, %1" : "=v"(v) : "v"((unsigned)(size_t)p) : "memory"); return v; }
DEV void job_finish(const Args& a, LAS char* stg, LAS char* tab, int jb, int lane) {
    const int c = lane & 15, q = lane >> 4;
    const __amdgpu_buffer_rsrc_t rw = rsrc(a.ws + WS_W2T, 40 * TS);
    if (jb < 400) {
        const int cb = jb / 40, nt = jb - 40 * cb;
        u32x2 re[4], im[4]; f32x4 c01[4], c23[4];
#pragma unroll
        for (int k = 0; k < 4; ++k) {
            re[k] = lds_rd8(stg + c * 128 + 32 * k + 8 * q); im[k] = lds_rd8(stg + 2048 + c * 128 + 32 * k + 8 * q);
            c01[k] = lds_rd16(tab + (16 * k + 4 * q) * 8); c23[k] = lds_rd16(tab + (16 * k + 4 * q) * 8 + 16);
        }
        asm volatile("s_waitcnt lgkmcnt(0)" : "+v"(re[0]), "+v"(re[1]), "+v"(re[2]), "+v"(re[3]), "+v"(im[0]), "+v"(im[1]), "+v"(im[2]), "+v"(im[3]),
                     "+v"(c01[0]), "+v"(c01[1]), "+v"(c01[2]), "+v"(c01[3]), "+v"(c23[0]), "+v"(c23[1]), "+v"(c23[2]), "+v"(c23[3]) :: "memory");
        u32x4 w[4];
#pragma unroll
        for (int k = 0; k < 4; ++k) {
            const float r0 = bf_lo(re[k][0]), r1 = bf_hi(re[k][0]), r2 = bf_lo(re[k][1]), r3 = bf_hi(re[k][1]);
            const float i0 = bf_lo(im[k][0]), i1 = bf_hi(im[k][0]), i2 = bf_lo(im[k][1]), i3 = bf_hi(im[k][1]);
            w[k] = (u32x4){pk2(r0 * c01[k][0] - i0 * c01[k][1], -(r0 * c01[k][1] + i0 * c01[k][0])),
                           pk2(r1 * c01[k][2] - i1 * c01[k][3], -(r1 * c01[k][3] + i1 * c01[k][2])),
                           pk2(r2 * c23[k][0] - i2 * c23[k][1], -(r2 * c23[k][1] + i2 * c23[k][0])),
                           pk2(r3 * c23[k][2] - i3 * c23[k][3], -(r3 * c23[k][3] + i3 * c23[k][2]))};
        }
#pragma unroll
        for (int k = 0; k < 4; ++k) st16_wt(rw, lane * 16, nt * TS + (4 * cb + k) * 1024, w[k]);
    } else if (jb < 500) {
        const int jo = jb - 400, s5 = jo / 5, n8 = jo - 5 * s5;
        u32x2 lo[8], hi[8];
#pragma unroll
        for (int k = 0; k < 8; ++k) {
            LAS char* b = stg + (4 * q + (c >> 2)) * 256 + 32 * k + 8 * (c & 3);
            lo[k] = lds_rdtr(b); hi[k] = lds_rdtr(b + 4096);
        }
        asm volatile("s_waitcnt lgkmcnt(0)" : "+v"(lo[0]), "+v"(lo[1]), "+v"(lo[2]), "+v"(lo[3]), "+v"(lo[4]), "+v"(lo[5]), "+v"(lo[6]), "+v"(lo[7]),
                     "+v"(hi[0]), "+v"(hi[1]), "+v"(hi[2]), "+v"(hi[3]), "+v"(hi[4]), "+v"(hi[5]), "+v"(hi[6]), "+v"(hi[7]) :: "memory");
#pragma unroll
        for (int k = 0; k < 8; ++k)
            st16_wt(rw, lane * 16, (8 * n8 + k) * TS + (40 + s5) * 1024, (u32x4){lo[k][0], lo[k][1], hi[k][0], hi[k][1]});
    }
}

DEV void b_loader(const Args& a, LAS char* sm, int ti, int gj, int lw, int lane) {
    STAMP(a.ws, lw + 4, 0);
    const unsigned nonce = __hip_atomic_load((GAS unsigned*)(a.ws + WS_NONCE), RLX_AGENT);
    unsigned off[9];
    {
        const int pos = lane & 7, rl = lane >> 3;
#pragma unroll
        for (int u = 0; u < 4; ++u) { const int R = 8 * (lw + 4 * u) + rl; off[u] = R * 1280 + ((pos ^ (R & 7)) << 4); }
#pragma unroll
        for (int u = 4; u < 9; ++u) {
            const int wi = lw + 4 * (u - 4), sec = wi >= 10, ch = 64 * (wi - 10 * sec) + lane, rho = ch / 10, cc = ch - 10 * rho;
            off[u] = (rho ^ (((rho >> 3) & 1) << 2)) * 3840 + cc * 16 + 1280 + 1280 * sec;
        }
    }
    const char* xb = a.x + (size_t)ti * (128 * 1280);
    const char* wb = a.wqkv + 160 * gj;
    auto issue_a = [&](int s) LAM {
        LAS char* dst = sm + (s % NS_B) * SLOT_B + lw * 1024;
#pragma unroll
        for (int u = 0; u < 4; ++u) glds16(xb + 128 * s + off[u], dst + 4096 * u);
        glds16(wb + (size_t)(64 * 3840) * s + off[4], dst + 4096 * 4);
    };
    auto issue_b = [&](int s) LAM {
        LAS char* dst = sm + (s % NS_B) * SLOT_B + lw * 1024;
#pragma unroll
        for (int u = 5; u < 9; ++u) glds16(wb + (size_t)(64 * 3840) * s + off[u], dst + 4096 * u);
    };
    STAMP(a.ws, lw + 4, 1);
    const int jb = (ti * 8 + gj) * 4 + lw;
    LAS char* stg = sm + B_STG + lw * 8192;
    issue_a(0); issue_b(0);
    if (JOBMODE == 0) job_issue(a, stg, jb, lane);
    issue_a(1); issue_b(1); issue_a(2); issue_b(2);
    WAIT_VM(18);
    bar();
    if (JOBMODE == 0) job_finish(a, stg, sm + B_TAB + lw * 512, jb, lane);
#pragma unroll 1
    for (int s = 0; s < 7; ++s) {
        if (s > 0) ring_wait(sm + L_FREE, s);
        issue_a(s + 3);
        if (JOBMODE == 0 && s < 2 && jb < 400) WAIT_VM(18); else if (JOBMODE == 0 && s < 2 && jb < 500) WAIT_VM(22); else WAIT_VM(14);
        ring_put(sm + L_FULL + 4 * lw, s + 2);
        issue_b(s + 3);
    }
    WAIT_VM(9);
    ring_put(sm + L_FULL + 4 * lw, 9);
    WAIT_VM(0);
    ring_put(sm + L_FULL + 4 * lw, 10);
    const unsigned tag = mk_tag(nonce);
    if (JOBMODE == 0) signal_stores(sm, L_DONE2, a.ws + WS_FW2T + (ti * 8 + gj) * 8, tag, lane, 4u);
    STAMP(a.ws, lw + 4, 2);
    if (JOBMODE == 2) {
        ring_wait(sm + L_FREE, 8);
        job_issue(a, stg, jb, lane);
        WAIT_VM(0);
        job_finish(a, stg, sm + B_TAB + lw * 512, jb, lane);
        STAMP(a.ws, lw + 4, 6);
    }
    __builtin_amdgcn_s_setprio(2);
    const int tl = lw * 64 + lane;
    {
        int o0[2], o1[2];
#pragma unroll
        for (int it = 0; it < 2; ++it) {
            const int g = 2 * (tl + 256 * it < 420 ? tl + 256 * it : 0);
            const int h = g / 210, rem = g - 210 * h, e = rem / 10, d = 2 * (rem - 10 * e);
            const bool kv = rem < 200;
            o0[it] = kv ? B_KVT + ((20 * h + e) * 84 + 20 * h + d) * 4 : B_Z + (20 * h + 2 * (rem - 200)) * 4;
            o1[it] = o0[it] + (kv ? 80 * 84 * 4 : 320);
        }
        asm volatile("" : "+v"(o0[0]), "+v"(o0[1]), "+v"(o1[0]), "+v"(o1[1]));
        const __amdgpu_buffer_rsrc_t rk = rsrc(a.ws + WS_KV, 16 * 8 * 840 * 8);
        word_wait(sm + L_KV, 4u);
        STAMP(a.ws, lw + 4, 3);
        u32x4 pv[2];
#pragma unroll
        for (int it = 0; it < 2; ++it) {
            const f32x4 v = *(LAS f32x4*)(sm + o0[it]) + *(LAS f32x4*)(sm + o1[it]);
            pv[it] = (u32x4){pk2(v[0], v[1]), tag, pk2(v[2], v[3]), tag};
        }
        asm volatile("" : "+v"(pv[0]), "+v"(pv[1]) :: "memory");
        st16_wt(rk, tl * 16, (ti * 8 + gj) * 6720, pv[0]);
        if (tl + 256 < 420) st16_wt(rk, (tl + 256) * 16, (ti * 8 + gj) * 6720, pv[1]);
        asm volatile("s_nop 1" ::"v"(pv[0]), "v"(pv[1]));
    }
    STAMP(a.ws, lw + 4, 4);
    if (lw == 0) {
        const int cnt = ti < 8 ? 53 : 52, g0 = ti < 8 ? 53 * ti : 424 + 52 * (ti - 8);
        const int g = g0 + (lane < cnt ? lane : 0);
        GAS u64* src = (GAS u64*)(a.ws + WS_KV) + gj * 840 + g;
        unsigned v[16];
        for (unsigned spins = 0;;) {
            bool ok = true;
#pragma unroll
            for (int i = 0; i < 16; ++i) {
                const u64 x = __hip_atomic_load(src + i * (8 * 840), RLX_AGENT);
                v[i] = (unsigned)x; ok &= (unsigned)(x >> 32) == tag;
            }
            if (wave_all(ok) || ++spins > SPIN_CAP) break;
            __builtin_amdgcn_s_sleep(1);
        }
        float s0 = 0.f, s1 = 0.f;
#pragma unroll
        for (int i = 0; i < 16; ++i) { s0 += bf_lo(v[i]); s1 += bf_hi(v[i]); }
        if (lane < cnt) __hip_atomic_store((GAS u64*)(a.ws + WS_TOT) + gj * 840 + g, granule(tag, pk2(s0, s1)), RLX_AGENT);
        word_set(sm + L_H2, 1u);
    }
    STAMP(a.ws, lw + 4, 5);
    __builtin_amdgcn_s_setprio(0);
    if (JOBMODE == 2) signal_stores(sm, L_DONE2, a.ws + WS_FW2T + (ti * 8 + gj) * 8, tag, lane, 4u);
    p2_main(a, sm, lw, lane, nonce);
}

DEV void b_consumer(const Args& a, LAS char* sm, int ti, int gj, int cw, int lane) {
    const int mh = cw >> 1, nh = cw & 1, c = lane & 15, q = lane >> 4, tid = cw * 64 + lane;
    STAMP(a.ws, cw, 0);
    if (cw == 3) warm_up(a, sm, lane);
    const unsigned tag = mk_tag(__hip_atomic_load((GAS unsigned*)(a.ws + WS_NONCE), RLX_AGENT));
    if (JOBMODE != 1) job_table(a, sm + B_TAB + cw * 512, (ti * 8 + gj) * 4 + cw, lane);
    {
        const __amdgpu_buffer_rsrc_t rk = rsrc(a.ws + WS_KV, 16 * 8 * 840 * 8);
        const u32x4 z = {0u, 0u, 0u, 0u};
        st16_wt(rk, tid * 16, (ti * 8 + gj) * 6720, z);
        if (tid + 256 < 420) st16_wt(rk, (tid + 256) * 16, (ti * 8 + gj) * 6720, z);
        const int cnt = ti < 8 ? 53 : 52, g0 = ti < 8 ? 53 * ti : 424 + 52 * (ti - 8);
        if (tid < cnt) __hip_atomic_store((GAS u64*)(a.ws + WS_TOT) + gj * 840 + g0 + tid, (u64)0, RLX_AGENT);
    }
    __builtin_amdgcn_s_setprio(1);
    f32x4 an[5][4];
#pragma unroll
    for (int i = 0; i < 4; ++i)
#pragma unroll
        for (int t = 0; t < 5; ++t) an[t][i] = (f32x4){0.f, 0.f, 0.f, 0.f};
    int xo[2], wA[2], wB[2];
#pragma unroll
    for (int kk = 0; kk < 2; ++kk) {
        xo[kk] = (64 * mh + c) * 128 + (((4 * kk + q) ^ (c & 7)) << 4);
        const int b = 16384 + (8 * q + 4 * (kk ^ (q & 1)) + (c >> 2)) * 160 + 8 * (c & 3);
        wA[kk] = b + 96 * nh; wB[kk] = b + 64 + 10240 * nh;
    }
    bf16x8 xa[2][4], wb[2][5];
    auto rdp = [&](int buf, LAS char* s0, int kk) LAM {
#pragma unroll
        for (int i = 0; i < 4; ++i) xa[buf][i] = lds_frag(s0 + xo[kk] + 2048 * i);
        LAS char* w0 = s0 + kk * 5120;
        wb[buf][0] = lds_tr_frag(w0 + wA[0], w0 + wA[1]);
        wb[buf][1] = lds_tr_frag(w0 + wA[0] + 32, w0 + wA[1] + 32);
        wb[buf][2] = lds_tr_frag(w0 + wB[0], w0 + wB[1]);
        wb[buf][3] = lds_tr_frag(w0 + wA[0] + 10240, w0 + wA[1] + 10240);
        wb[buf][4] = lds_tr_frag(w0 + wA[0] + 10272, w0 + wA[1] + 10272);
    };
    auto rd = [&](int buf, int slot, int kk) LAM { rdp(buf, sm + slot * SLOT_B, kk); };
    auto mm_head = [&](int buf) LAM {
#pragma unroll
        for (int i = 0; i < 4; ++i) an[0][i] = mfma(xa[buf][i], wb[buf][0], an[0][i]);
    };
    auto mm_tail = [&](int buf) LAM {
#pragma unroll
        for (int t = 1; t < 5; ++t)
#pragma unroll
            for (int i = 0; i < 4; ++i) an[t][i] = mfma(xa[buf][i], wb[buf][t], an[t][i]);
    };
    if (tid < L_WORDS) ((LAS unsigned*)(sm + L_FULL))[tid] = tid < 4 ? 1u : 0u;
#pragma unroll
    for (int t = 0; t < 5; ++t)
        asm volatile("" : "+v"(an[t][0]), "+v"(an[t][1]), "+v"(an[t][2]), "+v"(an[t][3]));
    STAMP(a.ws, cw, 1);
    lds_bar();
    STAMP(a.ws, cw, 2);
    rd(0, 0, 0);
#pragma unroll 1
    for (int s = 0; s < 10; ++s) {
        wait_lgkm0();
        SCHED();
        rd(1, s & 3, 1);
        u32x4 fl = *(volatile LAS u32x4*)(sm + L_FULL);
        mm_head(0); mm_tail(0);
        interleave<20, 15, IL_B>();
        SCHED();
        wait_lgkm0();
        asm volatile("" ::: "memory");
        *(volatile LAS unsigned*)(sm + L_FREE + 4 * cw) = s + 1;
        const unsigned need = s < 9 ? s + 2 : 0;
        unsigned landed = __builtin_amdgcn_readfirstlane(umin4(fl));
        LAS char* s0 = sm + ((s + 1) & 3) * SLOT_B;
        asm volatile("" : "+s"(landed), "+v"(s0));
        mm_head(1);
        SCHED();
        if (__builtin_expect(landed < need, 0)) ring_spin(sm + L_FULL, need);
        asm volatile("" ::: "memory");
        SCHED();
        rdp(0, s0, 0);
        mm_tail(1);
        interleave<16, 14, 14>();
        SCHED();
    }
    STAMP(a.ws, cw, 3);
    bf16x8 fr[5][2];
#pragma unroll
    for (int t = 0; t < 2; ++t)
#pragma unroll
        for (int i = 0; i < 4; ++i)
#pragma unroll
            for (int r = 0; r < 4; ++r) an[t][i][r] = phi(an[t][i][r]);
    if (nh == 0) {
#pragma unroll
        for (int i = 0; i < 4; ++i)
#pragma unroll
            for (int r = 0; r < 4; ++r) an[2][i][r] = phi(an[2][i][r]);
    }
#pragma unroll
    for (int t = 0; t < 5; ++t) { fr[t][0] = pack_pair(an[t][0], an[t][1]); fr[t][1] = pack_pair(an[t][2], an[t][3]); }
    {
        LAS char* xs = sm + B_X1 + cw * 2048 + lane * 16;
        *(LAS bf16x8*)xs = fr[2][0];
        *(LAS bf16x8*)(xs + 1024) = fr[2][1];
    }
    wait_lgkm0();
    word_set(sm + L_X1 + 4 * cw, 1u);
    STAMP(a.ws, cw, 4);
    word_wait(sm + L_X1 + 4 * (cw ^ 1), 1u);
    ring_spin(sm + L_FREE, 10);
    asm volatile("" ::: "memory");
    {
        LAS char* xr = sm + B_X1 + (cw ^ 1) * 2048 + lane * 16;
        const bf16x8 R0 = *(LAS bf16x8*)xr, R1 = *(LAS bf16x8*)(xr + 1024);
        const bf16x8 ones = as_frag((u32x4){0x3f803f80u, 0x3f803f80u, 0x3f803f80u, 0x3f803f80u});
        const f32x4 z4 = {0.f, 0.f, 0.f, 0.f};
        LAS float* kvt = (LAS float*)(sm + B_KVT) + mh * (80 * 84) + c * 84 + 4 * q;
        LAS float* zz = (LAS float*)(sm + B_Z) + mh * 80 + 4 * q;
        auto prod = [&](const bf16x8& k0, const bf16x8& k1, const bf16x8& v0, const bf16x8& v1, int ta, int tb) LAM {
            *(LAS f32x4*)(kvt + 16 * tb * 84 + 16 * ta) = mfma(k1, v1, mfma(k0, v0, z4));
        };
        auto zsum = [&](const bf16x8& k0, const bf16x8& k1, int ta) LAM {
            const f32x4 d = mfma(k1, ones, mfma(k0, ones, z4));
            if (c == 0) *(LAS f32x4*)(zz + 16 * ta) = d;
        };
        const int b0 = 3 * nh;
        prod(fr[0][0], fr[0][1], fr[3][0], fr[3][1], b0, b0);
        prod(fr[0][0], fr[0][1], fr[4][0], fr[4][1], b0, b0 + 1);
        prod(fr[1][0], fr[1][1], fr[3][0], fr[3][1], b0 + 1, b0);
        prod(fr[1][0], fr[1][1], fr[4][0], fr[4][1], b0 + 1, b0 + 1);
        zsum(fr[0][0], fr[0][1], b0);
        zsum(fr[1][0], fr[1][1], b0 + 1);
        if (nh == 0) {
            prod(fr[2][0], fr[2][1], fr[4][0], fr[4][1], 2, 1);
            prod(fr[1][0], fr[1][1], R0, R1, 1, 2);
            prod(fr[2][0], fr[2][1], R0, R1, 2, 2);
            zsum(fr[2][0], fr[2][1], 2);
        } else {
            prod(fr[0][0], fr[0][1], fr[2][0], fr[2][1], 3, 2);
            prod(R0, R1, fr[3][0], fr[3][1], 2, 3);
        }
    }
    WAIT_VM(0);
    wait_lgkm0();
    if (lane == 0) word_add(sm + L_KV);
    __builtin_amdgcn_s_setprio(0);
    STAMP(a.ws, cw, 5);
    if (JOBMODE == 1) {
        const int jb = (ti * 8 + gj) * 4 + cw;
        LAS char* stg = sm + cw * 8192;
        job_table(a, sm + B_TAB + cw * 512, jb, lane);
        word_wait(sm + L_H2, 1u);
        job_issue(a, stg, jb, lane);
        WAIT_VM(0);
        wait_lgkm0();
        job_finish(a, stg, sm + B_TAB + cw * 512, jb, lane);
        STAMP(a.ws, cw, 6);
        signal_stores(sm, L_DONE, a.ws + WS_FW2T + (ti * 8 + gj) * 8, tag, lane, 4u);
        STAMP(a.ws, cw, 7);
    } else if (lane == 0) word_add(sm + L_DONE);
    p2_final(a, sm, cw, lane, tag);
}

}

__global__ __launch_bounds__(512, 2) NOPK void gm_fused(const void* x, const void* lre, const void* lim, const void* bre,
                                                   const void* bim, const void* cre, const void* cim, const void* logdt,
                                                   const void* dskip, const void* wqkv, const void* wo, const void* gw,
                                                   void* ws, void* out) {
    __shared__ __attribute__((aligned(1024))) char smem[LDS_BYTES];
    LAS char* sm = (LAS char*)smem;
    u64 pc0;
    asm volatile("s_getpc_b64 %0" : "=s"(pc0));
    const char* code = (const char*)(pc0 & ~127ull);
#define MAPMODE 1
    const int b = blockIdx.x, xcd = b & 7, idx = b >> 3;
    const bool roleA = MAPMODE == 1 ? idx < 16 : xcd < 4;
    const int ti = MAPMODE == 1 ? 4 * (xcd >> 1) + (idx & 3) : 8 * ((xcd & 3) >> 1) + (idx & 7);
    const int gj = MAPMODE == 1 ? 4 * (xcd & 1) + ((idx >> 2) & 3) : 4 * (xcd & 1) + (idx >> 3);
    const int wave = __builtin_amdgcn_readfirstlane(threadIdx.x >> 6), lane = threadIdx.x & 63;
    const Args a{(const char*)x, (const u16*)lre, (const u16*)lim, (const char*)bre, (const char*)bim, (const char*)cre,
                 (const char*)cim, (const u16*)logdt, (const char*)wqkv, (const char*)wo, (const u16*)gw,
                 (const u16*)dskip, (char*)ws, (float*)out, code};
    STAMPL(ws, 0);
    if (roleA) {
        if (wave < 4) a_consumer(a, sm, ti, gj, wave, lane);
        else a_loader(a, sm, ti, gj, wave - 4, lane);
    } else {
        if (wave < 4) b_consumer(a, sm, ti, gj, wave, lane);
        else b_loader(a, sm, ti, gj, wave - 4, lane);
    }
    STAMPL(ws, 1);
}

extern "C" void kernel_launch(void* const* d_in, const int* in_sizes, int n_in, void* d_out, int out_size, void* d_ws,
                              size_t ws_size, hipStream_t stream) {
    gm_fused<<<256, 512, 0, stream>>>(d_in[0], d_in[1], d_in[2], d_in[3], d_in[4], d_in[5], d_in[6], d_in[7], d_in[8],
                                      d_in[10], d_in[11], d_in[12], d_ws, d_out);
}
```

```cpp
#include <hip/hip_runtime.h>
#include <hip/hip_bf16.h>
#include <stdint.h>

namespace {

typedef __attribute__((ext_vector_type(8))) short bf16x8;
typedef __attribute__((ext_vector_type(4))) short s16x4;
typedef __attribute__((ext_vector_type(4))) float f32x4;
typedef __attribute__((ext_vector_type(2))) float f32x2;
typedef __attribute__((ext_vector_type(2))) __bf16 bf16x2_t;
typedef __attribute__((ext_vector_type(4))) unsigned u32x4;
typedef __attribute__((ext_vector_type(2))) unsigned u32x2;
typedef unsigned long long u64;
typedef unsigned short u16;
#define LAS __attribute__((address_space(3)))
#define GAS __attribute__((address_space(1)))
#if defined(__HIP_DEVICE_COMPILE__)
#define NOPK __attribute__((target("no-packed-fp32-ops")))
#else
#define NOPK
#endif
#define DEV __device__ __forceinline__
#define LAM __attribute__((always_inline))
#define RLX_AGENT __ATOMIC_RELAXED, __HIP_MEMORY_SCOPE_AGENT

constexpr size_t WS_NONCE = 0;
constexpr size_t WS_EA    = 4096;
constexpr size_t WS_KV    = WS_EA + 16 * 8 * 160 * 8;
constexpr size_t WS_TOT   = WS_KV + 16 * 8 * 840 * 8;
constexpr size_t WS_G     = WS_TOT + 8 * 840 * 8;
constexpr size_t WS_FACT  = WS_G + 2048 * 8;
constexpr size_t WS_FW2T  = WS_FACT + 16 * 256;
constexpr size_t WS_W2T   = (WS_FW2T + 128 * 8 + 1023) / 1024 * 1024;
constexpr size_t TS = 61440;
constexpr size_t WS_ACT   = WS_W2T + 40 * TS;
constexpr size_t WS_PROF  = 32u << 20;

#define STAMP(ws, wave, i) do {} while (0)
#define STAMPC(ws, wave, i) do {} while (0)
#define TIMED(acc, stmt) do { stmt; } while (0)
#define STAMPV(ws, wave, i, v) do {} while (0)
#define STAMP2(ws, wave, i) do {} while (0)

#define STAMPL(ws, i) do {} while (0)

constexpr int LDS_BYTES = 163840;
constexpr int SLOT_A = 47104, NS_A = 3;
constexpr int A_TAB  = 141312;
constexpr int A_G    = A_TAB + 3840;
constexpr int A_ESEG = A_G + 1024;
constexpr int A_HSEG = A_ESEG + 5120;
constexpr int A_TOT  = A_HSEG + 5120;
constexpr int A_HT = 47104, HT_STRIDE = 272;
static_assert(A_TOT + 3360 <= 159776, "lds");
constexpr int SLOT_B = 36864, NS_B = 4;
constexpr int B_X1  = 147456;
constexpr int B_KVT = 0;
constexpr int B_Z   = B_KVT + 2 * 80 * 84 * 4;
constexpr int B_TAB = 155648;
constexpr int B_STG = 110592;
static_assert(B_Z + 640 <= B_X1, "lds");

#define JOBMODE 2
#define SLP_W2T 16
#define SLP_ACT 4
#define SENT_LB 0
#define SENT_TOT 0
#define SLP_LB 2
#define SLP_TOT 2
#define HEAD_A 4
#define IL_A 25
#define IL_B 16
constexpr unsigned SPIN_CAP = 1u << 18;

DEV unsigned pk2(float a, float b) {
    return __builtin_bit_cast(unsigned, __builtin_convertvector((f32x2){a, b}, bf16x2_t));
}
DEV float bfr(float a) { return __uint_as_float(pk2(a, 0.f) << 16); }
DEV float bf_lo(unsigned u) { return __uint_as_float(u << 16); }
DEV float bf_hi(unsigned u) { return __uint_as_float(u & 0xffff0000u); }
DEV float bf2f(u16 u) { return __uint_as_float((unsigned)u << 16); }
DEV float swp(float x) {
    return __builtin_bit_cast(float, __builtin_amdgcn_mov_dpp(__builtin_bit_cast(int, x), 0xB1, 0xF, 0xF, true));
}
DEV float cmul_add(float a, float bs, float e, float u) { return fmaf(bs, swp(e), fmaf(a, e, u)); }
DEV f32x4 mfma(bf16x8 a, bf16x8 b, f32x4 c) { return __builtin_amdgcn_mfma_f32_16x16x32_bf16(a, b, c, 0, 0, 0); }
DEV bf16x8 as_frag(u32x4 v) { return __builtin_bit_cast(bf16x8, v); }
DEV bf16x8 lds_frag(LAS char* p) { return *(LAS bf16x8*)p; }
DEV bf16x8 lds_tr_frag(LAS char* p0, LAS char* p1) {
    s16x4 lo = __builtin_amdgcn_ds_read_tr16_b64_v4i16((LAS s16x4*)p0);
    s16x4 hi = __builtin_amdgcn_ds_read_tr16_b64_v4i16((LAS s16x4*)p1);
    return __builtin_shufflevector(lo, hi, 0, 1, 2, 3, 4, 5, 6, 7);
}
DEV void glds16_wt(const char* g, unsigned l) {
    unsigned keep;
    asm volatile("s_mov_b32 %0, m0\n\ts_mov_b32 m0, %2\n\ts_nop 0\n\tglobal_load_lds_dwordx4 %1, off sc1\n\ts_mov_b32 m0, %0"
                 : "=&s"(keep) : "v"(g), "s"(l) : "memory");
}
#define AUX_X 0
#define AUX_W 0
#define AUX_J 0
template <int AUX> DEV void glds16a(const char* g, LAS char* l) {
    __builtin_amdgcn_global_load_lds((const GAS unsigned*)g, (LAS unsigned*)l, 16, 0, AUX);
}
DEV void glds16x(const char* g, LAS char* l) { glds16a<AUX_X>(g, l); }
DEV void glds16w(const char* g, LAS char* l) { glds16a<AUX_W>(g, l); }
DEV void glds16(const char* g, LAS char* l) { glds16a<AUX_J>(g, l); }
DEV void bar() { asm volatile("" ::: "memory"); __builtin_amdgcn_s_barrier(); asm volatile("" ::: "memory"); }
DEV void wait_lgkm0() { __builtin_amdgcn_s_waitcnt(0xC07F); }
DEV void lds_bar() { asm volatile("" ::: "memory"); wait_lgkm0(); bar(); }
constexpr int L_FULL = 159776, L_FREE = L_FULL + 16;
constexpr int L_DONE = L_FULL + 32;
constexpr int L_KV = L_FULL + 36, L_W2T = L_FULL + 40, L_ACT = L_FULL + 44;
constexpr int L_X1 = L_FULL + 48, L_ZERO = L_FULL + 64;
constexpr int L_DONE2 = L_FULL + 72, L_ACT2 = L_FULL + 76;
constexpr int L_H2 = L_FULL + 80;
constexpr int L_WR = L_FULL + 84;
constexpr int L_TOT = L_FULL + 88, L_LBS = L_FULL + 92;
constexpr int L_HS = L_FULL + 96, L_HT = L_FULL + 100;
constexpr int L_WORDS = 26;
DEV unsigned umin4(const u32x4& f) { const unsigned a = f[0] < f[1] ? f[0] : f[1], b = f[2] < f[3] ? f[2] : f[3]; return a < b ? a : b; }
DEV void ring_put(LAS char* p, unsigned v) {
    asm volatile("ds_write_b32 %0, %1" ::"v"((unsigned)(size_t)p), "v"(v) : "memory");
}
DEV void ring_wait(LAS char* p, unsigned need) {
    for (unsigned spins = 0; spins < SPIN_CAP; ++spins) {
        u32x4 f;
        asm volatile("ds_read_b128 %0, %1\n\ts_waitcnt lgkmcnt(0)" : "=&v"(f) : "v"((unsigned)(size_t)p) : "memory");
        if ((unsigned)__builtin_amdgcn_readfirstlane(umin4(f)) >= need) break;
        __builtin_amdgcn_s_sleep(1);
    }
}
DEV void word_wait(LAS char* p, unsigned need) {
    for (unsigned spins = 0; spins < SPIN_CAP; ++spins) {
        if ((unsigned)__builtin_amdgcn_readfirstlane(*(volatile LAS unsigned*)p) >= need) break;
        __builtin_amdgcn_s_sleep(1);
    }
    asm volatile("" ::: "memory");
}
DEV void word_set(LAS char* p, unsigned v) { asm volatile("" ::: "memory"); *(volatile LAS unsigned*)p = v; }
DEV unsigned word_add(LAS char* p) {
    return __hip_atomic_fetch_add((LAS unsigned*)p, 1u, __ATOMIC_RELAXED, __HIP_MEMORY_SCOPE_WORKGROUP);
}
DEV void ring_spin(LAS char* p, unsigned need) {
    for (unsigned spins = 0; spins < SPIN_CAP; ++spins) {
        const u32x4 f = *(volatile LAS u32x4*)p;
        if ((unsigned)__builtin_amdgcn_readfirstlane(umin4(f)) >= need) break;
    }
}
#define WAIT_VM(n) asm volatile("s_waitcnt vmcnt(" #n ")" ::: "memory")
#define SCHED() __builtin_amdgcn_sched_barrier(0)
#define KEEP(x) asm volatile("" ::"v"(x))
template <int NM, int NR, int NS> DEV void interleave() {
#pragma unroll
    for (int i = 0; i < NS; ++i) {
        __builtin_amdgcn_sched_group_barrier(0x8, 1, 0);
        if ((i + 1) * NR / NS != i * NR / NS) __builtin_amdgcn_sched_group_barrier(0x100, 1, 0);
    }
    __builtin_amdgcn_sched_group_barrier(0x8, NM - NS, 0);
}
DEV bool wave_all(bool p) { return __builtin_amdgcn_ballot_w64(p) == __builtin_amdgcn_ballot_w64(true); }
DEV float lane_xor(float v, int m) {
    const int x = __builtin_bit_cast(int, v);
    const int y = m == 1 ? __builtin_amdgcn_mov_dpp(x, 0xB1, 0xF, 0xF, true) : m == 2 ? __builtin_amdgcn_mov_dpp(x, 0x4E, 0xF, 0xF, true)
                                                                              : __builtin_amdgcn_ds_swizzle(x, 0x101F);
    return __builtin_bit_cast(float, y);
}
DEV float fexp(float x) { return __builtin_amdgcn_exp2f(x * 1.44269504088896341f); }
DEV unsigned mk_tag(unsigned n) { return ((n + 1u) * 0x9E3779B1u) | 1u; }
DEV u64 granule(unsigned tag, unsigned v) { return ((u64)tag << 32) | v; }
DEV u64 flag_of(unsigned tag) { return granule(tag, tag * 0x85EBCA6Bu + 0x5BD1E995u); }
DEV __amdgpu_buffer_rsrc_t rsrc(const char* p, unsigned bytes) { return __builtin_amdgcn_make_buffer_rsrc((void*)p, 0, bytes, 0x00020000); }
DEV void st16_wt(const __amdgpu_buffer_rsrc_t& r, unsigned voff, unsigned soff, u32x4 v) { __builtin_amdgcn_raw_buffer_store_b128(v, r, voff + soff, 0, 16); }
DEV void st8_wt(const __amdgpu_buffer_rsrc_t& r, unsigned voff, unsigned soff, u32x2 v) { __builtin_amdgcn_raw_buffer_store_b64(v, r, voff + soff, 0, 16); }
DEV bf16x8 ld16_wt(const __amdgpu_buffer_rsrc_t& r, unsigned voff, unsigned soff) {
    return __builtin_bit_cast(bf16x8, __builtin_amdgcn_raw_buffer_load_b128(r, voff, soff, 16));
}
DEV void signal_stores(LAS char* sm, int word, char* flag, unsigned tag, int lane, unsigned waves) {
    asm volatile("s_waitcnt vmcnt(0)" ::: "memory");
    if (lane == 0) {
        const unsigned old = __hip_atomic_fetch_add((LAS unsigned*)(sm + word), 1u, __ATOMIC_RELAXED, __HIP_MEMORY_SCOPE_WORKGROUP);
        if (old == waves - 1u) __hip_atomic_store((GAS u64*)flag, flag_of(tag), RLX_AGENT);
    }
}
DEV float phi(float t) {
    const float inf = __builtin_inff();
    const float e = __builtin_amdgcn_exp2f(__builtin_amdgcn_fmed3f(t * 1.44269504088896341f, -inf, 0.f));
    return (e + 1e-6f) + __builtin_amdgcn_fmed3f(t, 0.f, inf);
}
DEV bf16x8 pack_pair(const f32x4& lo, const f32x4& hi) {
    return as_frag((u32x4){pk2(lo[0], lo[1]), pk2(lo[2], lo[3]), pk2(hi[0], hi[1]), pk2(hi[2], hi[3])});
}

DEV void scan_step(float& u0, float& u1, float& u2, float& u3, float& u4, const float (&e)[5], const float (&a)[5],
                   const float (&b)[5]) {
    asm("v_fmac_f32 %0, %10, %5\n\tv_fmac_f32 %1, %11, %6\n\tv_fmac_f32 %2, %12, %7\n\t"
        "v_fmac_f32 %3, %13, %8\n\tv_fmac_f32 %4, %14, %9\n\t"
        "v_fmac_f32_dpp %0, %5, %15 quad_perm:[1,0,3,2] row_mask:0xf bank_mask:0xf bound_ctrl:1\n\t"
        "v_fmac_f32_dpp %1, %6, %16 quad_perm:[1,0,3,2] row_mask:0xf bank_mask:0xf bound_ctrl:1\n\t"
        "v_fmac_f32_dpp %2, %7, %17 quad_perm:[1,0,3,2] row_mask:0xf bank_mask:0xf bound_ctrl:1\n\t"
        "v_fmac_f32_dpp %3, %8, %18 quad_perm:[1,0,3,2] row_mask:0xf bank_mask:0xf bound_ctrl:1\n\t"
        "v_fmac_f32_dpp %4, %9, %19 quad_perm:[1,0,3,2] row_mask:0xf bank_mask:0xf bound_ctrl:1"
        : "+v"(u0), "+v"(u1), "+v"(u2), "+v"(u3), "+v"(u4)
        : "v"(e[0]), "v"(e[1]), "v"(e[2]), "v"(e[3]), "v"(e[4]), "v"(a[0]), "v"(a[1]), "v"(a[2]), "v"(a[3]), "v"(a[4]),
          "v"(b[0]), "v"(b[1]), "v"(b[2]), "v"(b[3]), "v"(b[4]));
}

DEV void scan_step_nd(float (&t)[5], float u0, float u1, float u2, float u3, float u4, const float (&e)[5],
                      const float (&a)[5], const float (&b)[5]) {
    asm("v_fma_f32 %0, %15, %10, %5\n\tv_fma_f32 %1, %16, %11, %6\n\tv_fma_f32 %2, %17, %12, %7\n\t"
        "v_fma_f32 %3, %18, %13, %8\n\tv_fma_f32 %4, %19, %14, %9\n\t"
        "v_fmac_f32_dpp %0, %10, %20 quad_perm:[1,0,3,2] row_mask:0xf bank_mask:0xf bound_ctrl:1\n\t"
        "v_fmac_f32_dpp %1, %11, %21 quad_perm:[1,0,3,2] row_mask:0xf bank_mask:0xf bound_ctrl:1\n\t"
        "v_fmac_f32_dpp %2, %12, %22 quad_perm:[1,0,3,2] row_mask:0xf bank_mask:0xf bound_ctrl:1\n\t"
        "v_fmac_f32_dpp %3, %13, %23 quad_perm:[1,0,3,2] row_mask:0xf bank_mask:0xf bound_ctrl:1\n\t"
        "v_fmac_f32_dpp %4, %14, %24 quad_perm:[1,0,3,2] row_mask:0xf bank_mask:0xf bound_ctrl:1"
        : "=&v"(t[0]), "=&v"(t[1]), "=&v"(t[2]), "=&v"(t[3]), "=&v"(t[4])
        : "v"(u0), "v"(u1), "v"(u2), "v"(u3), "v"(u4), "v"(e[0]), "v"(e[1]), "v"(e[2]), "v"(e[3]), "v"(e[4]),
          "v"(a[0]), "v"(a[1]), "v"(a[2]), "v"(a[3]), "v"(a[4]), "v"(b[0]), "v"(b[1]), "v"(b[2]), "v"(b[3]), "v"(b[4]));
}

struct Args {
    const char* x; const u16* lre; const u16* lim; const char* bre; const char* bim; const char* cre; const char* cim;
    const u16* logdt; const char* wqkv; const char* wo; const u16* gw; const u16* dskip; char* ws; float* out;
    const char* code;
};

DEV void channel_consts(const Args& a, int p, float& ar, float& ai, float& cr, float& ci) {
    const float lr = bf2f(a.lre[p]), li = bf2f(a.lim[p]);
    const float dt = bfr(fexp(bf2f(a.logdt[p])));
    const float zr = dt * lr, zi = dt * li, ex = fexp(zr);
    const float rv = zi * 0.15915494309189535f, rr = rv - __builtin_rintf(rv);
    ar = ex * __builtin_amdgcn_cosf(rr); ai = ex * __builtin_amdgcn_sinf(rr);
    const float nr = ar - 1.f, ni = ai, dn = lr * lr + li * li;
    cr = (nr * lr + ni * li) / dn; ci = (ni * lr - nr * li) / dn;
}

constexpr int L_JUNK = 160000;
DEV void touch(const char* p, LAS char* sm) {
    unsigned keep;
    asm volatile("s_mov_b32 %0, m0\n\ts_mov_b32 m0, %2\n\ts_nop 0\n\tglobal_load_lds_dword %1, off\n\ts_mov_b32 m0, %0"
                 : "=&s"(keep) : "v"(p), "s"((unsigned)(size_t)(sm + L_JUNK)) : "memory");
}
#define WARM_TLB 0
#define WARM_CODE 0
#define CODE_BYTES 65536
DEV void warm_up(const Args& a, LAS char* sm, int lane) {
    const int idx = blockIdx.x >> 3;
    if (WARM_TLB) {
        const int e = (idx * 10 + lane) % 298;
        const char* p = e < 176 ? a.ws + (size_t)e * 65536
                      : e < 256 ? (const char*)a.out + (size_t)(e - 176) * 65536
                      : e < 269 ? a.cre + (size_t)(e - 256) * 65536
                      : e < 282 ? a.cim + (size_t)(e - 269) * 65536
                      : e < 295 ? a.wo + (size_t)(e - 282) * 65536
                      : e == 295 ? (const char*)a.dskip : e == 296 ? (const char*)a.gw : (const char*)a.logdt;
        touch(p, sm);
    }
    if (WARM_CODE) {
        const char* pc = a.code;
        const int l = idx * 16 + (lane & 15);
        if (lane < 16 && l * 128 < CODE_BYTES) touch(pc + l * 128, sm);
    }
}

constexpr int P2_RED = 0;
#define P2_PW 4
#define P2_PA 5
DEV void p2_main(const Args& a, LAS char* sm, int lw, int lane, unsigned nonce) {
    const int b = blockIdx.x, xcd = b & 7, idx = b >> 3;
    const int tt = (xcd >> 1) * 8 + (idx & 7), cg = (xcd & 1) * 4 + (idx >> 3);
    const int c = lane & 15, q = lane >> 4;
    const unsigned tag = mk_tag(nonce);
    const u64 fl = flag_of(tag);
    constexpr int PW = P2_PW, PA = P2_PA;
    if (lw == 3) {
        const __amdgpu_buffer_rsrc_t rf = rsrc(a.ws + WS_FW2T, 128 * 8);
        const unsigned f0 = (unsigned)fl, f1 = (unsigned)(fl >> 32);
        for (unsigned spins = 0; spins < SPIN_CAP; ++spins) {
            const u32x4 v = __builtin_amdgcn_raw_buffer_load_b128(rf, lane * 16, 0, 16);
            if (wave_all((v[0] == f0) & (v[1] == f1) & (v[2] == f0) & (v[3] == f1))) break;
            __builtin_amdgcn_s_sleep(SLP_W2T);
        }
        word_set(sm + L_W2T, 1u);
    } else word_wait(sm + L_W2T, 1u);
    STAMP2(a.ws, lw + 4, 0);
    const __amdgpu_buffer_rsrc_t rw = rsrc(a.ws + WS_W2T, 40 * TS), ra = rsrc(a.ws + WS_ACT, 128 * TS);
    const unsigned wbase = 5 * cg * TS, abase = 4 * tt * TS, vo = lane * 16;
    const unsigned ko = (40 + 5 * lw) * 1024, kh = (10 * lw - 5) * 1024;
    auto koff = [&](int st) LAM { return (st < 5 ? ko : kh) + st * 1024; };
    bf16x8 fw[PW][5], fa[PA][4];
    auto ldw = [&](int st, int sl) LAM {
#pragma unroll
        for (int n = 0; n < 5; ++n) fw[sl][n] = ld16_wt(rw, vo, wbase + n * TS + koff(st));
    };
    auto lda = [&](int st) LAM {
#pragma unroll
        for (int t = 0; t < 4; ++t) fa[st % PA][t] = ld16_wt(ra, vo, abase + t * TS + koff(st));
    };
    auto wait_act = [&](int part, int word) LAM {
        if (lw == 3) {
            GAS u64* fp = (GAS u64*)(a.ws + WS_FACT) + (tt >> 1) * 32 + part * 16 + (lane & 7);
            for (unsigned spins = 0; spins < SPIN_CAP; ++spins) {
                const u64 v = __hip_atomic_load(fp, RLX_AGENT);
                if (wave_all(v == fl)) break;
                __builtin_amdgcn_s_sleep(SLP_ACT);
            }
            word_set(sm + word, 1u);
        } else word_wait(sm + word, 1u);
    };
    auto unit = [&](int i) LAM { const int u = 4 * lw + i; return u < 9 ? 5120 * u : 90624 + 5120 * (u - 9); };
    {
        const char* src = a.ws + WS_W2T + wbase + (10 * lw + 4) * 1024 + vo;
#pragma unroll
        for (int i = 0; i < 4; ++i)
#pragma unroll
            for (int n = 0; n < 5; ++n) glds16_wt(src + (size_t)n * TS + i * 1024, (unsigned)(size_t)(sm + unit(i) + n * 1024));
    }
#pragma unroll
    for (int st = 0; st < PW; ++st) ldw(st, st);
    SCHED();
    WAIT_VM(0);
    wait_act(1, L_ACT2);
    STAMP2(a.ws, lw + 4, 1);
    f32x4 acc[5][4];
#pragma unroll
    for (int n = 0; n < 5; ++n)
#pragma unroll
        for (int t = 0; t < 4; ++t) acc[n][t] = (f32x4){0.f, 0.f, 0.f, 0.f};
    auto slot = [&](int st) LAM { return st < 9 ? st % 4 : st == 13 ? 1 : st == 14 ? 2 : (st & 1) ? 3 : 0; };
    auto step = [&](int st) LAM {
        SCHED();
        if (st >= 8 && st < 12) {
#pragma unroll
            for (int n = 0; n < 5; ++n) fw[slot(st + 1)][n] = lds_frag(sm + unit(st - 8) + vo + n * 1024);
        }
#pragma unroll
        for (int n = 0; n < 5; ++n)
#pragma unroll
            for (int t = 0; t < 4; ++t) acc[n][t] = mfma(fw[slot(st)][n], fa[st % PA][t], acc[n][t]);
        SCHED();
        if (st < 5) ldw(st + 4, slot(st + 4));
        if (st == 9) ldw(13, slot(13));
        if (st == 10) ldw(14, slot(14));
        if (st == 11) { wait_lgkm0(); if (lane == 0) word_add(sm + L_WR); }
    };
    static_assert(PA == 5 && PW == 4, "ring layout");
#pragma unroll
    for (int st = 0; st < 5; ++st) lda(st);
#pragma unroll
    for (int st = 0; st < 5; ++st) step(st);
    STAMP2(a.ws, lw + 4, 4);
    wait_act(0, L_ACT);
    STAMP2(a.ws, lw + 4, 5);
    if (tt == 31 && cg == 0 && lw == 3 && lane == 0) __hip_atomic_store((GAS unsigned*)(a.ws + WS_NONCE), nonce + 1u, RLX_AGENT);
#pragma unroll
    for (int st = 5; st < 9; ++st) lda(st);
#pragma unroll
    for (int st = 5; st < 15; ++st) {
        if (st + 4 < 15) lda(st + 4);
        step(st);
    }
    STAMP2(a.ws, lw + 4, 2);
    word_wait(sm + L_WR, 4u);
    word_wait(sm + L_DONE, 4u);
    LAS float* rw_ = (LAS float*)(sm + P2_RED) + lw * (64 * 84);
#pragma unroll
    for (int t = 0; t < 4; ++t)
#pragma unroll
        for (int n = 0; n < 5; ++n) *(LAS f32x4*)(rw_ + (16 * t + c) * 84 + 16 * n + 4 * q) = acc[n][t];
    lds_bar();
    STAMP2(a.ws, lw + 4, 3);
}

DEV void p2_final(const Args& a, LAS char* sm, int cw, int lane, unsigned tag) {
    const int b = blockIdx.x, xcd = b & 7, idx = b >> 3;
    const int tt = (xcd >> 1) * 8 + (idx & 7), cg = (xcd & 1) * 4 + (idx >> 3);
    const int tid = cw * 64 + lane;
    u32x2 xv[5], dv[5]; float gv[5];
    int tok[5], n0[5];
#pragma unroll
    for (int i = 0; i < 5; ++i) {
        const int id = tid + 256 * i, r = id / 20, c4 = id - 20 * r;
        tok[i] = 64 * tt + 16 * ((r & 15) >> 2) + 4 * (r >> 4) + (r & 3); n0[i] = 80 * cg + 4 * c4;
        xv[i] = *(const u32x2*)((const u16*)a.x + tok[i] * 640 + n0[i]);
        dv[i] = *(const u32x2*)(a.dskip + n0[i]);
    }
    for (unsigned spins = 0; spins < SPIN_CAP; ++spins) {
        u64 v[5];
#pragma unroll
        for (int i = 0; i < 5; ++i) v[i] = __hip_atomic_load((GAS u64*)(a.ws + WS_G) + tok[i], RLX_AGENT);
        bool ok = true;
#pragma unroll
        for (int i = 0; i < 5; ++i) { gv[i] = __uint_as_float((unsigned)v[i]); ok &= (unsigned)(v[i] >> 32) == tag; }
        if (wave_all(ok)) break;
        __builtin_amdgcn_s_sleep(2);
    }
    f32x4 sk[5];
#pragma unroll
    for (int i = 0; i < 5; ++i) {
        const unsigned p01 = pk2(bf_lo(xv[i][0]) * bf_lo(dv[i][0]), bf_hi(xv[i][0]) * bf_hi(dv[i][0]));
        const unsigned p23 = pk2(bf_lo(xv[i][1]) * bf_lo(dv[i][1]), bf_hi(xv[i][1]) * bf_hi(dv[i][1]));
        sk[i] = (f32x4){gv[i] * bf_lo(p01), gv[i] * bf_hi(p01), gv[i] * bf_lo(p23), gv[i] * bf_hi(p23)};
    }
    STAMP2(a.ws, cw, 0);
    bar();
    STAMP2(a.ws, cw, 2);
#pragma unroll
    for (int i = 0; i < 5; ++i) {
        const int id = tid + 256 * i, r = id / 20, c4 = id - 20 * r;
        LAS float* p = (LAS float*)(sm + P2_RED) + r * 84 + 4 * c4;
        sk[i] += (*(LAS f32x4*)p + *(LAS f32x4*)(p + 64 * 84)) + (*(LAS f32x4*)(p + 128 * 84) + *(LAS f32x4*)(p + 192 * 84));
    }
    asm volatile("" : "+v"(sk[0]), "+v"(sk[1]), "+v"(sk[2]), "+v"(sk[3]), "+v"(sk[4]));
#define OUT_AUX 16
    {
        const __amdgpu_buffer_rsrc_t ro = rsrc((const char*)a.out, 2048 * 640 * 4);
#pragma unroll
        for (int i = 0; i < 5; ++i)
            __builtin_amdgcn_raw_buffer_store_b128(__builtin_bit_cast(u32x4, sk[i]), ro, (tok[i] * 640 + n0[i]) * 4, 0, OUT_AUX);
    }
    STAMP2(a.ws, cw, 3);
}

DEV void a_loader(const Args& a, LAS char* sm, int ti, int gj, int lw, int lane) {
    STAMP(a.ws, lw + 4, 0);
    const unsigned nonce = __hip_atomic_load((GAS unsigned*)(a.ws + WS_NONCE), RLX_AGENT);
    unsigned off[12];
    {
        const int pos = lane & 7, rl = lane >> 3, im = lw & 1;
#pragma unroll
        for (int u = 0; u < 4; ++u) {
            const int R = 8 * (lw + 4 * u) + rl, rho = R & 15;
            const int tok = (R & 64) + 16 * (rho >> 2) + 4 * ((R >> 4) & 3) + (rho & 3);
            off[u] = tok * 1280 + ((pos ^ (R & 7)) << 4);
        }
#pragma unroll
        for (int u = 4; u < 9; ++u) off[u] = (8 * ((lw + 4 * (u - 4)) >> 1) + rl) * 1280 + ((pos ^ rl ^ (im << 2)) << 4);
#pragma unroll
        for (int u = 9; u < 12; ++u) {
            const int ch = 64 * (lw + 4 * (u - 9)) + lane, rho = ch / 10, cc = ch - 10 * rho;
            off[u] = (rho ^ (((rho >> 3) & 1) << 2)) * 3840 + cc * 16;
        }
    }
    const char* xb = a.x + (size_t)ti * (128 * 1280);
    const char* bb = ((lw & 1) ? a.bim : a.bre) + (size_t)gj * (80 * 1280);
    const char* qb = a.wqkv + 160 * gj;
    const bool full = lw < 2;
    auto issue_a = [&](int s) LAM {
        LAS char* dst = sm + (s % NS_A) * SLOT_A + lw * 1024;
#pragma unroll
        for (int u = 0; u < 4; ++u) glds16x(xb + 128 * s + off[u], dst + 4096 * u);
#pragma unroll
        for (int u = 4; u < 6; ++u) glds16w(bb + 128 * s + off[u], dst + 4096 * u);
    };
    auto issue_b = [&](int s) LAM {
        LAS char* dst = sm + (s % NS_A) * SLOT_A + lw * 1024;
#pragma unroll
        for (int u = 6; u < 9; ++u) glds16w(bb + 128 * s + off[u], dst + 4096 * u);
#pragma unroll
        for (int u = 9; u < 12; ++u) if (u < 11 || full) glds16w(qb + (size_t)(64 * 3840) * s + off[u], dst + 4096 * u);
    };
    STAMP(a.ws, lw + 4, 1);
    issue_a(0); issue_b(0); issue_a(1); issue_b(1);
    if (full) WAIT_VM(12); else WAIT_VM(11);
    bar();
    u64 t_vm = 0, t_free = 0;
#pragma unroll 1
    for (int s = 0; s < 8; ++s) {
        if (s > 0) TIMED(t_free, ring_wait(sm + L_FREE, s));
        issue_a(s + 2);
        TIMED(t_vm, WAIT_VM(6));
        ring_put(sm + L_FULL + 4 * lw, s + 2);
        issue_b(s + 2);
    }
    WAIT_VM(0);
    ring_put(sm + L_FULL + 4 * lw, 10);
    STAMPV(a.ws, lw + 4, 14, t_vm); STAMPV(a.ws, lw + 4, 15, t_free);
    STAMP(a.ws, lw + 4, 2);
    __builtin_amdgcn_s_setprio(2);
    const unsigned tag = mk_tag(nonce);
    const int tl = lw * 64 + lane;
    if (tl < 128) {
        GAS u64* gp = (GAS u64*)(a.ws + WS_G) + 128 * ti + tl;
        u64 v;
        for (unsigned spins = 0;;) {
            v = __hip_atomic_load(gp, RLX_AGENT);
            if (wave_all((unsigned)(v >> 32) == tag) || ++spins > SPIN_CAP) break;
            __builtin_amdgcn_s_sleep(1);
        }
        const float g = __uint_as_float((unsigned)v);
        ((LAS float*)(sm + A_G))[tl] = g;
        ((LAS float*)(sm + A_G))[128 + tl] = bfr(1.f - g);
    }
    STAMP(a.ws, lw + 4, 9);
    lds_bar();
    STAMP(a.ws, lw + 4, 3);
    if (lw < 3) {
        const int col = tl < 160 ? tl : 159;
        LAS float* tab = (LAS float*)(sm + A_TAB);
        GAS u64* ea = (GAS u64*)(a.ws + WS_EA) + gj * 160;
        const float a16 = tab[2 * 160 + col], b16 = tab[3 * 160 + col], a128 = tab[4 * 160 + col], b128 = tab[5 * 160 + col];
        LAS float* es = (LAS float*)(sm + A_ESEG);
        float pre[8], P = 0.f;
#pragma unroll
        for (int s = 0; s < 8; ++s) { pre[s] = P; P = cmul_add(a16, b16, P, es[s * 160 + col]); }
        if (tl < 160) __hip_atomic_store(ea + ti * 1280 + col, granule(tag, __float_as_uint(P)), RLX_AGENT);
        STAMP(a.ws, lw + 4, 4);
        float H = 0.f;
        if (ti > 0) {
            if (SENT_LB == 0) {
            } else if (lw == 0) {
                GAS u64* sp = ea + (lane < ti ? lane : 0) * 1280 + 159;
                for (unsigned spins = 0; spins < SPIN_CAP; ++spins) {
                    if (wave_all((unsigned)(__hip_atomic_load(sp, RLX_AGENT) >> 32) == tag)) break;
                    __builtin_amdgcn_s_sleep(SLP_LB);
                }
                word_set(sm + L_LBS, 1u);
            } else word_wait(sm + L_LBS, 1u);
            float ev[15];
            for (unsigned spins = 0; spins < SPIN_CAP; ++spins) {
                u64 v[15];
#pragma unroll
                for (int i = 0; i < 15; ++i) v[i] = __hip_atomic_load(ea + (i < ti ? i : 0) * 1280 + col, RLX_AGENT);
                bool ok = true;
#pragma unroll
                for (int i = 0; i < 15; ++i) { ev[i] = __uint_as_float((unsigned)v[i]); ok &= (unsigned)(v[i] >> 32) == tag; }
                if (wave_all(ok)) break;
                __builtin_amdgcn_s_sleep(SLP_LB);
            }
#pragma unroll
            for (int i = 0; i < 15; ++i) if (i < ti) H = cmul_add(a128, b128, H, ev[i]);
        }
        STAMP(a.ws, lw + 4, 6);
        LAS float* hs = (LAS float*)(sm + A_HSEG);
#pragma unroll
        for (int s = 0; s < 8; ++s) { if (tl < 160) hs[s * 160 + col] = H + pre[s]; H = cmul_add(a16, b16, H, 0.f); }
        wait_lgkm0();
        if (lane == 0) word_add(sm + L_HS);
    } else {
        const int i16 = lane & 15;
        GAS u64* sp = (GAS u64*)(a.ws + WS_TOT) + gj * 840 + (i16 < 8 ? 53 * i16 + 52 : 424 + 52 * (i16 - 8) + 51);
        for (unsigned spins = 0; SENT_TOT && spins < SPIN_CAP; ++spins) {
            if (wave_all((unsigned)(__hip_atomic_load(sp, RLX_AGENT) >> 32) == tag)) break;
            __builtin_amdgcn_s_sleep(SLP_TOT);
        }
        STAMP(a.ws, lw + 4, 4);
        const __amdgpu_buffer_rsrc_t rs = rsrc(a.ws + WS_TOT, 8 * 840 * 8);
        u32x4 v[7];
        for (unsigned spins = 0; spins < SPIN_CAP; ++spins) {
            bool ok = true;
#pragma unroll
            for (int k = 0; k < 7; ++k) v[k] = __builtin_amdgcn_raw_buffer_load_b128(rs, gj * 6720 + (lane + 64 * k < 420 ? lane + 64 * k : 419) * 16, 0, 16);
#pragma unroll
            for (int k = 0; k < 7; ++k) ok &= (v[k][1] == tag) & (v[k][3] == tag);
            if (wave_all(ok)) break;
            __builtin_amdgcn_s_sleep(SLP_TOT);
        }
#pragma unroll
        for (int k = 0; k < 7; ++k) if (lane + 64 * k < 420) ((LAS u32x2*)(sm + A_TOT))[lane + 64 * k] = (u32x2){v[k][0], v[k][2]};
        wait_lgkm0();
        word_set(sm + L_TOT, 1u);
        STAMP(a.ws, lw + 4, 5);
    }
    __builtin_amdgcn_s_setprio(0);
    STAMP(a.ws, lw + 4, 8);
    p2_main(a, sm, lw, lane, nonce);
}

template <int NH>
DEV void a_pass1(f32x4 (&acc)[4][5], const float (&ca)[5], const float (&cb)[5], LAS float* es) {
    float e[5];
#pragma unroll
    for (int n = 0; n < 5; ++n) e[n] = acc[2 * NH][n][0];
#pragma unroll
    for (int t = 1; t < 16; ++t) {
        const int i = (t >> 2) ^ (2 * NH), r = t & 3;
        float y[5];
        scan_step_nd(y, acc[i][0][r], acc[i][1][r], acc[i][2][r], acc[i][3][r], acc[i][4][r], e, ca, cb);
#pragma unroll
        for (int n = 0; n < 5; ++n) e[n] = y[n];
    }
#pragma unroll
    for (int n = 0; n < 5; ++n) es[16 * n] = e[n];
}
template <int NH>
DEV void a_pass2(f32x4 (&acc)[4][5], const float (&ca)[5], const float (&cb)[5], LAS float* hs, LAS float* gt, LAS char* ht) {
    float e[5];
#pragma unroll
    for (int n = 0; n < 5; ++n) e[n] = hs[16 * n];
    asm volatile("s_nop 1" : "+v"(e[0]), "+v"(e[1]), "+v"(e[2]), "+v"(e[3]), "+v"(e[4]));
#pragma unroll
    for (int m = 0; m < 4; ++m) {
        const int i = m ^ (2 * NH);
        const f32x4 g = *(LAS f32x4*)(gt + 4 * m);
#pragma unroll
        for (int r = 0; r < 4; ++r) {
            float u0 = acc[i][0][r], u1 = acc[i][1][r], u2 = acc[i][2][r], u3 = acc[i][3][r], u4 = acc[i][4][r];
            scan_step(u0, u1, u2, u3, u4, e, ca, cb);
            e[0] = u0; e[1] = u1; e[2] = u2; e[3] = u3; e[4] = u4;
            acc[i][0][r] = u0 * g[r]; acc[i][1][r] = u1 * g[r]; acc[i][2][r] = u2 * g[r]; acc[i][3][r] = u3 * g[r]; acc[i][4][r] = u4 * g[r];
        }
#pragma unroll
        for (int n = 0; n < 5; ++n)
            *(LAS u32x2*)(ht + 16 * n * HT_STRIDE + 32 * m) = (u32x2){pk2(acc[i][n][0], acc[i][n][1]), pk2(acc[i][n][2], acc[i][n][3])};
    }
}

DEV void a_consumer(const Args& a, LAS char* sm, int ti, int gj, int cw, int lane) {
    const int mh = cw >> 1, nh = cw & 1, c = lane & 15, q = lane >> 4, tid = cw * 64 + lane;
    STAMP(a.ws, cw, 0);
    if (cw == 1) warm_up(a, sm, lane);
    const unsigned nonce = __hip_atomic_load((GAS unsigned*)(a.ws + WS_NONCE), RLX_AGENT);
    const unsigned tag = mk_tag(nonce);
    if (cw >= 2) {
        const int tok = 128 * ti + 16 * gj + 8 * (cw - 2) + (lane >> 3);
        const u32x4* xr = (const u32x4*)(a.x + (size_t)tok * 1280) + (lane & 7);
        const u32x4* gr = (const u32x4*)a.gw + (lane & 7);
        u32x4 xv[10], gv[10];
#pragma unroll
        for (int i = 0; i < 10; ++i) { xv[i] = xr[8 * i]; gv[i] = gr[8 * i]; }
        float s = 0.f;
#pragma unroll
        for (int i = 0; i < 10; ++i)
#pragma unroll
            for (int k = 0; k < 4; ++k) {
                const unsigned pr = pk2(bf_lo(xv[i][k]) * bf_lo(gv[i][k]), bf_hi(xv[i][k]) * bf_hi(gv[i][k]));
                s += bf_lo(pr); s += bf_hi(pr);
            }
        s += lane_xor(s, 1); s += lane_xor(s, 2); s += lane_xor(s, 4);
        const float lg = bfr(s), g = bfr(1.f / (1.f + fexp(-lg)));
        if ((lane & 7) == 0) __hip_atomic_store((GAS u64*)(a.ws + WS_G) + tok, granule(tag, __float_as_uint(g)), RLX_AGENT);
    } else if (tid < 80) {
        float ar, ai, cr, ci;
        channel_consts(a, 80 * gj + tid, ar, ai, cr, ci);
        float pr = ar, pi = ai;
#pragma unroll
        for (int k = 0; k < 4; ++k) { const float t = pr * pr - pi * pi; pi = 2.f * pr * pi; pr = t; }
        const float r16 = pr, i16 = pi;
#pragma unroll
        for (int k = 0; k < 3; ++k) { const float t = pr * pr - pi * pi; pi = 2.f * pr * pi; pr = t; }
        LAS f32x2* tab = (LAS f32x2*)(sm + A_TAB);
        tab[0 * 80 + tid] = (f32x2){ar, ar};
        tab[1 * 80 + tid] = (f32x2){-ai, ai};
        tab[2 * 80 + tid] = (f32x2){r16, r16};
        tab[3 * 80 + tid] = (f32x2){-i16, i16};
        tab[4 * 80 + tid] = (f32x2){pr, pr};
        tab[5 * 80 + tid] = (f32x2){-pi, pi};
    }
    if (tid < 160) __hip_atomic_store((GAS u64*)(a.ws + WS_EA) + (ti * 8 + gj) * 160 + tid, (u64)0, RLX_AGENT);
    __builtin_amdgcn_s_setprio(1);
    f32x4 acc[4][5], at[5][2];
#pragma unroll
    for (int n = 0; n < 5; ++n) {
#pragma unroll
        for (int i = 0; i < 4; ++i) acc[i][n] = (f32x4){0.f, 0.f, 0.f, 0.f};
        at[n][0] = at[n][1] = (f32x4){0.f, 0.f, 0.f, 0.f};
    }
    int xlo[2], xhi[2], wa[2], wq[2];
#pragma unroll
    for (int kk = 0; kk < 2; ++kk) {
        const int sw = ((4 * kk + q) ^ (c & 7)) << 4;
        xlo[kk] = (64 * mh + 32 * nh + c) * 128 + sw;
        xhi[kk] = (64 * mh + 32 * (1 - nh) + c) * 128 + sw;
        wa[kk] = 16384 + (80 * nh + 8 * (c & 1) + (c >> 1)) * 128 + (((4 * kk + q) ^ (c >> 1) ^ ((c & 1) << 2)) << 4);
        wq[kk] = 36864 + (8 * q + 4 * (kk ^ (q & 1)) + (c >> 2)) * 160 + 8 * (c & 3);
    }
    bf16x8 xa[2][4], wb[2][5], wt[2][5];
    auto rdp = [&](int buf, LAS char* s0, int kk) LAM {
        LAS char* q0 = s0 + wq[0] + kk * 5120; LAS char* q1 = s0 + wq[1] + kk * 5120;
        xa[buf][0] = lds_frag(s0 + xlo[kk]);
        xa[buf][1] = lds_frag(s0 + xlo[kk] + 2048);
        wt[buf][0] = lds_tr_frag(q0, q1);
        xa[buf][2] = lds_frag(s0 + xhi[kk]);
        xa[buf][3] = lds_frag(s0 + xhi[kk] + 2048);
        wt[buf][1] = lds_tr_frag(q0 + 32, q1 + 32);
        wb[buf][0] = lds_frag(s0 + wa[kk]);
        wb[buf][1] = lds_frag(s0 + wa[kk] + 2048);
        wt[buf][2] = lds_tr_frag(q0 + 64, q1 + 64);
        wb[buf][2] = lds_frag(s0 + wa[kk] + 4096);
        wb[buf][3] = lds_frag(s0 + wa[kk] + 6144);
        wt[buf][3] = lds_tr_frag(q0 + 96, q1 + 96);
        wb[buf][4] = lds_frag(s0 + wa[kk] + 8192);
        wt[buf][4] = lds_tr_frag(q0 + 128, q1 + 128);
    };
    auto rd = [&](int buf, int slot, int kk) LAM { rdp(buf, sm + slot * SLOT_A, kk); };
    auto mm_head = [&](int buf) LAM {
#pragma unroll
        for (int n = 0; n < HEAD_A / 4; ++n)
#pragma unroll
            for (int i = 0; i < 4; ++i) acc[i][n] = mfma(xa[buf][i], wb[buf][n], acc[i][n]);
    };
    auto mm_tail = [&](int buf) LAM {
#pragma unroll
        for (int n = HEAD_A / 4; n < 5; ++n)
#pragma unroll
            for (int i = 0; i < 4; ++i) acc[i][n] = mfma(xa[buf][i], wb[buf][n], acc[i][n]);
#pragma unroll
        for (int n = 0; n < 5; ++n)
#pragma unroll
            for (int i = 0; i < 2; ++i) at[n][i] = mfma(wt[buf][n], xa[buf][i], at[n][i]);
    };
    if (tid < L_WORDS) ((LAS unsigned*)(sm + L_FULL))[tid] = tid < 4 ? 1u : 0u;
#pragma unroll
    for (int n = 0; n < 5; ++n)
        asm volatile("" : "+v"(acc[0][n]), "+v"(acc[1][n]), "+v"(acc[2][n]), "+v"(acc[3][n]), "+v"(at[n][0]), "+v"(at[n][1]));
    STAMP(a.ws, cw, 1);
    lds_bar();
    STAMP(a.ws, cw, 2);
    STAMPC(a.ws, cw, 12);
    rd(0, 0, 0);
    int slot = 0;
    u64 t_spin = 0, n_spin = 0;
#pragma unroll 1
    for (int s = 0; s < 10; ++s) {
        wait_lgkm0();
        SCHED();
        rd(1, slot, 1);
        u32x4 fl = *(volatile LAS u32x4*)(sm + L_FULL);
        mm_head(0); mm_tail(0);
        interleave<30, 20, IL_A>();
        SCHED();
        wait_lgkm0();
        asm volatile("" ::: "memory");
        *(volatile LAS unsigned*)(sm + L_FREE + 4 * cw) = s + 1;
        slot = slot == 2 ? 0 : slot + 1;
        const unsigned need = s < 9 ? s + 2 : 0;
        unsigned landed = __builtin_amdgcn_readfirstlane(umin4(fl));
        LAS char* s0 = sm + slot * SLOT_A;
        asm volatile("" : "+s"(landed), "+v"(s0));
        mm_head(1);
        SCHED();
        if (__builtin_expect(landed < need, 0)) { TIMED(t_spin, ring_spin(sm + L_FULL, need)); n_spin++; }
        asm volatile("" ::: "memory");
        SCHED();
        rdp(0, s0, 0);
        mm_tail(1);
        interleave<30 - HEAD_A, 19, IL_A - HEAD_A + 1>();
        SCHED();
    }
    __builtin_amdgcn_s_setprio(0);
    asm volatile("s_nop 7\n\ts_nop 7" ::: "memory");
    STAMP(a.ws, cw, 3);
    STAMPC(a.ws, cw, 13);
    STAMPV(a.ws, cw, 14, t_spin); STAMPV(a.ws, cw, 15, n_spin);
    LAS float* tab = (LAS float*)(sm + A_TAB);
    float ca[5], cb[5];
#pragma unroll
    for (int n = 0; n < 5; ++n) { const int col = 80 * nh + 16 * n + c; ca[n] = tab[col]; cb[n] = tab[160 + col]; }
    {
        LAS float* es = (LAS float*)(sm + A_ESEG) + (4 * mh + q) * 160 + 80 * nh + c;
        if (nh == 0) a_pass1<0>(acc, ca, cb, es); else a_pass1<1>(acc, ca, cb, es);
    }
    WAIT_VM(0);
    STAMP(a.ws, cw, 4);
    lds_bar();
    bf16x8 P[2][3];
#pragma unroll
    for (int i = 0; i < 2; ++i) {
#pragma unroll
        for (int t = 0; t < 5; ++t)
#pragma unroll
            for (int r = 0; r < 4; ++r) at[t][i][r] = phi(at[t][i][r]);
        P[i][0] = pack_pair(at[0][i], at[1][i]);
        P[i][1] = pack_pair(at[2][i], at[3][i]);
        P[i][2] = as_frag((u32x4){pk2(at[4][i][0], at[4][i][1]), pk2(at[4][i][2], at[4][i][3]), 0u, 0u});
    }
    int adr[21];
    {
        int dh[5], db[5], eh[5], eb[5];
#pragma unroll
        for (int k = 0; k < 5; ++k) {
            const int d0 = 16 * k + 4 * q, e0 = 16 * k + c;
            dh[k] = d0 / 20; db[k] = A_TOT + dh[k] * 840 + (d0 - 20 * dh[k]) * 2;
            eh[k] = e0 / 20; eb[k] = (e0 - 20 * eh[k]) * 40;
        }
        auto ad = [&](int k, int E) LAM { return dh[k] == eh[E] ? db[k] + eb[E] : L_ZERO; };
        adr[0] = ad(0, 0); adr[1] = ad(1, 0);
        adr[2] = ad(0, 1); adr[3] = ad(1, 1); adr[4] = ad(2, 1); adr[5] = ad(3, 1);
        adr[6] = ad(1, 2); adr[7] = ad(2, 2); adr[8] = ad(3, 2);
        adr[9] = ad(2, 3); adr[10] = ad(3, 3); adr[11] = ad(4, 3);
        adr[12] = ad(3, 4); adr[13] = ad(4, 4);
#pragma unroll
        for (int k = 0; k < 5; ++k) adr[14 + k] = dh[k] == (c & 3) ? db[k] + 800 : L_ZERO;
        adr[19] = L_ZERO; adr[20] = A_G + 4 * (128 + 64 * mh + 16 * (c >> 2) + 8 * nh + (c & 3));
    }
#pragma unroll
    for (int i = 0; i < 2; ++i) asm volatile("" : "+v"(P[i][0]), "+v"(P[i][1]), "+v"(P[i][2]));
    asm volatile("" : "+v"(adr[0]), "+v"(adr[1]), "+v"(adr[2]), "+v"(adr[3]), "+v"(adr[4]), "+v"(adr[5]), "+v"(adr[6]),
                      "+v"(adr[7]), "+v"(adr[8]), "+v"(adr[9]), "+v"(adr[10]), "+v"(adr[11]), "+v"(adr[12]), "+v"(adr[13]));
    asm volatile("" : "+v"(adr[14]), "+v"(adr[15]), "+v"(adr[16]), "+v"(adr[17]), "+v"(adr[18]), "+v"(adr[20]));
    const __amdgpu_buffer_rsrc_t ract = rsrc(a.ws + WS_ACT, 128 * TS);
    u32x4 os[2][2]; u32x2 o8[2];
    STAMP(a.ws, cw, 5);
    word_wait(sm + L_TOT, 1u);
    STAMP(a.ws, cw, 6);
    {
        auto h4 = [&](int i) LAM -> u32x2 { return *(LAS u32x2*)(sm + adr[i]); };
        auto fr2 = [&](const u32x2& lo, const u32x2& hi) LAM { return as_frag((u32x4){lo[0], lo[1], hi[0], hi[1]}); };
        const u32x2 zz = {0u, 0u};
        const bf16x8 Z0 = fr2(h4(14), h4(15)), Z1 = fr2(h4(16), h4(17)), Z2 = fr2(h4(18), zz);
        const bf16x8 F0 = fr2(h4(0), h4(1)), F1 = fr2(h4(2), h4(3)), F2 = fr2(h4(4), h4(5));
        const bf16x8 F3 = fr2(zz, h4(6)), F4 = fr2(h4(7), h4(8)), F5 = fr2(h4(9), h4(10));
        const bf16x8 F6 = fr2(h4(11), zz), F7 = fr2(zz, h4(12)), F8 = fr2(h4(13), zz);
        const float ogv[2] = {*(LAS float*)(sm + adr[20]), *(LAS float*)(sm + adr[20] + 16)};
        const f32x4 z4 = {0.f, 0.f, 0.f, 0.f};
        const int uu = gj >> 1, odd = gj & 1;
#pragma unroll
        for (int i = 0; i < 2; ++i) {
            const f32x4 dn = mfma(Z2, P[i][2], mfma(Z1, P[i][1], mfma(Z0, P[i][0], z4)));
            const f32x4 n0 = mfma(F0, P[i][0], z4);
            const f32x4 n1 = mfma(F2, P[i][1], mfma(F1, P[i][0], z4));
            const f32x4 n2 = mfma(F4, P[i][1], mfma(F3, P[i][0], z4));
            const f32x4 n3 = mfma(F6, P[i][2], mfma(F5, P[i][1], z4));
            const f32x4 n4 = mfma(F8, P[i][2], mfma(F7, P[i][1], z4));
            const float og = ogv[i];
            const float r0 = og * __builtin_amdgcn_rcpf(dn[0] + 1e-6f), r1 = og * __builtin_amdgcn_rcpf(dn[1] + 1e-6f);
            const float r2 = og * __builtin_amdgcn_rcpf(dn[2] + 1e-6f), r3 = og * __builtin_amdgcn_rcpf(dn[3] + 1e-6f);
            const float s1 = q < 1 ? r0 : r1, s2 = q < 2 ? r1 : r2, s3 = q < 3 ? r2 : r3;
            os[i][0] = (u32x4){pk2(n0[0] * r0, n0[1] * r0), pk2(n0[2] * r0, n0[3] * r0), pk2(n1[0] * s1, n1[1] * s1), pk2(n1[2] * s1, n1[3] * s1)};
            os[i][1] = (u32x4){pk2(n2[0] * s2, n2[1] * s2), pk2(n2[2] * s2, n2[3] * s2), pk2(n3[0] * s3, n3[1] * s3), pk2(n3[2] * s3, n3[3] * s3)};
            o8[i] = (u32x2){pk2(n4[0] * r3, n4[1] * r3), pk2(n4[2] * r3, n4[3] * r3)};
        }
#pragma unroll
        for (int i = 0; i < 2; ++i) {
            const unsigned ob = (8 * ti + 4 * mh + 2 * nh + i) * TS + (40 + 5 * uu) * 1024;
            st16_wt(ract, lane * 16, ob + (odd ? 3072 : 0), os[i][0]);
            st16_wt(ract, lane * 16, ob + (odd ? 4096 : 1024), os[i][1]);
            st8_wt(ract, lane * 16 + 8 * odd, ob + 2048, o8[i]);
        }
    }
    STAMP(a.ws, cw, 7);
    word_wait(sm + L_HS, 3u);
    STAMP(a.ws, cw, 8);
    {
        LAS float* hs = (LAS float*)(sm + A_HSEG) + (4 * mh + q) * 160 + 80 * nh + c;
        LAS float* gt = (LAS float*)(sm + A_G) + 64 * mh + 16 * q;
        LAS char* ht = sm + A_HT + (80 * nh + c) * HT_STRIDE + (64 * mh + 4 * q) * 2;
        if (nh == 0) a_pass2<0>(acc, ca, cb, hs, gt, ht); else a_pass2<1>(acc, ca, cb, hs, gt, ht);
    }
    signal_stores(sm, L_DONE2, a.ws + WS_FACT + ti * 256 + 128 + gj * 8, tag, lane, 4u);
    KEEP(os[0][0]); KEEP(os[0][1]); KEEP(os[1][0]); KEEP(os[1][1]);
    STAMP(a.ws, cw, 9);
    wait_lgkm0();
    if (lane == 0) word_add(sm + L_HT);
    word_wait(sm + L_HT, 4u);
    {
        const int qq = (lane & 15) >> 2, p = lane & 3;
        bf16x8 hf[10];
#pragma unroll
        for (int i = 0; i < 10; ++i) {
            const int f = cw + 4 * i, mt = f / 5, ks = f - 5 * mt;
            LAS char* b = sm + A_HT + (32 * ks + 8 * q + qq) * HT_STRIDE + 32 * mt + 8 * p;
            hf[i] = lds_tr_frag(b, b + 4 * HT_STRIDE);
        }
        SCHED();
#pragma unroll
        for (int i = 0; i < 10; ++i) {
            const int f = cw + 4 * i, mt = f / 5, ks = f - 5 * mt;
            st16_wt(ract, lane * 16, (8 * ti + mt) * TS + (5 * gj + ks) * 1024, __builtin_bit_cast(u32x4, hf[i]));
        }
    }
    wait_lgkm0();
    STAMP(a.ws, cw, 10);
    signal_stores(sm, L_DONE, a.ws + WS_FACT + ti * 256 + gj * 8, tag, lane, 4u);
    STAMP(a.ws, cw, 11);
    p2_final(a, sm, cw, lane, tag);
}

DEV void job_table(const Args& a, LAS char* tab, int jb, int lane) {
    if (jb < 400) {
        float ar, ai, cr, ci;
        channel_consts(a, 64 * (jb / 40) + lane, ar, ai, cr, ci);
        ((LAS f32x2*)tab)[lane] = (f32x2){2.f * cr, 2.f * ci};
    }
}
DEV void job_issue(const Args& a, LAS char* stg, int jb, int lane) {
    if (jb < 400) {
        const int cb = jb / 40, nt = jb - 40 * cb;
        const unsigned o = (16 * nt + (lane >> 3)) * 1280 + cb * 128 + (lane & 7) * 16;
        glds16(a.cre + o, stg); glds16(a.cre + o + 8 * 1280, stg + 1024);
        glds16(a.cim + o, stg + 2048); glds16(a.cim + o + 8 * 1280, stg + 3072);
    } else if (jb < 500) {
        const int jo = jb - 400, s5 = jo / 5, n8 = jo - 5 * s5, uu = s5 / 5, s = s5 - 5 * uu;
        const int lo = 160 * uu + (s == 0 ? 0 : s == 1 ? 32 : s == 2 ? 64 : s == 3 ? 80 : 112);
        const int hi = 160 * uu + (s == 0 ? 16 : s == 1 ? 48 : s == 2 ? 144 : s == 3 ? 96 : 128);
        const unsigned o = (lane >> 4) * 1280 + n8 * 256 + (lane & 15) * 16;
#pragma unroll
        for (int k = 0; k < 4; ++k) {
            glds16(a.wo + (lo + 4 * k) * 1280 + o, stg + 1024 * k);
            glds16(a.wo + (hi + 4 * k) * 1280 + o, stg + 4096 + 1024 * k);
        }
    }
}
DEV u32x2 lds_rd8(LAS char* p) { u32x2 v; asm volatile("ds_read_b64 %0, %1" : "=v"(v) : "v"((unsigned)(size_t)p) : "memory"); return v; }
DEV f32x4 lds_rd16(LAS char* p) { f32x4 v; asm volatile("ds_read_b128 %0, %1" : "=v"(v) : "v"((unsigned)(size_t)p) : "memory"); return v; }
DEV u32x2 lds_rdtr(LAS char* p) { u32x2 v; asm volatile("ds_read_b64_tr_b16 %0, %1" : "=v"(v) : "v"((unsigned)(size_t)p) : "memory"); return v; }
DEV void job_finish(const Args& a, LAS char* stg, LAS char* tab, int jb, int lane) {
    const int c = lane & 15, q = lane >> 4;
    const __amdgpu_buffer_rsrc_t rw = rsrc(a.ws + WS_W2T, 40 * TS);
    if (jb < 400) {
        const int cb = jb / 40, nt = jb - 40 * cb;
        u32x2 re[4], im[4]; f32x4 c01[4], c23[4];
#pragma unroll
        for (int k = 0; k < 4; ++k) {
            re[k] = lds_rd8(stg + c * 128 + 32 * k + 8 * q); im[k] = lds_rd8(stg + 2048 + c * 128 + 32 * k + 8 * q);
            c01[k] = lds_rd16(tab + (16 * k + 4 * q) * 8); c23[k] = lds_rd16(tab + (16 * k + 4 * q) * 8 + 16);
        }
        asm volatile("s_waitcnt lgkmcnt(0)" : "+v"(re[0]), "+v"(re[1]), "+v"(re[2]), "+v"(re[3]), "+v"(im[0]), "+v"(im[1]), "+v"(im[2]), "+v"(im[3]),
                     "+v"(c01[0]), "+v"(c01[1]), "+v"(c01[2]), "+v"(c01[3]), "+v"(c23[0]), "+v"(c23[1]), "+v"(c23[2]), "+v"(c23[3]) :: "memory");
        u32x4 w[4];
#pragma unroll
        for (int k = 0; k < 4; ++k) {
            const float r0 = bf_lo(re[k][0]), r1 = bf_hi(re[k][0]), r2 = bf_lo(re[k][1]), r3 = bf_hi(re[k][1]);
            const float i0 = bf_lo(im[k][0]), i1 = bf_hi(im[k][0]), i2 = bf_lo(im[k][1]), i3 = bf_hi(im[k][1]);
            w[k] = (u32x4){pk2(r0 * c01[k][0] - i0 * c01[k][1], -(r0 * c01[k][1] + i0 * c01[k][0])),
                           pk2(r1 * c01[k][2] - i1 * c01[k][3], -(r1 * c01[k][3] + i1 * c01[k][2])),
                           pk2(r2 * c23[k][0] - i2 * c23[k][1], -(r2 * c23[k][1] + i2 * c23[k][0])),
                           pk2(r3 * c23[k][2] - i3 * c23[k][3], -(r3 * c23[k][3] + i3 * c23[k][2]))};
        }
#pragma unroll
        for (int k = 0; k < 4; ++k) st16_wt(rw, lane * 16, nt * TS + (4 * cb + k) * 1024, w[k]);
    } else if (jb < 500) {
        const int jo = jb - 400, s5 = jo / 5, n8 = jo - 5 * s5;
        u32x2 lo[8], hi[8];
#pragma unroll
        for (int k = 0; k < 8; ++k) {
            LAS char* b = stg + (4 * q + (c >> 2)) * 256 + 32 * k + 8 * (c & 3);
            lo[k] = lds_rdtr(b); hi[k] = lds_rdtr(b + 4096);
        }
        asm volatile("s_waitcnt lgkmcnt(0)" : "+v"(lo[0]), "+v"(lo[1]), "+v"(lo[2]), "+v"(lo[3]), "+v"(lo[4]), "+v"(lo[5]), "+v"(lo[6]), "+v"(lo[7]),
                     "+v"(hi[0]), "+v"(hi[1]), "+v"(hi[2]), "+v"(hi[3]), "+v"(hi[4]), "+v"(hi[5]), "+v"(hi[6]), "+v"(hi[7]) :: "memory");
#pragma unroll
        for (int k = 0; k < 8; ++k)
            st16_wt(rw, lane * 16, (8 * n8 + k) * TS + (40 + s5) * 1024, (u32x4){lo[k][0], lo[k][1], hi[k][0], hi[k][1]});
    }
}

DEV void b_loader(const Args& a, LAS char* sm, int ti, int gj, int lw, int lane) {
    STAMP(a.ws, lw + 4, 0);
    const unsigned nonce = __hip_atomic_load((GAS unsigned*)(a.ws + WS_NONCE), RLX_AGENT);
    unsigned off[9];
    {
        const int pos = lane & 7, rl = lane >> 3;
#pragma unroll
        for (int u = 0; u < 4; ++u) { const int R = 8 * (lw + 4 * u) + rl; off[u] = R * 1280 + ((pos ^ (R & 7)) << 4); }
#pragma unroll
        for (int u = 4; u < 9; ++u) {
            const int wi = lw + 4 * (u - 4), sec = wi >= 10, ch = 64 * (wi - 10 * sec) + lane, rho = ch / 10, cc = ch - 10 * rho;
            off[u] = (rho ^ (((rho >> 3) & 1) << 2)) * 3840 + cc * 16 + 1280 + 1280 * sec;
        }
    }
    const char* xb = a.x + (size_t)ti * (128 * 1280);
    const char* wb = a.wqkv + 160 * gj;
    auto issue_a = [&](int s) LAM {
        LAS char* dst = sm + (s % NS_B) * SLOT_B + lw * 1024;
#pragma unroll
        for (int u = 0; u < 4; ++u) glds16x(xb + 128 * s + off[u], dst + 4096 * u);
        glds16w(wb + (size_t)(64 * 3840) * s + off[4], dst + 4096 * 4);
    };
    auto issue_b = [&](int s) LAM {
        LAS char* dst = sm + (s % NS_B) * SLOT_B + lw * 1024;
#pragma unroll
        for (int u = 5; u < 9; ++u) glds16w(wb + (size_t)(64 * 3840) * s + off[u], dst + 4096 * u);
    };
    STAMP(a.ws, lw + 4, 1);
    const int jb = (ti * 8 + gj) * 4 + lw;
    LAS char* stg = sm + B_STG + lw * 8192;
    issue_a(0); issue_b(0);
    if (JOBMODE == 0) job_issue(a, stg, jb, lane);
    issue_a(1); issue_b(1); issue_a(2); issue_b(2);
    WAIT_VM(18);
    bar();
    if (JOBMODE == 0) job_finish(a, stg, sm + B_TAB + lw * 512, jb, lane);
#pragma unroll 1
    for (int s = 0; s < 7; ++s) {
        if (s > 0) ring_wait(sm + L_FREE, s);
        issue_a(s + 3);
        if (JOBMODE == 0 && s < 2 && jb < 400) WAIT_VM(18); else if (JOBMODE == 0 && s < 2 && jb < 500) WAIT_VM(22); else WAIT_VM(14);
        ring_put(sm + L_FULL + 4 * lw, s + 2);
        issue_b(s + 3);
    }
    WAIT_VM(9);
    ring_put(sm + L_FULL + 4 * lw, 9);
    WAIT_VM(0);
    ring_put(sm + L_FULL + 4 * lw, 10);
    const unsigned tag = mk_tag(nonce);
    if (JOBMODE == 0) signal_stores(sm, L_DONE2, a.ws + WS_FW2T + (ti * 8 + gj) * 8, tag, lane, 4u);
    STAMP(a.ws, lw + 4, 2);
    if (JOBMODE == 2) {
        ring_wait(sm + L_FREE, 8);
        job_issue(a, stg, jb, lane);
        WAIT_VM(0);
        job_finish(a, stg, sm + B_TAB + lw * 512, jb, lane);
        STAMP(a.ws, lw + 4, 6);
    }
    __builtin_amdgcn_s_setprio(2);
    const int tl = lw * 64 + lane;
    {
        int o0[2], o1[2];
#pragma unroll
        for (int it = 0; it < 2; ++it) {
            const int g = 2 * (tl + 256 * it < 420 ? tl + 256 * it : 0);
            const int h = g / 210, rem = g - 210 * h, e = rem / 10, d = 2 * (rem - 10 * e);
            const bool kv = rem < 200;
            o0[it] = kv ? B_KVT + ((20 * h + e) * 84 + 20 * h + d) * 4 : B_Z + (20 * h + 2 * (rem - 200)) * 4;
            o1[it] = o0[it] + (kv ? 80 * 84 * 4 : 320);
        }
        asm volatile("" : "+v"(o0[0]), "+v"(o0[1]), "+v"(o1[0]), "+v"(o1[1]));
        const __amdgpu_buffer_rsrc_t rk = rsrc(a.ws + WS_KV, 16 * 8 * 840 * 8);
        word_wait(sm + L_KV, 4u);
        STAMP(a.ws, lw + 4, 3);
        u32x4 pv[2];
#pragma unroll
        for (int it = 0; it < 2; ++it) {
            const f32x4 v = *(LAS f32x4*)(sm + o0[it]) + *(LAS f32x4*)(sm + o1[it]);
            pv[it] = (u32x4){pk2(v[0], v[1]), tag, pk2(v[2], v[3]), tag};
        }
        asm volatile("" : "+v"(pv[0]), "+v"(pv[1]) :: "memory");
        st16_wt(rk, tl * 16, (ti * 8 + gj) * 6720, pv[0]);
        if (tl + 256 < 420) st16_wt(rk, (tl + 256) * 16, (ti * 8 + gj) * 6720, pv[1]);
        asm volatile("s_nop 1" ::"v"(pv[0]), "v"(pv[1]));
    }
    STAMP(a.ws, lw + 4, 4);
    if (lw == 0) {
        const int cnt = ti < 8 ? 53 : 52, g0 = ti < 8 ? 53 * ti : 424 + 52 * (ti - 8);
        const int g = g0 + (lane < cnt ? lane : 0);
        GAS u64* src = (GAS u64*)(a.ws + WS_KV) + gj * 840 + g;
        unsigned v[16];
        for (unsigned spins = 0;;) {
            bool ok = true;
#pragma unroll
            for (int i = 0; i < 16; ++i) {
                const u64 x = __hip_atomic_load(src + i * (8 * 840), RLX_AGENT);
                v[i] = (unsigned)x; ok &= (unsigned)(x >> 32) == tag;
            }
            if (wave_all(ok) || ++spins > SPIN_CAP) break;
            __builtin_amdgcn_s_sleep(1);
        }
        float s0 = 0.f, s1 = 0.f;
#pragma unroll
        for (int i = 0; i < 16; ++i) { s0 += bf_lo(v[i]); s1 += bf_hi(v[i]); }
        if (lane < cnt) __hip_atomic_store((GAS u64*)(a.ws + WS_TOT) + gj * 840 + g, granule(tag, pk2(s0, s1)), RLX_AGENT);
        word_set(sm + L_H2, 1u);
    }
    STAMP(a.ws, lw + 4, 5);
    __builtin_amdgcn_s_setprio(0);
    if (JOBMODE == 2) signal_stores(sm, L_DONE2, a.ws + WS_FW2T + (ti * 8 + gj) * 8, tag, lane, 4u);
    p2_main(a, sm, lw, lane, nonce);
}

DEV void b_consumer(const Args& a, LAS char* sm, int ti, int gj, int cw, int lane) {
    const int mh = cw >> 1, nh = cw & 1, c = lane & 15, q = lane >> 4, tid = cw * 64 + lane;
    STAMP(a.ws, cw, 0);
    if (cw == 3) warm_up(a, sm, lane);
    const unsigned tag = mk_tag(__hip_atomic_load((GAS unsigned*)(a.ws + WS_NONCE), RLX_AGENT));
    if (JOBMODE != 1) job_table(a, sm + B_TAB + cw * 512, (ti * 8 + gj) * 4 + cw, lane);
    {
        const __amdgpu_buffer_rsrc_t rk = rsrc(a.ws + WS_KV, 16 * 8 * 840 * 8);
        const u32x4 z = {0u, 0u, 0u, 0u};
        st16_wt(rk, tid * 16, (ti * 8 + gj) * 6720, z);
        if (tid + 256 < 420) st16_wt(rk, (tid + 256) * 16, (ti * 8 + gj) * 6720, z);
        const int cnt = ti < 8 ? 53 : 52, g0 = ti < 8 ? 53 * ti : 424 + 52 * (ti - 8);
        if (tid < cnt) __hip_atomic_store((GAS u64*)(a.ws + WS_TOT) + gj * 840 + g0 + tid, (u64)0, RLX_AGENT);
    }
    __builtin_amdgcn_s_setprio(1);
    f32x4 an[5][4];
#pragma unroll
    for (int i = 0; i < 4; ++i)
#pragma unroll
        for (int t = 0; t < 5; ++t) an[t][i] = (f32x4){0.f, 0.f, 0.f, 0.f};
    int xo[2], wA[2], wB[2];
#pragma unroll
    for (int kk = 0; kk < 2; ++kk) {
        xo[kk] = (64 * mh + c) * 128 + (((4 * kk + q) ^ (c & 7)) << 4);
        const int b = 16384 + (8 * q + 4 * (kk ^ (q & 1)) + (c >> 2)) * 160 + 8 * (c & 3);
        wA[kk] = b + 96 * nh; wB[kk] = b + 64 + 10240 * nh;
    }
    bf16x8 xa[2][4], wb[2][5];
    auto rdp = [&](int buf, LAS char* s0, int kk) LAM {
#pragma unroll
        for (int i = 0; i < 4; ++i) xa[buf][i] = lds_frag(s0 + xo[kk] + 2048 * i);
        LAS char* w0 = s0 + kk * 5120;
        wb[buf][0] = lds_tr_frag(w0 + wA[0], w0 + wA[1]);
        wb[buf][1] = lds_tr_frag(w0 + wA[0] + 32, w0 + wA[1] + 32);
        wb[buf][2] = lds_tr_frag(w0 + wB[0], w0 + wB[1]);
        wb[buf][3] = lds_tr_frag(w0 + wA[0] + 10240, w0 + wA[1] + 10240);
        wb[buf][4] = lds_tr_frag(w0 + wA[0] + 10272, w0 + wA[1] + 10272);
    };
    auto rd = [&](int buf, int slot, int kk) LAM { rdp(buf, sm + slot * SLOT_B, kk); };
    auto mm_head = [&](int buf) LAM {
#pragma unroll
        for (int i = 0; i < 4; ++i) an[0][i] = mfma(xa[buf][i], wb[buf][0], an[0][i]);
    };
    auto mm_tail = [&](int buf) LAM {
#pragma unroll
        for (int t = 1; t < 5; ++t)
#pragma unroll
            for (int i = 0; i < 4; ++i) an[t][i] = mfma(xa[buf][i], wb[buf][t], an[t][i]);
    };
    if (tid < L_WORDS) ((LAS unsigned*)(sm + L_FULL))[tid] = tid < 4 ? 1u : 0u;
#pragma unroll
    for (int t = 0; t < 5; ++t)
        asm volatile("" : "+v"(an[t][0]), "+v"(an[t][1]), "+v"(an[t][2]), "+v"(an[t][3]));
    STAMP(a.ws, cw, 1);
    lds_bar();
    STAMP(a.ws, cw, 2);
    rd(0, 0, 0);
#pragma unroll 1
    for (int s = 0; s < 10; ++s) {
        wait_lgkm0();
        SCHED();
        rd(1, s & 3, 1);
        u32x4 fl = *(volatile LAS u32x4*)(sm + L_FULL);
        mm_head(0); mm_tail(0);
        interleave<20, 15, IL_B>();
        SCHED();
        wait_lgkm0();
        asm volatile("" ::: "memory");
        *(volatile LAS unsigned*)(sm + L_FREE + 4 * cw) = s + 1;
        const unsigned need = s < 9 ? s + 2 : 0;
        unsigned landed = __builtin_amdgcn_readfirstlane(umin4(fl));
        LAS char* s0 = sm + ((s + 1) & 3) * SLOT_B;
        asm volatile("" : "+s"(landed), "+v"(s0));
        mm_head(1);
        SCHED();
        if (__builtin_expect(landed < need, 0)) ring_spin(sm + L_FULL, need);
        asm volatile("" ::: "memory");
        SCHED();
        rdp(0, s0, 0);
        mm_tail(1);
        interleave<16, 14, 14>();
        SCHED();
    }
    STAMP(a.ws, cw, 3);
    bf16x8 fr[5][2];
#pragma unroll
    for (int t = 0; t < 2; ++t)
#pragma unroll
        for (int i = 0; i < 4; ++i)
#pragma unroll
            for (int r = 0; r < 4; ++r) an[t][i][r] = phi(an[t][i][r]);
    if (nh == 0) {
#pragma unroll
        for (int i = 0; i < 4; ++i)
#pragma unroll
            for (int r = 0; r < 4; ++r) an[2][i][r] = phi(an[2][i][r]);
    }
#pragma unroll
    for (int t = 0; t < 5; ++t) { fr[t][0] = pack_pair(an[t][0], an[t][1]); fr[t][1] = pack_pair(an[t][2], an[t][3]); }
    {
        LAS char* xs = sm + B_X1 + cw * 2048 + lane * 16;
        *(LAS bf16x8*)xs = fr[2][0];
        *(LAS bf16x8*)(xs + 1024) = fr[2][1];
    }
    wait_lgkm0();
    word_set(sm + L_X1 + 4 * cw, 1u);
    STAMP(a.ws, cw, 4);
    word_wait(sm + L_X1 + 4 * (cw ^ 1), 1u);
    ring_spin(sm + L_FREE, 10);
    asm volatile("" ::: "memory");
    {
        LAS char* xr = sm + B_X1 + (cw ^ 1) * 2048 + lane * 16;
        const bf16x8 R0 = *(LAS bf16x8*)xr, R1 = *(LAS bf16x8*)(xr + 1024);
        const bf16x8 ones = as_frag((u32x4){0x3f803f80u, 0x3f803f80u, 0x3f803f80u, 0x3f803f80u});
        const f32x4 z4 = {0.f, 0.f, 0.f, 0.f};
        LAS float* kvt = (LAS float*)(sm + B_KVT) + mh * (80 * 84) + c * 84 + 4 * q;
        LAS float* zz = (LAS float*)(sm + B_Z) + mh * 80 + 4 * q;
        auto prod = [&](const bf16x8& k0, const bf16x8& k1, const bf16x8& v0, const bf16x8& v1, int ta, int tb) LAM {
            *(LAS f32x4*)(kvt + 16 * tb * 84 + 16 * ta) = mfma(k1, v1, mfma(k0, v0, z4));
        };
        auto zsum = [&](const bf16x8& k0, const bf16x8& k1, int ta) LAM {
            const f32x4 d = mfma(k1, ones, mfma(k0, ones, z4));
            if (c == 0) *(LAS f32x4*)(zz + 16 * ta) = d;
        };
        const int b0 = 3 * nh;
        prod(fr[0][0], fr[0][1], fr[3][0], fr[3][1], b0, b0);
        prod(fr[0][0], fr[0][1], fr[4][0], fr[4][1], b0, b0 + 1);
        prod(fr[1][0], fr[1][1], fr[3][0], fr[3][1], b0 + 1, b0);
        prod(fr[1][0], fr[1][1], fr[4][0], fr[4][1], b0 + 1, b0 + 1);
        zsum(fr[0][0], fr[0][1], b0);
        zsum(fr[1][0], fr[1][1], b0 + 1);
        if (nh == 0) {
            prod(fr[2][0], fr[2][1], fr[4][0], fr[4][1], 2, 1);
            prod(fr[1][0], fr[1][1], R0, R1, 1, 2);
            prod(fr[2][0], fr[2][1], R0, R1, 2, 2);
            zsum(fr[2][0], fr[2][1], 2);
        } else {
            prod(fr[0][0], fr[0][1], fr[2][0], fr[2][1], 3, 2);
            prod(R0, R1, fr[3][0], fr[3][1], 2, 3);
        }
    }
    WAIT_VM(0);
    wait_lgkm0();
    if (lane == 0) word_add(sm + L_KV);
    __builtin_amdgcn_s_setprio(0);
    STAMP(a.ws, cw, 5);
    if (JOBMODE == 1) {
        const int jb = (ti * 8 + gj) * 4 + cw;
        LAS char* stg = sm + cw * 8192;
        job_table(a, sm + B_TAB + cw * 512, jb, lane);
        word_wait(sm + L_H2, 1u);
        job_issue(a, stg, jb, lane);
        WAIT_VM(0);
        wait_lgkm0();
        job_finish(a, stg, sm + B_TAB + cw * 512, jb, lane);
        STAMP(a.ws, cw, 6);
        signal_stores(sm, L_DONE, a.ws + WS_FW2T + (ti * 8 + gj) * 8, tag, lane, 4u);
        STAMP(a.ws, cw, 7);
    } else if (lane == 0) word_add(sm + L_DONE);
    p2_final(a, sm, cw, lane, tag);
}

}

__global__ __launch_bounds__(512, 2) NOPK void gm_fused(const void* x, const void* lre, const void* lim, const void* bre,
                                                   const void* bim, const void* cre, const void* cim, const void* logdt,
                                                   const void* dskip, const void* wqkv, const void* wo, const void* gw,
                                                   void* ws, void* out) {
    __shared__ __attribute__((aligned(1024))) char smem[LDS_BYTES];
    LAS char* sm = (LAS char*)smem;
    u64 pc0;
    asm volatile("s_getpc_b64 %0" : "=s"(pc0));
    const char* code = (const char*)(pc0 & ~127ull);
#define MAPMODE 1
    const int b = blockIdx.x, xcd = b & 7, idx = b >> 3;
    const bool roleA = MAPMODE == 1 ? idx < 16 : xcd < 4;
    const int ti = MAPMODE == 1 ? 4 * (xcd >> 1) + (idx & 3) : 8 * ((xcd & 3) >> 1) + (idx & 7);
    const int gj = MAPMODE == 1 ? 4 * (xcd & 1) + ((idx >> 2) & 3) : 4 * (xcd & 1) + (idx >> 3);
    const int wave = __builtin_amdgcn_readfirstlane(threadIdx.x >> 6), lane = threadIdx.x & 63;
    const Args a{(const char*)x, (const u16*)lre, (const u16*)lim, (const char*)bre, (const char*)bim, (const char*)cre,
                 (const char*)cim, (const u16*)logdt, (const char*)wqkv, (const char*)wo, (const u16*)gw,
                 (const u16*)dskip, (char*)ws, (float*)out, code};
    STAMPL(ws, 0);
    if (roleA) {
        if (wave < 4) a_consumer(a, sm, ti, gj, wave, lane);
        else a_loader(a, sm, ti, gj, wave - 4, lane);
    } else {
        if (wave < 4) b_consumer(a, sm, ti, gj, wave, lane);
        else b_loader(a, sm, ti, gj, wave - 4, lane);
    }
    STAMPL(ws, 1);
}

extern "C" void kernel_launch(void* const* d_in, const int* in_sizes, int n_in, void* d_out, int out_size, void* d_ws,
                              size_t ws_size, hipStream_t stream) {
    gm_fused<<<256, 512, 0, stream>>>(d_in[0], d_in[1], d_in[2], d_in[3], d_in[4], d_in[5], d_in[6], d_in[7], d_in[8],
                                      d_in[10], d_in[11], d_in[12], d_ws, d_out);
}
```

```cpp
#include <hip/hip_runtime.h>
#include <hip/hip_bf16.h>
#include <stdint.h>

namespace {

typedef __attribute__((ext_vector_type(8))) short bf16x8;
typedef __attribute__((ext_vector_type(4))) short s16x4;
typedef __attribute__((ext_vector_type(4))) float f32x4;
typedef __attribute__((ext_vector_type(2))) float f32x2;
typedef __attribute__((ext_vector_type(2))) __bf16 bf16x2_t;
typedef __attribute__((ext_vector_type(4))) unsigned u32x4;
typedef __attribute__((ext_vector_type(2))) unsigned u32x2;
typedef unsigned long long u64;
typedef unsigned short u16;
#define LAS __attribute__((address_space(3)))
#define GAS __attribute__((address_space(1)))
#if defined(__HIP_DEVICE_COMPILE__)
#define NOPK __attribute__((target("no-packed-fp32-ops")))
#else
#define NOPK
#endif
#define DEV __device__ __forceinline__
#define LAM __attribute__((always_inline))
#define RLX_AGENT __ATOMIC_RELAXED, __HIP_MEMORY_SCOPE_AGENT

constexpr size_t WS_NONCE = 0;
constexpr size_t WS_EA    = 4096;
constexpr size_t WS_KV    = WS_EA + 16 * 8 * 160 * 8;
constexpr size_t WS_TOT   = WS_KV + 16 * 8 * 840 * 8;
constexpr size_t WS_G     = WS_TOT + 8 * 840 * 8;
constexpr size_t WS_FACT  = WS_G + 2048 * 8;
constexpr size_t WS_FW2T  = WS_FACT + 16 * 256;
constexpr int NREP = 16;
constexpr size_t WS_W2T   = (WS_FW2T + NREP * 1024 + 1023) / 1024 * 1024;
constexpr size_t TS = 61440;
constexpr size_t WS_ACT   = WS_W2T + 40 * TS;
constexpr size_t WS_PROF  = 32u << 20;

#define STAMP(ws, wave, i) do {} while (0)
#define STAMPC(ws, wave, i) do {} while (0)
#define TIMED(acc, stmt) do { stmt; } while (0)
#define STAMPV(ws, wave, i, v) do {} while (0)
#define STAMP2(ws, wave, i) do {} while (0)

#define STAMPL(ws, i) do {} while (0)

constexpr int LDS_BYTES = 163840;
constexpr int SLOT_A = 47104, NS_A = 3;
constexpr int A_TAB  = 141312;
constexpr int A_G    = A_TAB + 3840;
constexpr int A_ESEG = A_G + 1024;
constexpr int A_HSEG = A_ESEG + 5120;
constexpr int A_TOT  = A_HSEG + 5120;
constexpr int A_HT = 47104, HT_STRIDE = 272;
static_assert(A_TOT + 3360 <= 159776, "lds");
constexpr int SLOT_B = 36864, NS_B = 4;
constexpr int B_X1  = 147456;
constexpr int B_KVT = 0;
constexpr int B_Z   = B_KVT + 2 * 80 * 84 * 4;
constexpr int B_TAB = 155648;
constexpr int B_STG = 110592;
static_assert(B_Z + 640 <= B_X1, "lds");

#define JOBMODE 2
#define SLP_W2T 16
#define SLP_ACT 4
#define SIDE_W2T 1
#define PF_EARLY 1
#define SENT_LB 0
#define SENT_TOT 0
#define SLP_LB 2
#define SLP_TOT 2
#define HEAD_A 4
#define IL_A 25
#define IL_B 16
constexpr unsigned SPIN_CAP = 1u << 18;

DEV unsigned pk2(float a, float b) {
    return __builtin_bit_cast(unsigned, __builtin_convertvector((f32x2){a, b}, bf16x2_t));
}
DEV float bfr(float a) { return __uint_as_float(pk2(a, 0.f) << 16); }
DEV float bf_lo(unsigned u) { return __uint_as_float(u << 16); }
DEV float bf_hi(unsigned u) { return __uint_as_float(u & 0xffff0000u); }
DEV float bf2f(u16 u) { return __uint_as_float((unsigned)u << 16); }
DEV float swp(float x) {
    return __builtin_bit_cast(float, __builtin_amdgcn_mov_dpp(__builtin_bit_cast(int, x), 0xB1, 0xF, 0xF, true));
}
DEV float cmul_add(float a, float bs, float e, float u) { return fmaf(bs, swp(e), fmaf(a, e, u)); }
DEV f32x4 mfma(bf16x8 a, bf16x8 b, f32x4 c) { return __builtin_amdgcn_mfma_f32_16x16x32_bf16(a, b, c, 0, 0, 0); }
DEV bf16x8 as_frag(u32x4 v) { return __builtin_bit_cast(bf16x8, v); }
DEV bf16x8 lds_frag(LAS char* p) { return *(LAS bf16x8*)p; }
DEV bf16x8 lds_tr_frag(LAS char* p0, LAS char* p1) {
    s16x4 lo = __builtin_amdgcn_ds_read_tr16_b64_v4i16((LAS s16x4*)p0);
    s16x4 hi = __builtin_amdgcn_ds_read_tr16_b64_v4i16((LAS s16x4*)p1);
    return __builtin_shufflevector(lo, hi, 0, 1, 2, 3, 4, 5, 6, 7);
}
DEV void glds16_wt(const char* g, unsigned l) {
    unsigned keep;
    asm volatile("s_mov_b32 %0, m0\n\ts_mov_b32 m0, %2\n\ts_nop 0\n\tglobal_load_lds_dwordx4 %1, off sc1\n\ts_mov_b32 m0, %0"
                 : "=&s"(keep) : "v"(g), "s"(l) : "memory");
}
#define AUX_X 0
#define AUX_W 0
#define AUX_J 0
template <int AUX> DEV void glds16a(const char* g, LAS char* l) {
    __builtin_amdgcn_global_load_lds((const GAS unsigned*)g, (LAS unsigned*)l, 16, 0, AUX);
}
DEV void glds16x(const char* g, LAS char* l) { glds16a<AUX_X>(g, l); }
DEV void glds16w(const char* g, LAS char* l) { glds16a<AUX_W>(g, l); }
DEV void glds16(const char* g, LAS char* l) { glds16a<AUX_J>(g, l); }
DEV void bar() { asm volatile("" ::: "memory"); __builtin_amdgcn_s_barrier(); asm volatile("" ::: "memory"); }
DEV void wait_lgkm0() { __builtin_amdgcn_s_waitcnt(0xC07F); }
DEV void lds_bar() { asm volatile("" ::: "memory"); wait_lgkm0(); bar(); }
constexpr int L_FULL = 159776, L_FREE = L_FULL + 16;
constexpr int L_DONE = L_FULL + 32;
constexpr int L_KV = L_FULL + 36, L_W2T = L_FULL + 40, L_ACT = L_FULL + 44;
constexpr int L_X1 = L_FULL + 48, L_ZERO = L_FULL + 64;
constexpr int L_DONE2 = L_FULL + 72, L_ACT2 = L_FULL + 76;
constexpr int L_H2 = L_FULL + 80;
constexpr int L_WR = L_FULL + 84;
constexpr int L_TOT = L_FULL + 88, L_LBS = L_FULL + 92;
constexpr int L_HS = L_FULL + 96, L_HT = L_FULL + 100;
constexpr int L_PUB = L_FULL + 108;
constexpr int L_WORDS = 28;
DEV unsigned umin4(const u32x4& f) { const unsigned a = f[0] < f[1] ? f[0] : f[1], b = f[2] < f[3] ? f[2] : f[3]; return a < b ? a : b; }
DEV void ring_put(LAS char* p, unsigned v) {
    asm volatile("ds_write_b32 %0, %1" ::"v"((unsigned)(size_t)p), "v"(v) : "memory");
}
DEV void ring_wait(LAS char* p, unsigned need) {
    for (unsigned spins = 0; spins < SPIN_CAP; ++spins) {
        u32x4 f;
        asm volatile("ds_read_b128 %0, %1\n\ts_waitcnt lgkmcnt(0)" : "=&v"(f) : "v"((unsigned)(size_t)p) : "memory");
        if ((unsigned)__builtin_amdgcn_readfirstlane(umin4(f)) >= need) break;
        __builtin_amdgcn_s_sleep(1);
    }
}
DEV void word_wait(LAS char* p, unsigned need) {
    for (unsigned spins = 0; spins < SPIN_CAP; ++spins) {
        if ((unsigned)__builtin_amdgcn_readfirstlane(*(volatile LAS unsigned*)p) >= need) break;
        __builtin_amdgcn_s_sleep(1);
    }
    asm volatile("" ::: "memory");
}
DEV void word_set(LAS char* p, unsigned v) { asm volatile("" ::: "memory"); *(volatile LAS unsigned*)p = v; }
DEV unsigned word_add(LAS char* p) {
    return __hip_atomic_fetch_add((LAS unsigned*)p, 1u, __ATOMIC_RELAXED, __HIP_MEMORY_SCOPE_WORKGROUP);
}
DEV void ring_spin(LAS char* p, unsigned need) {
    for (unsigned spins = 0; spins < SPIN_CAP; ++spins) {
        const u32x4 f = *(volatile LAS u32x4*)p;
        if ((unsigned)__builtin_amdgcn_readfirstlane(umin4(f)) >= need) break;
    }
}
#define WAIT_VM(n) asm volatile("s_waitcnt vmcnt(" #n ")" ::: "memory")
#define SCHED() __builtin_amdgcn_sched_barrier(0)
#define KEEP(x) asm volatile("" ::"v"(x))
template <int NM, int NR, int NS> DEV void interleave() {
#pragma unroll
    for (int i = 0; i < NS; ++i) {
        __builtin_amdgcn_sched_group_barrier(0x8, 1, 0);
        if ((i + 1) * NR / NS != i * NR / NS) __builtin_amdgcn_sched_group_barrier(0x100, 1, 0);
    }
    __builtin_amdgcn_sched_group_barrier(0x8, NM - NS, 0);
}
DEV bool wave_all(bool p) { return __builtin_amdgcn_ballot_w64(p) == __builtin_amdgcn_ballot_w64(true); }
DEV float lane_xor(float v, int m) {
    const int x = __builtin_bit_cast(int, v);
    const int y = m == 1 ? __builtin_amdgcn_mov_dpp(x, 0xB1, 0xF, 0xF, true) : m == 2 ? __builtin_amdgcn_mov_dpp(x, 0x4E, 0xF, 0xF, true)
                                                                              : __builtin_amdgcn_ds_swizzle(x, 0x101F);
    return __builtin_bit_cast(float, y);
}
DEV float fexp(float x) { return __builtin_amdgcn_exp2f(x * 1.44269504088896341f); }
DEV unsigned mk_tag(unsigned n) { return ((n + 1u) * 0x9E3779B1u) | 1u; }
DEV u64 granule(unsigned tag, unsigned v) { return ((u64)tag << 32) | v; }
DEV u64 flag_of(unsigned tag) { return granule(tag, tag * 0x85EBCA6Bu + 0x5BD1E995u); }
DEV __amdgpu_buffer_rsrc_t rsrc(const char* p, unsigned bytes) { return __builtin_amdgcn_make_buffer_rsrc((void*)p, 0, bytes, 0x00020000); }
DEV void st16_wt(const __amdgpu_buffer_rsrc_t& r, unsigned voff, unsigned soff, u32x4 v) { __builtin_amdgcn_raw_buffer_store_b128(v, r, voff + soff, 0, 16); }
DEV void st8_wt(const __amdgpu_buffer_rsrc_t& r, unsigned voff, unsigned soff, u32x2 v) { __builtin_amdgcn_raw_buffer_store_b64(v, r, voff + soff, 0, 16); }
DEV bf16x8 ld16_wt(const __amdgpu_buffer_rsrc_t& r, unsigned voff, unsigned soff) {
    return __builtin_bit_cast(bf16x8, __builtin_amdgcn_raw_buffer_load_b128(r, voff, soff, 16));
}
DEV void signal_stores(LAS char* sm, int word, char* flag, unsigned tag, int lane, unsigned waves, int replicas = 1) {
    asm volatile("s_waitcnt vmcnt(0)" ::: "memory");
    unsigned old = 0;
    if (lane == 0) old = __hip_atomic_fetch_add((LAS unsigned*)(sm + word), 1u, __ATOMIC_RELAXED, __HIP_MEMORY_SCOPE_WORKGROUP);
    old = __builtin_amdgcn_readfirstlane(old);
    if (old == waves - 1u && lane < replicas) __hip_atomic_store((GAS u64*)(flag + lane * 1024), flag_of(tag), RLX_AGENT);
}
DEV float phi(float t) {
    const float e = __builtin_amdgcn_fmed3f(__builtin_amdgcn_exp2f(t * 1.44269504088896341f), 0.f, 1.f);
    return (e + 1e-6f) + __builtin_amdgcn_fmed3f(t, 0.f, __builtin_inff());
}
DEV bf16x8 pack_pair(const f32x4& lo, const f32x4& hi) {
    return as_frag((u32x4){pk2(lo[0], lo[1]), pk2(lo[2], lo[3]), pk2(hi[0], hi[1]), pk2(hi[2], hi[3])});
}

DEV void scan_step(float& u0, float& u1, float& u2, float& u3, float& u4, const float (&e)[5], const float (&a)[5],
                   const float (&b)[5]) {
    asm("v_fmac_f32 %0, %10, %5\n\tv_fmac_f32 %1, %11, %6\n\tv_fmac_f32 %2, %12, %7\n\t"
        "v_fmac_f32 %3, %13, %8\n\tv_fmac_f32 %4, %14, %9\n\t"
        "v_fmac_f32_dpp %0, %5, %15 quad_perm:[1,0,3,2] row_mask:0xf bank_mask:0xf bound_ctrl:1\n\t"
        "v_fmac_f32_dpp %1, %6, %16 quad_perm:[1,0,3,2] row_mask:0xf bank_mask:0xf bound_ctrl:1\n\t"
        "v_fmac_f32_dpp %2, %7, %17 quad_perm:[1,0,3,2] row_mask:0xf bank_mask:0xf bound_ctrl:1\n\t"
        "v_fmac_f32_dpp %3, %8, %18 quad_perm:[1,0,3,2] row_mask:0xf bank_mask:0xf bound_ctrl:1\n\t"
        "v_fmac_f32_dpp %4, %9, %19 quad_perm:[1,0,3,2] row_mask:0xf bank_mask:0xf bound_ctrl:1"
        : "+v"(u0), "+v"(u1), "+v"(u2), "+v"(u3), "+v"(u4)
        : "v"(e[0]), "v"(e[1]), "v"(e[2]), "v"(e[3]), "v"(e[4]), "v"(a[0]), "v"(a[1]), "v"(a[2]), "v"(a[3]), "v"(a[4]),
          "v"(b[0]), "v"(b[1]), "v"(b[2]), "v"(b[3]), "v"(b[4]));
}

DEV void scan_step_nd(float (&t)[5], float u0, float u1, float u2, float u3, float u4, const float (&e)[5],
                      const float (&a)[5], const float (&b)[5]) {
    asm("v_fma_f32 %0, %15, %10, %5\n\tv_fma_f32 %1, %16, %11, %6\n\tv_fma_f32 %2, %17, %12, %7\n\t"
        "v_fma_f32 %3, %18, %13, %8\n\tv_fma_f32 %4, %19, %14, %9\n\t"
        "v_fmac_f32_dpp %0, %10, %20 quad_perm:[1,0,3,2] row_mask:0xf bank_mask:0xf bound_ctrl:1\n\t"
        "v_fmac_f32_dpp %1, %11, %21 quad_perm:[1,0,3,2] row_mask:0xf bank_mask:0xf bound_ctrl:1\n\t"
        "v_fmac_f32_dpp %2, %12, %22 quad_perm:[1,0,3,2] row_mask:0xf bank_mask:0xf bound_ctrl:1\n\t"
        "v_fmac_f32_dpp %3, %13, %23 quad_perm:[1,0,3,2] row_mask:0xf bank_mask:0xf bound_ctrl:1\n\t"
        "v_fmac_f32_dpp %4, %14, %24 quad_perm:[1,0,3,2] row_mask:0xf bank_mask:0xf bound_ctrl:1"
        : "=&v"(t[0]), "=&v"(t[1]), "=&v"(t[2]), "=&v"(t[3]), "=&v"(t[4])
        : "v"(u0), "v"(u1), "v"(u2), "v"(u3), "v"(u4), "v"(e[0]), "v"(e[1]), "v"(e[2]), "v"(e[3]), "v"(e[4]),
          "v"(a[0]), "v"(a[1]), "v"(a[2]), "v"(a[3]), "v"(a[4]), "v"(b[0]), "v"(b[1]), "v"(b[2]), "v"(b[3]), "v"(b[4]));
}

struct Args {
    const char* x; const u16* lre; const u16* lim; const char* bre; const char* bim; const char* cre; const char* cim;
    const u16* logdt; const char* wqkv; const char* wo; const u16* gw; const u16* dskip; char* ws; float* out;
};

DEV void channel_consts(u16 lre, u16 lim, u16 logdt, float& ar, float& ai, float& cr, float& ci) {
    const float lr = bf2f(lre), li = bf2f(lim);
    const float dt = bfr(fexp(bf2f(logdt)));
    const float zr = dt * lr, zi = dt * li, ex = fexp(zr);
    const float rv = zi * 0.15915494309189535f, rr = rv - __builtin_rintf(rv);
    ar = ex * __builtin_amdgcn_cosf(rr); ai = ex * __builtin_amdgcn_sinf(rr);
    const float nr = ar - 1.f, ni = ai, dn = lr * lr + li * li;
    cr = (nr * lr + ni * li) / dn; ci = (ni * lr - nr * li) / dn;
}
DEV float dot2(unsigned x, unsigned y, float s) {
    return __builtin_amdgcn_fdot2_f32_bf16(__builtin_bit_cast(bf16x2_t, x), __builtin_bit_cast(bf16x2_t, y), s, false);
}

constexpr int P2_RED = 0;
#define P2_PW 4
#define P2_PA 5
DEV bool flags_up(const u32x4& v, u64 fl) {
    const unsigned f0 = (unsigned)fl, f1 = (unsigned)(fl >> 32);
    return wave_all((v[0] == f0) & (v[1] == f1) & (v[2] == f0) & (v[3] == f1));
}
DEV int p2_unit(int lw, int i) { const int u = 4 * lw + i; return u < 9 ? 5120 * u : 90624 + 5120 * (u - 9); }
DEV void p2_prefetch(const Args& a, LAS char* sm, int lw, int lane, unsigned tag, bf16x8 (&fw)[P2_PW][5]) {
    const int b = blockIdx.x, xcd = b & 7, idx = b >> 3;
    const int cg = (xcd & 1) * 4 + (idx >> 3);
    if (lw == 3) {
        const __amdgpu_buffer_rsrc_t rf = rsrc(a.ws + WS_FW2T + (xcd * 2 + (b >> 7)) * 1024, 1024);
        for (unsigned spins = 0; spins < SPIN_CAP; ++spins) {
            if (flags_up(__builtin_amdgcn_raw_buffer_load_b128(rf, lane * 16, 0, 16), flag_of(tag))) break;
            __builtin_amdgcn_s_sleep(SLP_W2T);
        }
        word_set(sm + L_W2T, 1u);
    } else word_wait(sm + L_W2T, 1u);
    STAMP2(a.ws, lw + 4, 0);
    const unsigned wbase = 5 * cg * TS, vo = lane * 16;
    const char* src = a.ws + WS_W2T + wbase + (10 * lw + 4) * 1024 + vo;
#pragma unroll
    for (int i = 0; i < 4; ++i)
#pragma unroll
        for (int n = 0; n < 5; ++n) glds16_wt(src + (size_t)n * TS + i * 1024, (unsigned)(size_t)(sm + p2_unit(lw, i) + n * 1024));
    const __amdgpu_buffer_rsrc_t rw = rsrc(a.ws + WS_W2T, 40 * TS);
#pragma unroll
    for (int st = 0; st < P2_PW; ++st)
#pragma unroll
        for (int n = 0; n < 5; ++n) fw[st][n] = ld16_wt(rw, vo, wbase + n * TS + (40 + 5 * lw + st) * 1024);
}
DEV void p2_main(const Args& a, LAS char* sm, int lw, int lane, unsigned nonce, bf16x8 (&fw)[P2_PW][5]) {
    const int b = blockIdx.x, xcd = b & 7, idx = b >> 3;
    const int tt = (xcd >> 1) * 8 + (idx & 7), cg = (xcd & 1) * 4 + (idx >> 3);
    const int c = lane & 15, q = lane >> 4;
    const unsigned tag = mk_tag(nonce);
    const u64 fl = flag_of(tag);
    constexpr int PW = P2_PW, PA = P2_PA;
    const __amdgpu_buffer_rsrc_t rw = rsrc(a.ws + WS_W2T, 40 * TS), ra = rsrc(a.ws + WS_ACT, 128 * TS);
    const unsigned wbase = 5 * cg * TS, abase = 4 * tt * TS, vo = lane * 16;
    const unsigned ko = (40 + 5 * lw) * 1024, kh = (10 * lw - 5) * 1024;
    auto koff = [&](int st) LAM { return (st < 5 ? ko : kh) + st * 1024; };
    bf16x8 fa[PA][4];
    auto ldw = [&](int st, int sl) LAM {
#pragma unroll
        for (int n = 0; n < 5; ++n) fw[sl][n] = ld16_wt(rw, vo, wbase + n * TS + koff(st));
    };
    auto lda = [&](int st) LAM {
#pragma unroll
        for (int t = 0; t < 4; ++t) fa[st % PA][t] = ld16_wt(ra, vo, abase + t * TS + koff(st));
    };
    auto wait_act = [&](int part, int word) LAM {
        if (lw == 3) {
            GAS u64* fp = (GAS u64*)(a.ws + WS_FACT) + (tt >> 1) * 32 + part * 16 + (lane & 7);
            for (unsigned spins = 0; spins < SPIN_CAP; ++spins) {
                const u64 v = __hip_atomic_load(fp, RLX_AGENT);
                if (wave_all(v == fl)) break;
                __builtin_amdgcn_s_sleep(SLP_ACT);
            }
            word_set(sm + word, 1u);
        } else word_wait(sm + word, 1u);
    };
    auto unit = [&](int i) LAM { return p2_unit(lw, i); };
    WAIT_VM(0);
    wait_act(1, L_ACT2);
    STAMP2(a.ws, lw + 4, 1);
    f32x4 acc[5][4];
#pragma unroll
    for (int n = 0; n < 5; ++n)
#pragma unroll
        for (int t = 0; t < 4; ++t) acc[n][t] = (f32x4){0.f, 0.f, 0.f, 0.f};
    auto slot = [&](int st) LAM { return st < 9 ? st % 4 : st == 13 ? 1 : st == 14 ? 2 : (st & 1) ? 3 : 0; };
    auto step = [&](int st) LAM {
        SCHED();
        if (st >= 8 && st < 12) {
#pragma unroll
            for (int n = 0; n < 5; ++n) fw[slot(st + 1)][n] = lds_frag(sm + unit(st - 8) + vo + n * 1024);
        }
#pragma unroll
        for (int n = 0; n < 5; ++n)
#pragma unroll
            for (int t = 0; t < 4; ++t) acc[n][t] = mfma(fw[slot(st)][n], fa[st % PA][t], acc[n][t]);
        SCHED();
        if (st < 5) ldw(st + 4, slot(st + 4));
        if (st == 9) ldw(13, slot(13));
        if (st == 10) ldw(14, slot(14));
        if (st == 11) { wait_lgkm0(); if (lane == 0) word_add(sm + L_WR); }
    };
    static_assert(PA == 5 && PW == 4, "ring layout");
#pragma unroll
    for (int st = 0; st < 5; ++st) lda(st);
#pragma unroll
    for (int st = 0; st < 5; ++st) step(st);
    STAMP2(a.ws, lw + 4, 4);
    wait_act(0, L_ACT);
    STAMP2(a.ws, lw + 4, 5);
    if (tt == 31 && cg == 0 && lw == 3 && lane == 0) __hip_atomic_store((GAS unsigned*)(a.ws + WS_NONCE), nonce + 1u, RLX_AGENT);
#pragma unroll
    for (int st = 5; st < 9; ++st) lda(st);
#pragma unroll
    for (int st = 5; st < 15; ++st) {
        if (st + 4 < 15) lda(st + 4);
        step(st);
    }
    STAMP2(a.ws, lw + 4, 2);
    word_wait(sm + L_WR, 4u);
    word_wait(sm + L_DONE, 4u);
    LAS float* rw_ = (LAS float*)(sm + P2_RED) + lw * (64 * 84);
#pragma unroll
    for (int t = 0; t < 4; ++t)
#pragma unroll
        for (int n = 0; n < 5; ++n) *(LAS f32x4*)(rw_ + (16 * t + c) * 84 + 16 * n + 4 * q) = acc[n][t];
    lds_bar();
    STAMP2(a.ws, lw + 4, 3);
}

DEV void p2_final(const Args& a, LAS char* sm, int cw, int lane, unsigned tag) {
    const int b = blockIdx.x, xcd = b & 7, idx = b >> 3;
    const int tt = (xcd >> 1) * 8 + (idx & 7), cg = (xcd & 1) * 4 + (idx >> 3);
    const int tid = cw * 64 + lane;
    u32x2 xv[5], dv[5]; float gv[5];
    int tok[5], n0[5];
#pragma unroll
    for (int i = 0; i < 5; ++i) {
        const int id = tid + 256 * i, r = id / 20, c4 = id - 20 * r;
        tok[i] = 64 * tt + 16 * ((r & 15) >> 2) + 4 * (r >> 4) + (r & 3); n0[i] = 80 * cg + 4 * c4;
        xv[i] = *(const u32x2*)((const u16*)a.x + tok[i] * 640 + n0[i]);
        dv[i] = *(const u32x2*)(a.dskip + n0[i]);
    }
    for (unsigned spins = 0; spins < SPIN_CAP; ++spins) {
        u64 v[5];
#pragma unroll
        for (int i = 0; i < 5; ++i) v[i] = __hip_atomic_load((GAS u64*)(a.ws + WS_G) + tok[i], RLX_AGENT);
        bool ok = true;
#pragma unroll
        for (int i = 0; i < 5; ++i) { gv[i] = __uint_as_float((unsigned)v[i]); ok &= (unsigned)(v[i] >> 32) == tag; }
        if (wave_all(ok)) break;
        __builtin_amdgcn_s_sleep(2);
    }
    f32x4 sk[5];
#pragma unroll
    for (int i = 0; i < 5; ++i) {
        const unsigned p01 = pk2(bf_lo(xv[i][0]) * bf_lo(dv[i][0]), bf_hi(xv[i][0]) * bf_hi(dv[i][0]));
        const unsigned p23 = pk2(bf_lo(xv[i][1]) * bf_lo(dv[i][1]), bf_hi(xv[i][1]) * bf_hi(dv[i][1]));
        sk[i] = (f32x4){gv[i] * bf_lo(p01), gv[i] * bf_hi(p01), gv[i] * bf_lo(p23), gv[i] * bf_hi(p23)};
    }
    STAMP2(a.ws, cw, 0);
    bar();
    STAMP2(a.ws, cw, 2);
#pragma unroll
    for (int i = 0; i < 5; ++i) {
        const int id = tid + 256 * i, r = id / 20, c4 = id - 20 * r;
        LAS float* p = (LAS float*)(sm + P2_RED) + r * 84 + 4 * c4;
        sk[i] += (*(LAS f32x4*)p + *(LAS f32x4*)(p + 64 * 84)) + (*(LAS f32x4*)(p + 128 * 84) + *(LAS f32x4*)(p + 192 * 84));
    }
    asm volatile("" : "+v"(sk[0]), "+v"(sk[1]), "+v"(sk[2]), "+v"(sk[3]), "+v"(sk[4]));
#define OUT_AUX 16
    {
        const __amdgpu_buffer_rsrc_t ro = rsrc((const char*)a.out, 2048 * 640 * 4);
#pragma unroll
        for (int i = 0; i < 5; ++i)
            __builtin_amdgcn_raw_buffer_store_b128(__builtin_bit_cast(u32x4, sk[i]), ro, (tok[i] * 640 + n0[i]) * 4, 0, OUT_AUX);
    }
    STAMP2(a.ws, cw, 3);
}

DEV void a_loader(const Args& a, LAS char* sm, int ti, int gj, int lw, int lane) {
    STAMP(a.ws, lw + 4, 0);
    const unsigned nonce = __hip_atomic_load((GAS unsigned*)(a.ws + WS_NONCE), RLX_AGENT);
    unsigned off[12];
    {
        const int pos = lane & 7, rl = lane >> 3, im = lw & 1;
#pragma unroll
        for (int u = 0; u < 4; ++u) {
            const int R = 8 * (lw + 4 * u) + rl, rho = R & 15;
            const int tok = (R & 64) + 16 * (rho >> 2) + 4 * ((R >> 4) & 3) + (rho & 3);
            off[u] = tok * 1280 + ((pos ^ (R & 7)) << 4);
        }
#pragma unroll
        for (int u = 4; u < 9; ++u) off[u] = (8 * ((lw + 4 * (u - 4)) >> 1) + rl) * 1280 + ((pos ^ rl ^ (im << 2)) << 4);
#pragma unroll
        for (int u = 9; u < 12; ++u) {
            const int ch = 64 * (lw + 4 * (u - 9)) + lane, rho = ch / 10, cc = ch - 10 * rho;
            off[u] = (rho ^ (((rho >> 3) & 1) << 2)) * 3840 + cc * 16;
        }
    }
    const char* xb = a.x + (size_t)ti * (128 * 1280);
    const char* bb = ((lw & 1) ? a.bim : a.bre) + (size_t)gj * (80 * 1280);
    const char* qb = a.wqkv + 160 * gj;
    const bool full = lw < 2;
    auto issue_a = [&](int s) LAM {
        LAS char* dst = sm + (s % NS_A) * SLOT_A + lw * 1024;
#pragma unroll
        for (int u = 0; u < 4; ++u) glds16x(xb + 128 * s + off[u], dst + 4096 * u);
#pragma unroll
        for (int u = 4; u < 6; ++u) glds16w(bb + 128 * s + off[u], dst + 4096 * u);
    };
    auto issue_b = [&](int s) LAM {
        LAS char* dst = sm + (s % NS_A) * SLOT_A + lw * 1024;
#pragma unroll
        for (int u = 6; u < 9; ++u) glds16w(bb + 128 * s + off[u], dst + 4096 * u);
#pragma unroll
        for (int u = 9; u < 12; ++u) if (u < 11 || full) glds16w(qb + (size_t)(64 * 3840) * s + off[u], dst + 4096 * u);
    };
    STAMP(a.ws, lw + 4, 1);
    issue_a(0); issue_b(0); issue_a(1); issue_b(1);
    if (full) WAIT_VM(12); else WAIT_VM(11);
    bar();
    u64 t_vm = 0, t_free = 0;
#pragma unroll 1
    for (int s = 0; s < 8; ++s) {
        if (s > 0) TIMED(t_free, ring_wait(sm + L_FREE, s));
        issue_a(s + 2);
        TIMED(t_vm, WAIT_VM(6));
        ring_put(sm + L_FULL + 4 * lw, s + 2);
        issue_b(s + 2);
    }
    WAIT_VM(0);
    ring_put(sm + L_FULL + 4 * lw, 10);
    STAMPV(a.ws, lw + 4, 14, t_vm); STAMPV(a.ws, lw + 4, 15, t_free);
    STAMP(a.ws, lw + 4, 2);
    __builtin_amdgcn_s_setprio(2);
    const unsigned tag = mk_tag(nonce);
    const int tl = lw * 64 + lane;
    if (tl < 128) {
        GAS u64* gp = (GAS u64*)(a.ws + WS_G) + 128 * ti + tl;
        u64 v;
        for (unsigned spins = 0;;) {
            v = __hip_atomic_load(gp, RLX_AGENT);
            if (wave_all((unsigned)(v >> 32) == tag) || ++spins > SPIN_CAP) break;
            __builtin_amdgcn_s_sleep(1);
        }
        const float g = __uint_as_float((unsigned)v);
        ((LAS float*)(sm + A_G))[tl] = g;
        ((LAS float*)(sm + A_G))[128 + tl] = bfr(1.f - g);
    }
    STAMP(a.ws, lw + 4, 9);
    lds_bar();
    STAMP(a.ws, lw + 4, 3);
    bf16x8 fw[P2_PW][5];
    if (lw < 3) {
        const int col = tl < 160 ? tl : 159;
        LAS float* tab = (LAS float*)(sm + A_TAB);
        GAS u64* ea = (GAS u64*)(a.ws + WS_EA) + gj * 160;
        const float a16 = tab[2 * 160 + col], b16 = tab[3 * 160 + col], a128 = tab[4 * 160 + col], b128 = tab[5 * 160 + col];
        LAS float* es = (LAS float*)(sm + A_ESEG);
        float pre[8], P = 0.f;
#pragma unroll
        for (int s = 0; s < 8; ++s) { pre[s] = P; P = cmul_add(a16, b16, P, es[s * 160 + col]); }
        if (tl < 160) __hip_atomic_store(ea + ti * 1280 + col, granule(tag, __float_as_uint(P)), RLX_AGENT);
        STAMP(a.ws, lw + 4, 4);
        if (PF_EARLY) p2_prefetch(a, sm, lw, lane, tag, fw);
        float H = 0.f;
        if (ti > 0) {
            if (SENT_LB == 0) {
            } else if (lw == 0) {
                GAS u64* sp = ea + (lane < ti ? lane : 0) * 1280 + 159;
                for (unsigned spins = 0; spins < SPIN_CAP; ++spins) {
                    if (wave_all((unsigned)(__hip_atomic_load(sp, RLX_AGENT) >> 32) == tag)) break;
                    __builtin_amdgcn_s_sleep(SLP_LB);
                }
                word_set(sm + L_LBS, 1u);
            } else word_wait(sm + L_LBS, 1u);
            float ev[15];
            for (unsigned spins = 0; spins < SPIN_CAP; ++spins) {
                u64 v[15];
#pragma unroll
                for (int i = 0; i < 15; ++i) v[i] = __hip_atomic_load(ea + (i < ti ? i : 0) * 1280 + col, RLX_AGENT);
                bool ok = true;
#pragma unroll
                for (int i = 0; i < 15; ++i) { ev[i] = __uint_as_float((unsigned)v[i]); ok &= (unsigned)(v[i] >> 32) == tag; }
                if (wave_all(ok)) break;
                __builtin_amdgcn_s_sleep(SLP_LB);
            }
#pragma unroll
            for (int i = 0; i < 15; ++i) if (i < ti) H = cmul_add(a128, b128, H, ev[i]);
        }
        STAMP(a.ws, lw + 4, 6);
        LAS float* hs = (LAS float*)(sm + A_HSEG);
#pragma unroll
        for (int s = 0; s < 8; ++s) { if (tl < 160) hs[s * 160 + col] = H + pre[s]; H = cmul_add(a16, b16, H, 0.f); }
        wait_lgkm0();
        if (lane == 0) word_add(sm + L_HS);
        if (!PF_EARLY) p2_prefetch(a, sm, lw, lane, tag, fw);
    } else {
        p2_prefetch(a, sm, lw, lane, tag, fw);
        const int i16 = lane & 15;
        GAS u64* sp = (GAS u64*)(a.ws + WS_TOT) + gj * 840 + (i16 < 8 ? 53 * i16 + 52 : 424 + 52 * (i16 - 8) + 51);
        for (unsigned spins = 0; SENT_TOT && spins < SPIN_CAP; ++spins) {
            if (wave_all((unsigned)(__hip_atomic_load(sp, RLX_AGENT) >> 32) == tag)) break;
            __builtin_amdgcn_s_sleep(SLP_TOT);
        }
        STAMP(a.ws, lw + 4, 4);
        const __amdgpu_buffer_rsrc_t rs = rsrc(a.ws + WS_TOT, 8 * 840 * 8);
        u32x4 v[7];
        for (unsigned spins = 0; spins < SPIN_CAP; ++spins) {
            bool ok = true;
#pragma unroll
            for (int k = 0; k < 7; ++k) v[k] = __builtin_amdgcn_raw_buffer_load_b128(rs, gj * 6720 + (lane + 64 * k < 420 ? lane + 64 * k : 419) * 16, 0, 16);
#pragma unroll
            for (int k = 0; k < 7; ++k) ok &= (v[k][1] == tag) & (v[k][3] == tag);
            if (wave_all(ok)) break;
            __builtin_amdgcn_s_sleep(SLP_TOT);
        }
#pragma unroll
        for (int k = 0; k < 7; ++k) if (lane + 64 * k < 420) ((LAS u32x2*)(sm + A_TOT))[lane + 64 * k] = (u32x2){v[k][0], v[k][2]};
        wait_lgkm0();
        word_set(sm + L_TOT, 1u);
        STAMP(a.ws, lw + 4, 5);
    }
    __builtin_amdgcn_s_setprio(0);
    STAMP(a.ws, lw + 4, 8);
    p2_main(a, sm, lw, lane, nonce, fw);
}

template <int NH>
DEV void a_pass1(f32x4 (&acc)[4][5], const float (&ca)[5], const float (&cb)[5], LAS float* es) {
    float e[5];
#pragma unroll
    for (int n = 0; n < 5; ++n) e[n] = acc[2 * NH][n][0];
#pragma unroll
    for (int t = 1; t < 16; ++t) {
        const int i = (t >> 2) ^ (2 * NH), r = t & 3;
        float y[5];
        scan_step_nd(y, acc[i][0][r], acc[i][1][r], acc[i][2][r], acc[i][3][r], acc[i][4][r], e, ca, cb);
#pragma unroll
        for (int n = 0; n < 5; ++n) e[n] = y[n];
    }
#pragma unroll
    for (int n = 0; n < 5; ++n) es[16 * n] = e[n];
}
template <int NH>
DEV void a_pass2(f32x4 (&acc)[4][5], const float (&ca)[5], const float (&cb)[5], LAS float* hs, LAS float* gt, LAS char* ht,
                 LAS char* hr, const __amdgpu_buffer_rsrc_t& ract, unsigned vo, unsigned ob, LAS char* sm, char* oflag,
                 unsigned tag, int lane) {
    constexpr int KS = NH == 0 ? 0 : 3;
    float e[5];
#pragma unroll
    for (int n = 0; n < 5; ++n) e[n] = hs[16 * n];
    asm volatile("s_nop 1" : "+v"(e[0]), "+v"(e[1]), "+v"(e[2]), "+v"(e[3]), "+v"(e[4]));
    bf16x8 hf[4][2];
    auto rd = [&](int m) LAM {
#pragma unroll
        for (int k = 0; k < 2; ++k) {
            LAS char* b = hr + 32 * (KS + k) * HT_STRIDE + 32 * m;
            hf[m][k] = lds_tr_frag(b, b + 4 * HT_STRIDE);
        }
    };
    auto st = [&](int m) LAM {
#pragma unroll
        for (int k = 0; k < 2; ++k) st16_wt(ract, vo, ob + m * TS + (KS + k) * 1024, __builtin_bit_cast(u32x4, hf[m][k]));
    };
#pragma unroll
    for (int m = 0; m < 4; ++m) {
        const int i = m ^ (2 * NH);
        const f32x4 g = *(LAS f32x4*)(gt + 4 * m);
#pragma unroll
        for (int r = 0; r < 4; ++r) {
            float u0 = acc[i][0][r], u1 = acc[i][1][r], u2 = acc[i][2][r], u3 = acc[i][3][r], u4 = acc[i][4][r];
            scan_step(u0, u1, u2, u3, u4, e, ca, cb);
            e[0] = u0; e[1] = u1; e[2] = u2; e[3] = u3; e[4] = u4;
            acc[i][0][r] = u0 * g[r]; acc[i][1][r] = u1 * g[r]; acc[i][2][r] = u2 * g[r]; acc[i][3][r] = u3 * g[r]; acc[i][4][r] = u4 * g[r];
        }
        SCHED();
        if (m >= 2) st(m - 2);
        if (m == 3) {
            asm volatile("s_waitcnt vmcnt(4)" ::: "memory");
            if (lane == 0 && word_add(sm + L_DONE2) == 3u) __hip_atomic_store((GAS u64*)oflag, flag_of(tag), RLX_AGENT);
        }
#pragma unroll
        for (int n = 0; n < 5; ++n)
            *(LAS u32x2*)(ht + 16 * n * HT_STRIDE + 32 * m) = (u32x2){pk2(acc[i][n][0], acc[i][n][1]), pk2(acc[i][n][2], acc[i][n][3])};
        rd(m);
        SCHED();
    }
    st(2); st(3);
#pragma unroll
    for (int m = 0; m < 4; ++m) { KEEP(hf[m][0]); KEEP(hf[m][1]); }
}

DEV void a_consumer(const Args& a, LAS char* sm, int ti, int gj, int cw, int lane) {
    const int mh = cw >> 1, nh = cw & 1, c = lane & 15, q = lane >> 4, tid = cw * 64 + lane;
    STAMP(a.ws, cw, 0);
    const unsigned nonce = __hip_atomic_load((GAS unsigned*)(a.ws + WS_NONCE), RLX_AGENT);
    const int gtok = 128 * ti + 16 * gj + 8 * (cw & 1) + (lane >> 3), pch = 80 * gj + (tid < 80 ? tid : 0);
    u32x4 xv[10], gv[10];
    u16 rlre = 0, rlim = 0, rldt = 0;
    if (cw >= 2) {
        const u32x4* xr = (const u32x4*)(a.x + (size_t)gtok * 1280) + (lane & 7);
        const u32x4* gr = (const u32x4*)a.gw + (lane & 7);
#pragma unroll
        for (int i = 0; i < 10; ++i) { xv[i] = xr[8 * i]; gv[i] = gr[8 * i]; }
    } else { rlre = a.lre[pch]; rlim = a.lim[pch]; rldt = a.logdt[pch]; }
    __builtin_amdgcn_s_setprio(1);
    f32x4 acc[4][5], at[5][2];
#pragma unroll
    for (int n = 0; n < 5; ++n) {
#pragma unroll
        for (int i = 0; i < 4; ++i) acc[i][n] = (f32x4){0.f, 0.f, 0.f, 0.f};
        at[n][0] = at[n][1] = (f32x4){0.f, 0.f, 0.f, 0.f};
    }
    int xlo[2], xhi[2], wa[2], wq[2];
#pragma unroll
    for (int kk = 0; kk < 2; ++kk) {
        const int sw = ((4 * kk + q) ^ (c & 7)) << 4;
        xlo[kk] = (64 * mh + 32 * nh + c) * 128 + sw;
        xhi[kk] = (64 * mh + 32 * (1 - nh) + c) * 128 + sw;
        wa[kk] = 16384 + (80 * nh + 8 * (c & 1) + (c >> 1)) * 128 + (((4 * kk + q) ^ (c >> 1) ^ ((c & 1) << 2)) << 4);
        wq[kk] = 36864 + (8 * q + 4 * (kk ^ (q & 1)) + (c >> 2)) * 160 + 8 * (c & 3);
    }
    bf16x8 xa[2][4], wb[2][5], wt[2][5];
    auto rdp = [&](int buf, LAS char* s0, int kk) LAM {
        LAS char* q0 = s0 + wq[0] + kk * 5120; LAS char* q1 = s0 + wq[1] + kk * 5120;
        xa[buf][0] = lds_frag(s0 + xlo[kk]);
        xa[buf][1] = lds_frag(s0 + xlo[kk] + 2048);
        wt[buf][0] = lds_tr_frag(q0, q1);
        xa[buf][2] = lds_frag(s0 + xhi[kk]);
        xa[buf][3] = lds_frag(s0 + xhi[kk] + 2048);
        wt[buf][1] = lds_tr_frag(q0 + 32, q1 + 32);
        wb[buf][0] = lds_frag(s0 + wa[kk]);
        wb[buf][1] = lds_frag(s0 + wa[kk] + 2048);
        wt[buf][2] = lds_tr_frag(q0 + 64, q1 + 64);
        wb[buf][2] = lds_frag(s0 + wa[kk] + 4096);
        wb[buf][3] = lds_frag(s0 + wa[kk] + 6144);
        wt[buf][3] = lds_tr_frag(q0 + 96, q1 + 96);
        wb[buf][4] = lds_frag(s0 + wa[kk] + 8192);
        wt[buf][4] = lds_tr_frag(q0 + 128, q1 + 128);
    };
    auto rd = [&](int buf, int slot, int kk) LAM { rdp(buf, sm + slot * SLOT_A, kk); };
    auto mm_head = [&](int buf) LAM {
#pragma unroll
        for (int n = 0; n < HEAD_A / 4; ++n)
#pragma unroll
            for (int i = 0; i < 4; ++i) acc[i][n] = mfma(xa[buf][i], wb[buf][n], acc[i][n]);
    };
    auto mm_tail = [&](int buf) LAM {
#pragma unroll
        for (int n = HEAD_A / 4; n < 5; ++n)
#pragma unroll
            for (int i = 0; i < 4; ++i) acc[i][n] = mfma(xa[buf][i], wb[buf][n], acc[i][n]);
#pragma unroll
        for (int n = 0; n < 5; ++n)
#pragma unroll
            for (int i = 0; i < 2; ++i) at[n][i] = mfma(wt[buf][n], xa[buf][i], at[n][i]);
    };
    if (tid < L_WORDS) ((LAS unsigned*)(sm + L_FULL))[tid] = tid < 4 ? 1u : 0u;
#pragma unroll
    for (int n = 0; n < 5; ++n)
        asm volatile("" : "+v"(acc[0][n]), "+v"(acc[1][n]), "+v"(acc[2][n]), "+v"(acc[3][n]), "+v"(at[n][0]), "+v"(at[n][1]));
    asm volatile("" : "+v"(xlo[0]), "+v"(xlo[1]), "+v"(xhi[0]), "+v"(xhi[1]), "+v"(wa[0]), "+v"(wa[1]), "+v"(wq[0]), "+v"(wq[1]));
    const unsigned tag = mk_tag(nonce);
    if (cw >= 2) {
        float s0 = 0.f, s1 = 0.f;
#pragma unroll
        for (int i = 0; i < 10; ++i) {
            s0 = dot2(xv[i][0], gv[i][0], s0); s1 = dot2(xv[i][1], gv[i][1], s1);
            s0 = dot2(xv[i][2], gv[i][2], s0); s1 = dot2(xv[i][3], gv[i][3], s1);
        }
        float s = s0 + s1;
        s += lane_xor(s, 1); s += lane_xor(s, 2); s += lane_xor(s, 4);
        const float lg = bfr(s), g = bfr(1.f / (1.f + fexp(-lg)));
        if ((lane & 7) == 0) __hip_atomic_store((GAS u64*)(a.ws + WS_G) + gtok, granule(tag, __float_as_uint(g)), RLX_AGENT);
    } else if (tid < 80) {
        float ar, ai, cr, ci;
        channel_consts(rlre, rlim, rldt, ar, ai, cr, ci);
        float pr = ar, pi = ai;
#pragma unroll
        for (int k = 0; k < 4; ++k) { const float t = pr * pr - pi * pi; pi = 2.f * pr * pi; pr = t; }
        const float r16 = pr, i16 = pi;
#pragma unroll
        for (int k = 0; k < 3; ++k) { const float t = pr * pr - pi * pi; pi = 2.f * pr * pi; pr = t; }
        LAS f32x2* tab = (LAS f32x2*)(sm + A_TAB);
        tab[0 * 80 + tid] = (f32x2){ar, ar};
        tab[1 * 80 + tid] = (f32x2){-ai, ai};
        tab[2 * 80 + tid] = (f32x2){r16, r16};
        tab[3 * 80 + tid] = (f32x2){-i16, i16};
        tab[4 * 80 + tid] = (f32x2){pr, pr};
        tab[5 * 80 + tid] = (f32x2){-pi, pi};
    }
    if (tid < 160) __hip_atomic_store((GAS u64*)(a.ws + WS_EA) + (ti * 8 + gj) * 160 + tid, (u64)0, RLX_AGENT);
    STAMP(a.ws, cw, 1);
    lds_bar();
    STAMP(a.ws, cw, 2);
    STAMPC(a.ws, cw, 14);
    rd(0, 0, 0);
    int slot = 0;
    u64 t_spin = 0, n_spin = 0;
#pragma unroll 1
    for (int s = 0; s < 10; ++s) {
        wait_lgkm0();
        SCHED();
        rd(1, slot, 1);
        u32x4 fl = *(volatile LAS u32x4*)(sm + L_FULL);
        mm_head(0); mm_tail(0);
        interleave<30, 20, IL_A>();
        SCHED();
        wait_lgkm0();
        asm volatile("" ::: "memory");
        *(volatile LAS unsigned*)(sm + L_FREE + 4 * cw) = s + 1;
        slot = slot == 2 ? 0 : slot + 1;
        const unsigned need = s < 9 ? s + 2 : 0;
        unsigned landed = __builtin_amdgcn_readfirstlane(umin4(fl));
        LAS char* s0 = sm + slot * SLOT_A;
        asm volatile("" : "+s"(landed), "+v"(s0));
        mm_head(1);
        SCHED();
        if (__builtin_expect(landed < need, 0)) { TIMED(t_spin, ring_spin(sm + L_FULL, need)); n_spin++; }
        asm volatile("" ::: "memory");
        SCHED();
        rdp(0, s0, 0);
        mm_tail(1);
        interleave<30 - HEAD_A, 19, IL_A - HEAD_A + 1>();
        SCHED();
    }
    __builtin_amdgcn_s_setprio(0);
    asm volatile("s_nop 7\n\ts_nop 7" ::: "memory");
    STAMP(a.ws, cw, 3);
    STAMPC(a.ws, cw, 15);
    LAS float* tab = (LAS float*)(sm + A_TAB);
    float ca[5], cb[5];
#pragma unroll
    for (int n = 0; n < 5; ++n) { const int col = 80 * nh + 16 * n + c; ca[n] = tab[col]; cb[n] = tab[160 + col]; }
    {
        LAS float* es = (LAS float*)(sm + A_ESEG) + (4 * mh + q) * 160 + 80 * nh + c;
        if (nh == 0) a_pass1<0>(acc, ca, cb, es); else a_pass1<1>(acc, ca, cb, es);
    }
    WAIT_VM(0);
    STAMP(a.ws, cw, 4);
    lds_bar();
    bf16x8 P[2][3];
#pragma unroll
    for (int i = 0; i < 2; ++i) {
#pragma unroll
        for (int t = 0; t < 5; ++t)
#pragma unroll
            for (int r = 0; r < 4; ++r) at[t][i][r] = phi(at[t][i][r]);
        P[i][0] = pack_pair(at[0][i], at[1][i]);
        P[i][1] = pack_pair(at[2][i], at[3][i]);
        P[i][2] = as_frag((u32x4){pk2(at[4][i][0], at[4][i][1]), pk2(at[4][i][2], at[4][i][3]), 0u, 0u});
    }
    int adr[21];
    {
        int dh[5], db[5], eh[5], eb[5];
#pragma unroll
        for (int k = 0; k < 5; ++k) {
            const int d0 = 16 * k + 4 * q, e0 = 16 * k + c;
            dh[k] = d0 / 20; db[k] = A_TOT + dh[k] * 840 + (d0 - 20 * dh[k]) * 2;
            eh[k] = e0 / 20; eb[k] = (e0 - 20 * eh[k]) * 40;
        }
        auto ad = [&](int k, int E) LAM { return dh[k] == eh[E] ? db[k] + eb[E] : L_ZERO; };
        adr[0] = ad(0, 0); adr[1] = ad(1, 0);
        adr[2] = ad(0, 1); adr[3] = ad(1, 1); adr[4] = ad(2, 1); adr[5] = ad(3, 1);
        adr[6] = ad(1, 2); adr[7] = ad(2, 2); adr[8] = ad(3, 2);
        adr[9] = ad(2, 3); adr[10] = ad(3, 3); adr[11] = ad(4, 3);
        adr[12] = ad(3, 4); adr[13] = ad(4, 4);
#pragma unroll
        for (int k = 0; k < 5; ++k) adr[14 + k] = dh[k] == (c & 3) ? db[k] + 800 : L_ZERO;
        adr[19] = L_ZERO; adr[20] = A_G + 4 * (128 + 64 * mh + 16 * (c >> 2) + 8 * nh + (c & 3));
    }
#pragma unroll
    for (int i = 0; i < 2; ++i) asm volatile("" : "+v"(P[i][0]), "+v"(P[i][1]), "+v"(P[i][2]));
    asm volatile("" : "+v"(adr[0]), "+v"(adr[1]), "+v"(adr[2]), "+v"(adr[3]), "+v"(adr[4]), "+v"(adr[5]), "+v"(adr[6]),
                      "+v"(adr[7]), "+v"(adr[8]), "+v"(adr[9]), "+v"(adr[10]), "+v"(adr[11]), "+v"(adr[12]), "+v"(adr[13]));
    asm volatile("" : "+v"(adr[14]), "+v"(adr[15]), "+v"(adr[16]), "+v"(adr[17]), "+v"(adr[18]), "+v"(adr[20]));
    const __amdgpu_buffer_rsrc_t ract = rsrc(a.ws + WS_ACT, 128 * TS);
    u32x4 os[2][2]; u32x2 o8[2];
    STAMP(a.ws, cw, 5);
    word_wait(sm + L_TOT, 1u);
    STAMP(a.ws, cw, 6);
    {
        auto h4 = [&](int i) LAM -> u32x2 { return *(LAS u32x2*)(sm + adr[i]); };
        auto fr2 = [&](const u32x2& lo, const u32x2& hi) LAM { return as_frag((u32x4){lo[0], lo[1], hi[0], hi[1]}); };
        const u32x2 zz = {0u, 0u};
        const bf16x8 Z0 = fr2(h4(14), h4(15)), Z1 = fr2(h4(16), h4(17)), Z2 = fr2(h4(18), zz);
        const bf16x8 F0 = fr2(h4(0), h4(1)), F1 = fr2(h4(2), h4(3)), F2 = fr2(h4(4), h4(5));
        const bf16x8 F3 = fr2(zz, h4(6)), F4 = fr2(h4(7), h4(8)), F5 = fr2(h4(9), h4(10));
        const bf16x8 F6 = fr2(h4(11), zz), F7 = fr2(zz, h4(12)), F8 = fr2(h4(13), zz);
        const float ogv[2] = {*(LAS float*)(sm + adr[20]), *(LAS float*)(sm + adr[20] + 16)};
        const f32x4 z4 = {0.f, 0.f, 0.f, 0.f};
        const int uu = gj >> 1, odd = gj & 1;
#pragma unroll
        for (int i = 0; i < 2; ++i) {
            const f32x4 dn = mfma(Z2, P[i][2], mfma(Z1, P[i][1], mfma(Z0, P[i][0], z4)));
            const f32x4 n0 = mfma(F0, P[i][0], z4);
            const f32x4 n1 = mfma(F2, P[i][1], mfma(F1, P[i][0], z4));
            const f32x4 n2 = mfma(F4, P[i][1], mfma(F3, P[i][0], z4));
            const f32x4 n3 = mfma(F6, P[i][2], mfma(F5, P[i][1], z4));
            const f32x4 n4 = mfma(F8, P[i][2], mfma(F7, P[i][1], z4));
            const float og = ogv[i];
            const float r0 = og * __builtin_amdgcn_rcpf(dn[0] + 1e-6f), r1 = og * __builtin_amdgcn_rcpf(dn[1] + 1e-6f);
            const float r2 = og * __builtin_amdgcn_rcpf(dn[2] + 1e-6f), r3 = og * __builtin_amdgcn_rcpf(dn[3] + 1e-6f);
            const float s1 = q < 1 ? r0 : r1, s2 = q < 2 ? r1 : r2, s3 = q < 3 ? r2 : r3;
            os[i][0] = (u32x4){pk2(n0[0] * r0, n0[1] * r0), pk2(n0[2] * r0, n0[3] * r0), pk2(n1[0] * s1, n1[1] * s1), pk2(n1[2] * s1, n1[3] * s1)};
            os[i][1] = (u32x4){pk2(n2[0] * s2, n2[1] * s2), pk2(n2[2] * s2, n2[3] * s2), pk2(n3[0] * s3, n3[1] * s3), pk2(n3[2] * s3, n3[3] * s3)};
            o8[i] = (u32x2){pk2(n4[0] * r3, n4[1] * r3), pk2(n4[2] * r3, n4[3] * r3)};
        }
#pragma unroll
        for (int i = 0; i < 2; ++i) {
            const unsigned ob = (8 * ti + 4 * mh + 2 * nh + i) * TS + (40 + 5 * uu) * 1024;
            st16_wt(ract, lane * 16, ob + (odd ? 3072 : 0), os[i][0]);
            st16_wt(ract, lane * 16, ob + (odd ? 4096 : 1024), os[i][1]);
            st8_wt(ract, lane * 16 + 8 * odd, ob + 2048, o8[i]);
        }
    }
    STAMP(a.ws, cw, 7);
    word_wait(sm + L_HS, 3u);
    STAMP(a.ws, cw, 8);
    {
        const int qq = (lane & 15) >> 2, p = lane & 3;
        LAS float* hs = (LAS float*)(sm + A_HSEG) + (4 * mh + q) * 160 + 80 * nh + c;
        LAS float* gt = (LAS float*)(sm + A_G) + 64 * mh + 16 * q;
        LAS char* ht = sm + A_HT + (80 * nh + c) * HT_STRIDE + (64 * mh + 4 * q) * 2;
        LAS char* hr = sm + A_HT + (8 * q + qq) * HT_STRIDE + 128 * mh + 8 * p;
        const unsigned ob = (8 * ti + 4 * mh) * TS + 5 * gj * 1024;
        char* oflag = a.ws + WS_FACT + ti * 256 + 128 + gj * 8;
        if (nh == 0) a_pass2<0>(acc, ca, cb, hs, gt, ht, hr, ract, lane * 16, ob, sm, oflag, tag, lane);
        else a_pass2<1>(acc, ca, cb, hs, gt, ht, hr, ract, lane * 16, ob, sm, oflag, tag, lane);
        KEEP(os[0][0]); KEEP(os[0][1]); KEEP(os[1][0]); KEEP(os[1][1]);
        STAMP(a.ws, cw, 9);
        wait_lgkm0();
        if (lane == 0) word_add(sm + L_HT + 4 * mh);
        word_wait(sm + L_HT + 4 * mh, 2u);
        STAMP(a.ws, cw, 12);
        bf16x8 hf[2];
#pragma unroll
        for (int i = 0; i < 2; ++i) {
            LAS char* b = hr + 64 * HT_STRIDE + 32 * (2 * nh + i);
            hf[i] = lds_tr_frag(b, b + 4 * HT_STRIDE);
        }
#pragma unroll
        for (int i = 0; i < 2; ++i) st16_wt(ract, lane * 16, ob + (2 * nh + i) * TS + 2048, __builtin_bit_cast(u32x4, hf[i]));
    }
    wait_lgkm0();
    STAMP(a.ws, cw, 10);
    signal_stores(sm, L_DONE, a.ws + WS_FACT + ti * 256 + gj * 8, tag, lane, 4u);
    STAMP(a.ws, cw, 11);
    p2_final(a, sm, cw, lane, tag);
}

DEV void job_table(const Args& a, LAS char* tab, int jb, int lane) {
    if (jb < 400) {
        float ar, ai, cr, ci;
        const int p = 64 * (jb / 40) + lane;
        channel_consts(a.lre[p], a.lim[p], a.logdt[p], ar, ai, cr, ci);
        ((LAS f32x2*)tab)[lane] = (f32x2){2.f * cr, 2.f * ci};
    }
}
DEV void job_issue(const Args& a, LAS char* stg, int jb, int lane) {
    if (jb < 400) {
        const int cb = jb / 40, nt = jb - 40 * cb;
        const unsigned o = (16 * nt + (lane >> 3)) * 1280 + cb * 128 + (lane & 7) * 16;
        glds16(a.cre + o, stg); glds16(a.cre + o + 8 * 1280, stg + 1024);
        glds16(a.cim + o, stg + 2048); glds16(a.cim + o + 8 * 1280, stg + 3072);
    } else if (jb < 500) {
        const int jo = jb - 400, s5 = jo / 5, n8 = jo - 5 * s5, uu = s5 / 5, s = s5 - 5 * uu;
        const int lo = 160 * uu + (s == 0 ? 0 : s == 1 ? 32 : s == 2 ? 64 : s == 3 ? 80 : 112);
        const int hi = 160 * uu + (s == 0 ? 16 : s == 1 ? 48 : s == 2 ? 144 : s == 3 ? 96 : 128);
        const unsigned o = (lane >> 4) * 1280 + n8 * 256 + (lane & 15) * 16;
#pragma unroll
        for (int k = 0; k < 4; ++k) {
            glds16(a.wo + (lo + 4 * k) * 1280 + o, stg + 1024 * k);
            glds16(a.wo + (hi + 4 * k) * 1280 + o, stg + 4096 + 1024 * k);
        }
    }
}
DEV u32x2 lds_rd8(LAS char* p) { u32x2 v; asm volatile("ds_read_b64 %0, %1" : "=v"(v) : "v"((unsigned)(size_t)p) : "memory"); return v; }
DEV f32x4 lds_rd16(LAS char* p) { f32x4 v; asm volatile("ds_read_b128 %0, %1" : "=v"(v) : "v"((unsigned)(size_t)p) : "memory"); return v; }
DEV u32x2 lds_rdtr(LAS char* p) { u32x2 v; asm volatile("ds_read_b64_tr_b16 %0, %1" : "=v"(v) : "v"((unsigned)(size_t)p) : "memory"); return v; }
DEV void job_finish(const Args& a, LAS char* stg, LAS char* tab, int jb, int lane) {
    const int c = lane & 15, q = lane >> 4;
    const __amdgpu_buffer_rsrc_t rw = rsrc(a.ws + WS_W2T, 40 * TS);
    if (jb < 400) {
        const int cb = jb / 40, nt = jb - 40 * cb;
        u32x2 re[4], im[4]; f32x4 c01[4], c23[4];
#pragma unroll
        for (int k = 0; k < 4; ++k) {
            re[k] = lds_rd8(stg + c * 128 + 32 * k + 8 * q); im[k] = lds_rd8(stg + 2048 + c * 128 + 32 * k + 8 * q);
            c01[k] = lds_rd16(tab + (16 * k + 4 * q) * 8); c23[k] = lds_rd16(tab + (16 * k + 4 * q) * 8 + 16);
        }
        asm volatile("s_waitcnt lgkmcnt(0)" : "+v"(re[0]), "+v"(re[1]), "+v"(re[2]), "+v"(re[3]), "+v"(im[0]), "+v"(im[1]), "+v"(im[2]), "+v"(im[3]),
                     "+v"(c01[0]), "+v"(c01[1]), "+v"(c01[2]), "+v"(c01[3]), "+v"(c23[0]), "+v"(c23[1]), "+v"(c23[2]), "+v"(c23[3]) :: "memory");
        u32x4 w[4];
#pragma unroll
        for (int k = 0; k < 4; ++k) {
            const float r0 = bf_lo(re[k][0]), r1 = bf_hi(re[k][0]), r2 = bf_lo(re[k][1]), r3 = bf_hi(re[k][1]);
            const float i0 = bf_lo(im[k][0]), i1 = bf_hi(im[k][0]), i2 = bf_lo(im[k][1]), i3 = bf_hi(im[k][1]);
            w[k] = (u32x4){pk2(r0 * c01[k][0] - i0 * c01[k][1], -(r0 * c01[k][1] + i0 * c01[k][0])),
                           pk2(r1 * c01[k][2] - i1 * c01[k][3], -(r1 * c01[k][3] + i1 * c01[k][2])),
                           pk2(r2 * c23[k][0] - i2 * c23[k][1], -(r2 * c23[k][1] + i2 * c23[k][0])),
                           pk2(r3 * c23[k][2] - i3 * c23[k][3], -(r3 * c23[k][3] + i3 * c23[k][2]))};
        }
#pragma unroll
        for (int k = 0; k < 4; ++k) st16_wt(rw, lane * 16, nt * TS + (4 * cb + k) * 1024, w[k]);
    } else if (jb < 500) {
        const int jo = jb - 400, s5 = jo / 5, n8 = jo - 5 * s5;
        u32x2 lo[8], hi[8];
#pragma unroll
        for (int k = 0; k < 8; ++k) {
            LAS char* b = stg + (4 * q + (c >> 2)) * 256 + 32 * k + 8 * (c & 3);
            lo[k] = lds_rdtr(b); hi[k] = lds_rdtr(b + 4096);
        }
        asm volatile("s_waitcnt lgkmcnt(0)" : "+v"(lo[0]), "+v"(lo[1]), "+v"(lo[2]), "+v"(lo[3]), "+v"(lo[4]), "+v"(lo[5]), "+v"(lo[6]), "+v"(lo[7]),
                     "+v"(hi[0]), "+v"(hi[1]), "+v"(hi[2]), "+v"(hi[3]), "+v"(hi[4]), "+v"(hi[5]), "+v"(hi[6]), "+v"(hi[7]) :: "memory");
#pragma unroll
        for (int k = 0; k < 8; ++k)
            st16_wt(rw, lane * 16, (8 * n8 + k) * TS + (40 + s5) * 1024, (u32x4){lo[k][0], lo[k][1], hi[k][0], hi[k][1]});
    }
}

DEV void b_loader(const Args& a, LAS char* sm, int ti, int gj, int lw, int lane) {
    STAMP(a.ws, lw + 4, 0);
    const unsigned nonce = __hip_atomic_load((GAS unsigned*)(a.ws + WS_NONCE), RLX_AGENT);
    unsigned off[9];
    {
        const int pos = lane & 7, rl = lane >> 3;
#pragma unroll
        for (int u = 0; u < 4; ++u) { const int R = 8 * (lw + 4 * u) + rl; off[u] = R * 1280 + ((pos ^ (R & 7)) << 4); }
#pragma unroll
        for (int u = 4; u < 9; ++u) {
            const int wi = lw + 4 * (u - 4), sec = wi >= 10, ch = 64 * (wi - 10 * sec) + lane, rho = ch / 10, cc = ch - 10 * rho;
            off[u] = (rho ^ (((rho >> 3) & 1) << 2)) * 3840 + cc * 16 + 1280 + 1280 * sec;
        }
    }
    const char* xb = a.x + (size_t)ti * (128 * 1280);
    const char* wb = a.wqkv + 160 * gj;
    auto issue_a = [&](int s) LAM {
        LAS char* dst = sm + (s % NS_B) * SLOT_B + lw * 1024;
#pragma unroll
        for (int u = 0; u < 4; ++u) glds16x(xb + 128 * s + off[u], dst + 4096 * u);
        glds16w(wb + (size_t)(64 * 3840) * s + off[4], dst + 4096 * 4);
    };
    auto issue_b = [&](int s) LAM {
        LAS char* dst = sm + (s % NS_B) * SLOT_B + lw * 1024;
#pragma unroll
        for (int u = 5; u < 9; ++u) glds16w(wb + (size_t)(64 * 3840) * s + off[u], dst + 4096 * u);
    };
    STAMP(a.ws, lw + 4, 1);
    const int jb = (ti * 8 + gj) * 4 + lw;
    LAS char* stg = sm + B_STG + lw * 8192;
    issue_a(0); issue_b(0);
    if (JOBMODE == 0) job_issue(a, stg, jb, lane);
    issue_a(1); issue_b(1); issue_a(2); issue_b(2);
    WAIT_VM(18);
    bar();
    if (JOBMODE == 0) job_finish(a, stg, sm + B_TAB + lw * 512, jb, lane);
#pragma unroll 1
    for (int s = 0; s < 7; ++s) {
        if (s > 0) ring_wait(sm + L_FREE, s);
        issue_a(s + 3);
        if (JOBMODE == 0 && s < 2 && jb < 400) WAIT_VM(18); else if (JOBMODE == 0 && s < 2 && jb < 500) WAIT_VM(22); else WAIT_VM(14);
        ring_put(sm + L_FULL + 4 * lw, s + 2);
        issue_b(s + 3);
    }
    WAIT_VM(9);
    ring_put(sm + L_FULL + 4 * lw, 9);
    WAIT_VM(0);
    ring_put(sm + L_FULL + 4 * lw, 10);
    const unsigned tag = mk_tag(nonce);
    if (JOBMODE == 0) signal_stores(sm, L_DONE2, a.ws + WS_FW2T + (ti * 8 + gj) * 8, tag, lane, 4u, NREP);
    STAMP(a.ws, lw + 4, 2);
    if (JOBMODE == 2) {
        ring_wait(sm + L_FREE, 8);
        job_issue(a, stg, jb, lane);
        WAIT_VM(0);
        job_finish(a, stg, sm + B_TAB + lw * 512, jb, lane);
        STAMP(a.ws, lw + 4, 6);
        signal_stores(sm, L_DONE2, a.ws + WS_FW2T + (ti * 8 + gj) * 8, tag, lane, 4u, NREP);
    }
    __builtin_amdgcn_s_setprio(2);
    const int tl = lw * 64 + lane;
    {
        int o0[2], o1[2];
#pragma unroll
        for (int it = 0; it < 2; ++it) {
            const int g = 2 * (tl + 256 * it < 420 ? tl + 256 * it : 0);
            const int h = g / 210, rem = g - 210 * h, e = rem / 10, d = 2 * (rem - 10 * e);
            const bool kv = rem < 200;
            o0[it] = kv ? B_KVT + ((20 * h + e) * 84 + 20 * h + d) * 4 : B_Z + (20 * h + 2 * (rem - 200)) * 4;
            o1[it] = o0[it] + (kv ? 80 * 84 * 4 : 320);
        }
        asm volatile("" : "+v"(o0[0]), "+v"(o0[1]), "+v"(o1[0]), "+v"(o1[1]));
        const __amdgpu_buffer_rsrc_t rk = rsrc(a.ws + WS_KV, 16 * 8 * 840 * 8);
        word_wait(sm + L_KV, 4u);
        STAMP(a.ws, lw + 4, 3);
        u32x4 pv[2];
#pragma unroll
        for (int it = 0; it < 2; ++it) {
            const f32x4 v = *(LAS f32x4*)(sm + o0[it]) + *(LAS f32x4*)(sm + o1[it]);
            pv[it] = (u32x4){pk2(v[0], v[1]), tag, pk2(v[2], v[3]), tag};
        }
        asm volatile("" : "+v"(pv[0]), "+v"(pv[1]) :: "memory");
        if (lane == 0) word_add(sm + L_PUB);
        st16_wt(rk, tl * 16, (ti * 8 + gj) * 6720, pv[0]);
        if (tl + 256 < 420) st16_wt(rk, (tl + 256) * 16, (ti * 8 + gj) * 6720, pv[1]);
        asm volatile("s_nop 1" ::"v"(pv[0]), "v"(pv[1]));
    }
    STAMP(a.ws, lw + 4, 4);
    if (lw == 0) {
        const int cnt = ti < 8 ? 53 : 52, g0 = ti < 8 ? 53 * ti : 424 + 52 * (ti - 8);
        const int g = g0 + (lane < cnt ? lane : 0);
        GAS u64* src = (GAS u64*)(a.ws + WS_KV) + gj * 840 + g;
        unsigned v[16];
        for (unsigned spins = 0;;) {
            bool ok = true;
#pragma unroll
            for (int i = 0; i < 16; ++i) {
                const u64 x = __hip_atomic_load(src + i * (8 * 840), RLX_AGENT);
                v[i] = (unsigned)x; ok &= (unsigned)(x >> 32) == tag;
            }
            if (wave_all(ok) || ++spins > SPIN_CAP) break;
            __builtin_amdgcn_s_sleep(1);
        }
        float s0 = 0.f, s1 = 0.f;
#pragma unroll
        for (int i = 0; i < 16; ++i) { s0 += bf_lo(v[i]); s1 += bf_hi(v[i]); }
        if (lane < cnt) __hip_atomic_store((GAS u64*)(a.ws + WS_TOT) + gj * 840 + g, granule(tag, pk2(s0, s1)), RLX_AGENT);
        word_set(sm + L_H2, 1u);
    }
    STAMP(a.ws, lw + 4, 5);
    __builtin_amdgcn_s_setprio(0);
    bf16x8 fw[P2_PW][5];
    word_wait(sm + L_PUB, 4u);
    p2_prefetch(a, sm, lw, lane, tag, fw);
    p2_main(a, sm, lw, lane, nonce, fw);
}

DEV void b_consumer(const Args& a, LAS char* sm, int ti, int gj, int cw, int lane) {
    const int mh = cw >> 1, nh = cw & 1, c = lane & 15, q = lane >> 4, tid = cw * 64 + lane;
    STAMP(a.ws, cw, 0);
    const unsigned tag = mk_tag(__hip_atomic_load((GAS unsigned*)(a.ws + WS_NONCE), RLX_AGENT));
    if (JOBMODE != 1) job_table(a, sm + B_TAB + cw * 512, (ti * 8 + gj) * 4 + cw, lane);
    {
        const __amdgpu_buffer_rsrc_t rk = rsrc(a.ws + WS_KV, 16 * 8 * 840 * 8);
        const u32x4 z = {0u, 0u, 0u, 0u};
        st16_wt(rk, tid * 16, (ti * 8 + gj) * 6720, z);
        if (tid + 256 < 420) st16_wt(rk, (tid + 256) * 16, (ti * 8 + gj) * 6720, z);
        const int cnt = ti < 8 ? 53 : 52, g0 = ti < 8 ? 53 * ti : 424 + 52 * (ti - 8);
        if (tid < cnt) __hip_atomic_store((GAS u64*)(a.ws + WS_TOT) + gj * 840 + g0 + tid, (u64)0, RLX_AGENT);
    }
    __builtin_amdgcn_s_setprio(1);
    f32x4 an[5][4];
#pragma unroll
    for (int i = 0; i < 4; ++i)
#pragma unroll
        for (int t = 0; t < 5; ++t) an[t][i] = (f32x4){0.f, 0.f, 0.f, 0.f};
    int xo[2], wA[2], wB[2];
#pragma unroll
    for (int kk = 0; kk < 2; ++kk) {
        xo[kk] = (64 * mh + c) * 128 + (((4 * kk + q) ^ (c & 7)) << 4);
        const int b = 16384 + (8 * q + 4 * (kk ^ (q & 1)) + (c >> 2)) * 160 + 8 * (c & 3);
        wA[kk] = b + 96 * nh; wB[kk] = b + 64 + 10240 * nh;
    }
    bf16x8 xa[2][4], wb[2][5];
    auto rdp = [&](int buf, LAS char* s0, int kk) LAM {
#pragma unroll
        for (int i = 0; i < 4; ++i) xa[buf][i] = lds_frag(s0 + xo[kk] + 2048 * i);
        LAS char* w0 = s0 + kk * 5120;
        wb[buf][0] = lds_tr_frag(w0 + wA[0], w0 + wA[1]);
        wb[buf][1] = lds_tr_frag(w0 + wA[0] + 32, w0 + wA[1] + 32);
        wb[buf][2] = lds_tr_frag(w0 + wB[0], w0 + wB[1]);
        wb[buf][3] = lds_tr_frag(w0 + wA[0] + 10240, w0 + wA[1] + 10240);
        wb[buf][4] = lds_tr_frag(w0 + wA[0] + 10272, w0 + wA[1] + 10272);
    };
    auto rd = [&](int buf, int slot, int kk) LAM { rdp(buf, sm + slot * SLOT_B, kk); };
    auto mm_head = [&](int buf) LAM {
#pragma unroll
        for (int i = 0; i < 4; ++i) an[0][i] = mfma(xa[buf][i], wb[buf][0], an[0][i]);
    };
    auto mm_tail = [&](int buf) LAM {
#pragma unroll
        for (int t = 1; t < 5; ++t)
#pragma unroll
            for (int i = 0; i < 4; ++i) an[t][i] = mfma(xa[buf][i], wb[buf][t], an[t][i]);
    };
    if (tid < L_WORDS) ((LAS unsigned*)(sm + L_FULL))[tid] = tid < 4 ? 1u : 0u;
#pragma unroll
    for (int t = 0; t < 5; ++t)
        asm volatile("" : "+v"(an[t][0]), "+v"(an[t][1]), "+v"(an[t][2]), "+v"(an[t][3]));
    STAMP(a.ws, cw, 1);
    lds_bar();
    STAMP(a.ws, cw, 2);
    rd(0, 0, 0);
#pragma unroll 1
    for (int s = 0; s < 10; ++s) {
        wait_lgkm0();
        SCHED();
        rd(1, s & 3, 1);
        u32x4 fl = *(volatile LAS u32x4*)(sm + L_FULL);
        mm_head(0); mm_tail(0);
        interleave<20, 15, IL_B>();
        SCHED();
        wait_lgkm0();
        asm volatile("" ::: "memory");
        *(volatile LAS unsigned*)(sm + L_FREE + 4 * cw) = s + 1;
        const unsigned need = s < 9 ? s + 2 : 0;
        unsigned landed = __builtin_amdgcn_readfirstlane(umin4(fl));
        LAS char* s0 = sm + ((s + 1) & 3) * SLOT_B;
        asm volatile("" : "+s"(landed), "+v"(s0));
        mm_head(1);
        SCHED();
        if (__builtin_expect(landed < need, 0)) ring_spin(sm + L_FULL, need);
        asm volatile("" ::: "memory");
        SCHED();
        rdp(0, s0, 0);
        mm_tail(1);
        interleave<16, 14, 14>();
        SCHED();
    }
    STAMP(a.ws, cw, 3);
    bf16x8 fr[5][2];
#pragma unroll
    for (int t = 0; t < 2; ++t)
#pragma unroll
        for (int i = 0; i < 4; ++i)
#pragma unroll
            for (int r = 0; r < 4; ++r) an[t][i][r] = phi(an[t][i][r]);
    if (nh == 0) {
#pragma unroll
        for (int i = 0; i < 4; ++i)
#pragma unroll
            for (int r = 0; r < 4; ++r) an[2][i][r] = phi(an[2][i][r]);
    }
#pragma unroll
    for (int t = 0; t < 5; ++t) { fr[t][0] = pack_pair(an[t][0], an[t][1]); fr[t][1] = pack_pair(an[t][2], an[t][3]); }
    {
        LAS char* xs = sm + B_X1 + cw * 2048 + lane * 16;
        *(LAS bf16x8*)xs = fr[2][0];
        *(LAS bf16x8*)(xs + 1024) = fr[2][1];
    }
    wait_lgkm0();
    word_set(sm + L_X1 + 4 * cw, 1u);
    STAMP(a.ws, cw, 4);
    word_wait(sm + L_X1 + 4 * (cw ^ 1), 1u);
    ring_spin(sm + L_FREE, 10);
    asm volatile("" ::: "memory");
    STAMP(a.ws, cw, 6);
    {
        LAS char* xr = sm + B_X1 + (cw ^ 1) * 2048 + lane * 16;
        const bf16x8 R0 = *(LAS bf16x8*)xr, R1 = *(LAS bf16x8*)(xr + 1024);
        const bf16x8 ones = as_frag((u32x4){0x3f803f80u, 0x3f803f80u, 0x3f803f80u, 0x3f803f80u});
        const f32x4 z4 = {0.f, 0.f, 0.f, 0.f};
        LAS float* kvt = (LAS float*)(sm + B_KVT) + mh * (80 * 84) + c * 84 + 4 * q;
        LAS float* zz = (LAS float*)(sm + B_Z) + mh * 80 + 4 * q;
        auto prod = [&](const bf16x8& k0, const bf16x8& k1, const bf16x8& v0, const bf16x8& v1, int ta, int tb) LAM {
            *(LAS f32x4*)(kvt + 16 * tb * 84 + 16 * ta) = mfma(k1, v1, mfma(k0, v0, z4));
        };
        auto zsum = [&](const bf16x8& k0, const bf16x8& k1, int ta) LAM {
            const f32x4 d = mfma(k1, ones, mfma(k0, ones, z4));
            if (c == 0) *(LAS f32x4*)(zz + 16 * ta) = d;
        };
        const int b0 = 3 * nh;
        prod(fr[0][0], fr[0][1], fr[3][0], fr[3][1], b0, b0);
        prod(fr[0][0], fr[0][1], fr[4][0], fr[4][1], b0, b0 + 1);
        prod(fr[1][0], fr[1][1], fr[3][0], fr[3][1], b0 + 1, b0);
        prod(fr[1][0], fr[1][1], fr[4][0], fr[4][1], b0 + 1, b0 + 1);
        zsum(fr[0][0], fr[0][1], b0);
        zsum(fr[1][0], fr[1][1], b0 + 1);
        if (nh == 0) {
            prod(fr[2][0], fr[2][1], fr[4][0], fr[4][1], 2, 1);
            prod(fr[1][0], fr[1][1], R0, R1, 1, 2);
            prod(fr[2][0], fr[2][1], R0, R1, 2, 2);
            zsum(fr[2][0], fr[2][1], 2);
        } else {
            prod(fr[0][0], fr[0][1], fr[2][0], fr[2][1], 3, 2);
            prod(R0, R1, fr[3][0], fr[3][1], 2, 3);
        }
    }
    STAMP(a.ws, cw, 7);
    WAIT_VM(0);
    wait_lgkm0();
    if (lane == 0) word_add(sm + L_KV);
    __builtin_amdgcn_s_setprio(0);
    STAMP(a.ws, cw, 5);
    if (JOBMODE == 1) {
        const int jb = (ti * 8 + gj) * 4 + cw;
        LAS char* stg = sm + cw * 8192;
        job_table(a, sm + B_TAB + cw * 512, jb, lane);
        word_wait(sm + L_H2, 1u);
        job_issue(a, stg, jb, lane);
        WAIT_VM(0);
        wait_lgkm0();
        job_finish(a, stg, sm + B_TAB + cw * 512, jb, lane);
        STAMP(a.ws, cw, 6);
        signal_stores(sm, L_DONE, a.ws + WS_FW2T + (ti * 8 + gj) * 8, tag, lane, 4u, NREP);
        STAMP(a.ws, cw, 7);
    } else if (lane == 0) word_add(sm + L_DONE);
    p2_final(a, sm, cw, lane, tag);
}

}

__global__ __launch_bounds__(512, 2) NOPK void gm_fused(const void* x, const void* lre, const void* lim, const void* bre,
                                                   const void* bim, const void* cre, const void* cim, const void* logdt,
                                                   const void* dskip, const void* wqkv, const void* wo, const void* gw,
                                                   void* ws, void* out) {
    __shared__ __attribute__((aligned(1024))) char smem[LDS_BYTES];
    LAS char* sm = (LAS char*)smem;
#define MAPMODE 1
    const int b = blockIdx.x, xcd = b & 7, idx = b >> 3;
    const bool roleA = MAPMODE == 1 ? idx < 16 : MAPMODE == 2 ? idx >= 16 : xcd < 4;
    const int ti = MAPMODE != 0 ? 4 * (xcd >> 1) + (idx & 3) : 8 * ((xcd & 3) >> 1) + (idx & 7);
    const int gj = MAPMODE != 0 ? 4 * (xcd & 1) + ((idx >> 2) & 3) : 4 * (xcd & 1) + (idx >> 3);
    const int wave = __builtin_amdgcn_readfirstlane(threadIdx.x >> 6), lane = threadIdx.x & 63;
    const Args a{(const char*)x, (const u16*)lre, (const u16*)lim, (const char*)bre, (const char*)bim, (const char*)cre,
                 (const char*)cim, (const u16*)logdt, (const char*)wqkv, (const char*)wo, (const u16*)gw,
                 (const u16*)dskip, (char*)ws, (float*)out};
    STAMPL(ws, 0);
    if (roleA) {
        if (wave < 4) a_consumer(a, sm, ti, gj, wave, lane);
        else a_loader(a, sm, ti, gj, wave - 4, lane);
    } else {
        if (wave < 4) b_consumer(a, sm, ti, gj, wave, lane);
        else b_loader(a, sm, ti, gj, wave - 4, lane);
    }
    STAMPL(ws, 1);
}

extern "C" void kernel_launch(void* const* d_in, const int* in_sizes, int n_in, void* d_out, int out_size, void* d_ws,
                              size_t ws_size, hipStream_t stream) {
    gm_fused<<<256, 512, 0, stream>>>(d_in[0], d_in[1], d_in[2], d_in[3], d_in[4], d_in[5], d_in[6], d_in[7], d_in[8],
                                      d_in[10], d_in[11], d_in[12], d_ws, d_out);
}
```

```cpp
#include <hip/hip_runtime.h>
#include <hip/hip_bf16.h>
#include <stdint.h>

namespace {

typedef __attribute__((ext_vector_type(8))) short bf16x8;
typedef __attribute__((ext_vector_type(4))) short s16x4;
typedef __attribute__((ext_vector_type(4))) float f32x4;
typedef __attribute__((ext_vector_type(2))) float f32x2;
typedef __attribute__((ext_vector_type(2))) __bf16 bf16x2_t;
typedef __attribute__((ext_vector_type(4))) unsigned u32x4;
typedef __attribute__((ext_vector_type(2))) unsigned u32x2;
typedef unsigned long long u64;
typedef unsigned short u16;
#define LAS __attribute__((address_space(3)))
#define GAS __attribute__((address_space(1)))
#if defined(__HIP_DEVICE_COMPILE__)
#define NOPK __attribute__((target("no-packed-fp32-ops")))
#else
#define NOPK
#endif
#define DEV __device__ __forceinline__
#define LAM __attribute__((always_inline))
#define RLX_AGENT __ATOMIC_RELAXED, __HIP_MEMORY_SCOPE_AGENT

constexpr size_t WS_NONCE = 0;
constexpr size_t WS_EA    = 4096;
constexpr size_t WS_KV    = WS_EA + 16 * 8 * 160 * 8;
constexpr size_t WS_TOT   = WS_KV + 16 * 8 * 840 * 8;
constexpr size_t WS_G     = WS_TOT + 8 * 840 * 8;
constexpr size_t WS_FACT  = WS_G + 2048 * 8;
constexpr size_t WS_FW2T  = WS_FACT + 16 * 256;
constexpr int NREP = 16;
constexpr size_t WS_W2T   = (WS_FW2T + NREP * 1024 + 1023) / 1024 * 1024;
constexpr size_t TS = 61440;
constexpr size_t WS_ACT   = WS_W2T + 40 * TS;
constexpr size_t WS_PROF  = 32u << 20;

#define STAMP(ws, wave, i) do {} while (0)
#define STAMPC(ws, wave, i) do {} while (0)
#define TIMED(acc, stmt) do { stmt; } while (0)
#define STAMPV(ws, wave, i, v) do {} while (0)
#define STAMP2(ws, wave, i) do {} while (0)

#define STAMPL(ws, i) do {} while (0)

constexpr int LDS_BYTES = 163840;
constexpr int SLOT_A = 36864, NS_A = 4;
constexpr int A_STG  = 110592;
constexpr int A_TAB  = 147456;
constexpr int A_JTAB = A_TAB + 3840;
constexpr int A_G    = A_JTAB + 2048;
constexpr int A_SEG  = A_G + 512;
constexpr int A_HT = 47104, HT_STRIDE = 272;
static_assert(A_SEG + 5120 <= 159776, "lds");
constexpr int SLOT_B = 47104, NS_B = 3;
constexpr int B_KVT = 0;
constexpr int B_Z   = B_KVT + 2 * 80 * 84 * 4;
constexpr int B_X1  = 141312;
constexpr int B_G   = B_X1 + 8192;
constexpr int B_TOT = B_G + 512;
static_assert(B_TOT + 3360 <= 159776, "lds");

#define SLP_W2T 16
#define SLP_ACT 4
#define SLP_LB 2
#define SLP_TOT 2
#define IL_A 16
#define IL_B 27
constexpr unsigned SPIN_CAP = 1u << 18;

DEV unsigned pk2(float a, float b) {
    return __builtin_bit_cast(unsigned, __builtin_convertvector((f32x2){a, b}, bf16x2_t));
}
DEV float bfr(float a) { return __uint_as_float(pk2(a, 0.f) << 16); }
DEV float bf_lo(unsigned u) { return __uint_as_float(u << 16); }
DEV float bf_hi(unsigned u) { return __uint_as_float(u & 0xffff0000u); }
DEV float bf2f(u16 u) { return __uint_as_float((unsigned)u << 16); }
DEV float swp(float x) {
    return __builtin_bit_cast(float, __builtin_amdgcn_mov_dpp(__builtin_bit_cast(int, x), 0xB1, 0xF, 0xF, true));
}
DEV float cmul_add(float a, float bs, float e, float u) { return fmaf(bs, swp(e), fmaf(a, e, u)); }
DEV f32x4 mfma(bf16x8 a, bf16x8 b, f32x4 c) { return __builtin_amdgcn_mfma_f32_16x16x32_bf16(a, b, c, 0, 0, 0); }
DEV bf16x8 as_frag(u32x4 v) { return __builtin_bit_cast(bf16x8, v); }
DEV bf16x8 lds_frag(LAS char* p) { return *(LAS bf16x8*)p; }
DEV bf16x8 lds_tr_frag(LAS char* p0, LAS char* p1) {
    s16x4 lo = __builtin_amdgcn_ds_read_tr16_b64_v4i16((LAS s16x4*)p0);
    s16x4 hi = __builtin_amdgcn_ds_read_tr16_b64_v4i16((LAS s16x4*)p1);
    return __builtin_shufflevector(lo, hi, 0, 1, 2, 3, 4, 5, 6, 7);
}
DEV void glds16_wt(const char* g, unsigned l) {
    unsigned keep;
    asm volatile("s_mov_b32 %0, m0\n\ts_mov_b32 m0, %2\n\ts_nop 0\n\tglobal_load_lds_dwordx4 %1, off sc1\n\ts_mov_b32 m0, %0"
                 : "=&s"(keep) : "v"(g), "s"(l) : "memory");
}
DEV void glds16(const char* g, LAS char* l) {
    __builtin_amdgcn_global_load_lds((const GAS unsigned*)g, (LAS unsigned*)l, 16, 0, 0);
}
DEV void bar() { asm volatile("" ::: "memory"); __builtin_amdgcn_s_barrier(); asm volatile("" ::: "memory"); }
DEV void wait_lgkm0() { __builtin_amdgcn_s_waitcnt(0xC07F); }
DEV void lds_bar() { asm volatile("" ::: "memory"); wait_lgkm0(); bar(); }
constexpr int L_FULL = 159776, L_FREE = L_FULL + 16;
constexpr int L_DONE = L_FULL + 32;
constexpr int L_KV = L_FULL + 36;
constexpr int L_W2T = L_FULL + 40;
constexpr int L_ACTH = L_FULL + 44, L_ACTO = L_FULL + 76;
constexpr int L_X1 = L_FULL + 48, L_ZERO = L_FULL + 64;
constexpr int L_DONE2 = L_FULL + 72;
constexpr int L_WR = L_FULL + 84;
constexpr int L_TOT = L_FULL + 88;
constexpr int L_HS = L_FULL + 96, L_HT = L_FULL + 100;
constexpr int L_PUB = L_FULL + 108;
constexpr int L_WORDS = 28;
DEV unsigned umin4(const u32x4& f) { const unsigned a = f[0] < f[1] ? f[0] : f[1], b = f[2] < f[3] ? f[2] : f[3]; return a < b ? a : b; }
DEV void ring_put(LAS char* p, unsigned v) {
    asm volatile("ds_write_b32 %0, %1" ::"v"((unsigned)(size_t)p), "v"(v) : "memory");
}
DEV void ring_wait(LAS char* p, unsigned need) {
    for (unsigned spins = 0; spins < SPIN_CAP; ++spins) {
        u32x4 f;
        asm volatile("ds_read_b128 %0, %1\n\ts_waitcnt lgkmcnt(0)" : "=&v"(f) : "v"((unsigned)(size_t)p) : "memory");
        if ((unsigned)__builtin_amdgcn_readfirstlane(umin4(f)) >= need) break;
        __builtin_amdgcn_s_sleep(1);
    }
}
DEV void word_wait(LAS char* p, unsigned need) {
    for (unsigned spins = 0; spins < SPIN_CAP; ++spins) {
        if ((unsigned)__builtin_amdgcn_readfirstlane(*(volatile LAS unsigned*)p) >= need) break;
        __builtin_amdgcn_s_sleep(1);
    }
    asm volatile("" ::: "memory");
}
DEV void word_set(LAS char* p, unsigned v) { asm volatile("" ::: "memory"); *(volatile LAS unsigned*)p = v; }
DEV unsigned word_add(LAS char* p) {
    return __hip_atomic_fetch_add((LAS unsigned*)p, 1u, __ATOMIC_RELAXED, __HIP_MEMORY_SCOPE_WORKGROUP);
}
DEV void ring_spin(LAS char* p, unsigned need) {
    for (unsigned spins = 0; spins < SPIN_CAP; ++spins) {
        const u32x4 f = *(volatile LAS u32x4*)p;
        if ((unsigned)__builtin_amdgcn_readfirstlane(umin4(f)) >= need) break;
    }
}
#define WAIT_VM(n) asm volatile("s_waitcnt vmcnt(" #n ")" ::: "memory")
#define SCHED() __builtin_amdgcn_sched_barrier(0)
#define KEEP(x) asm volatile("" ::"v"(x))
template <int NM, int NR, int NS> DEV void interleave() {
#pragma unroll
    for (int i = 0; i < NS; ++i) {
        __builtin_amdgcn_sched_group_barrier(0x8, 1, 0);
        if ((i + 1) * NR / NS != i * NR / NS) __builtin_amdgcn_sched_group_barrier(0x100, 1, 0);
    }
    __builtin_amdgcn_sched_group_barrier(0x8, NM - NS, 0);
}
DEV bool wave_all(bool p) { return __builtin_amdgcn_ballot_w64(p) == __builtin_amdgcn_ballot_w64(true); }
DEV float lane_xor(float v, int m) {
    const int x = __builtin_bit_cast(int, v);
    const int y = m == 1 ? __builtin_amdgcn_mov_dpp(x, 0xB1, 0xF, 0xF, true) : m == 2 ? __builtin_amdgcn_mov_dpp(x, 0x4E, 0xF, 0xF, true)
                                                                              : __builtin_amdgcn_ds_swizzle(x, 0x101F);
    return __builtin_bit_cast(float, y);
}
DEV float fexp(float x) { return __builtin_amdgcn_exp2f(x * 1.44269504088896341f); }
DEV unsigned mk_tag(unsigned n) { return ((n + 1u) * 0x9E3779B1u) | 1u; }
DEV u64 granule(unsigned tag, unsigned v) { return ((u64)tag << 32) | v; }
DEV u64 flag_of(unsigned tag) { return granule(tag, tag * 0x85EBCA6Bu + 0x5BD1E995u); }
DEV __amdgpu_buffer_rsrc_t rsrc(const char* p, unsigned bytes) { return __builtin_amdgcn_make_buffer_rsrc((void*)p, 0, bytes, 0x00020000); }
DEV void st16_wt(const __amdgpu_buffer_rsrc_t& r, unsigned voff, unsigned soff, u32x4 v) { __builtin_amdgcn_raw_buffer_store_b128(v, r, voff + soff, 0, 16); }
DEV void st8_wt(const __amdgpu_buffer_rsrc_t& r, unsigned voff, unsigned soff, u32x2 v) { __builtin_amdgcn_raw_buffer_store_b64(v, r, voff + soff, 0, 16); }
DEV bf16x8 ld16_wt(const __amdgpu_buffer_rsrc_t& r, unsigned voff, unsigned soff) {
    return __builtin_bit_cast(bf16x8, __builtin_amdgcn_raw_buffer_load_b128(r, voff, soff, 16));
}
DEV void signal_stores(LAS char* sm, int word, char* flag, unsigned tag, int lane, unsigned waves, int replicas = 1) {
    asm volatile("s_waitcnt vmcnt(0)" ::: "memory");
    unsigned old = 0;
    if (lane == 0) old = __hip_atomic_fetch_add((LAS unsigned*)(sm + word), 1u, __ATOMIC_RELAXED, __HIP_MEMORY_SCOPE_WORKGROUP);
    old = __builtin_amdgcn_readfirstlane(old);
    if (old == waves - 1u && lane < replicas) __hip_atomic_store((GAS u64*)(flag + lane * 1024), flag_of(tag), RLX_AGENT);
}
DEV float phi(float t) {
    const float e = __builtin_amdgcn_fmed3f(__builtin_amdgcn_exp2f(t * 1.44269504088896341f), 0.f, 1.f);
    return (e + 1e-6f) + __builtin_amdgcn_fmed3f(t, 0.f, __builtin_inff());
}
DEV bf16x8 pack_pair(const f32x4& lo, const f32x4& hi) {
    return as_frag((u32x4){pk2(lo[0], lo[1]), pk2(lo[2], lo[3]), pk2(hi[0], hi[1]), pk2(hi[2], hi[3])});
}

DEV void scan_step(float& u0, float& u1, float& u2, float& u3, float& u4, const float (&e)[5], const float (&a)[5],
                   const float (&b)[5]) {
    asm("v_fmac_f32 %0, %10, %5\n\tv_fmac_f32 %1, %11, %6\n\tv_fmac_f32 %2, %12, %7\n\t"
        "v_fmac_f32 %3, %13, %8\n\tv_fmac_f32 %4, %14, %9\n\t"
        "v_fmac_f32_dpp %0, %5, %15 quad_perm:[1,0,3,2] row_mask:0xf bank_mask:0xf bound_ctrl:1\n\t"
        "v_fmac_f32_dpp %1, %6, %16 quad_perm:[1,0,3,2] row_mask:0xf bank_mask:0xf bound_ctrl:1\n\t"
        "v_fmac_f32_dpp %2, %7, %17 quad_perm:[1,0,3,2] row_mask:0xf bank_mask:0xf bound_ctrl:1\n\t"
        "v_fmac_f32_dpp %3, %8, %18 quad_perm:[1,0,3,2] row_mask:0xf bank_mask:0xf bound_ctrl:1\n\t"
        "v_fmac_f32_dpp %4, %9, %19 quad_perm:[1,0,3,2] row_mask:0xf bank_mask:0xf bound_ctrl:1"
        : "+v"(u0), "+v"(u1), "+v"(u2), "+v"(u3), "+v"(u4)
        : "v"(e[0]), "v"(e[1]), "v"(e[2]), "v"(e[3]), "v"(e[4]), "v"(a[0]), "v"(a[1]), "v"(a[2]), "v"(a[3]), "v"(a[4]),
          "v"(b[0]), "v"(b[1]), "v"(b[2]), "v"(b[3]), "v"(b[4]));
}

DEV void scan_step_nd(float (&t)[5], float u0, float u1, float u2, float u3, float u4, const float (&e)[5],
                      const float (&a)[5], const float (&b)[5]) {
    asm("v_fma_f32 %0, %15, %10, %5\n\tv_fma_f32 %1, %16, %11, %6\n\tv_fma_f32 %2, %17, %12, %7\n\t"
        "v_fma_f32 %3, %18, %13, %8\n\tv_fma_f32 %4, %19, %14, %9\n\t"
        "v_fmac_f32_dpp %0, %10, %20 quad_perm:[1,0,3,2] row_mask:0xf bank_mask:0xf bound_ctrl:1\n\t"
        "v_fmac_f32_dpp %1, %11, %21 quad_perm:[1,0,3,2] row_mask:0xf bank_mask:0xf bound_ctrl:1\n\t"
        "v_fmac_f32_dpp %2, %12, %22 quad_perm:[1,0,3,2] row_mask:0xf bank_mask:0xf bound_ctrl:1\n\t"
        "v_fmac_f32_dpp %3, %13, %23 quad_perm:[1,0,3,2] row_mask:0xf bank_mask:0xf bound_ctrl:1\n\t"
        "v_fmac_f32_dpp %4, %14, %24 quad_perm:[1,0,3,2] row_mask:0xf bank_mask:0xf bound_ctrl:1"
        : "=&v"(t[0]), "=&v"(t[1]), "=&v"(t[2]), "=&v"(t[3]), "=&v"(t[4])
        : "v"(u0), "v"(u1), "v"(u2), "v"(u3), "v"(u4), "v"(e[0]), "v"(e[1]), "v"(e[2]), "v"(e[3]), "v"(e[4]),
          "v"(a[0]), "v"(a[1]), "v"(a[2]), "v"(a[3]), "v"(a[4]), "v"(b[0]), "v"(b[1]), "v"(b[2]), "v"(b[3]), "v"(b[4]));
}

struct Args {
    const char* x; const u16* lre; const u16* lim; const char* bre; const char* bim; const char* cre; const char* cim;
    const u16* logdt; const char* wqkv; const char* wo; const u16* gw; const u16* dskip; char* ws; float* out;
};

DEV void channel_consts(u16 lre, u16 lim, u16 logdt, float& ar, float& ai, float& cr, float& ci) {
    const float lr = bf2f(lre), li = bf2f(lim);
    const float dt = bfr(fexp(bf2f(logdt)));
    const float zr = dt * lr, zi = dt * li, ex = fexp(zr);
    const float rv = zi * 0.15915494309189535f, rr = rv - __builtin_rintf(rv);
    ar = ex * __builtin_amdgcn_cosf(rr); ai = ex * __builtin_amdgcn_sinf(rr);
    const float nr = ar - 1.f, ni = ai, dn = lr * lr + li * li;
    cr = (nr * lr + ni * li) / dn; ci = (ni * lr - nr * li) / dn;
}
DEV float dot2(unsigned x, unsigned y, float s) {
    return __builtin_amdgcn_fdot2_f32_bf16(__builtin_bit_cast(bf16x2_t, x), __builtin_bit_cast(bf16x2_t, y), s, false);
}

constexpr int P2_RED = 0;
constexpr int P2_PW = 4, P2_PA = 5;
DEV bool flags_up(const u32x4& v, u64 fl) {
    const unsigned f0 = (unsigned)fl, f1 = (unsigned)(fl >> 32);
    return wave_all((v[0] == f0) & (v[1] == f1) & (v[2] == f0) & (v[3] == f1));
}
DEV void wait_flags8(const Args& a, int ti, int part, u64 fl) {
    GAS u64* fp = (GAS u64*)(a.ws + WS_FACT) + ti * 32 + part * 16 + (threadIdx.x & 7);
    for (unsigned spins = 0; spins < SPIN_CAP; ++spins) {
        if (wave_all(__hip_atomic_load(fp, RLX_AGENT) == fl)) break;
        __builtin_amdgcn_s_sleep(SLP_ACT);
    }
}
DEV unsigned p2_koff(int lw, int st) { return (st < 10 ? 10 * lw + st : 30 + 5 * lw + st) * 1024; }
DEV int p2_unit(int lw, int i) { return 5120 * (7 * lw + i); }
DEV void p2_prefetch(const Args& a, LAS char* sm, int lw, int lane, unsigned tag, int poller, bf16x8 (&fw)[P2_PW][5]) {
    const int b = blockIdx.x, xcd = b & 7, idx = b >> 3;
    const int cg = (xcd & 1) * 4 + (idx >> 3);
    if (lw == poller) {
        const __amdgpu_buffer_rsrc_t rf = rsrc(a.ws + WS_FW2T + (xcd * 2 + (b >> 7)) * 1024, 1024);
        for (unsigned spins = 0; spins < SPIN_CAP; ++spins) {
            if (flags_up(__builtin_amdgcn_raw_buffer_load_b128(rf, lane * 16, 0, 16), flag_of(tag))) break;
            __builtin_amdgcn_s_sleep(SLP_W2T);
        }
        word_set(sm + L_W2T, 1u);
    } else word_wait(sm + L_W2T, 1u);
    STAMP2(a.ws, lw + 4, 0);
    const unsigned wbase = 5 * cg * TS, vo = lane * 16;
    const char* src = a.ws + WS_W2T + wbase + vo;
#pragma unroll
    for (int i = 0; i < 7; ++i)
#pragma unroll
        for (int n = 0; n < 5; ++n)
            glds16_wt(src + (size_t)n * TS + p2_koff(lw, 4 + i), (unsigned)(size_t)(sm + p2_unit(lw, i) + n * 1024));
    const __amdgpu_buffer_rsrc_t rw = rsrc(a.ws + WS_W2T, 40 * TS);
#pragma unroll
    for (int st = 0; st < P2_PW; ++st)
#pragma unroll
        for (int n = 0; n < 5; ++n) fw[st][n] = ld16_wt(rw, vo, wbase + n * TS + p2_koff(lw, st));
}
DEV void p2_main(const Args& a, LAS char* sm, int lw, int lane, unsigned nonce, int poller, bf16x8 (&fw)[P2_PW][5]) {
    const int b = blockIdx.x, xcd = b & 7, idx = b >> 3;
    const int tt = (xcd >> 1) * 8 + (idx & 7), cg = (xcd & 1) * 4 + (idx >> 3);
    const int c = lane & 15, q = lane >> 4;
    const unsigned tag = mk_tag(nonce);
    const u64 fl = flag_of(tag);
    constexpr int PA = P2_PA;
    const __amdgpu_buffer_rsrc_t rw = rsrc(a.ws + WS_W2T, 40 * TS), ra = rsrc(a.ws + WS_ACT, 128 * TS);
    const unsigned wbase = 5 * cg * TS, abase = 4 * tt * TS, vo = lane * 16;
    bf16x8 fa[PA][4];
    auto ldw = [&](int st, int sl) LAM {
#pragma unroll
        for (int n = 0; n < 5; ++n) fw[sl][n] = ld16_wt(rw, vo, wbase + n * TS + p2_koff(lw, st));
    };
    auto lda = [&](int st) LAM {
#pragma unroll
        for (int t = 0; t < 4; ++t) fa[st % PA][t] = ld16_wt(ra, vo, abase + t * TS + p2_koff(lw, st));
    };
    WAIT_VM(0);
    if (lw == poller) { wait_flags8(a, tt >> 1, 0, fl); word_set(sm + L_ACTH, 1u); }
    else word_wait(sm + L_ACTH, 1u);
    STAMP2(a.ws, lw + 4, 1);
#pragma unroll
    for (int st = 0; st < 3; ++st) lda(st);
    f32x4 acc[5][4];
    const f32x4 z4 = {0.f, 0.f, 0.f, 0.f};
    auto slot = [&](int st) LAM { return st < 4 ? st : st <= 10 ? (st & 1) : st == 11 ? 2 : st == 12 ? 3 : st == 13 ? 1 : 0; };
#pragma unroll
    for (int st = 0; st < 15; ++st) {
        if (st <= 4) lda(st + 3);
        if (st == 6) word_wait(sm + L_ACTO, 1u);
        if (st >= 4 && st + 4 < 15) lda(st + 4);
        SCHED();
        if (st >= 3 && st < 10) {
#pragma unroll
            for (int n = 0; n < 5; ++n) fw[slot(st + 1)][n] = lds_frag(sm + p2_unit(lw, st - 3) + vo + n * 1024);
        }
#pragma unroll
        for (int n = 0; n < 5; ++n)
#pragma unroll
            for (int t = 0; t < 4; ++t) acc[n][t] = mfma(fw[slot(st)][n], fa[st % PA][t], st == 0 ? z4 : acc[n][t]);
        SCHED();
        if (st == 7) ldw(11, slot(11));
        if (st == 9) { wait_lgkm0(); if (lane == 0) word_add(sm + L_WR); ldw(12, slot(12)); }
        if (st == 10) ldw(13, slot(13));
        if (st == 11) ldw(14, slot(14));
    }
    STAMP2(a.ws, lw + 4, 2);
    word_wait(sm + L_WR, 4u);
    word_wait(sm + L_DONE, 4u);
    LAS float* rw_ = (LAS float*)(sm + P2_RED) + lw * (64 * 84);
#pragma unroll
    for (int t = 0; t < 4; ++t)
#pragma unroll
        for (int n = 0; n < 5; ++n) *(LAS f32x4*)(rw_ + (16 * t + c) * 84 + 16 * n + 4 * q) = acc[n][t];
    lds_bar();
    STAMP2(a.ws, lw + 4, 3);
}

DEV void p2_final(const Args& a, LAS char* sm, int cw, int lane, unsigned nonce) {
    const unsigned tag = mk_tag(nonce);
    const int b = blockIdx.x, xcd = b & 7, idx = b >> 3;
    const int tt = (xcd >> 1) * 8 + (idx & 7), cg = (xcd & 1) * 4 + (idx >> 3);
    const int tid = cw * 64 + lane;
    u32x2 xv[5], dv[5]; float gv[5];
    int tok[5], n0[5];
#pragma unroll
    for (int i = 0; i < 5; ++i) {
        const int id = tid + 256 * i, r = id / 20, c4 = id - 20 * r;
        tok[i] = 64 * tt + 16 * ((r & 15) >> 2) + 4 * (r >> 4) + (r & 3); n0[i] = 80 * cg + 4 * c4;
        xv[i] = *(const u32x2*)((const u16*)a.x + tok[i] * 640 + n0[i]);
        dv[i] = *(const u32x2*)(a.dskip + n0[i]);
    }
    for (unsigned spins = 0; spins < SPIN_CAP; ++spins) {
        u64 v[5];
#pragma unroll
        for (int i = 0; i < 5; ++i) v[i] = __hip_atomic_load((GAS u64*)(a.ws + WS_G) + tok[i], RLX_AGENT);
        bool ok = true;
#pragma unroll
        for (int i = 0; i < 5; ++i) { gv[i] = __uint_as_float((unsigned)v[i]); ok &= (unsigned)(v[i] >> 32) == tag; }
        if (wave_all(ok)) break;
        __builtin_amdgcn_s_sleep(2);
    }
    f32x4 sk[5];
#pragma unroll
    for (int i = 0; i < 5; ++i) {
        const unsigned p01 = pk2(bf_lo(xv[i][0]) * bf_lo(dv[i][0]), bf_hi(xv[i][0]) * bf_hi(dv[i][0]));
        const unsigned p23 = pk2(bf_lo(xv[i][1]) * bf_lo(dv[i][1]), bf_hi(xv[i][1]) * bf_hi(dv[i][1]));
        sk[i] = (f32x4){gv[i] * bf_lo(p01), gv[i] * bf_hi(p01), gv[i] * bf_lo(p23), gv[i] * bf_hi(p23)};
    }
    asm volatile("" : "+v"(sk[0]), "+v"(sk[1]), "+v"(sk[2]), "+v"(sk[3]), "+v"(sk[4]));
    if (cw == 0) {
        wait_flags8(a, tt >> 1, 1, flag_of(tag));
        word_set(sm + L_ACTO, 1u);
        if (tt == 31 && cg == 0) {
            word_wait(sm + L_ACTH, 1u);
            if (lane == 0) __hip_atomic_store((GAS unsigned*)(a.ws + WS_NONCE), nonce + 1u, RLX_AGENT);
        }
    }
    STAMP2(a.ws, cw, 0);
    bar();
    STAMP2(a.ws, cw, 2);
#pragma unroll
    for (int i = 0; i < 5; ++i) {
        const int id = tid + 256 * i, r = id / 20, c4 = id - 20 * r;
        LAS float* p = (LAS float*)(sm + P2_RED) + r * 84 + 4 * c4;
        sk[i] += (*(LAS f32x4*)p + *(LAS f32x4*)(p + 64 * 84)) + (*(LAS f32x4*)(p + 128 * 84) + *(LAS f32x4*)(p + 192 * 84));
    }
    asm volatile("" : "+v"(sk[0]), "+v"(sk[1]), "+v"(sk[2]), "+v"(sk[3]), "+v"(sk[4]));
#define OUT_AUX 16
    {
        const __amdgpu_buffer_rsrc_t ro = rsrc((const char*)a.out, 2048 * 640 * 4);
#pragma unroll
        for (int i = 0; i < 5; ++i)
            __builtin_amdgcn_raw_buffer_store_b128(__builtin_bit_cast(u32x4, sk[i]), ro, (tok[i] * 640 + n0[i]) * 4, 0, OUT_AUX);
    }
    STAMP2(a.ws, cw, 3);
}

DEV void job_issue(const Args& a, LAS char* stg, int jb, int lane) {
    if (jb < 400) {
        const int cb = jb / 40, nt = jb - 40 * cb;
        const unsigned o = (16 * nt + (lane >> 3)) * 1280 + cb * 128 + (lane & 7) * 16;
        glds16(a.cre + o, stg); glds16(a.cre + o + 8 * 1280, stg + 1024);
        glds16(a.cim + o, stg + 2048); glds16(a.cim + o + 8 * 1280, stg + 3072);
    } else if (jb < 500) {
        const int jo = jb - 400, s5 = jo / 5, n8 = jo - 5 * s5, uu = s5 / 5, s = s5 - 5 * uu;
        const int lo = 160 * uu + (s == 0 ? 0 : s == 1 ? 32 : s == 2 ? 64 : s == 3 ? 80 : 112);
        const int hi = 160 * uu + (s == 0 ? 16 : s == 1 ? 48 : s == 2 ? 144 : s == 3 ? 96 : 128);
        const unsigned o = (lane >> 4) * 1280 + n8 * 256 + (lane & 15) * 16;
#pragma unroll
        for (int k = 0; k < 4; ++k) {
            glds16(a.wo + (lo + 4 * k) * 1280 + o, stg + 1024 * k);
            glds16(a.wo + (hi + 4 * k) * 1280 + o, stg + 4096 + 1024 * k);
        }
    }
}
DEV u32x2 lds_rd8(LAS char* p) { u32x2 v; asm volatile("ds_read_b64 %0, %1" : "=v"(v) : "v"((unsigned)(size_t)p) : "memory"); return v; }
DEV f32x4 lds_rd16(LAS char* p) { f32x4 v; asm volatile("ds_read_b128 %0, %1" : "=v"(v) : "v"((unsigned)(size_t)p) : "memory"); return v; }
DEV u32x2 lds_rdtr(LAS char* p) { u32x2 v; asm volatile("ds_read_b64_tr_b16 %0, %1" : "=v"(v) : "v"((unsigned)(size_t)p) : "memory"); return v; }
DEV void job_finish(const Args& a, LAS char* stg, LAS char* tab, int jb, int lane) {
    const int c = lane & 15, q = lane >> 4;
    const __amdgpu_buffer_rsrc_t rw = rsrc(a.ws + WS_W2T, 40 * TS);
    if (jb < 400) {
        const int cb = jb / 40, nt = jb - 40 * cb;
        u32x2 re[4], im[4]; f32x4 c01[4], c23[4];
#pragma unroll
        for (int k = 0; k < 4; ++k) {
            re[k] = lds_rd8(stg + c * 128 + 32 * k + 8 * q); im[k] = lds_rd8(stg + 2048 + c * 128 + 32 * k + 8 * q);
            c01[k] = lds_rd16(tab + (16 * k + 4 * q) * 8); c23[k] = lds_rd16(tab + (16 * k + 4 * q) * 8 + 16);
        }
        asm volatile("s_waitcnt lgkmcnt(0)" : "+v"(re[0]), "+v"(re[1]), "+v"(re[2]), "+v"(re[3]), "+v"(im[0]), "+v"(im[1]), "+v"(im[2]), "+v"(im[3]),
                     "+v"(c01[0]), "+v"(c01[1]), "+v"(c01[2]), "+v"(c01[3]), "+v"(c23[0]), "+v"(c23[1]), "+v"(c23[2]), "+v"(c23[3]) :: "memory");
        u32x4 w[4];
#pragma unroll
        for (int k = 0; k < 4; ++k) {
            const float r0 = bf_lo(re[k][0]), r1 = bf_hi(re[k][0]), r2 = bf_lo(re[k][1]), r3 = bf_hi(re[k][1]);
            const float i0 = bf_lo(im[k][0]), i1 = bf_hi(im[k][0]), i2 = bf_lo(im[k][1]), i3 = bf_hi(im[k][1]);
            w[k] = (u32x4){pk2(r0 * c01[k][0] - i0 * c01[k][1], -(r0 * c01[k][1] + i0 * c01[k][0])),
                           pk2(r1 * c01[k][2] - i1 * c01[k][3], -(r1 * c01[k][3] + i1 * c01[k][2])),
                           pk2(r2 * c23[k][0] - i2 * c23[k][1], -(r2 * c23[k][1] + i2 * c23[k][0])),
                           pk2(r3 * c23[k][2] - i3 * c23[k][3], -(r3 * c23[k][3] + i3 * c23[k][2]))};
        }
#pragma unroll
        for (int k = 0; k < 4; ++k) st16_wt(rw, lane * 16, nt * TS + (4 * cb + k) * 1024, w[k]);
    } else if (jb < 500) {
        const int jo = jb - 400, s5 = jo / 5, n8 = jo - 5 * s5;
        u32x2 lo[8], hi[8];
#pragma unroll
        for (int k = 0; k < 8; ++k) {
            LAS char* b = stg + (4 * q + (c >> 2)) * 256 + 32 * k + 8 * (c & 3);
            lo[k] = lds_rdtr(b); hi[k] = lds_rdtr(b + 4096);
        }
        asm volatile("s_waitcnt lgkmcnt(0)" : "+v"(lo[0]), "+v"(lo[1]), "+v"(lo[2]), "+v"(lo[3]), "+v"(lo[4]), "+v"(lo[5]), "+v"(lo[6]), "+v"(lo[7]),
                     "+v"(hi[0]), "+v"(hi[1]), "+v"(hi[2]), "+v"(hi[3]), "+v"(hi[4]), "+v"(hi[5]), "+v"(hi[6]), "+v"(hi[7]) :: "memory");
#pragma unroll
        for (int k = 0; k < 8; ++k)
            st16_wt(rw, lane * 16, (8 * n8 + k) * TS + (40 + s5) * 1024, (u32x4){lo[k][0], lo[k][1], hi[k][0], hi[k][1]});
    }
}

#define TOUCH 1
#define TOUCH_S0 5
constexpr int L_JUNK = 160000;
DEV void touch_streams(const Args& a, LAS char* sm, int cw, int lane) {
    const int b = blockIdx.x, xcd = b & 7, tq = xcd >> 1, gh = xcd & 1;
#pragma unroll
    for (int r = 0; r < 2; ++r) {
        const int e = (b >> 3) * 4 + cw + 128 * r, s = TOUCH_S0 + e / 33, k = e - 33 * (e / 33);
        if (s < 10) {
            const char* p;
            if (k < 8) p = a.x + (size_t)((4 * tq + (k >> 1)) * 128 + 64 * (k & 1) + lane) * 1280 + 128 * s;
            else if (k < 18) {
                const int li = 64 * (k - 8) + lane, g = li / 160, rem = li - 160 * g;
                p = (rem < 80 ? a.bre : a.bim) + (size_t)(80 * (4 * gh + g) + (rem < 80 ? rem : rem - 80)) * 1280 + 128 * s;
            } else {
                const int li = 64 * (k - 18) + lane, kr = li / 15, rem = li - 15 * kr, sec = rem / 5;
                p = a.wqkv + (size_t)(64 * s + kr) * 3840 + sec * 1280 + 640 * gh + 128 * (rem - 5 * sec);
            }
            unsigned keep;
            asm volatile("s_mov_b32 %0, m0\n\ts_mov_b32 m0, %2\n\ts_nop 0\n\tglobal_load_lds_dword %1, off\n\ts_mov_b32 m0, %0"
                         : "=&s"(keep) : "v"(p), "s"((unsigned)(size_t)(sm + L_JUNK)) : "memory");
        }
    }
}
DEV float gate_of(const Args& a, int tok, unsigned tag) {
    u64 v = 0;
    for (unsigned spins = 0; spins < SPIN_CAP; ++spins) {
        v = __hip_atomic_load((GAS u64*)(a.ws + WS_G) + tok, RLX_AGENT);
        if (wave_all((unsigned)(v >> 32) == tag)) break;
        __builtin_amdgcn_s_sleep(1);
    }
    return __uint_as_float((unsigned)v);
}
DEV int x_token(int R) { const int rho = R & 15; return (R & 64) + 16 * (rho >> 2) + 4 * ((R >> 4) & 3) + (rho & 3); }

DEV void a_loader(const Args& a, LAS char* sm, int ti, int gj, int lw, int lane) {
    STAMP(a.ws, lw + 4, 0);
    const unsigned nonce = __hip_atomic_load((GAS unsigned*)(a.ws + WS_NONCE), RLX_AGENT);
    unsigned off[9];
    {
        const int pos = lane & 7, rl = lane >> 3, im = lw & 1;
#pragma unroll
        for (int u = 0; u < 4; ++u) {
            const int R = 8 * (lw + 4 * u) + rl;
            off[u] = x_token(R) * 1280 + ((pos ^ (R & 7)) << 4);
        }
#pragma unroll
        for (int u = 4; u < 9; ++u) off[u] = (8 * ((lw + 4 * (u - 4)) >> 1) + rl) * 1280 + ((pos ^ rl ^ (im << 2)) << 4);
    }
    const char* xb = a.x + (size_t)ti * (128 * 1280);
    const char* bb = ((lw & 1) ? a.bim : a.bre) + (size_t)gj * (80 * 1280);
    auto issue_a = [&](int s) LAM {
        LAS char* dst = sm + (s % NS_A) * SLOT_A + lw * 1024;
#pragma unroll
        for (int u = 0; u < 4; ++u) glds16(xb + 128 * s + off[u], dst + 4096 * u);
        glds16(bb + 128 * s + off[4], dst + 4096 * 4);
    };
    auto issue_b = [&](int s) LAM {
        LAS char* dst = sm + (s % NS_A) * SLOT_A + lw * 1024;
#pragma unroll
        for (int u = 5; u < 9; ++u) glds16(bb + 128 * s + off[u], dst + 4096 * u);
    };
    STAMP(a.ws, lw + 4, 1);
    issue_a(0); issue_b(0); issue_a(1); issue_b(1); issue_a(2); issue_b(2);
    WAIT_VM(18);
    bar();
#pragma unroll 1
    for (int s = 0; s < 7; ++s) {
        if (s > 0) ring_wait(sm + L_FREE, s);
        issue_a(s + 3);
        WAIT_VM(14);
        ring_put(sm + L_FULL + 4 * lw, s + 2);
        issue_b(s + 3);
    }
    WAIT_VM(9);
    ring_put(sm + L_FULL + 4 * lw, 9);
    WAIT_VM(0);
    ring_put(sm + L_FULL + 4 * lw, 10);
    STAMP(a.ws, lw + 4, 2);
    const unsigned tag = mk_tag(nonce);
    const int tl = lw * 64 + lane;
    const int jb = (ti * 8 + gj) * 4 + lw;
    ring_wait(sm + L_FREE, 8);
    job_issue(a, sm + A_STG + lw * 8192, jb, lane);
    if (tl < 128) ((LAS float*)(sm + A_G))[tl] = gate_of(a, 128 * ti + tl, tag);
    WAIT_VM(0);
    job_finish(a, sm + A_STG + lw * 8192, sm + A_JTAB + lw * 512, jb, lane);
    STAMP(a.ws, lw + 4, 9);
    __builtin_amdgcn_s_setprio(2);
    lds_bar();
    STAMP(a.ws, lw + 4, 3);
    if (lw < 3) {
        const int col = tl < 160 ? tl : 159;
        LAS float* tab = (LAS float*)(sm + A_TAB);
        GAS u64* ea = (GAS u64*)(a.ws + WS_EA) + gj * 160;
        const float a16 = tab[2 * 160 + col], b16 = tab[3 * 160 + col], a128 = tab[4 * 160 + col], b128 = tab[5 * 160 + col];
        LAS float* sg = (LAS float*)(sm + A_SEG);
        float pre[8], P = 0.f;
#pragma unroll
        for (int s = 0; s < 8; ++s) { pre[s] = P; P = cmul_add(a16, b16, P, sg[s * 160 + col]); }
        if (tl < 160) __hip_atomic_store(ea + ti * 1280 + col, granule(tag, __float_as_uint(P)), RLX_AGENT);
        STAMP(a.ws, lw + 4, 4);
        signal_stores(sm, L_DONE2, a.ws + WS_FW2T + (ti * 8 + gj) * 8, tag, lane, 4u, NREP);
        float H = 0.f;
        if (ti > 0) {
            float ev[15];
            for (unsigned spins = 0; spins < SPIN_CAP; ++spins) {
                u64 v[15];
#pragma unroll
                for (int i = 0; i < 15; ++i) v[i] = __hip_atomic_load(ea + (i < ti ? i : 0) * 1280 + col, RLX_AGENT);
                bool ok = true;
#pragma unroll
                for (int i = 0; i < 15; ++i) { ev[i] = __uint_as_float((unsigned)v[i]); ok &= (unsigned)(v[i] >> 32) == tag; }
                if (wave_all(ok)) break;
                __builtin_amdgcn_s_sleep(SLP_LB);
            }
#pragma unroll
            for (int i = 0; i < 15; ++i) if (i < ti) H = cmul_add(a128, b128, H, ev[i]);
        }
        STAMP(a.ws, lw + 4, 6);
#pragma unroll
        for (int s = 0; s < 8; ++s) { if (tl < 160) sg[s * 160 + col] = H + pre[s]; H = cmul_add(a16, b16, H, 0.f); }
        wait_lgkm0();
        if (lane == 0) word_add(sm + L_HS);
    } else signal_stores(sm, L_DONE2, a.ws + WS_FW2T + (ti * 8 + gj) * 8, tag, lane, 4u, NREP);
    __builtin_amdgcn_s_setprio(0);
    STAMP(a.ws, lw + 4, 8);
    word_wait(sm + L_DONE, 4u);
    bf16x8 fw[P2_PW][5];
    p2_prefetch(a, sm, lw, lane, tag, 3, fw);
    p2_main(a, sm, lw, lane, nonce, 3, fw);
}

DEV void a_pass1(f32x4 (&acc)[4][5], const float (&ca)[5], const float (&cb)[5], LAS float* es) {
    float e[5];
#pragma unroll
    for (int n = 0; n < 5; ++n) e[n] = acc[0][n][0];
#pragma unroll
    for (int t = 1; t < 16; ++t) {
        const int i = t >> 2, r = t & 3;
        float y[5];
        scan_step_nd(y, acc[i][0][r], acc[i][1][r], acc[i][2][r], acc[i][3][r], acc[i][4][r], e, ca, cb);
#pragma unroll
        for (int n = 0; n < 5; ++n) e[n] = y[n];
    }
#pragma unroll
    for (int n = 0; n < 5; ++n) es[16 * n] = e[n];
}
DEV void a_pass2(f32x4 (&acc)[4][5], const float (&ca)[5], const float (&cb)[5], LAS float* hs, LAS float* gt, LAS char* ht,
                 LAS char* hr, const __amdgpu_buffer_rsrc_t& ract, unsigned vo, unsigned ob) {
    float e[5];
#pragma unroll
    for (int n = 0; n < 5; ++n) e[n] = hs[16 * n];
    asm volatile("s_nop 1" : "+v"(e[0]), "+v"(e[1]), "+v"(e[2]), "+v"(e[3]), "+v"(e[4]));
    bf16x8 hf[4][2];
    auto rd = [&](int m) LAM {
#pragma unroll
        for (int k = 0; k < 2; ++k) {
            LAS char* b = hr + 32 * k * HT_STRIDE + 32 * m;
            hf[m][k] = lds_tr_frag(b, b + 4 * HT_STRIDE);
        }
    };
    auto st = [&](int m) LAM {
#pragma unroll
        for (int k = 0; k < 2; ++k) st16_wt(ract, vo, ob + m * TS + k * 1024, __builtin_bit_cast(u32x4, hf[m][k]));
    };
#pragma unroll
    for (int m = 0; m < 4; ++m) {
        const f32x4 g = *(LAS f32x4*)(gt + 4 * m);
#pragma unroll
        for (int r = 0; r < 4; ++r) {
            float u0 = acc[m][0][r], u1 = acc[m][1][r], u2 = acc[m][2][r], u3 = acc[m][3][r], u4 = acc[m][4][r];
            scan_step(u0, u1, u2, u3, u4, e, ca, cb);
            e[0] = u0; e[1] = u1; e[2] = u2; e[3] = u3; e[4] = u4;
            acc[m][0][r] = u0 * g[r]; acc[m][1][r] = u1 * g[r]; acc[m][2][r] = u2 * g[r]; acc[m][3][r] = u3 * g[r]; acc[m][4][r] = u4 * g[r];
        }
        SCHED();
        if (m >= 2) st(m - 2);
#pragma unroll
        for (int n = 0; n < 5; ++n)
            *(LAS u32x2*)(ht + 16 * n * HT_STRIDE + 32 * m) = (u32x2){pk2(acc[m][n][0], acc[m][n][1]), pk2(acc[m][n][2], acc[m][n][3])};
        rd(m);
        SCHED();
    }
    st(2); st(3);
#pragma unroll
    for (int m = 0; m < 4; ++m) { KEEP(hf[m][0]); KEEP(hf[m][1]); }
}

DEV void a_consumer(const Args& a, LAS char* sm, int ti, int gj, int cw, int lane) {
    const int mh = cw >> 1, nh = cw & 1, c = lane & 15, q = lane >> 4, tid = cw * 64 + lane;
    STAMP(a.ws, cw, 0);
    const unsigned nonce = __hip_atomic_load((GAS unsigned*)(a.ws + WS_NONCE), RLX_AGENT);
    const int jb = (ti * 8 + gj) * 4 + cw, pj = (jb < 400 ? 64 * (jb / 40) : 0) + lane;
    const u16 jlre = a.lre[pj], jlim = a.lim[pj], jldt = a.logdt[pj];
    const int gtok = 128 * ti + 16 * gj + 8 * (cw & 1) + (lane >> 3), pch = 80 * gj + (tid < 80 ? tid : 0);
    u32x4 xv[10], gv[10];
    u16 rlre = 0, rlim = 0, rldt = 0;
    if (cw >= 2) {
        const u32x4* xr = (const u32x4*)(a.x + (size_t)gtok * 1280) + (lane & 7);
        const u32x4* gr = (const u32x4*)a.gw + (lane & 7);
#pragma unroll
        for (int i = 0; i < 10; ++i) { xv[i] = xr[8 * i]; gv[i] = gr[8 * i]; }
    } else { rlre = a.lre[pch]; rlim = a.lim[pch]; rldt = a.logdt[pch]; }
    __builtin_amdgcn_s_setprio(1);
    f32x4 acc[4][5];
#pragma unroll
    for (int n = 0; n < 5; ++n)
#pragma unroll
        for (int m = 0; m < 4; ++m) acc[m][n] = (f32x4){0.f, 0.f, 0.f, 0.f};
    int xo[2], wa[2];
#pragma unroll
    for (int kk = 0; kk < 2; ++kk) {
        xo[kk] = (64 * mh + c) * 128 + (((4 * kk + q) ^ (c & 7)) << 4);
        wa[kk] = 16384 + (80 * nh + 8 * (c & 1) + (c >> 1)) * 128 + (((4 * kk + q) ^ (c >> 1) ^ ((c & 1) << 2)) << 4);
    }
    bf16x8 xa[2][4], wb[2][5];
    auto rdp = [&](int buf, LAS char* s0, int kk) LAM {
#pragma unroll
        for (int m = 0; m < 4; ++m) xa[buf][m] = lds_frag(s0 + xo[kk] + 2048 * m);
#pragma unroll
        for (int n = 0; n < 5; ++n) wb[buf][n] = lds_frag(s0 + wa[kk] + 2048 * n);
    };
    auto mm_head = [&](int buf) LAM {
#pragma unroll
        for (int m = 0; m < 4; ++m) acc[m][0] = mfma(xa[buf][m], wb[buf][0], acc[m][0]);
    };
    auto mm_tail = [&](int buf) LAM {
#pragma unroll
        for (int n = 1; n < 5; ++n)
#pragma unroll
            for (int m = 0; m < 4; ++m) acc[m][n] = mfma(xa[buf][m], wb[buf][n], acc[m][n]);
    };
    if (tid < L_WORDS) ((LAS unsigned*)(sm + L_FULL))[tid] = tid < 4 ? 1u : 0u;
#pragma unroll
    for (int n = 0; n < 5; ++n)
        asm volatile("" : "+v"(acc[0][n]), "+v"(acc[1][n]), "+v"(acc[2][n]), "+v"(acc[3][n]));
    asm volatile("" : "+v"(xo[0]), "+v"(xo[1]), "+v"(wa[0]), "+v"(wa[1]));
    const unsigned tag = mk_tag(nonce);
    if (cw >= 2) {
        float s0 = 0.f, s1 = 0.f;
#pragma unroll
        for (int i = 0; i < 10; ++i) {
            s0 = dot2(xv[i][0], gv[i][0], s0); s1 = dot2(xv[i][1], gv[i][1], s1);
            s0 = dot2(xv[i][2], gv[i][2], s0); s1 = dot2(xv[i][3], gv[i][3], s1);
        }
        float s = s0 + s1;
        s += lane_xor(s, 1); s += lane_xor(s, 2); s += lane_xor(s, 4);
        const float lg = bfr(s), g = bfr(1.f / (1.f + fexp(-lg)));
        if ((lane & 7) == 0) __hip_atomic_store((GAS u64*)(a.ws + WS_G) + gtok, granule(tag, __float_as_uint(g)), RLX_AGENT);
    } else if (tid < 80) {
        float ar, ai, cr, ci;
        channel_consts(rlre, rlim, rldt, ar, ai, cr, ci);
        float pr = ar, pi = ai;
#pragma unroll
        for (int k = 0; k < 4; ++k) { const float t = pr * pr - pi * pi; pi = 2.f * pr * pi; pr = t; }
        const float r16 = pr, i16 = pi;
#pragma unroll
        for (int k = 0; k < 3; ++k) { const float t = pr * pr - pi * pi; pi = 2.f * pr * pi; pr = t; }
        LAS f32x2* tab = (LAS f32x2*)(sm + A_TAB);
        tab[0 * 80 + tid] = (f32x2){ar, ar};
        tab[1 * 80 + tid] = (f32x2){-ai, ai};
        tab[2 * 80 + tid] = (f32x2){r16, r16};
        tab[3 * 80 + tid] = (f32x2){-i16, i16};
        tab[4 * 80 + tid] = (f32x2){pr, pr};
        tab[5 * 80 + tid] = (f32x2){-pi, pi};
    }
    {
        float ar, ai, cr, ci;
        channel_consts(jlre, jlim, jldt, ar, ai, cr, ci);
        ((LAS f32x2*)(sm + A_JTAB + cw * 512))[lane] = (f32x2){2.f * cr, 2.f * ci};
    }
    if (tid < 160) __hip_atomic_store((GAS u64*)(a.ws + WS_EA) + (ti * 8 + gj) * 160 + tid, (u64)0, RLX_AGENT);
    if (TOUCH) touch_streams(a, sm, cw, lane);
    STAMP(a.ws, cw, 1);
    lds_bar();
    STAMP(a.ws, cw, 2);
    STAMPC(a.ws, cw, 14);
    rdp(0, sm, 0);
#pragma unroll 1
    for (int s = 0; s < 10; ++s) {
        wait_lgkm0();
        SCHED();
        rdp(1, sm + (s & 3) * SLOT_A, 1);
        u32x4 fl = *(volatile LAS u32x4*)(sm + L_FULL);
        mm_head(0); mm_tail(0);
        interleave<20, 10, IL_A>();
        SCHED();
        wait_lgkm0();
        asm volatile("" ::: "memory");
        *(volatile LAS unsigned*)(sm + L_FREE + 4 * cw) = s + 1;
        const unsigned need = s < 9 ? s + 2 : 0;
        unsigned landed = __builtin_amdgcn_readfirstlane(umin4(fl));
        LAS char* s0 = sm + ((s + 1) & 3) * SLOT_A;
        asm volatile("" : "+s"(landed), "+v"(s0));
        mm_head(1);
        SCHED();
        if (__builtin_expect(landed < need, 0)) ring_spin(sm + L_FULL, need);
        asm volatile("" ::: "memory");
        SCHED();
        rdp(0, s0, 0);
        mm_tail(1);
        interleave<16, 9, IL_A - 3>();
        SCHED();
    }
    __builtin_amdgcn_s_setprio(0);
    asm volatile("s_nop 7\n\ts_nop 7" ::: "memory");
    STAMP(a.ws, cw, 3);
    STAMPC(a.ws, cw, 15);
    LAS float* tab = (LAS float*)(sm + A_TAB);
    LAS float* sg = (LAS float*)(sm + A_SEG) + (4 * mh + q) * 160 + 80 * nh + c;
    float ca[5], cb[5];
#pragma unroll
    for (int n = 0; n < 5; ++n) { const int col = 80 * nh + 16 * n + c; ca[n] = tab[col]; cb[n] = tab[160 + col]; }
    a_pass1(acc, ca, cb, sg);
    WAIT_VM(0);
    STAMP(a.ws, cw, 4);
    lds_bar();
    STAMP(a.ws, cw, 5);
    word_wait(sm + L_HS, 3u);
    STAMP(a.ws, cw, 8);
    {
        const int qq = (lane & 15) >> 2, p = lane & 3;
        const __amdgpu_buffer_rsrc_t ract = rsrc(a.ws + WS_ACT, 128 * TS);
        LAS float* gt = (LAS float*)(sm + A_G) + 64 * mh + 16 * q;
        LAS char* ht = sm + A_HT + (80 * nh + c) * HT_STRIDE + (64 * mh + 4 * q) * 2;
        LAS char* hr = sm + A_HT + (8 * q + qq) * HT_STRIDE + 128 * mh + 8 * p;
        const unsigned ob = (8 * ti + 4 * mh) * TS + 5 * gj * 1024;
        a_pass2(acc, ca, cb, sg, gt, ht, hr + 96 * nh * HT_STRIDE, ract, lane * 16, ob + 3072 * nh);
        STAMP(a.ws, cw, 9);
        wait_lgkm0();
        if (lane == 0) word_add(sm + L_HT + 4 * mh);
        word_wait(sm + L_HT + 4 * mh, 2u);
        bf16x8 hf[2];
#pragma unroll
        for (int i = 0; i < 2; ++i) {
            LAS char* b = hr + 64 * HT_STRIDE + 32 * (2 * nh + i);
            hf[i] = lds_tr_frag(b, b + 4 * HT_STRIDE);
        }
#pragma unroll
        for (int i = 0; i < 2; ++i) st16_wt(ract, lane * 16, ob + (2 * nh + i) * TS + 2048, __builtin_bit_cast(u32x4, hf[i]));
    }
    wait_lgkm0();
    STAMP(a.ws, cw, 10);
    signal_stores(sm, L_DONE, a.ws + WS_FACT + ti * 256 + gj * 8, tag, lane, 4u);
    STAMP(a.ws, cw, 11);
    p2_final(a, sm, cw, lane, nonce);
}

DEV void b_loader(const Args& a, LAS char* sm, int ti, int gj, int lw, int lane) {
    STAMP(a.ws, lw + 4, 0);
    const unsigned nonce = __hip_atomic_load((GAS unsigned*)(a.ws + WS_NONCE), RLX_AGENT);
    unsigned off[12];
    {
        const int pos = lane & 7, rl = lane >> 3;
#pragma unroll
        for (int u = 0; u < 4; ++u) {
            const int R = 8 * (lw + 4 * u) + rl;
            off[u] = x_token(R) * 1280 + ((pos ^ (R & 7)) << 4);
        }
#pragma unroll
        for (int u = 4; u < 12; ++u) {
            const int wi = lw + 4 * (u - 4), sec = wi / 10, ch = 64 * (wi - 10 * sec) + lane, rho = ch / 10, cc = ch - 10 * rho;
            off[u] = (rho ^ (((rho >> 3) & 1) << 2)) * 3840 + cc * 16 + (sec == 0 ? 1280 : sec == 1 ? 2560 : 0);
        }
    }
    const char* xb = a.x + (size_t)ti * (128 * 1280);
    const char* wb = a.wqkv + 160 * gj;
    const bool full = lw < 2;
    auto issue_a = [&](int s) LAM {
        LAS char* dst = sm + (s % NS_B) * SLOT_B + lw * 1024;
#pragma unroll
        for (int u = 0; u < 4; ++u) glds16(xb + 128 * s + off[u], dst + 4096 * u);
#pragma unroll
        for (int u = 4; u < 6; ++u) glds16(wb + (size_t)(64 * 3840) * s + off[u], dst + 4096 * u);
    };
    auto issue_b = [&](int s) LAM {
        LAS char* dst = sm + (s % NS_B) * SLOT_B + lw * 1024;
#pragma unroll
        for (int u = 6; u < 12; ++u) if (u < 11 || full) glds16(wb + (size_t)(64 * 3840) * s + off[u], dst + 4096 * u);
    };
    STAMP(a.ws, lw + 4, 1);
    issue_a(0); issue_b(0); issue_a(1); issue_b(1);
    if (full) WAIT_VM(12); else WAIT_VM(11);
    bar();
#pragma unroll 1
    for (int s = 0; s < 8; ++s) {
        if (s > 0) ring_wait(sm + L_FREE, s);
        issue_a(s + 2);
        WAIT_VM(6);
        ring_put(sm + L_FULL + 4 * lw, s + 2);
        issue_b(s + 2);
    }
    WAIT_VM(0);
    ring_put(sm + L_FULL + 4 * lw, 10);
    STAMP(a.ws, lw + 4, 2);
    __builtin_amdgcn_s_setprio(2);
    const unsigned tag = mk_tag(nonce);
    const int tl = lw * 64 + lane;
    if (tl < 128) ((LAS float*)(sm + B_G))[tl] = bfr(1.f - gate_of(a, 128 * ti + tl, tag));
    {
        int o0[2], o1[2];
#pragma unroll
        for (int it = 0; it < 2; ++it) {
            const int g = 2 * (tl + 256 * it < 420 ? tl + 256 * it : 0);
            const int h = g / 210, rem = g - 210 * h, e = rem / 10, d = 2 * (rem - 10 * e);
            const bool kv = rem < 200;
            o0[it] = kv ? B_KVT + ((20 * h + e) * 84 + 20 * h + d) * 4 : B_Z + (20 * h + 2 * (rem - 200)) * 4;
            o1[it] = o0[it] + (kv ? 80 * 84 * 4 : 320);
        }
        asm volatile("" : "+v"(o0[0]), "+v"(o0[1]), "+v"(o1[0]), "+v"(o1[1]));
        const __amdgpu_buffer_rsrc_t rk = rsrc(a.ws + WS_KV, 16 * 8 * 840 * 8);
        word_wait(sm + L_KV, 4u);
        STAMP(a.ws, lw + 4, 3);
        u32x4 pv[2];
#pragma unroll
        for (int it = 0; it < 2; ++it) {
            const f32x4 v = *(LAS f32x4*)(sm + o0[it]) + *(LAS f32x4*)(sm + o1[it]);
            pv[it] = (u32x4){pk2(v[0], v[1]), tag, pk2(v[2], v[3]), tag};
        }
        asm volatile("" : "+v"(pv[0]), "+v"(pv[1]) :: "memory");
        wait_lgkm0();
        if (lane == 0) word_add(sm + L_PUB);
        st16_wt(rk, tl * 16, (ti * 8 + gj) * 6720, pv[0]);
        if (tl + 256 < 420) st16_wt(rk, (tl + 256) * 16, (ti * 8 + gj) * 6720, pv[1]);
        asm volatile("s_nop 1" ::"v"(pv[0]), "v"(pv[1]));
    }
    STAMP(a.ws, lw + 4, 4);
    if (lw == 0) {
        const int cnt = ti < 8 ? 53 : 52, g0 = ti < 8 ? 53 * ti : 424 + 52 * (ti - 8);
        const int g = g0 + (lane < cnt ? lane : 0);
        GAS u64* src = (GAS u64*)(a.ws + WS_KV) + gj * 840 + g;
        unsigned v[16];
        for (unsigned spins = 0; spins < SPIN_CAP; ++spins) {
            u64 x[16];
#pragma unroll
            for (int i = 0; i < 16; ++i) x[i] = __hip_atomic_load(src + i * (8 * 840), RLX_AGENT);
            bool ok = true;
#pragma unroll
            for (int i = 0; i < 16; ++i) { v[i] = (unsigned)x[i]; ok &= (unsigned)(x[i] >> 32) == tag; }
            if (wave_all(ok)) break;
            __builtin_amdgcn_s_sleep(1);
        }
        float s0 = 0.f, s1 = 0.f;
#pragma unroll
        for (int i = 0; i < 16; ++i) { s0 += bf_lo(v[i]); s1 += bf_hi(v[i]); }
        if (lane < cnt) __hip_atomic_store((GAS u64*)(a.ws + WS_TOT) + gj * 840 + g, granule(tag, pk2(s0, s1)), RLX_AGENT);
        STAMP(a.ws, lw + 4, 5);
    } else if (lw == 3) {
        const __amdgpu_buffer_rsrc_t rs = rsrc(a.ws + WS_TOT, 8 * 840 * 8);
        u32x4 v[7];
        for (unsigned spins = 0; spins < SPIN_CAP; ++spins) {
            bool ok = true;
#pragma unroll
            for (int k = 0; k < 7; ++k) v[k] = __builtin_amdgcn_raw_buffer_load_b128(rs, gj * 6720 + (lane + 64 * k < 420 ? lane + 64 * k : 419) * 16, 0, 16);
#pragma unroll
            for (int k = 0; k < 7; ++k) ok &= (v[k][1] == tag) & (v[k][3] == tag);
            if (wave_all(ok)) break;
            __builtin_amdgcn_s_sleep(SLP_TOT);
        }
#pragma unroll
        for (int k = 0; k < 7; ++k) if (lane + 64 * k < 420) ((LAS u32x2*)(sm + B_TOT))[lane + 64 * k] = (u32x2){v[k][0], v[k][2]};
        wait_lgkm0();
        word_set(sm + L_TOT, 1u);
        STAMP(a.ws, lw + 4, 5);
    }
    __builtin_amdgcn_s_setprio(0);
    bf16x8 fw[P2_PW][5];
    word_wait(sm + L_PUB, 4u);
    p2_prefetch(a, sm, lw, lane, tag, 2, fw);
    p2_main(a, sm, lw, lane, nonce, 2, fw);
}

DEV void b_consumer(const Args& a, LAS char* sm, int ti, int gj, int cw, int lane) {
    const int mh = cw >> 1, nh = cw & 1, c = lane & 15, q = lane >> 4, tid = cw * 64 + lane;
    STAMP(a.ws, cw, 0);
    const unsigned nonce = __hip_atomic_load((GAS unsigned*)(a.ws + WS_NONCE), RLX_AGENT), tag = mk_tag(nonce);
    {
        const __amdgpu_buffer_rsrc_t rk = rsrc(a.ws + WS_KV, 16 * 8 * 840 * 8);
        const u32x4 z = {0u, 0u, 0u, 0u};
        st16_wt(rk, tid * 16, (ti * 8 + gj) * 6720, z);
        if (tid + 256 < 420) st16_wt(rk, (tid + 256) * 16, (ti * 8 + gj) * 6720, z);
        const int cnt = ti < 8 ? 53 : 52, g0 = ti < 8 ? 53 * ti : 424 + 52 * (ti - 8);
        if (tid < cnt) __hip_atomic_store((GAS u64*)(a.ws + WS_TOT) + gj * 840 + g0 + tid, (u64)0, RLX_AGENT);
    }
    __builtin_amdgcn_s_setprio(1);
    f32x4 an[5][4], at[5][2];
#pragma unroll
    for (int t = 0; t < 5; ++t) {
#pragma unroll
        for (int i = 0; i < 4; ++i) an[t][i] = (f32x4){0.f, 0.f, 0.f, 0.f};
        at[t][0] = at[t][1] = (f32x4){0.f, 0.f, 0.f, 0.f};
    }
    int xlo[2], xhi[2], wr[2];
#pragma unroll
    for (int kk = 0; kk < 2; ++kk) {
        const int sw = ((4 * kk + q) ^ (c & 7)) << 4;
        xlo[kk] = (64 * mh + 32 * nh + c) * 128 + sw;
        xhi[kk] = (64 * mh + 32 * (1 - nh) + c) * 128 + sw;
        wr[kk] = 16384 + (8 * q + 4 * (kk ^ (q & 1)) + (c >> 2)) * 160 + 8 * (c & 3);
    }
    bf16x8 xa[2][4], wb[2][5], wt[2][5];
    auto rdp = [&](int buf, LAS char* s0, int kk) LAM {
        LAS char* q0 = s0 + kk * 5120 + wr[0]; LAS char* q1 = s0 + kk * 5120 + wr[1];
        LAS char* a0 = q0 + 96 * nh; LAS char* a1 = q1 + 96 * nh;
        xa[buf][0] = lds_frag(s0 + xlo[kk]);
        wb[buf][0] = lds_tr_frag(a0, a1);
        wb[buf][1] = lds_tr_frag(a0 + 32, a1 + 32);
        xa[buf][1] = lds_frag(s0 + xlo[kk] + 2048);
        wb[buf][2] = lds_tr_frag(q0 + 64 + 10240 * nh, q1 + 64 + 10240 * nh);
        wb[buf][3] = lds_tr_frag(a0 + 10240, a1 + 10240);
        xa[buf][2] = lds_frag(s0 + xhi[kk]);
        wb[buf][4] = lds_tr_frag(a0 + 10272, a1 + 10272);
        wt[buf][0] = lds_tr_frag(q0 + 20480, q1 + 20480);
        xa[buf][3] = lds_frag(s0 + xhi[kk] + 2048);
        wt[buf][1] = lds_tr_frag(q0 + 20512, q1 + 20512);
        wt[buf][2] = lds_tr_frag(q0 + 20544, q1 + 20544);
        wt[buf][3] = lds_tr_frag(q0 + 20576, q1 + 20576);
        wt[buf][4] = lds_tr_frag(q0 + 20608, q1 + 20608);
    };
    auto mm_head = [&](int buf) LAM {
#pragma unroll
        for (int i = 0; i < 4; ++i) an[0][i] = mfma(xa[buf][i], wb[buf][0], an[0][i]);
    };
    auto mm_tail = [&](int buf) LAM {
#pragma unroll
        for (int t = 1; t < 5; ++t)
#pragma unroll
            for (int i = 0; i < 4; ++i) an[t][i] = mfma(xa[buf][i], wb[buf][t], an[t][i]);
#pragma unroll
        for (int t = 0; t < 5; ++t)
#pragma unroll
            for (int i = 0; i < 2; ++i) at[t][i] = mfma(wt[buf][t], xa[buf][i], at[t][i]);
    };
    if (tid < L_WORDS) ((LAS unsigned*)(sm + L_FULL))[tid] = tid < 4 ? 1u : 0u;
#pragma unroll
    for (int t = 0; t < 5; ++t)
        asm volatile("" : "+v"(an[t][0]), "+v"(an[t][1]), "+v"(an[t][2]), "+v"(an[t][3]), "+v"(at[t][0]), "+v"(at[t][1]));
    if (TOUCH) touch_streams(a, sm, cw, lane);
    STAMP(a.ws, cw, 1);
    lds_bar();
    STAMP(a.ws, cw, 2);
    STAMPC(a.ws, cw, 14);
    rdp(0, sm, 0);
    int slot = 0;
#pragma unroll 1
    for (int s = 0; s < 10; ++s) {
        wait_lgkm0();
        SCHED();
        rdp(1, sm + slot * SLOT_B, 1);
        u32x4 fl = *(volatile LAS u32x4*)(sm + L_FULL);
        mm_head(0); mm_tail(0);
        interleave<30, 25, IL_B>();
        SCHED();
        wait_lgkm0();
        asm volatile("" ::: "memory");
        *(volatile LAS unsigned*)(sm + L_FREE + 4 * cw) = s + 1;
        slot = slot == 2 ? 0 : slot + 1;
        const unsigned need = s < 9 ? s + 2 : 0;
        unsigned landed = __builtin_amdgcn_readfirstlane(umin4(fl));
        LAS char* s0 = sm + slot * SLOT_B;
        asm volatile("" : "+s"(landed), "+v"(s0));
        mm_head(1);
        SCHED();
        if (__builtin_expect(landed < need, 0)) ring_spin(sm + L_FULL, need);
        asm volatile("" ::: "memory");
        SCHED();
        rdp(0, s0, 0);
        mm_tail(1);
        interleave<26, 24, 24>();
        SCHED();
    }
    STAMP(a.ws, cw, 3);
    STAMPC(a.ws, cw, 15);
    bf16x8 fr[5][2];
#pragma unroll
    for (int t = 0; t < 2; ++t)
#pragma unroll
        for (int i = 0; i < 4; ++i)
#pragma unroll
            for (int r = 0; r < 4; ++r) an[t][i][r] = phi(an[t][i][r]);
    if (nh == 0) {
#pragma unroll
        for (int i = 0; i < 4; ++i)
#pragma unroll
            for (int r = 0; r < 4; ++r) an[2][i][r] = phi(an[2][i][r]);
    }
#pragma unroll
    for (int t = 0; t < 5; ++t) { fr[t][0] = pack_pair(an[t][0], an[t][1]); fr[t][1] = pack_pair(an[t][2], an[t][3]); }
    {
        LAS char* xs = sm + B_X1 + cw * 2048 + lane * 16;
        *(LAS bf16x8*)(xs + (nh << 10)) = fr[2][0];
        *(LAS bf16x8*)(xs + ((1 ^ nh) << 10)) = fr[2][1];
    }
    wait_lgkm0();
    word_set(sm + L_X1 + 4 * cw, 1u);
#pragma unroll
    for (int t = 0; t < 5; ++t) asm volatile("" : "+v"(fr[t][0]), "+v"(fr[t][1]));
    STAMP(a.ws, cw, 4);
    word_wait(sm + L_X1 + 4 * (cw ^ 1), 1u);
    ring_spin(sm + L_FREE, 10);
    asm volatile("" ::: "memory");
    {
        LAS char* xr = sm + B_X1 + (cw ^ 1) * 2048 + lane * 16;
        const bf16x8 R0 = *(LAS bf16x8*)(xr + (nh << 10)), R1 = *(LAS bf16x8*)(xr + ((1 ^ nh) << 10));
        const bf16x8 ones = as_frag((u32x4){0x3f803f80u, 0x3f803f80u, 0x3f803f80u, 0x3f803f80u});
        const f32x4 z4 = {0.f, 0.f, 0.f, 0.f};
        LAS float* kvt = (LAS float*)(sm + B_KVT) + mh * (80 * 84) + c * 84 + 4 * q;
        LAS float* zz = (LAS float*)(sm + B_Z) + mh * 80 + 4 * q;
        auto prod = [&](const bf16x8& k0, const bf16x8& k1, const bf16x8& v0, const bf16x8& v1, int ta, int tb) LAM {
            *(LAS f32x4*)(kvt + 16 * tb * 84 + 16 * ta) = mfma(k1, v1, mfma(k0, v0, z4));
        };
        auto zsum = [&](const bf16x8& k0, const bf16x8& k1, int ta) LAM {
            const f32x4 d = mfma(k1, ones, mfma(k0, ones, z4));
            if (c == 0) *(LAS f32x4*)(zz + 16 * ta) = d;
        };
        const int b0 = 3 * nh;
        prod(fr[0][0], fr[0][1], fr[3][0], fr[3][1], b0, b0);
        prod(fr[0][0], fr[0][1], fr[4][0], fr[4][1], b0, b0 + 1);
        prod(fr[1][0], fr[1][1], fr[3][0], fr[3][1], b0 + 1, b0);
        prod(fr[1][0], fr[1][1], fr[4][0], fr[4][1], b0 + 1, b0 + 1);
        zsum(fr[0][0], fr[0][1], b0);
        zsum(fr[1][0], fr[1][1], b0 + 1);
        if (nh == 0) {
            prod(fr[2][0], fr[2][1], fr[4][0], fr[4][1], 2, 1);
            prod(fr[1][0], fr[1][1], R0, R1, 1, 2);
            prod(fr[2][0], fr[2][1], R0, R1, 2, 2);
            zsum(fr[2][0], fr[2][1], 2);
        } else {
            prod(fr[0][0], fr[0][1], fr[2][0], fr[2][1], 3, 2);
            prod(R0, R1, fr[3][0], fr[3][1], 2, 3);
        }
    }
    WAIT_VM(0);
    wait_lgkm0();
    if (lane == 0) word_add(sm + L_KV);
    __builtin_amdgcn_s_setprio(0);
    STAMP(a.ws, cw, 5);
    bf16x8 P[2][3];
#pragma unroll
    for (int i = 0; i < 2; ++i) {
#pragma unroll
        for (int t = 0; t < 5; ++t)
#pragma unroll
            for (int r = 0; r < 4; ++r) at[t][i][r] = phi(at[t][i][r]);
        P[i][0] = pack_pair(at[0][i], at[1][i]);
        P[i][1] = pack_pair(at[2][i], at[3][i]);
        P[i][2] = as_frag((u32x4){pk2(at[4][i][0], at[4][i][1]), pk2(at[4][i][2], at[4][i][3]), 0u, 0u});
    }
    int adr[21];
    {
        int dh[5], db[5], eh[5], eb[5];
#pragma unroll
        for (int k = 0; k < 5; ++k) {
            const int d0 = 16 * k + 4 * q, e0 = 16 * k + c;
            dh[k] = d0 / 20; db[k] = B_TOT + dh[k] * 840 + (d0 - 20 * dh[k]) * 2;
            eh[k] = e0 / 20; eb[k] = (e0 - 20 * eh[k]) * 40;
        }
        auto ad = [&](int k, int E) LAM { return dh[k] == eh[E] ? db[k] + eb[E] : L_ZERO; };
        adr[0] = ad(0, 0); adr[1] = ad(1, 0);
        adr[2] = ad(0, 1); adr[3] = ad(1, 1); adr[4] = ad(2, 1); adr[5] = ad(3, 1);
        adr[6] = ad(1, 2); adr[7] = ad(2, 2); adr[8] = ad(3, 2);
        adr[9] = ad(2, 3); adr[10] = ad(3, 3); adr[11] = ad(4, 3);
        adr[12] = ad(3, 4); adr[13] = ad(4, 4);
#pragma unroll
        for (int k = 0; k < 5; ++k) adr[14 + k] = dh[k] == (c & 3) ? db[k] + 800 : L_ZERO;
        adr[19] = L_ZERO; adr[20] = B_G + 4 * (64 * mh + 16 * (c >> 2) + 8 * nh + (c & 3));
    }
#pragma unroll
    for (int i = 0; i < 2; ++i) asm volatile("" : "+v"(P[i][0]), "+v"(P[i][1]), "+v"(P[i][2]));
    asm volatile("" : "+v"(adr[0]), "+v"(adr[1]), "+v"(adr[2]), "+v"(adr[3]), "+v"(adr[4]), "+v"(adr[5]), "+v"(adr[6]),
                      "+v"(adr[7]), "+v"(adr[8]), "+v"(adr[9]), "+v"(adr[10]), "+v"(adr[11]), "+v"(adr[12]), "+v"(adr[13]));
    asm volatile("" : "+v"(adr[14]), "+v"(adr[15]), "+v"(adr[16]), "+v"(adr[17]), "+v"(adr[18]), "+v"(adr[20]));
    const __amdgpu_buffer_rsrc_t ract = rsrc(a.ws + WS_ACT, 128 * TS);
    u32x4 os[2][2]; u32x2 o8[2];
    STAMP(a.ws, cw, 6);
    word_wait(sm + L_TOT, 1u);
    word_wait(sm + L_PUB, 4u);
    STAMP(a.ws, cw, 7);
    {
        auto h4 = [&](int i) LAM -> u32x2 { return *(LAS u32x2*)(sm + adr[i]); };
        auto fr2 = [&](const u32x2& lo, const u32x2& hi) LAM { return as_frag((u32x4){lo[0], lo[1], hi[0], hi[1]}); };
        const u32x2 zz = {0u, 0u};
        const bf16x8 Z0 = fr2(h4(14), h4(15)), Z1 = fr2(h4(16), h4(17)), Z2 = fr2(h4(18), zz);
        const bf16x8 F0 = fr2(h4(0), h4(1)), F1 = fr2(h4(2), h4(3)), F2 = fr2(h4(4), h4(5));
        const bf16x8 F3 = fr2(zz, h4(6)), F4 = fr2(h4(7), h4(8)), F5 = fr2(h4(9), h4(10));
        const bf16x8 F6 = fr2(h4(11), zz), F7 = fr2(zz, h4(12)), F8 = fr2(h4(13), zz);
        const float ogv[2] = {*(LAS float*)(sm + adr[20]), *(LAS float*)(sm + adr[20] + 16)};
        const f32x4 z4 = {0.f, 0.f, 0.f, 0.f};
        const int uu = gj >> 1, odd = gj & 1;
#pragma unroll
        for (int i = 0; i < 2; ++i) {
            const f32x4 dn = mfma(Z2, P[i][2], mfma(Z1, P[i][1], mfma(Z0, P[i][0], z4)));
            const f32x4 n0 = mfma(F0, P[i][0], z4);
            const f32x4 n1 = mfma(F2, P[i][1], mfma(F1, P[i][0], z4));
            const f32x4 n2 = mfma(F4, P[i][1], mfma(F3, P[i][0], z4));
            const f32x4 n3 = mfma(F6, P[i][2], mfma(F5, P[i][1], z4));
            const f32x4 n4 = mfma(F8, P[i][2], mfma(F7, P[i][1], z4));
            const float og = ogv[i];
            const float r0 = og * __builtin_amdgcn_rcpf(dn[0] + 1e-6f), r1 = og * __builtin_amdgcn_rcpf(dn[1] + 1e-6f);
            const float r2 = og * __builtin_amdgcn_rcpf(dn[2] + 1e-6f), r3 = og * __builtin_amdgcn_rcpf(dn[3] + 1e-6f);
            const float s1 = q < 1 ? r0 : r1, s2 = q < 2 ? r1 : r2, s3 = q < 3 ? r2 : r3;
            os[i][0] = (u32x4){pk2(n0[0] * r0, n0[1] * r0), pk2(n0[2] * r0, n0[3] * r0), pk2(n1[0] * s1, n1[1] * s1), pk2(n1[2] * s1, n1[3] * s1)};
            os[i][1] = (u32x4){pk2(n2[0] * s2, n2[1] * s2), pk2(n2[2] * s2, n2[3] * s2), pk2(n3[0] * s3, n3[1] * s3), pk2(n3[2] * s3, n3[3] * s3)};
            o8[i] = (u32x2){pk2(n4[0] * r3, n4[1] * r3), pk2(n4[2] * r3, n4[3] * r3)};
        }
#pragma unroll
        for (int i = 0; i < 2; ++i) {
            const unsigned ob = (8 * ti + 4 * mh + 2 * nh + i) * TS + (40 + 5 * uu) * 1024;
            st16_wt(ract, lane * 16, ob + (odd ? 3072 : 0), os[i][0]);
            st16_wt(ract, lane * 16, ob + (odd ? 4096 : 1024), os[i][1]);
            st8_wt(ract, lane * 16 + 8 * odd, ob + 2048, o8[i]);
        }
    }
    KEEP(os[0][0]); KEEP(os[0][1]); KEEP(os[1][0]); KEEP(os[1][1]);
    STAMP(a.ws, cw, 8);
    signal_stores(sm, L_DONE, a.ws + WS_FACT + ti * 256 + 128 + gj * 8, tag, lane, 4u);
    STAMP(a.ws, cw, 9);
    p2_final(a, sm, cw, lane, nonce);
}

}

__global__ __launch_bounds__(512, 2) NOPK void gm_fused(const void* x, const void* lre, const void* lim, const void* bre,
                                                   const void* bim, const void* cre, const void* cim, const void* logdt,
                                                   const void* dskip, const void* wqkv, const void* wo, const void* gw,
                                                   void* ws, void* out) {
    __shared__ __attribute__((aligned(1024))) char smem[LDS_BYTES];
    LAS char* sm = (LAS char*)smem;
#define MAPMODE 2
    const int b = blockIdx.x, xcd = b & 7, idx = b >> 3;
    const bool roleA = MAPMODE == 1 ? idx < 16 : MAPMODE == 2 ? idx >= 16 : xcd < 4;
    const int ti = MAPMODE != 0 ? 4 * (xcd >> 1) + (idx & 3) : 8 * ((xcd & 3) >> 1) + (idx & 7);
    const int gj = MAPMODE != 0 ? 4 * (xcd & 1) + ((idx >> 2) & 3) : 4 * (xcd & 1) + (idx >> 3);
    const int wave = __builtin_amdgcn_readfirstlane(threadIdx.x >> 6), lane = threadIdx.x & 63;
    const Args a{(const char*)x, (const u16*)lre, (const u16*)lim, (const char*)bre, (const char*)bim, (const char*)cre,
                 (const char*)cim, (const u16*)logdt, (const char*)wqkv, (const char*)wo, (const u16*)gw,
                 (const u16*)dskip, (char*)ws, (float*)out};
    STAMPL(ws, 0);
    if (roleA) {
        if (wave < 4) a_consumer(a, sm, ti, gj, wave, lane);
        else a_loader(a, sm, ti, gj, wave - 4, lane);
    } else {
        if (wave < 4) b_consumer(a, sm, ti, gj, wave, lane);
        else b_loader(a, sm, ti, gj, wave - 4, lane);
    }
    STAMPL(ws, 1);
}

extern "C" void kernel_launch(void* const* d_in, const int* in_sizes, int n_in, void* d_out, int out_size, void* d_ws,
                              size_t ws_size, hipStream_t stream) {
    gm_fused<<<256, 512, 0, stream>>>(d_in[0], d_in[1], d_in[2], d_in[3], d_in[4], d_in[5], d_in[6], d_in[7], d_in[8],
                                      d_in[10], d_in[11], d_in[12], d_ws, d_out);
}
```

```cpp
#include <hip/hip_runtime.h>
#include <hip/hip_bf16.h>
#include <stdint.h>

namespace {

typedef __attribute__((ext_vector_type(8))) short bf16x8;
typedef __attribute__((ext_vector_type(4))) short s16x4;
typedef __attribute__((ext_vector_type(4))) float f32x4;
typedef __attribute__((ext_vector_type(2))) float f32x2;
typedef __attribute__((ext_vector_type(2))) __bf16 bf16x2_t;
typedef __attribute__((ext_vector_type(4))) unsigned u32x4;
typedef __attribute__((ext_vector_type(2))) unsigned u32x2;
typedef unsigned long long u64;
typedef unsigned short u16;
#define LAS __attribute__((address_space(3)))
#define GAS __attribute__((address_space(1)))
#if defined(__HIP_DEVICE_COMPILE__)
#define NOPK __attribute__((target("no-packed-fp32-ops")))
#else
#define NOPK
#endif
#define DEV __device__ __forceinline__
#define LAM __attribute__((always_inline))
#define RLX_AGENT __ATOMIC_RELAXED, __HIP_MEMORY_SCOPE_AGENT

constexpr size_t WS_NONCE = 0;
constexpr size_t WS_EA    = 4096;
constexpr size_t WS_KV    = WS_EA + 16 * 8 * 160 * 8;
constexpr size_t WS_TOT   = WS_KV + 16 * 8 * 840 * 8;
constexpr size_t WS_G     = WS_TOT + 8 * 840 * 8;
constexpr size_t WS_FACT  = WS_G + 2048 * 8;
constexpr size_t WS_FW2T  = WS_FACT + 16 * 256;
constexpr int NREP = 16;
constexpr size_t WS_W2T   = (WS_FW2T + NREP * 1024 + 1023) / 1024 * 1024;
constexpr size_t TS = 61440;
constexpr size_t WS_ACT   = WS_W2T + 40 * TS;
constexpr size_t WS_PROF  = 32u << 20;

#define STAMP(ws, wave, i) do {} while (0)
#define STAMPC(ws, wave, i) do {} while (0)
#define TIMED(acc, stmt) do { stmt; } while (0)
#define STAMPV(ws, wave, i, v) do {} while (0)
#define STAMP2(ws, wave, i) do {} while (0)

#define STAMPL(ws, i) do {} while (0)

constexpr int LDS_BYTES = 163840;
constexpr int SLOT_A = 36864, NS_A = 4;
constexpr int A_STG  = 110592;
constexpr int A_TAB  = 147456;
constexpr int A_JTAB = A_TAB + 3840;
constexpr int A_G    = A_JTAB + 2048;
constexpr int A_SEG  = A_G + 512;
constexpr int A_HT = 47104, HT_STRIDE = 272;
static_assert(A_SEG + 5120 <= 159776, "lds");
constexpr int SLOT_B = 47104, NS_B = 3;
constexpr int B_KVT = 0;
constexpr int B_Z   = B_KVT + 2 * 80 * 84 * 4;
constexpr int B_X1  = 141312;
constexpr int B_G   = B_X1 + 8192;
constexpr int B_TOT = B_G + 512;
static_assert(B_TOT + 3360 <= 159776, "lds");

#define SLP_W2T 16
#define SLP_ACT 4
#define SLP_LB 2
#define SLP_TOT 2
#define IL_A 16
#define IL_B 27
constexpr unsigned SPIN_CAP = 1u << 18;

DEV unsigned pk2(float a, float b) {
    return __builtin_bit_cast(unsigned, __builtin_convertvector((f32x2){a, b}, bf16x2_t));
}
DEV float bfr(float a) { return __uint_as_float(pk2(a, 0.f) << 16); }
DEV float bf_lo(unsigned u) { return __uint_as_float(u << 16); }
DEV float bf_hi(unsigned u) { return __uint_as_float(u & 0xffff0000u); }
DEV float bf2f(u16 u) { return __uint_as_float((unsigned)u << 16); }
DEV float swp(float x) {
    return __builtin_bit_cast(float, __builtin_amdgcn_mov_dpp(__builtin_bit_cast(int, x), 0xB1, 0xF, 0xF, true));
}
DEV float cmul_add(float a, float bs, float e, float u) { return fmaf(bs, swp(e), fmaf(a, e, u)); }
DEV f32x4 mfma(bf16x8 a, bf16x8 b, f32x4 c) { return __builtin_amdgcn_mfma_f32_16x16x32_bf16(a, b, c, 0, 0, 0); }
DEV bf16x8 as_frag(u32x4 v) { return __builtin_bit_cast(bf16x8, v); }
DEV bf16x8 lds_frag(LAS char* p) { return *(LAS bf16x8*)p; }
DEV bf16x8 lds_tr_frag(LAS char* p0, LAS char* p1) {
    s16x4 lo = __builtin_amdgcn_ds_read_tr16_b64_v4i16((LAS s16x4*)p0);
    s16x4 hi = __builtin_amdgcn_ds_read_tr16_b64_v4i16((LAS s16x4*)p1);
    return __builtin_shufflevector(lo, hi, 0, 1, 2, 3, 4, 5, 6, 7);
}
DEV void glds16_wt(const char* g, unsigned l) {
    unsigned keep;
    asm volatile("s_mov_b32 %0, m0\n\ts_mov_b32 m0, %2\n\ts_nop 0\n\tglobal_load_lds_dwordx4 %1, off sc1\n\ts_mov_b32 m0, %0"
                 : "=&s"(keep) : "v"(g), "s"(l) : "memory");
}
DEV void glds16(const char* g, LAS char* l) {
    __builtin_amdgcn_global_load_lds((const GAS unsigned*)g, (LAS unsigned*)l, 16, 0, 0);
}
DEV void bar() { asm volatile("" ::: "memory"); __builtin_amdgcn_s_barrier(); asm volatile("" ::: "memory"); }
DEV void wait_lgkm0() { __builtin_amdgcn_s_waitcnt(0xC07F); }
DEV void lds_bar() { asm volatile("" ::: "memory"); wait_lgkm0(); bar(); }
constexpr int L_FULL = 159776, L_FREE = L_FULL + 16;
constexpr int L_DONE = L_FULL + 32;
constexpr int L_KV = L_FULL + 36;
constexpr int L_W2T = L_FULL + 40;
constexpr int L_ACTH = L_FULL + 44, L_ACTO = L_FULL + 76;
constexpr int L_X1 = L_FULL + 48, L_ZERO = L_FULL + 64;
constexpr int L_DONE2 = L_FULL + 72;
constexpr int L_WR = L_FULL + 84;
constexpr int L_TOT = L_FULL + 88;
constexpr int L_HS = L_FULL + 96, L_HT = L_FULL + 100;
constexpr int L_PUB = L_FULL + 108;
constexpr int L_WORDS = 28;
DEV unsigned umin4(const u32x4& f) { const unsigned a = f[0] < f[1] ? f[0] : f[1], b = f[2] < f[3] ? f[2] : f[3]; return a < b ? a : b; }
DEV void ring_put(LAS char* p, unsigned v) {
    asm volatile("ds_write_b32 %0, %1" ::"v"((unsigned)(size_t)p), "v"(v) : "memory");
}
DEV void ring_wait(LAS char* p, unsigned need) {
#pragma unroll 1
    for (unsigned spins = 0; spins < SPIN_CAP; ++spins) {
        u32x4 f;
        asm volatile("ds_read_b128 %0, %1\n\ts_waitcnt lgkmcnt(0)" : "=&v"(f) : "v"((unsigned)(size_t)p) : "memory");
        if ((unsigned)__builtin_amdgcn_readfirstlane(umin4(f)) >= need) break;
        __builtin_amdgcn_s_sleep(1);
    }
}
DEV void word_wait(LAS char* p, unsigned need) {
#pragma unroll 1
    for (unsigned spins = 0; spins < SPIN_CAP; ++spins) {
        if ((unsigned)__builtin_amdgcn_readfirstlane(*(volatile LAS unsigned*)p) >= need) break;
        __builtin_amdgcn_s_sleep(1);
    }
    asm volatile("" ::: "memory");
}
DEV void word_set(LAS char* p, unsigned v) { asm volatile("" ::: "memory"); *(volatile LAS unsigned*)p = v; }
DEV unsigned word_add(LAS char* p) {
    return __hip_atomic_fetch_add((LAS unsigned*)p, 1u, __ATOMIC_RELAXED, __HIP_MEMORY_SCOPE_WORKGROUP);
}
DEV void ring_spin(LAS char* p, unsigned need) {
#pragma unroll 1
    for (unsigned spins = 0; spins < SPIN_CAP; ++spins) {
        const u32x4 f = *(volatile LAS u32x4*)p;
        if ((unsigned)__builtin_amdgcn_readfirstlane(umin4(f)) >= need) break;
    }
}
#define WAIT_VM(n) asm volatile("s_waitcnt vmcnt(" #n ")" ::: "memory")
#define SCHED() __builtin_amdgcn_sched_barrier(0)
#define KEEP(x) asm volatile("" ::"v"(x))
template <int NM, int NR, int NS> DEV void interleave() {
#pragma unroll
    for (int i = 0; i < NS; ++i) {
        __builtin_amdgcn_sched_group_barrier(0x8, 1, 0);
        if ((i + 1) * NR / NS != i * NR / NS) __builtin_amdgcn_sched_group_barrier(0x100, 1, 0);
    }
    __builtin_amdgcn_sched_group_barrier(0x8, NM - NS, 0);
}
DEV bool wave_all(bool p) { return __builtin_amdgcn_ballot_w64(p) == __builtin_amdgcn_ballot_w64(true); }
DEV float lane_xor(float v, int m) {
    const int x = __builtin_bit_cast(int, v);
    const int y = m == 1 ? __builtin_amdgcn_mov_dpp(x, 0xB1, 0xF, 0xF, true) : m == 2 ? __builtin_amdgcn_mov_dpp(x, 0x4E, 0xF, 0xF, true)
                                                                              : __builtin_amdgcn_ds_swizzle(x, 0x101F);
    return __builtin_bit_cast(float, y);
}
DEV float fexp(float x) { return __builtin_amdgcn_exp2f(x * 1.44269504088896341f); }
DEV unsigned mk_tag(unsigned n) { return ((n + 1u) * 0x9E3779B1u) | 1u; }
DEV u64 granule(unsigned tag, unsigned v) { return ((u64)tag << 32) | v; }
DEV u64 flag_of(unsigned tag) { return granule(tag, tag * 0x85EBCA6Bu + 0x5BD1E995u); }
DEV __amdgpu_buffer_rsrc_t rsrc(const char* p, unsigned bytes) { return __builtin_amdgcn_make_buffer_rsrc((void*)p, 0, bytes, 0x00020000); }
DEV void st16_wt(const __amdgpu_buffer_rsrc_t& r, unsigned voff, unsigned soff, u32x4 v) { __builtin_amdgcn_raw_buffer_store_b128(v, r, voff + soff, 0, 16); }
DEV void st8_wt(const __amdgpu_buffer_rsrc_t& r, unsigned voff, unsigned soff, u32x2 v) { __builtin_amdgcn_raw_buffer_store_b64(v, r, voff + soff, 0, 16); }
DEV bf16x8 ld16_wt(const __amdgpu_buffer_rsrc_t& r, unsigned voff, unsigned soff) {
    return __builtin_bit_cast(bf16x8, __builtin_amdgcn_raw_buffer_load_b128(r, voff, soff, 16));
}
DEV void signal_stores(LAS char* sm, int word, char* flag, unsigned tag, int lane, unsigned waves, int replicas = 1) {
    asm volatile("s_waitcnt vmcnt(0)" ::: "memory");
    unsigned old = 0;
    if (lane == 0) old = __hip_atomic_fetch_add((LAS unsigned*)(sm + word), 1u, __ATOMIC_RELAXED, __HIP_MEMORY_SCOPE_WORKGROUP);
    old = __builtin_amdgcn_readfirstlane(old);
    if (old == waves - 1u && lane < replicas) __hip_atomic_store((GAS u64*)(flag + lane * 1024), flag_of(tag), RLX_AGENT);
}
DEV float phi(float t) {
    const float e = __builtin_amdgcn_fmed3f(__builtin_amdgcn_exp2f(t * 1.44269504088896341f), 0.f, 1.f);
    return (e + 1e-6f) + __builtin_amdgcn_fmed3f(t, 0.f, __builtin_inff());
}
DEV bf16x8 pack_pair(const f32x4& lo, const f32x4& hi) {
    return as_frag((u32x4){pk2(lo[0], lo[1]), pk2(lo[2], lo[3]), pk2(hi[0], hi[1]), pk2(hi[2], hi[3])});
}

DEV void scan_step(float& u0, float& u1, float& u2, float& u3, float& u4, const float (&e)[5], const float (&a)[5],
                   const float (&b)[5]) {
    asm("v_fmac_f32 %0, %10, %5\n\tv_fmac_f32 %1, %11, %6\n\tv_fmac_f32 %2, %12, %7\n\t"
        "v_fmac_f32 %3, %13, %8\n\tv_fmac_f32 %4, %14, %9\n\t"
        "v_fmac_f32_dpp %0, %5, %15 quad_perm:[1,0,3,2] row_mask:0xf bank_mask:0xf bound_ctrl:1\n\t"
        "v_fmac_f32_dpp %1, %6, %16 quad_perm:[1,0,3,2] row_mask:0xf bank_mask:0xf bound_ctrl:1\n\t"
        "v_fmac_f32_dpp %2, %7, %17 quad_perm:[1,0,3,2] row_mask:0xf bank_mask:0xf bound_ctrl:1\n\t"
        "v_fmac_f32_dpp %3, %8, %18 quad_perm:[1,0,3,2] row_mask:0xf bank_mask:0xf bound_ctrl:1\n\t"
        "v_fmac_f32_dpp %4, %9, %19 quad_perm:[1,0,3,2] row_mask:0xf bank_mask:0xf bound_ctrl:1"
        : "+v"(u0), "+v"(u1), "+v"(u2), "+v"(u3), "+v"(u4)
        : "v"(e[0]), "v"(e[1]), "v"(e[2]), "v"(e[3]), "v"(e[4]), "v"(a[0]), "v"(a[1]), "v"(a[2]), "v"(a[3]), "v"(a[4]),
          "v"(b[0]), "v"(b[1]), "v"(b[2]), "v"(b[3]), "v"(b[4]));
}

DEV void scan_step_nd(float (&t)[5], float u0, float u1, float u2, float u3, float u4, const float (&e)[5],
                      const float (&a)[5], const float (&b)[5]) {
    asm("v_fma_f32 %0, %15, %10, %5\n\tv_fma_f32 %1, %16, %11, %6\n\tv_fma_f32 %2, %17, %12, %7\n\t"
        "v_fma_f32 %3, %18, %13, %8\n\tv_fma_f32 %4, %19, %14, %9\n\t"
        "v_fmac_f32_dpp %0, %10, %20 quad_perm:[1,0,3,2] row_mask:0xf bank_mask:0xf bound_ctrl:1\n\t"
        "v_fmac_f32_dpp %1, %11, %21 quad_perm:[1,0,3,2] row_mask:0xf bank_mask:0xf bound_ctrl:1\n\t"
        "v_fmac_f32_dpp %2, %12, %22 quad_perm:[1,0,3,2] row_mask:0xf bank_mask:0xf bound_ctrl:1\n\t"
        "v_fmac_f32_dpp %3, %13, %23 quad_perm:[1,0,3,2] row_mask:0xf bank_mask:0xf bound_ctrl:1\n\t"
        "v_fmac_f32_dpp %4, %14, %24 quad_perm:[1,0,3,2] row_mask:0xf bank_mask:0xf bound_ctrl:1"
        : "=&v"(t[0]), "=&v"(t[1]), "=&v"(t[2]), "=&v"(t[3]), "=&v"(t[4])
        : "v"(u0), "v"(u1), "v"(u2), "v"(u3), "v"(u4), "v"(e[0]), "v"(e[1]), "v"(e[2]), "v"(e[3]), "v"(e[4]),
          "v"(a[0]), "v"(a[1]), "v"(a[2]), "v"(a[3]), "v"(a[4]), "v"(b[0]), "v"(b[1]), "v"(b[2]), "v"(b[3]), "v"(b[4]));
}

struct Args {
    const char* x; const u16* lre; const u16* lim; const char* bre; const char* bim; const char* cre; const char* cim;
    const u16* logdt; const char* wqkv; const char* wo; const u16* gw; const u16* dskip; char* ws; float* out;
};

DEV void channel_consts(u16 lre, u16 lim, u16 logdt, float& ar, float& ai, float& cr, float& ci) {
    const float lr = bf2f(lre), li = bf2f(lim);
    const float dt = bfr(fexp(bf2f(logdt)));
    const float zr = dt * lr, zi = dt * li, ex = fexp(zr);
    const float rv = zi * 0.15915494309189535f, rr = rv - __builtin_rintf(rv);
    ar = ex * __builtin_amdgcn_cosf(rr); ai = ex * __builtin_amdgcn_sinf(rr);
    const float nr = ar - 1.f, ni = ai, dn = lr * lr + li * li;
    cr = (nr * lr + ni * li) / dn; ci = (ni * lr - nr * li) / dn;
}
DEV float dot2(unsigned x, unsigned y, float s) {
    return __builtin_amdgcn_fdot2_f32_bf16(__builtin_bit_cast(bf16x2_t, x), __builtin_bit_cast(bf16x2_t, y), s, false);
}

constexpr int P2_RED = 0;
constexpr int P2_PW = 4, P2_PA = 5;
DEV bool flags_up(const u32x4& v, u64 fl) {
    const unsigned f0 = (unsigned)fl, f1 = (unsigned)(fl >> 32);
    return wave_all((v[0] == f0) & (v[1] == f1) & (v[2] == f0) & (v[3] == f1));
}
DEV void wait_flags8(const Args& a, int ti, int part, u64 fl) {
    GAS u64* fp = (GAS u64*)(a.ws + WS_FACT) + ti * 32 + part * 16 + (threadIdx.x & 7);
#pragma unroll 1
    for (unsigned spins = 0; spins < SPIN_CAP; ++spins) {
        if (wave_all(__hip_atomic_load(fp, RLX_AGENT) == fl)) break;
        __builtin_amdgcn_s_sleep(SLP_ACT);
    }
}
DEV unsigned p2_koff(int lw, int st) { return (st < 10 ? 10 * lw + st : 30 + 5 * lw + st) * 1024; }
DEV int p2_unit(int lw, int i) { return 5120 * (7 * lw + i); }
DEV void p2_prefetch(const Args& a, LAS char* sm, int lw, int lane, unsigned tag, int poller, bf16x8 (&fw)[P2_PW][5]) {
    const int b = blockIdx.x, xcd = b & 7, idx = b >> 3;
    const int cg = (xcd & 1) * 4 + (idx >> 3);
    if (lw == poller) {
        const __amdgpu_buffer_rsrc_t rf = rsrc(a.ws + WS_FW2T + (xcd * 2 + (b >> 7)) * 1024, 1024);
#pragma unroll 1
        for (unsigned spins = 0; spins < SPIN_CAP; ++spins) {
            if (flags_up(__builtin_amdgcn_raw_buffer_load_b128(rf, lane * 16, 0, 16), flag_of(tag))) break;
            __builtin_amdgcn_s_sleep(SLP_W2T);
        }
        word_set(sm + L_W2T, 1u);
    } else word_wait(sm + L_W2T, 1u);
    STAMP2(a.ws, lw + 4, 0);
    const unsigned wbase = 5 * cg * TS, vo = lane * 16;
    const char* src = a.ws + WS_W2T + wbase + vo;
#pragma unroll
    for (int i = 0; i < 7; ++i)
#pragma unroll
        for (int n = 0; n < 5; ++n)
            glds16_wt(src + (size_t)n * TS + p2_koff(lw, 4 + i), (unsigned)(size_t)(sm + p2_unit(lw, i) + n * 1024));
    const __amdgpu_buffer_rsrc_t rw = rsrc(a.ws + WS_W2T, 40 * TS);
#pragma unroll
    for (int st = 0; st < P2_PW; ++st)
#pragma unroll
        for (int n = 0; n < 5; ++n) fw[st][n] = ld16_wt(rw, vo, wbase + n * TS + p2_koff(lw, st));
}
DEV void p2_main(const Args& a, LAS char* sm, int lw, int lane, unsigned nonce, int poller, bf16x8 (&fw)[P2_PW][5]) {
    const int b = blockIdx.x, xcd = b & 7, idx = b >> 3;
    const int tt = (xcd >> 1) * 8 + (idx & 7), cg = (xcd & 1) * 4 + (idx >> 3);
    const int c = lane & 15, q = lane >> 4;
    const unsigned tag = mk_tag(nonce);
    const u64 fl = flag_of(tag);
    constexpr int PA = P2_PA;
    const __amdgpu_buffer_rsrc_t rw = rsrc(a.ws + WS_W2T, 40 * TS), ra = rsrc(a.ws + WS_ACT, 128 * TS);
    const unsigned wbase = 5 * cg * TS, abase = 4 * tt * TS, vo = lane * 16;
    bf16x8 fa[PA][4];
    auto ldw = [&](int st, int sl) LAM {
#pragma unroll
        for (int n = 0; n < 5; ++n) fw[sl][n] = ld16_wt(rw, vo, wbase + n * TS + p2_koff(lw, st));
    };
    auto lda = [&](int st) LAM {
#pragma unroll
        for (int t = 0; t < 4; ++t) fa[st % PA][t] = ld16_wt(ra, vo, abase + t * TS + p2_koff(lw, st));
    };
    WAIT_VM(0);
    if (lw == poller) { wait_flags8(a, tt >> 1, 0, fl); word_set(sm + L_ACTH, 1u); }
    else word_wait(sm + L_ACTH, 1u);
    STAMP2(a.ws, lw + 4, 1);
#pragma unroll
    for (int st = 0; st < 3; ++st) lda(st);
    f32x4 acc[5][4];
    const f32x4 z4 = {0.f, 0.f, 0.f, 0.f};
    auto slot = [&](int st) LAM { return st < 4 ? st : st <= 10 ? (st & 1) : st == 11 ? 2 : st == 12 ? 3 : st == 13 ? 1 : 0; };
#pragma unroll
    for (int st = 0; st < 15; ++st) {
        if (st <= 4) lda(st + 3);
        if (st == 6) word_wait(sm + L_ACTO, 1u);
        if (st >= 4 && st + 4 < 15) lda(st + 4);
        SCHED();
        if (st >= 3 && st < 10) {
#pragma unroll
            for (int n = 0; n < 5; ++n) fw[slot(st + 1)][n] = lds_frag(sm + p2_unit(lw, st - 3) + vo + n * 1024);
        }
#pragma unroll
        for (int n = 0; n < 5; ++n)
#pragma unroll
            for (int t = 0; t < 4; ++t) acc[n][t] = mfma(fw[slot(st)][n], fa[st % PA][t], st == 0 ? z4 : acc[n][t]);
        SCHED();
        if (st == 7) ldw(11, slot(11));
        if (st == 9) { wait_lgkm0(); if (lane == 0) word_add(sm + L_WR); ldw(12, slot(12)); }
        if (st == 10) ldw(13, slot(13));
        if (st == 11) ldw(14, slot(14));
    }
    STAMP2(a.ws, lw + 4, 2);
    word_wait(sm + L_WR, 4u);
    word_wait(sm + L_DONE, 4u);
    LAS float* rw_ = (LAS float*)(sm + P2_RED) + lw * (64 * 84);
#pragma unroll
    for (int t = 0; t < 4; ++t)
#pragma unroll
        for (int n = 0; n < 5; ++n) *(LAS f32x4*)(rw_ + (16 * t + c) * 84 + 16 * n + 4 * q) = acc[n][t];
    lds_bar();
    STAMP2(a.ws, lw + 4, 3);
}

DEV void p2_final(const Args& a, LAS char* sm, int cw, int lane, unsigned nonce) {
    const unsigned tag = mk_tag(nonce);
    const int b = blockIdx.x, xcd = b & 7, idx = b >> 3;
    const int tt = (xcd >> 1) * 8 + (idx & 7), cg = (xcd & 1) * 4 + (idx >> 3);
    const int tid = cw * 64 + lane;
    u32x2 xv[5], dv[5]; float gv[5];
    int tok[5], n0[5];
#pragma unroll
    for (int i = 0; i < 5; ++i) {
        const int id = tid + 256 * i, r = id / 20, c4 = id - 20 * r;
        tok[i] = 64 * tt + 16 * ((r & 15) >> 2) + 4 * (r >> 4) + (r & 3); n0[i] = 80 * cg + 4 * c4;
        xv[i] = *(const u32x2*)((const u16*)a.x + tok[i] * 640 + n0[i]);
        dv[i] = *(const u32x2*)(a.dskip + n0[i]);
    }
#pragma unroll 1
    for (unsigned spins = 0; spins < SPIN_CAP; ++spins) {
        u64 v[5];
#pragma unroll
        for (int i = 0; i < 5; ++i) v[i] = __hip_atomic_load((GAS u64*)(a.ws + WS_G) + tok[i], RLX_AGENT);
        bool ok = true;
#pragma unroll
        for (int i = 0; i < 5; ++i) { gv[i] = __uint_as_float((unsigned)v[i]); ok &= (unsigned)(v[i] >> 32) == tag; }
        if (wave_all(ok)) break;
        __builtin_amdgcn_s_sleep(2);
    }
    f32x4 sk[5];
#pragma unroll
    for (int i = 0; i < 5; ++i) {
        const unsigned p01 = pk2(bf_lo(xv[i][0]) * bf_lo(dv[i][0]), bf_hi(xv[i][0]) * bf_hi(dv[i][0]));
        const unsigned p23 = pk2(bf_lo(xv[i][1]) * bf_lo(dv[i][1]), bf_hi(xv[i][1]) * bf_hi(dv[i][1]));
        sk[i] = (f32x4){gv[i] * bf_lo(p01), gv[i] * bf_hi(p01), gv[i] * bf_lo(p23), gv[i] * bf_hi(p23)};
    }
    asm volatile("" : "+v"(sk[0]), "+v"(sk[1]), "+v"(sk[2]), "+v"(sk[3]), "+v"(sk[4]));
    if (cw == 0) {
        wait_flags8(a, tt >> 1, 1, flag_of(tag));
        word_set(sm + L_ACTO, 1u);
        if (tt == 31 && cg == 0) {
            word_wait(sm + L_ACTH, 1u);
            if (lane == 0) __hip_atomic_store((GAS unsigned*)(a.ws + WS_NONCE), nonce + 1u, RLX_AGENT);
        }
    }
    STAMP2(a.ws, cw, 0);
    bar();
    STAMP2(a.ws, cw, 2);
#pragma unroll
    for (int i = 0; i < 5; ++i) {
        const int id = tid + 256 * i, r = id / 20, c4 = id - 20 * r;
        LAS float* p = (LAS float*)(sm + P2_RED) + r * 84 + 4 * c4;
        sk[i] += (*(LAS f32x4*)p + *(LAS f32x4*)(p + 64 * 84)) + (*(LAS f32x4*)(p + 128 * 84) + *(LAS f32x4*)(p + 192 * 84));
    }
    asm volatile("" : "+v"(sk[0]), "+v"(sk[1]), "+v"(sk[2]), "+v"(sk[3]), "+v"(sk[4]));
#define OUT_AUX 16
    {
        const __amdgpu_buffer_rsrc_t ro = rsrc((const char*)a.out, 2048 * 640 * 4);
#pragma unroll
        for (int i = 0; i < 5; ++i)
            __builtin_amdgcn_raw_buffer_store_b128(__builtin_bit_cast(u32x4, sk[i]), ro, (tok[i] * 640 + n0[i]) * 4, 0, OUT_AUX);
    }
    STAMP2(a.ws, cw, 3);
}

DEV void job_issue(const Args& a, LAS char* stg, int jb, int lane) {
    if (jb < 400) {
        const int cb = jb / 40, nt = jb - 40 * cb;
        const unsigned o = (16 * nt + (lane >> 3)) * 1280 + cb * 128 + (lane & 7) * 16;
        glds16(a.cre + o, stg); glds16(a.cre + o + 8 * 1280, stg + 1024);
        glds16(a.cim + o, stg + 2048); glds16(a.cim + o + 8 * 1280, stg + 3072);
    } else if (jb < 500) {
        const int jo = jb - 400, s5 = jo / 5, n8 = jo - 5 * s5, uu = s5 / 5, s = s5 - 5 * uu;
        const int lo = 160 * uu + (s == 0 ? 0 : s == 1 ? 32 : s == 2 ? 64 : s == 3 ? 80 : 112);
        const int hi = 160 * uu + (s == 0 ? 16 : s == 1 ? 48 : s == 2 ? 144 : s == 3 ? 96 : 128);
        const unsigned o = (lane >> 4) * 1280 + n8 * 256 + (lane & 15) * 16;
#pragma unroll
        for (int k = 0; k < 4; ++k) {
            glds16(a.wo + (lo + 4 * k) * 1280 + o, stg + 1024 * k);
            glds16(a.wo + (hi + 4 * k) * 1280 + o, stg + 4096 + 1024 * k);
        }
    }
}
DEV u32x2 lds_rd8(LAS char* p) { u32x2 v; asm volatile("ds_read_b64 %0, %1" : "=v"(v) : "v"((unsigned)(size_t)p) : "memory"); return v; }
DEV f32x4 lds_rd16(LAS char* p) { f32x4 v; asm volatile("ds_read_b128 %0, %1" : "=v"(v) : "v"((unsigned)(size_t)p) : "memory"); return v; }
DEV u32x2 lds_rdtr(LAS char* p) { u32x2 v; asm volatile("ds_read_b64_tr_b16 %0, %1" : "=v"(v) : "v"((unsigned)(size_t)p) : "memory"); return v; }
DEV void job_finish(const Args& a, LAS char* stg, LAS char* tab, int jb, int lane) {
    const int c = lane & 15, q = lane >> 4;
    const __amdgpu_buffer_rsrc_t rw = rsrc(a.ws + WS_W2T, 40 * TS);
    if (jb < 400) {
        const int cb = jb / 40, nt = jb - 40 * cb;
        u32x2 re[4], im[4]; f32x4 c01[4], c23[4];
#pragma unroll
        for (int k = 0; k < 4; ++k) {
            re[k] = lds_rd8(stg + c * 128 + 32 * k + 8 * q); im[k] = lds_rd8(stg + 2048 + c * 128 + 32 * k + 8 * q);
            c01[k] = lds_rd16(tab + (16 * k + 4 * q) * 8); c23[k] = lds_rd16(tab + (16 * k + 4 * q) * 8 + 16);
        }
        asm volatile("s_waitcnt lgkmcnt(0)" : "+v"(re[0]), "+v"(re[1]), "+v"(re[2]), "+v"(re[3]), "+v"(im[0]), "+v"(im[1]), "+v"(im[2]), "+v"(im[3]),
                     "+v"(c01[0]), "+v"(c01[1]), "+v"(c01[2]), "+v"(c01[3]), "+v"(c23[0]), "+v"(c23[1]), "+v"(c23[2]), "+v"(c23[3]) :: "memory");
        u32x4 w[4];
#pragma unroll
        for (int k = 0; k < 4; ++k) {
            const float r0 = bf_lo(re[k][0]), r1 = bf_hi(re[k][0]), r2 = bf_lo(re[k][1]), r3 = bf_hi(re[k][1]);
            const float i0 = bf_lo(im[k][0]), i1 = bf_hi(im[k][0]), i2 = bf_lo(im[k][1]), i3 = bf_hi(im[k][1]);
            w[k] = (u32x4){pk2(r0 * c01[k][0] - i0 * c01[k][1], -(r0 * c01[k][1] + i0 * c01[k][0])),
                           pk2(r1 * c01[k][2] - i1 * c01[k][3], -(r1 * c01[k][3] + i1 * c01[k][2])),
                           pk2(r2 * c23[k][0] - i2 * c23[k][1], -(r2 * c23[k][1] + i2 * c23[k][0])),
                           pk2(r3 * c23[k][2] - i3 * c23[k][3], -(r3 * c23[k][3] + i3 * c23[k][2]))};
        }
#pragma unroll
        for (int k = 0; k < 4; ++k) st16_wt(rw, lane * 16, nt * TS + (4 * cb + k) * 1024, w[k]);
    } else if (jb < 500) {
        const int jo = jb - 400, s5 = jo / 5, n8 = jo - 5 * s5;
        u32x2 lo[8], hi[8];
#pragma unroll
        for (int k = 0; k < 8; ++k) {
            LAS char* b = stg + (4 * q + (c >> 2)) * 256 + 32 * k + 8 * (c & 3);
            lo[k] = lds_rdtr(b); hi[k] = lds_rdtr(b + 4096);
        }
        asm volatile("s_waitcnt lgkmcnt(0)" : "+v"(lo[0]), "+v"(lo[1]), "+v"(lo[2]), "+v"(lo[3]), "+v"(lo[4]), "+v"(lo[5]), "+v"(lo[6]), "+v"(lo[7]),
                     "+v"(hi[0]), "+v"(hi[1]), "+v"(hi[2]), "+v"(hi[3]), "+v"(hi[4]), "+v"(hi[5]), "+v"(hi[6]), "+v"(hi[7]) :: "memory");
#pragma unroll
        for (int k = 0; k < 8; ++k)
            st16_wt(rw, lane * 16, (8 * n8 + k) * TS + (40 + s5) * 1024, (u32x4){lo[k][0], lo[k][1], hi[k][0], hi[k][1]});
    }
}

#define TOUCH 1
#define TOUCH_JOB 0
#define TOUCH_S0 6
constexpr int L_JUNK = 160000;
DEV void touch(const char* p, LAS char* sm) {
    unsigned keep;
    asm volatile("s_mov_b32 %0, m0\n\ts_mov_b32 m0, %2\n\ts_nop 0\n\tglobal_load_lds_dword %1, off\n\ts_mov_b32 m0, %0"
                 : "=&s"(keep) : "v"(p), "s"((unsigned)(size_t)(sm + L_JUNK)) : "memory");
}
DEV void touch_job(const Args& a, LAS char* sm, int jb, int lane) {
    if (jb < 400) {
        const int cb = jb / 40, nt = jb - 40 * cb;
        touch((lane & 32 ? a.cim : a.cre) + (16 * nt + (lane & 15)) * 1280 + cb * 128, sm);
    } else if (jb < 500) {
        const int jo = jb - 400, s5 = jo / 5, n8 = jo - 5 * s5, uu = s5 / 5, s = s5 - 5 * uu;
        const int lo = 160 * uu + (s == 0 ? 0 : s == 1 ? 32 : s == 2 ? 64 : s == 3 ? 80 : 112);
        const int hi = 160 * uu + (s == 0 ? 16 : s == 1 ? 48 : s == 2 ? 144 : s == 3 ? 96 : 128);
        touch(a.wo + ((lane & 32 ? hi : lo) + (lane & 15)) * 1280 + n8 * 256 + (lane & 16 ? 128 : 0), sm);
    }
}
DEV void touch_streams(const Args& a, LAS char* sm, int cw, int lane) {
    const int b = blockIdx.x, xcd = b & 7, tq = xcd >> 1, gh = xcd & 1;
#pragma unroll
    for (int r = 0; r < 2; ++r) {
        const int e = (b >> 3) * 4 + cw + 128 * r, s = TOUCH_S0 + e / 33, k = e - 33 * (e / 33);
        if (s < 10) {
            const char* p;
            if (k < 8) p = a.x + (size_t)((4 * tq + (k >> 1)) * 128 + 64 * (k & 1) + lane) * 1280 + 128 * s;
            else if (k < 18) {
                const int li = 64 * (k - 8) + lane, g = li / 160, rem = li - 160 * g;
                p = (rem < 80 ? a.bre : a.bim) + (size_t)(80 * (4 * gh + g) + (rem < 80 ? rem : rem - 80)) * 1280 + 128 * s;
            } else {
                const int li = 64 * (k - 18) + lane, kr = li / 15, rem = li - 15 * kr, sec = rem / 5;
                p = a.wqkv + (size_t)(64 * s + kr) * 3840 + sec * 1280 + 640 * gh + 128 * (rem - 5 * sec);
            }
            touch(p, sm);
        }
    }
}
DEV float gate_of(const Args& a, int tok, unsigned tag) {
    u64 v = 0;
#pragma unroll 1
    for (unsigned spins = 0; spins < SPIN_CAP; ++spins) {
        v = __hip_atomic_load((GAS u64*)(a.ws + WS_G) + tok, RLX_AGENT);
        if (wave_all((unsigned)(v >> 32) == tag)) break;
        __builtin_amdgcn_s_sleep(1);
    }
    return __uint_as_float((unsigned)v);
}
DEV int x_token(int R) { const int rho = R & 15; return (R & 64) + 16 * (rho >> 2) + 4 * ((R >> 4) & 3) + (rho & 3); }

DEV void a_loader(const Args& a, LAS char* sm, int ti, int gj, int lw, int lane) {
    STAMP(a.ws, lw + 4, 0);
    const unsigned nonce = __hip_atomic_load((GAS unsigned*)(a.ws + WS_NONCE), RLX_AGENT);
    unsigned off[9];
    {
        const int pos = lane & 7, rl = lane >> 3, im = lw & 1;
#pragma unroll
        for (int u = 0; u < 4; ++u) {
            const int R = 8 * (lw + 4 * u) + rl;
            off[u] = x_token(R) * 1280 + ((pos ^ (R & 7)) << 4);
        }
#pragma unroll
        for (int u = 4; u < 9; ++u) off[u] = (8 * ((lw + 4 * (u - 4)) >> 1) + rl) * 1280 + ((pos ^ rl ^ (im << 2)) << 4);
    }
    const char* xb = a.x + (size_t)ti * (128 * 1280);
    const char* bb = ((lw & 1) ? a.bim : a.bre) + (size_t)gj * (80 * 1280);
    auto issue_a = [&](int s) LAM {
        LAS char* dst = sm + (s % NS_A) * SLOT_A + lw * 1024;
#pragma unroll
        for (int u = 0; u < 4; ++u) glds16(xb + 128 * s + off[u], dst + 4096 * u);
        glds16(bb + 128 * s + off[4], dst + 4096 * 4);
    };
    auto issue_b = [&](int s) LAM {
        LAS char* dst = sm + (s % NS_A) * SLOT_A + lw * 1024;
#pragma unroll
        for (int u = 5; u < 9; ++u) glds16(bb + 128 * s + off[u], dst + 4096 * u);
    };
    STAMP(a.ws, lw + 4, 1);
    issue_a(0); issue_b(0); issue_a(1); issue_b(1); issue_a(2); issue_b(2);
    WAIT_VM(18);
    bar();
#pragma unroll 1
    for (int s = 0; s < 7; ++s) {
        if (s > 0) ring_wait(sm + L_FREE, s);
        issue_a(s + 3);
        WAIT_VM(14);
        ring_put(sm + L_FULL + 4 * lw, s + 2);
        issue_b(s + 3);
    }
    WAIT_VM(9);
    ring_put(sm + L_FULL + 4 * lw, 9);
    WAIT_VM(0);
    ring_put(sm + L_FULL + 4 * lw, 10);
    STAMP(a.ws, lw + 4, 2);
    const unsigned tag = mk_tag(nonce);
    const int tl = lw * 64 + lane;
    const int jb = (ti * 8 + gj) * 4 + lw;
    ring_wait(sm + L_FREE, 8);
    job_issue(a, sm + A_STG + lw * 8192, jb, lane);
    if (tl < 128) ((LAS float*)(sm + A_G))[tl] = gate_of(a, 128 * ti + tl, tag);
    WAIT_VM(0);
    job_finish(a, sm + A_STG + lw * 8192, sm + A_JTAB + lw * 512, jb, lane);
    STAMP(a.ws, lw + 4, 9);
    __builtin_amdgcn_s_setprio(2);
    lds_bar();
    STAMP(a.ws, lw + 4, 3);
    if (lw < 3) {
        const int col = tl < 160 ? tl : 159;
        LAS float* tab = (LAS float*)(sm + A_TAB);
        GAS u64* ea = (GAS u64*)(a.ws + WS_EA) + gj * 160;
        const float a16 = tab[2 * 160 + col], b16 = tab[3 * 160 + col], a128 = tab[4 * 160 + col], b128 = tab[5 * 160 + col];
        LAS float* sg = (LAS float*)(sm + A_SEG);
        float pre[8], P = 0.f;
#pragma unroll
        for (int s = 0; s < 8; ++s) { pre[s] = P; P = cmul_add(a16, b16, P, sg[s * 160 + col]); }
        if (tl < 160) __hip_atomic_store(ea + ti * 1280 + col, granule(tag, __float_as_uint(P)), RLX_AGENT);
        STAMP(a.ws, lw + 4, 4);
        signal_stores(sm, L_DONE2, a.ws + WS_FW2T + (ti * 8 + gj) * 8, tag, lane, 4u, NREP);
        float H = 0.f;
        if (ti > 0) {
            float ev[15];
#pragma unroll 1
            for (unsigned spins = 0; spins < SPIN_CAP; ++spins) {
                u64 v[15];
#pragma unroll
                for (int i = 0; i < 15; ++i) v[i] = __hip_atomic_load(ea + (i < ti ? i : 0) * 1280 + col, RLX_AGENT);
                bool ok = true;
#pragma unroll
                for (int i = 0; i < 15; ++i) { ev[i] = __uint_as_float((unsigned)v[i]); ok &= (unsigned)(v[i] >> 32) == tag; }
                if (wave_all(ok)) break;
                __builtin_amdgcn_s_sleep(SLP_LB);
            }
#pragma unroll
            for (int i = 0; i < 15; ++i) if (i < ti) H = cmul_add(a128, b128, H, ev[i]);
        }
        STAMP(a.ws, lw + 4, 6);
#pragma unroll
        for (int s = 0; s < 8; ++s) { if (tl < 160) sg[s * 160 + col] = H + pre[s]; H = cmul_add(a16, b16, H, 0.f); }
        wait_lgkm0();
        if (lane == 0) word_add(sm + L_HS);
    } else signal_stores(sm, L_DONE2, a.ws + WS_FW2T + (ti * 8 + gj) * 8, tag, lane, 4u, NREP);
    __builtin_amdgcn_s_setprio(0);
    STAMP(a.ws, lw + 4, 8);
    word_wait(sm + L_DONE, 4u);
    bf16x8 fw[P2_PW][5];
    p2_prefetch(a, sm, lw, lane, tag, 3, fw);
    p2_main(a, sm, lw, lane, nonce, 3, fw);
}

DEV void a_pass1(f32x4 (&acc)[4][5], const float (&ca)[5], const float (&cb)[5], LAS float* es) {
    float e[5];
#pragma unroll
    for (int n = 0; n < 5; ++n) e[n] = acc[0][n][0];
#pragma unroll
    for (int t = 1; t < 16; ++t) {
        const int i = t >> 2, r = t & 3;
        float y[5];
        scan_step_nd(y, acc[i][0][r], acc[i][1][r], acc[i][2][r], acc[i][3][r], acc[i][4][r], e, ca, cb);
#pragma unroll
        for (int n = 0; n < 5; ++n) e[n] = y[n];
    }
#pragma unroll
    for (int n = 0; n < 5; ++n) es[16 * n] = e[n];
}
DEV void a_pass2(f32x4 (&acc)[4][5], const float (&ca)[5], const float (&cb)[5], LAS float* hs, LAS float* gt, LAS char* ht,
                 LAS char* hr, const __amdgpu_buffer_rsrc_t& ract, unsigned vo, unsigned ob) {
    float e[5];
#pragma unroll
    for (int n = 0; n < 5; ++n) e[n] = hs[16 * n];
    asm volatile("s_nop 1" : "+v"(e[0]), "+v"(e[1]), "+v"(e[2]), "+v"(e[3]), "+v"(e[4]));
    bf16x8 hf[4][2];
    auto rd = [&](int m) LAM {
#pragma unroll
        for (int k = 0; k < 2; ++k) {
            LAS char* b = hr + 32 * k * HT_STRIDE + 32 * m;
            hf[m][k] = lds_tr_frag(b, b + 4 * HT_STRIDE);
        }
    };
    auto st = [&](int m) LAM {
#pragma unroll
        for (int k = 0; k < 2; ++k) st16_wt(ract, vo, ob + m * TS + k * 1024, __builtin_bit_cast(u32x4, hf[m][k]));
    };
#pragma unroll
    for (int m = 0; m < 4; ++m) {
        const f32x4 g = *(LAS f32x4*)(gt + 4 * m);
#pragma unroll
        for (int r = 0; r < 4; ++r) {
            float u0 = acc[m][0][r], u1 = acc[m][1][r], u2 = acc[m][2][r], u3 = acc[m][3][r], u4 = acc[m][4][r];
            scan_step(u0, u1, u2, u3, u4, e, ca, cb);
            e[0] = u0; e[1] = u1; e[2] = u2; e[3] = u3; e[4] = u4;
            acc[m][0][r] = u0 * g[r]; acc[m][1][r] = u1 * g[r]; acc[m][2][r] = u2 * g[r]; acc[m][3][r] = u3 * g[r]; acc[m][4][r] = u4 * g[r];
        }
        SCHED();
        if (m >= 2) st(m - 2);
#pragma unroll
        for (int n = 0; n < 5; ++n)
            *(LAS u32x2*)(ht + 16 * n * HT_STRIDE + 32 * m) = (u32x2){pk2(acc[m][n][0], acc[m][n][1]), pk2(acc[m][n][2], acc[m][n][3])};
        rd(m);
        SCHED();
    }
    st(2); st(3);
#pragma unroll
    for (int m = 0; m < 4; ++m) { KEEP(hf[m][0]); KEEP(hf[m][1]); }
}

DEV void a_consumer(const Args& a, LAS char* sm, int ti, int gj, int cw, int lane) {
    const int mh = cw >> 1, nh = cw & 1, c = lane & 15, q = lane >> 4, tid = cw * 64 + lane;
    STAMP(a.ws, cw, 0);
    const unsigned nonce = __hip_atomic_load((GAS unsigned*)(a.ws + WS_NONCE), RLX_AGENT);
    const int jb = (ti * 8 + gj) * 4 + cw, pj = (jb < 400 ? 64 * (jb / 40) : 0) + lane;
    const u16 jlre = a.lre[pj], jlim = a.lim[pj], jldt = a.logdt[pj];
    const int gtok = 128 * ti + 16 * gj + 8 * (cw & 1) + (lane >> 3), pch = 80 * gj + (tid < 80 ? tid : 0);
    u32x4 xv[10], gv[10];
    u16 rlre = 0, rlim = 0, rldt = 0;
    if (cw >= 2) {
        const u32x4* xr = (const u32x4*)(a.x + (size_t)gtok * 1280) + (lane & 7);
        const u32x4* gr = (const u32x4*)a.gw + (lane & 7);
#pragma unroll
        for (int i = 0; i < 10; ++i) { xv[i] = xr[8 * i]; gv[i] = gr[8 * i]; }
    } else { rlre = a.lre[pch]; rlim = a.lim[pch]; rldt = a.logdt[pch]; }
    __builtin_amdgcn_s_setprio(1);
    f32x4 acc[4][5];
#pragma unroll
    for (int n = 0; n < 5; ++n)
#pragma unroll
        for (int m = 0; m < 4; ++m) acc[m][n] = (f32x4){0.f, 0.f, 0.f, 0.f};
    int xo[2], wa[2];
#pragma unroll
    for (int kk = 0; kk < 2; ++kk) {
        xo[kk] = (64 * mh + c) * 128 + (((4 * kk + q) ^ (c & 7)) << 4);
        wa[kk] = 16384 + (80 * nh + 8 * (c & 1) + (c >> 1)) * 128 + (((4 * kk + q) ^ (c >> 1) ^ ((c & 1) << 2)) << 4);
    }
    bf16x8 xa[2][4], wb[2][5];
    auto rdp = [&](int buf, LAS char* s0, int kk) LAM {
#pragma unroll
        for (int m = 0; m < 4; ++m) xa[buf][m] = lds_frag(s0 + xo[kk] + 2048 * m);
#pragma unroll
        for (int n = 0; n < 5; ++n) wb[buf][n] = lds_frag(s0 + wa[kk] + 2048 * n);
    };
    auto mm_head = [&](int buf) LAM {
#pragma unroll
        for (int m = 0; m < 4; ++m) acc[m][0] = mfma(xa[buf][m], wb[buf][0], acc[m][0]);
    };
    auto mm_tail = [&](int buf) LAM {
#pragma unroll
        for (int n = 1; n < 5; ++n)
#pragma unroll
            for (int m = 0; m < 4; ++m) acc[m][n] = mfma(xa[buf][m], wb[buf][n], acc[m][n]);
    };
    if (tid < L_WORDS) ((LAS unsigned*)(sm + L_FULL))[tid] = tid < 4 ? 1u : 0u;
#pragma unroll
    for (int n = 0; n < 5; ++n)
        asm volatile("" : "+v"(acc[0][n]), "+v"(acc[1][n]), "+v"(acc[2][n]), "+v"(acc[3][n]));
    asm volatile("" : "+v"(xo[0]), "+v"(xo[1]), "+v"(wa[0]), "+v"(wa[1]));
    const unsigned tag = mk_tag(nonce);
    if (cw >= 2) {
        float s0 = 0.f, s1 = 0.f;
#pragma unroll
        for (int i = 0; i < 10; ++i) {
            s0 = dot2(xv[i][0], gv[i][0], s0); s1 = dot2(xv[i][1], gv[i][1], s1);
            s0 = dot2(xv[i][2], gv[i][2], s0); s1 = dot2(xv[i][3], gv[i][3], s1);
        }
        float s = s0 + s1;
        s += lane_xor(s, 1); s += lane_xor(s, 2); s += lane_xor(s, 4);
        const float lg = bfr(s), g = bfr(1.f / (1.f + fexp(-lg)));
        if ((lane & 7) == 0) __hip_atomic_store((GAS u64*)(a.ws + WS_G) + gtok, granule(tag, __float_as_uint(g)), RLX_AGENT);
    } else if (tid < 80) {
        float ar, ai, cr, ci;
        channel_consts(rlre, rlim, rldt, ar, ai, cr, ci);
        float pr = ar, pi = ai;
#pragma unroll
        for (int k = 0; k < 4; ++k) { const float t = pr * pr - pi * pi; pi = 2.f * pr * pi; pr = t; }
        const float r16 = pr, i16 = pi;
#pragma unroll
        for (int k = 0; k < 3; ++k) { const float t = pr * pr - pi * pi; pi = 2.f * pr * pi; pr = t; }
        LAS f32x2* tab = (LAS f32x2*)(sm + A_TAB);
        tab[0 * 80 + tid] = (f32x2){ar, ar};
        tab[1 * 80 + tid] = (f32x2){-ai, ai};
        tab[2 * 80 + tid] = (f32x2){r16, r16};
        tab[3 * 80 + tid] = (f32x2){-i16, i16};
        tab[4 * 80 + tid] = (f32x2){pr, pr};
        tab[5 * 80 + tid] = (f32x2){-pi, pi};
    }
    {
        float ar, ai, cr, ci;
        channel_consts(jlre, jlim, jldt, ar, ai, cr, ci);
        ((LAS f32x2*)(sm + A_JTAB + cw * 512))[lane] = (f32x2){2.f * cr, 2.f * ci};
    }
    if (tid < 160) __hip_atomic_store((GAS u64*)(a.ws + WS_EA) + (ti * 8 + gj) * 160 + tid, (u64)0, RLX_AGENT);
    if (TOUCH) touch_streams(a, sm, cw, lane);
    if (TOUCH_JOB) touch_job(a, sm, jb, lane);
    STAMP(a.ws, cw, 1);
    lds_bar();
    STAMP(a.ws, cw, 2);
    STAMPC(a.ws, cw, 14);
    rdp(0, sm, 0);
#pragma unroll 1
    for (int s = 0; s < 10; ++s) {
        wait_lgkm0();
        SCHED();
        rdp(1, sm + (s & 3) * SLOT_A, 1);
        u32x4 fl = *(volatile LAS u32x4*)(sm + L_FULL);
        mm_head(0); mm_tail(0);
        interleave<20, 10, IL_A>();
        SCHED();
        wait_lgkm0();
        asm volatile("" ::: "memory");
        *(volatile LAS unsigned*)(sm + L_FREE + 4 * cw) = s + 1;
        const unsigned need = s < 9 ? s + 2 : 0;
        unsigned landed = __builtin_amdgcn_readfirstlane(umin4(fl));
        LAS char* s0 = sm + ((s + 1) & 3) * SLOT_A;
        asm volatile("" : "+s"(landed), "+v"(s0));
        mm_head(1);
        SCHED();
        if (__builtin_expect(landed < need, 0)) ring_spin(sm + L_FULL, need);
        asm volatile("" ::: "memory");
        SCHED();
        rdp(0, s0, 0);
        mm_tail(1);
        interleave<16, 9, IL_A - 3>();
        SCHED();
    }
    __builtin_amdgcn_s_setprio(0);
    asm volatile("s_nop 7\n\ts_nop 7" ::: "memory");
    STAMP(a.ws, cw, 3);
    STAMPC(a.ws, cw, 15);
    LAS float* tab = (LAS float*)(sm + A_TAB);
    LAS float* sg = (LAS float*)(sm + A_SEG) + (4 * mh + q) * 160 + 80 * nh + c;
    float ca[5], cb[5];
#pragma unroll
    for (int n = 0; n < 5; ++n) { const int col = 80 * nh + 16 * n + c; ca[n] = tab[col]; cb[n] = tab[160 + col]; }
    a_pass1(acc, ca, cb, sg);
    WAIT_VM(0);
    STAMP(a.ws, cw, 4);
    lds_bar();
    STAMP(a.ws, cw, 5);
    word_wait(sm + L_HS, 3u);
    STAMP(a.ws, cw, 8);
    {
        const int qq = (lane & 15) >> 2, p = lane & 3;
        const __amdgpu_buffer_rsrc_t ract = rsrc(a.ws + WS_ACT, 128 * TS);
        LAS float* gt = (LAS float*)(sm + A_G) + 64 * mh + 16 * q;
        LAS char* ht = sm + A_HT + (80 * nh + c) * HT_STRIDE + (64 * mh + 4 * q) * 2;
        LAS char* hr = sm + A_HT + (8 * q + qq) * HT_STRIDE + 128 * mh + 8 * p;
        const unsigned ob = (8 * ti + 4 * mh) * TS + 5 * gj * 1024;
        a_pass2(acc, ca, cb, sg, gt, ht, hr + 96 * nh * HT_STRIDE, ract, lane * 16, ob + 3072 * nh);
        STAMP(a.ws, cw, 9);
        wait_lgkm0();
        if (lane == 0) word_add(sm + L_HT + 4 * mh);
        word_wait(sm + L_HT + 4 * mh, 2u);
        bf16x8 hf[2];
#pragma unroll
        for (int i = 0; i < 2; ++i) {
            LAS char* b = hr + 64 * HT_STRIDE + 32 * (2 * nh + i);
            hf[i] = lds_tr_frag(b, b + 4 * HT_STRIDE);
        }
#pragma unroll
        for (int i = 0; i < 2; ++i) st16_wt(ract, lane * 16, ob + (2 * nh + i) * TS + 2048, __builtin_bit_cast(u32x4, hf[i]));
    }
    wait_lgkm0();
    STAMP(a.ws, cw, 10);
    signal_stores(sm, L_DONE, a.ws + WS_FACT + ti * 256 + gj * 8, tag, lane, 4u);
    STAMP(a.ws, cw, 11);
    p2_final(a, sm, cw, lane, nonce);
}

DEV void b_loader(const Args& a, LAS char* sm, int ti, int gj, int lw, int lane) {
    STAMP(a.ws, lw + 4, 0);
    const unsigned nonce = __hip_atomic_load((GAS unsigned*)(a.ws + WS_NONCE), RLX_AGENT);
    unsigned off[12];
    {
        const int pos = lane & 7, rl = lane >> 3;
#pragma unroll
        for (int u = 0; u < 4; ++u) {
            const int R = 8 * (lw + 4 * u) + rl;
            off[u] = x_token(R) * 1280 + ((pos ^ (R & 7)) << 4);
        }
#pragma unroll
        for (int u = 4; u < 12; ++u) {
            const int wi = lw + 4 * (u - 4), sec = wi / 10, ch = 64 * (wi - 10 * sec) + lane, rho = ch / 10, cc = ch - 10 * rho;
            off[u] = (rho ^ (((rho >> 3) & 1) << 2)) * 3840 + cc * 16 + (sec == 0 ? 1280 : sec == 1 ? 2560 : 0);
        }
    }
    const char* xb = a.x + (size_t)ti * (128 * 1280);
    const char* wb = a.wqkv + 160 * gj;
    const bool full = lw < 2;
    auto issue_a = [&](int s) LAM {
        LAS char* dst = sm + (s % NS_B) * SLOT_B + lw * 1024;
#pragma unroll
        for (int u = 0; u < 4; ++u) glds16(xb + 128 * s + off[u], dst + 4096 * u);
#pragma unroll
        for (int u = 4; u < 6; ++u) glds16(wb + (size_t)(64 * 3840) * s + off[u], dst + 4096 * u);
    };
    auto issue_b = [&](int s) LAM {
        LAS char* dst = sm + (s % NS_B) * SLOT_B + lw * 1024;
#pragma unroll
        for (int u = 6; u < 12; ++u) if (u < 11 || full) glds16(wb + (size_t)(64 * 3840) * s + off[u], dst + 4096 * u);
    };
    STAMP(a.ws, lw + 4, 1);
    issue_a(0); issue_b(0); issue_a(1); issue_b(1);
    if (full) WAIT_VM(12); else WAIT_VM(11);
    bar();
#pragma unroll 1
    for (int s = 0; s < 8; ++s) {
        if (s > 0) ring_wait(sm + L_FREE, s);
        issue_a(s + 2);
        WAIT_VM(6);
        ring_put(sm + L_FULL + 4 * lw, s + 2);
        issue_b(s + 2);
    }
    WAIT_VM(0);
    ring_put(sm + L_FULL + 4 * lw, 10);
    STAMP(a.ws, lw + 4, 2);
    __builtin_amdgcn_s_setprio(2);
    const unsigned tag = mk_tag(nonce);
    const int tl = lw * 64 + lane;
    if (tl < 128) ((LAS float*)(sm + B_G))[tl] = bfr(1.f - gate_of(a, 128 * ti + tl, tag));
    {
        int o0[2], o1[2];
#pragma unroll
        for (int it = 0; it < 2; ++it) {
            const int g = 2 * (tl + 256 * it < 420 ? tl + 256 * it : 0);
            const int h = g / 210, rem = g - 210 * h, e = rem / 10, d = 2 * (rem - 10 * e);
            const bool kv = rem < 200;
            o0[it] = kv ? B_KVT + ((20 * h + e) * 84 + 20 * h + d) * 4 : B_Z + (20 * h + 2 * (rem - 200)) * 4;
            o1[it] = o0[it] + (kv ? 80 * 84 * 4 : 320);
        }
        asm volatile("" : "+v"(o0[0]), "+v"(o0[1]), "+v"(o1[0]), "+v"(o1[1]));
        const __amdgpu_buffer_rsrc_t rk = rsrc(a.ws + WS_KV, 16 * 8 * 840 * 8);
        word_wait(sm + L_KV, 4u);
        STAMP(a.ws, lw + 4, 3);
        u32x4 pv[2];
#pragma unroll
        for (int it = 0; it < 2; ++it) {
            const f32x4 v = *(LAS f32x4*)(sm + o0[it]) + *(LAS f32x4*)(sm + o1[it]);
            pv[it] = (u32x4){pk2(v[0], v[1]), tag, pk2(v[2], v[3]), tag};
        }
        asm volatile("" : "+v"(pv[0]), "+v"(pv[1]) :: "memory");
        wait_lgkm0();
        if (lane == 0) word_add(sm + L_PUB);
        st16_wt(rk, tl * 16, (ti * 8 + gj) * 6720, pv[0]);
        if (tl + 256 < 420) st16_wt(rk, (tl + 256) * 16, (ti * 8 + gj) * 6720, pv[1]);
        asm volatile("s_nop 1" ::"v"(pv[0]), "v"(pv[1]));
    }
    STAMP(a.ws, lw + 4, 4);
    if (lw == 0) {
        const int cnt = ti < 8 ? 53 : 52, g0 = ti < 8 ? 53 * ti : 424 + 52 * (ti - 8);
        const int g = g0 + (lane < cnt ? lane : 0);
        GAS u64* src = (GAS u64*)(a.ws + WS_KV) + gj * 840 + g;
        unsigned v[16];
#pragma unroll 1
        for (unsigned spins = 0; spins < SPIN_CAP; ++spins) {
            u64 x[16];
#pragma unroll
            for (int i = 0; i < 16; ++i) x[i] = __hip_atomic_load(src + i * (8 * 840), RLX_AGENT);
            bool ok = true;
#pragma unroll
            for (int i = 0; i < 16; ++i) { v[i] = (unsigned)x[i]; ok &= (unsigned)(x[i] >> 32) == tag; }
            if (wave_all(ok)) break;
            __builtin_amdgcn_s_sleep(1);
        }
        float s0 = 0.f, s1 = 0.f;
#pragma unroll
        for (int i = 0; i < 16; ++i) { s0 += bf_lo(v[i]); s1 += bf_hi(v[i]); }
        if (lane < cnt) __hip_atomic_store((GAS u64*)(a.ws + WS_TOT) + gj * 840 + g, granule(tag, pk2(s0, s1)), RLX_AGENT);
        STAMP(a.ws, lw + 4, 5);
    } else if (lw == 3) {
        const __amdgpu_buffer_rsrc_t rs = rsrc(a.ws + WS_TOT, 8 * 840 * 8);
        u32x4 v[7];
#pragma unroll 1
        for (unsigned spins = 0; spins < SPIN_CAP; ++spins) {
            bool ok = true;
#pragma unroll
            for (int k = 0; k < 7; ++k) v[k] = __builtin_amdgcn_raw_buffer_load_b128(rs, gj * 6720 + (lane + 64 * k < 420 ? lane + 64 * k : 419) * 16, 0, 16);
#pragma unroll
            for (int k = 0; k < 7; ++k) ok &= (v[k][1] == tag) & (v[k][3] == tag);
            if (wave_all(ok)) break;
            __builtin_amdgcn_s_sleep(SLP_TOT);
        }
#pragma unroll
        for (int k = 0; k < 7; ++k) if (lane + 64 * k < 420) ((LAS u32x2*)(sm + B_TOT))[lane + 64 * k] = (u32x2){v[k][0], v[k][2]};
        wait_lgkm0();
        word_set(sm + L_TOT, 1u);
        STAMP(a.ws, lw + 4, 5);
    }
    __builtin_amdgcn_s_setprio(0);
    bf16x8 fw[P2_PW][5];
    word_wait(sm + L_PUB, 4u);
    p2_prefetch(a, sm, lw, lane, tag, 2, fw);
    p2_main(a, sm, lw, lane, nonce, 2, fw);
}

DEV void b_consumer(const Args& a, LAS char* sm, int ti, int gj, int cw, int lane) {
    const int mh = cw >> 1, nh = cw & 1, c = lane & 15, q = lane >> 4, tid = cw * 64 + lane;
    STAMP(a.ws, cw, 0);
    const unsigned nonce = __hip_atomic_load((GAS unsigned*)(a.ws + WS_NONCE), RLX_AGENT), tag = mk_tag(nonce);
    {
        const __amdgpu_buffer_rsrc_t rk = rsrc(a.ws + WS_KV, 16 * 8 * 840 * 8);
        const u32x4 z = {0u, 0u, 0u, 0u};
        st16_wt(rk, tid * 16, (ti * 8 + gj) * 6720, z);
        if (tid + 256 < 420) st16_wt(rk, (tid + 256) * 16, (ti * 8 + gj) * 6720, z);
        const int cnt = ti < 8 ? 53 : 52, g0 = ti < 8 ? 53 * ti : 424 + 52 * (ti - 8);
        if (tid < cnt) __hip_atomic_store((GAS u64*)(a.ws + WS_TOT) + gj * 840 + g0 + tid, (u64)0, RLX_AGENT);
    }
    __builtin_amdgcn_s_setprio(1);
    f32x4 an[5][4], at[5][2];
#pragma unroll
    for (int t = 0; t < 5; ++t) {
#pragma unroll
        for (int i = 0; i < 4; ++i) an[t][i] = (f32x4){0.f, 0.f, 0.f, 0.f};
        at[t][0] = at[t][1] = (f32x4){0.f, 0.f, 0.f, 0.f};
    }
    int xlo[2], xhi[2], wr[2];
#pragma unroll
    for (int kk = 0; kk < 2; ++kk) {
        const int sw = ((4 * kk + q) ^ (c & 7)) << 4;
        xlo[kk] = (64 * mh + 32 * nh + c) * 128 + sw;
        xhi[kk] = (64 * mh + 32 * (1 - nh) + c) * 128 + sw;
        wr[kk] = 16384 + (8 * q + 4 * (kk ^ (q & 1)) + (c >> 2)) * 160 + 8 * (c & 3);
    }
    bf16x8 xa[2][4], wb[2][5], wt[2][5];
    auto rdp = [&](int buf, LAS char* s0, int kk) LAM {
        LAS char* q0 = s0 + kk * 5120 + wr[0]; LAS char* q1 = s0 + kk * 5120 + wr[1];
        LAS char* a0 = q0 + 96 * nh; LAS char* a1 = q1 + 96 * nh;
        xa[buf][0] = lds_frag(s0 + xlo[kk]);
        wb[buf][0] = lds_tr_frag(a0, a1);
        wb[buf][1] = lds_tr_frag(a0 + 32, a1 + 32);
        xa[buf][1] = lds_frag(s0 + xlo[kk] + 2048);
        wb[buf][2] = lds_tr_frag(q0 + 64 + 10240 * nh, q1 + 64 + 10240 * nh);
        wb[buf][3] = lds_tr_frag(a0 + 10240, a1 + 10240);
        xa[buf][2] = lds_frag(s0 + xhi[kk]);
        wb[buf][4] = lds_tr_frag(a0 + 10272, a1 + 10272);
        wt[buf][0] = lds_tr_frag(q0 + 20480, q1 + 20480);
        xa[buf][3] = lds_frag(s0 + xhi[kk] + 2048);
        wt[buf][1] = lds_tr_frag(q0 + 20512, q1 + 20512);
        wt[buf][2] = lds_tr_frag(q0 + 20544, q1 + 20544);
        wt[buf][3] = lds_tr_frag(q0 + 20576, q1 + 20576);
        wt[buf][4] = lds_tr_frag(q0 + 20608, q1 + 20608);
    };
    auto mm_head = [&](int buf) LAM {
#pragma unroll
        for (int i = 0; i < 4; ++i) an[0][i] = mfma(xa[buf][i], wb[buf][0], an[0][i]);
    };
    auto mm_tail = [&](int buf) LAM {
#pragma unroll
        for (int t = 1; t < 5; ++t)
#pragma unroll
            for (int i = 0; i < 4; ++i) an[t][i] = mfma(xa[buf][i], wb[buf][t], an[t][i]);
#pragma unroll
        for (int t = 0; t < 5; ++t)
#pragma unroll
            for (int i = 0; i < 2; ++i) at[t][i] = mfma(wt[buf][t], xa[buf][i], at[t][i]);
    };
    if (tid < L_WORDS) ((LAS unsigned*)(sm + L_FULL))[tid] = tid < 4 ? 1u : 0u;
#pragma unroll
    for (int t = 0; t < 5; ++t)
        asm volatile("" : "+v"(an[t][0]), "+v"(an[t][1]), "+v"(an[t][2]), "+v"(an[t][3]), "+v"(at[t][0]), "+v"(at[t][1]));
    if (TOUCH) touch_streams(a, sm, cw, lane);
    STAMP(a.ws, cw, 1);
    lds_bar();
    STAMP(a.ws, cw, 2);
    STAMPC(a.ws, cw, 14);
    rdp(0, sm, 0);
    int slot = 0;
#pragma unroll 1
    for (int s = 0; s < 10; ++s) {
        wait_lgkm0();
        SCHED();
        rdp(1, sm + slot * SLOT_B, 1);
        u32x4 fl = *(volatile LAS u32x4*)(sm + L_FULL);
        mm_head(0); mm_tail(0);
        interleave<30, 25, IL_B>();
        SCHED();
        wait_lgkm0();
        asm volatile("" ::: "memory");
        *(volatile LAS unsigned*)(sm + L_FREE + 4 * cw) = s + 1;
        slot = slot == 2 ? 0 : slot + 1;
        const unsigned need = s < 9 ? s + 2 : 0;
        unsigned landed = __builtin_amdgcn_readfirstlane(umin4(fl));
        LAS char* s0 = sm + slot * SLOT_B;
        asm volatile("" : "+s"(landed), "+v"(s0));
        mm_head(1);
        SCHED();
        if (__builtin_expect(landed < need, 0)) ring_spin(sm + L_FULL, need);
        asm volatile("" ::: "memory");
        SCHED();
        rdp(0, s0, 0);
        mm_tail(1);
        interleave<26, 24, 24>();
        SCHED();
    }
    STAMP(a.ws, cw, 3);
    STAMPC(a.ws, cw, 15);
    bf16x8 fr[5][2];
#pragma unroll
    for (int t = 0; t < 2; ++t)
#pragma unroll
        for (int i = 0; i < 4; ++i)
#pragma unroll
            for (int r = 0; r < 4; ++r) an[t][i][r] = phi(an[t][i][r]);
    if (nh == 0) {
#pragma unroll
        for (int i = 0; i < 4; ++i)
#pragma unroll
            for (int r = 0; r < 4; ++r) an[2][i][r] = phi(an[2][i][r]);
    }
#pragma unroll
    for (int t = 0; t < 5; ++t) { fr[t][0] = pack_pair(an[t][0], an[t][1]); fr[t][1] = pack_pair(an[t][2], an[t][3]); }
    {
        LAS char* xs = sm + B_X1 + cw * 2048 + lane * 16;
        *(LAS bf16x8*)(xs + (nh << 10)) = fr[2][0];
        *(LAS bf16x8*)(xs + ((1 ^ nh) << 10)) = fr[2][1];
    }
    wait_lgkm0();
    word_set(sm + L_X1 + 4 * cw, 1u);
#pragma unroll
    for (int t = 0; t < 5; ++t) asm volatile("" : "+v"(fr[t][0]), "+v"(fr[t][1]));
    STAMP(a.ws, cw, 4);
    word_wait(sm + L_X1 + 4 * (cw ^ 1), 1u);
    ring_spin(sm + L_FREE, 10);
    asm volatile("" ::: "memory");
    {
        LAS char* xr = sm + B_X1 + (cw ^ 1) * 2048 + lane * 16;
        const bf16x8 R0 = *(LAS bf16x8*)(xr + (nh << 10)), R1 = *(LAS bf16x8*)(xr + ((1 ^ nh) << 10));
        const bf16x8 ones = as_frag((u32x4){0x3f803f80u, 0x3f803f80u, 0x3f803f80u, 0x3f803f80u});
        const f32x4 z4 = {0.f, 0.f, 0.f, 0.f};
        LAS float* kvt = (LAS float*)(sm + B_KVT) + mh * (80 * 84) + c * 84 + 4 * q;
        LAS float* zz = (LAS float*)(sm + B_Z) + mh * 80 + 4 * q;
        auto prod = [&](const bf16x8& k0, const bf16x8& k1, const bf16x8& v0, const bf16x8& v1, int ta, int tb) LAM {
            *(LAS f32x4*)(kvt + 16 * tb * 84 + 16 * ta) = mfma(k1, v1, mfma(k0, v0, z4));
        };
        auto zsum = [&](const bf16x8& k0, const bf16x8& k1, int ta) LAM {
            const f32x4 d = mfma(k1, ones, mfma(k0, ones, z4));
            if (c == 0) *(LAS f32x4*)(zz + 16 * ta) = d;
        };
        const int b0 = 3 * nh;
        prod(fr[0][0], fr[0][1], fr[3][0], fr[3][1], b0, b0);
        prod(fr[0][0], fr[0][1], fr[4][0], fr[4][1], b0, b0 + 1);
        prod(fr[1][0], fr[1][1], fr[3][0], fr[3][1], b0 + 1, b0);
        prod(fr[1][0], fr[1][1], fr[4][0], fr[4][1], b0 + 1, b0 + 1);
        zsum(fr[0][0], fr[0][1], b0);
        zsum(fr[1][0], fr[1][1], b0 + 1);
        if (nh == 0) {
            prod(fr[2][0], fr[2][1], fr[4][0], fr[4][1], 2, 1);
            prod(fr[1][0], fr[1][1], R0, R1, 1, 2);
            prod(fr[2][0], fr[2][1], R0, R1, 2, 2);
            zsum(fr[2][0], fr[2][1], 2);
        } else {
            prod(fr[0][0], fr[0][1], fr[2][0], fr[2][1], 3, 2);
            prod(R0, R1, fr[3][0], fr[3][1], 2, 3);
        }
    }
    WAIT_VM(0);
    wait_lgkm0();
    if (lane == 0) word_add(sm + L_KV);
    __builtin_amdgcn_s_setprio(0);
    STAMP(a.ws, cw, 5);
    bf16x8 P[2][3];
#pragma unroll
    for (int i = 0; i < 2; ++i) {
#pragma unroll
        for (int t = 0; t < 5; ++t)
#pragma unroll
            for (int r = 0; r < 4; ++r) at[t][i][r] = phi(at[t][i][r]);
        P[i][0] = pack_pair(at[0][i], at[1][i]);
        P[i][1] = pack_pair(at[2][i], at[3][i]);
        P[i][2] = as_frag((u32x4){pk2(at[4][i][0], at[4][i][1]), pk2(at[4][i][2], at[4][i][3]), 0u, 0u});
    }
    int adr[21];
    {
        int dh[5], db[5], eh[5], eb[5];
#pragma unroll
        for (int k = 0; k < 5; ++k) {
            const int d0 = 16 * k + 4 * q, e0 = 16 * k + c;
            dh[k] = d0 / 20; db[k] = B_TOT + dh[k] * 840 + (d0 - 20 * dh[k]) * 2;
            eh[k] = e0 / 20; eb[k] = (e0 - 20 * eh[k]) * 40;
        }
        auto ad = [&](int k, int E) LAM { return dh[k] == eh[E] ? db[k] + eb[E] : L_ZERO; };
        adr[0] = ad(0, 0); adr[1] = ad(1, 0);
        adr[2] = ad(0, 1); adr[3] = ad(1, 1); adr[4] = ad(2, 1); adr[5] = ad(3, 1);
        adr[6] = ad(1, 2); adr[7] = ad(2, 2); adr[8] = ad(3, 2);
        adr[9] = ad(2, 3); adr[10] = ad(3, 3); adr[11] = ad(4, 3);
        adr[12] = ad(3, 4); adr[13] = ad(4, 4);
#pragma unroll
        for (int k = 0; k < 5; ++k) adr[14 + k] = dh[k] == (c & 3) ? db[k] + 800 : L_ZERO;
        adr[19] = L_ZERO; adr[20] = B_G + 4 * (64 * mh + 16 * (c >> 2) + 8 * nh + (c & 3));
    }
#pragma unroll
    for (int i = 0; i < 2; ++i) asm volatile("" : "+v"(P[i][0]), "+v"(P[i][1]), "+v"(P[i][2]));
    asm volatile("" : "+v"(adr[0]), "+v"(adr[1]), "+v"(adr[2]), "+v"(adr[3]), "+v"(adr[4]), "+v"(adr[5]), "+v"(adr[6]),
                      "+v"(adr[7]), "+v"(adr[8]), "+v"(adr[9]), "+v"(adr[10]), "+v"(adr[11]), "+v"(adr[12]), "+v"(adr[13]));
    asm volatile("" : "+v"(adr[14]), "+v"(adr[15]), "+v"(adr[16]), "+v"(adr[17]), "+v"(adr[18]), "+v"(adr[20]));
    const __amdgpu_buffer_rsrc_t ract = rsrc(a.ws + WS_ACT, 128 * TS);
    u32x4 os[2][2]; u32x2 o8[2];
    STAMP(a.ws, cw, 6);
    word_wait(sm + L_TOT, 1u);
    word_wait(sm + L_PUB, 4u);
    STAMP(a.ws, cw, 7);
    {
        auto h4 = [&](int i) LAM -> u32x2 { return *(LAS u32x2*)(sm + adr[i]); };
        auto fr2 = [&](const u32x2& lo, const u32x2& hi) LAM { return as_frag((u32x4){lo[0], lo[1], hi[0], hi[1]}); };
        const u32x2 zz = {0u, 0u};
        const bf16x8 Z0 = fr2(h4(14), h4(15)), Z1 = fr2(h4(16), h4(17)), Z2 = fr2(h4(18), zz);
        const bf16x8 F0 = fr2(h4(0), h4(1)), F1 = fr2(h4(2), h4(3)), F2 = fr2(h4(4), h4(5));
        const bf16x8 F3 = fr2(zz, h4(6)), F4 = fr2(h4(7), h4(8)), F5 = fr2(h4(9), h4(10));
        const bf16x8 F6 = fr2(h4(11), zz), F7 = fr2(zz, h4(12)), F8 = fr2(h4(13), zz);
        const float ogv[2] = {*(LAS float*)(sm + adr[20]), *(LAS float*)(sm + adr[20] + 16)};
        const f32x4 z4 = {0.f, 0.f, 0.f, 0.f};
        const int uu = gj >> 1, odd = gj & 1;
#pragma unroll
        for (int i = 0; i < 2; ++i) {
            const f32x4 dn = mfma(Z2, P[i][2], mfma(Z1, P[i][1], mfma(Z0, P[i][0], z4)));
            const f32x4 n0 = mfma(F0, P[i][0], z4);
            const f32x4 n1 = mfma(F2, P[i][1], mfma(F1, P[i][0], z4));
            const f32x4 n2 = mfma(F4, P[i][1], mfma(F3, P[i][0], z4));
            const f32x4 n3 = mfma(F6, P[i][2], mfma(F5, P[i][1], z4));
            const f32x4 n4 = mfma(F8, P[i][2], mfma(F7, P[i][1], z4));
            const float og = ogv[i];
            const float r0 = og * __builtin_amdgcn_rcpf(dn[0] + 1e-6f), r1 = og * __builtin_amdgcn_rcpf(dn[1] + 1e-6f);
            const float r2 = og * __builtin_amdgcn_rcpf(dn[2] + 1e-6f), r3 = og * __builtin_amdgcn_rcpf(dn[3] + 1e-6f);
            const float s1 = q < 1 ? r0 : r1, s2 = q < 2 ? r1 : r2, s3 = q < 3 ? r2 : r3;
            os[i][0] = (u32x4){pk2(n0[0] * r0, n0[1] * r0), pk2(n0[2] * r0, n0[3] * r0), pk2(n1[0] * s1, n1[1] * s1), pk2(n1[2] * s1, n1[3] * s1)};
            os[i][1] = (u32x4){pk2(n2[0] * s2, n2[1] * s2), pk2(n2[2] * s2, n2[3] * s2), pk2(n3[0] * s3, n3[1] * s3), pk2(n3[2] * s3, n3[3] * s3)};
            o8[i] = (u32x2){pk2(n4[0] * r3, n4[1] * r3), pk2(n4[2] * r3, n4[3] * r3)};
        }
#pragma unroll
        for (int i = 0; i < 2; ++i) {
            const unsigned ob = (8 * ti + 4 * mh + 2 * nh + i) * TS + (40 + 5 * uu) * 1024;
            st16_wt(ract, lane * 16, ob + (odd ? 3072 : 0), os[i][0]);
            st16_wt(ract, lane * 16, ob + (odd ? 4096 : 1024), os[i][1]);
            st8_wt(ract, lane * 16 + 8 * odd, ob + 2048, o8[i]);
        }
    }
    KEEP(os[0][0]); KEEP(os[0][1]); KEEP(os[1][0]); KEEP(os[1][1]);
    STAMP(a.ws, cw, 8);
    signal_stores(sm, L_DONE, a.ws + WS_FACT + ti * 256 + 128 + gj * 8, tag, lane, 4u);
    STAMP(a.ws, cw, 9);
    p2_final(a, sm, cw, lane, nonce);
}

}

__global__ __launch_bounds__(512, 2) NOPK void gm_fused(const void* x, const void* lre, const void* lim, const void* bre,
                                                   const void* bim, const void* cre, const void* cim, const void* logdt,
                                                   const void* dskip, const void* wqkv, const void* wo, const void* gw,
                                                   void* ws, void* out) {
    __shared__ __attribute__((aligned(1024))) char smem[LDS_BYTES];
    LAS char* sm = (LAS char*)smem;
#define MAPMODE 2
    const int b = blockIdx.x, xcd = b & 7, idx = b >> 3;
    const bool roleA = MAPMODE == 1 ? idx < 16 : MAPMODE == 2 ? idx >= 16 : xcd < 4;
    const int ti = MAPMODE != 0 ? 4 * (xcd >> 1) + (idx & 3) : 8 * ((xcd & 3) >> 1) + (idx & 7);
    const int gj = MAPMODE != 0 ? 4 * (xcd & 1) + ((idx >> 2) & 3) : 4 * (xcd & 1) + (idx >> 3);
    const int wave = __builtin_amdgcn_readfirstlane(threadIdx.x >> 6), lane = threadIdx.x & 63;
    const Args a{(const char*)x, (const u16*)lre, (const u16*)lim, (const char*)bre, (const char*)bim, (const char*)cre,
                 (const char*)cim, (const u16*)logdt, (const char*)wqkv, (const char*)wo, (const u16*)gw,
                 (const u16*)dskip, (char*)ws, (float*)out};
    STAMPL(ws, 0);
    if (roleA) {
        if (wave < 4) a_consumer(a, sm, ti, gj, wave, lane);
        else a_loader(a, sm, ti, gj, wave - 4, lane);
    } else {
        if (wave < 4) b_consumer(a, sm, ti, gj, wave, lane);
        else b_loader(a, sm, ti, gj, wave - 4, lane);
    }
    STAMPL(ws, 1);
}

extern "C" void kernel_launch(void* const* d_in, const int* in_sizes, int n_in, void* d_out, int out_size, void* d_ws,
                              size_t ws_size, hipStream_t stream) {
    gm_fused<<<256, 512, 0, stream>>>(d_in[0], d_in[1], d_in[2], d_in[3], d_in[4], d_in[5], d_in[6], d_in[7], d_in[8],
                                      d_in[10], d_in[11], d_in[12], d_ws, d_out);
}
```

```cpp
#include <hip/hip_runtime.h>
#include <hip/hip_bf16.h>
#include <stdint.h>

namespace {

typedef __attribute__((ext_vector_type(8))) short bf16x8;
typedef __attribute__((ext_vector_type(4))) short s16x4;
typedef __attribute__((ext_vector_type(4))) float f32x4;
typedef __attribute__((ext_vector_type(2))) float f32x2;
typedef __attribute__((ext_vector_type(2))) __bf16 bf16x2_t;
typedef __attribute__((ext_vector_type(4))) unsigned u32x4;
typedef __attribute__((ext_vector_type(2))) unsigned u32x2;
typedef unsigned long long u64;
typedef unsigned short u16;
#define LAS __attribute__((address_space(3)))
#define GAS __attribute__((address_space(1)))
#if defined(__HIP_DEVICE_COMPILE__)
#define NOPK __attribute__((target("no-packed-fp32-ops")))
#else
#define NOPK
#endif
#define DEV __device__ __forceinline__
#define LAM __attribute__((always_inline))
#define RLX_AGENT __ATOMIC_RELAXED, __HIP_MEMORY_SCOPE_AGENT

constexpr size_t WS_NONCE = 0;
constexpr size_t WS_EA    = 4096;
constexpr size_t WS_KV    = WS_EA + 16 * 8 * 160 * 8;
constexpr size_t WS_TOT   = WS_KV + 16 * 8 * 840 * 8;
constexpr size_t WS_G     = WS_TOT + 8 * 840 * 8;
constexpr size_t WS_FACT  = WS_G + 2048 * 8;
constexpr size_t WS_FW2T  = WS_FACT + 16 * 256;
constexpr int NREP = 16;
constexpr size_t WS_W2T   = (WS_FW2T + NREP * 1024 + 1023) / 1024 * 1024;
constexpr size_t TS = 61440;
constexpr size_t WS_ACT   = WS_W2T + 40 * TS;
constexpr size_t WS_PROF  = 32u << 20;

#define STAMP(ws, wave, i) do {} while (0)
#define STAMPC(ws, wave, i) do {} while (0)
#define TIMED(acc, stmt) do { stmt; } while (0)
#define STAMPV(ws, wave, i, v) do {} while (0)
#define STAMP2(ws, wave, i) do {} while (0)

#define STAMPL(ws, i) do {} while (0)

constexpr int LDS_BYTES = 163840;
constexpr int SLOT_A = 36864, NS_A = 4;
constexpr int A_STG  = 110592;
constexpr int A_TAB  = 147456;
constexpr int A_JTAB = A_TAB + 3840;
constexpr int A_G    = A_JTAB + 2048;
constexpr int A_SEG  = A_G + 512;
constexpr int A_HT = 47104, HT_STRIDE = 272;
static_assert(A_SEG + 5120 <= 159776, "lds");
constexpr int SLOT_B = 47104, NS_B = 3;
constexpr int B_KVT = 0;
constexpr int B_Z   = B_KVT + 2 * 80 * 84 * 4;
constexpr int B_X1  = 141312;
constexpr int B_G   = B_X1 + 8192;
constexpr int B_TOT = B_G + 512;
static_assert(B_TOT + 3360 <= 159776, "lds");

#define SLP_W2T 8
#define SLP_ACT 4
#define SLP_LB 2
#define SLP_TOT 2
#define IL_A 16
#define IL_B 27
constexpr unsigned SPIN_CAP = 1u << 18;

DEV unsigned pk2(float a, float b) {
    return __builtin_bit_cast(unsigned, __builtin_convertvector((f32x2){a, b}, bf16x2_t));
}
DEV float bfr(float a) { return __uint_as_float(pk2(a, 0.f) << 16); }
DEV float bf_lo(unsigned u) { return __uint_as_float(u << 16); }
DEV float bf_hi(unsigned u) { return __uint_as_float(u & 0xffff0000u); }
DEV float bf2f(u16 u) { return __uint_as_float((unsigned)u << 16); }
DEV float swp(float x) {
    return __builtin_bit_cast(float, __builtin_amdgcn_mov_dpp(__builtin_bit_cast(int, x), 0xB1, 0xF, 0xF, true));
}
DEV float cmul_add(float a, float bs, float e, float u) { return fmaf(bs, swp(e), fmaf(a, e, u)); }
DEV f32x4 mfma(bf16x8 a, bf16x8 b, f32x4 c) { return __builtin_amdgcn_mfma_f32_16x16x32_bf16(a, b, c, 0, 0, 0); }
DEV bf16x8 as_frag(u32x4 v) { return __builtin_bit_cast(bf16x8, v); }
DEV bf16x8 lds_frag(LAS char* p) { return *(LAS bf16x8*)p; }
DEV bf16x8 lds_tr_frag(LAS char* p0, LAS char* p1) {
    s16x4 lo = __builtin_amdgcn_ds_read_tr16_b64_v4i16((LAS s16x4*)p0);
    s16x4 hi = __builtin_amdgcn_ds_read_tr16_b64_v4i16((LAS s16x4*)p1);
    return __builtin_shufflevector(lo, hi, 0, 1, 2, 3, 4, 5, 6, 7);
}
DEV void glds16_wt(const char* g, unsigned l) {
    unsigned keep;
    asm volatile("s_mov_b32 %0, m0\n\ts_mov_b32 m0, %2\n\ts_nop 0\n\tglobal_load_lds_dwordx4 %1, off sc1\n\ts_mov_b32 m0, %0"
                 : "=&s"(keep) : "v"(g), "s"(l) : "memory");
}
DEV void glds16(const char* g, LAS char* l) {
    __builtin_amdgcn_global_load_lds((const GAS unsigned*)g, (LAS unsigned*)l, 16, 0, 0);
}
DEV void bar() { asm volatile("" ::: "memory"); __builtin_amdgcn_s_barrier(); asm volatile("" ::: "memory"); }
DEV void wait_lgkm0() { __builtin_amdgcn_s_waitcnt(0xC07F); }
DEV void lds_bar() { asm volatile("" ::: "memory"); wait_lgkm0(); bar(); }
constexpr int L_FULL = 159776, L_FREE = L_FULL + 16;
constexpr int L_DONE = L_FULL + 32;
constexpr int L_KV = L_FULL + 36;
constexpr int L_W2T = L_FULL + 40;
constexpr int L_ACTH = L_FULL + 44, L_ACTO = L_FULL + 76;
constexpr int L_X1 = L_FULL + 48, L_ZERO = L_FULL + 64;
constexpr int L_DONE2 = L_FULL + 72;
constexpr int L_WR = L_FULL + 84;
constexpr int L_TOT = L_FULL + 88;
constexpr int L_HS = L_FULL + 96, L_HT = L_FULL + 100;
constexpr int L_PUB = L_FULL + 108;
constexpr int L_WORDS = 28;
DEV unsigned umin4(const u32x4& f) { const unsigned a = f[0] < f[1] ? f[0] : f[1], b = f[2] < f[3] ? f[2] : f[3]; return a < b ? a : b; }
DEV void ring_put(LAS char* p, unsigned v) {
    asm volatile("ds_write_b32 %0, %1" ::"v"((unsigned)(size_t)p), "v"(v) : "memory");
}
DEV void ring_wait(LAS char* p, unsigned need) {
#pragma unroll 1
    for (unsigned spins = 0; spins < SPIN_CAP; ++spins) {
        u32x4 f;
        asm volatile("ds_read_b128 %0, %1\n\ts_waitcnt lgkmcnt(0)" : "=&v"(f) : "v"((unsigned)(size_t)p) : "memory");
        if ((unsigned)__builtin_amdgcn_readfirstlane(umin4(f)) >= need) break;
        __builtin_amdgcn_s_sleep(1);
    }
}
DEV void word_wait(LAS char* p, unsigned need) {
#pragma unroll 1
    for (unsigned spins = 0; spins < SPIN_CAP; ++spins) {
        if ((unsigned)__builtin_amdgcn_readfirstlane(*(volatile LAS unsigned*)p) >= need) break;
        __builtin_amdgcn_s_sleep(1);
    }
    asm volatile("" ::: "memory");
}
DEV void word_set(LAS char* p, unsigned v) { asm volatile("" ::: "memory"); *(volatile LAS unsigned*)p = v; }
DEV unsigned word_add(LAS char* p) {
    return __hip_atomic_fetch_add((LAS unsigned*)p, 1u, __ATOMIC_RELAXED, __HIP_MEMORY_SCOPE_WORKGROUP);
}
DEV void ring_spin(LAS char* p, unsigned need) {
#pragma unroll 1
    for (unsigned spins = 0; spins < SPIN_CAP; ++spins) {
        const u32x4 f = *(volatile LAS u32x4*)p;
        if ((unsigned)__builtin_amdgcn_readfirstlane(umin4(f)) >= need) break;
    }
}
#define WAIT_VM(n) asm volatile("s_waitcnt vmcnt(" #n ")" ::: "memory")
#define SCHED() __builtin_amdgcn_sched_barrier(0)
#define KEEP(x) asm volatile("" ::"v"(x))
template <int NM, int NR, int NS> DEV void interleave() {
#pragma unroll
    for (int i = 0; i < NS; ++i) {
        __builtin_amdgcn_sched_group_barrier(0x8, 1, 0);
        if ((i + 1) * NR / NS != i * NR / NS) __builtin_amdgcn_sched_group_barrier(0x100, 1, 0);
    }
    __builtin_amdgcn_sched_group_barrier(0x8, NM - NS, 0);
}
DEV bool wave_all(bool p) { return __builtin_amdgcn_ballot_w64(p) == __builtin_amdgcn_ballot_w64(true); }
DEV float lane_xor(float v, int m) {
    const int x = __builtin_bit_cast(int, v);
    const int y = m == 1 ? __builtin_amdgcn_mov_dpp(x, 0xB1, 0xF, 0xF, true) : m == 2 ? __builtin_amdgcn_mov_dpp(x, 0x4E, 0xF, 0xF, true)
                                                                              : __builtin_amdgcn_ds_swizzle(x, 0x101F);
    return __builtin_bit_cast(float, y);
}
DEV float fexp(float x) { return __builtin_amdgcn_exp2f(x * 1.44269504088896341f); }
DEV unsigned mk_tag(unsigned n) { return ((n + 1u) * 0x9E3779B1u) | 1u; }
DEV u64 granule(unsigned tag, unsigned v) { return ((u64)tag << 32) | v; }
DEV u64 flag_of(unsigned tag) { return granule(tag, tag * 0x85EBCA6Bu + 0x5BD1E995u); }
DEV __amdgpu_buffer_rsrc_t rsrc(const char* p, unsigned bytes) { return __builtin_amdgcn_make_buffer_rsrc((void*)p, 0, bytes, 0x00020000); }
DEV void st16_wt(const __amdgpu_buffer_rsrc_t& r, unsigned voff, unsigned soff, u32x4 v) { __builtin_amdgcn_raw_buffer_store_b128(v, r, voff + soff, 0, 16); }
DEV void st8_wt(const __amdgpu_buffer_rsrc_t& r, unsigned voff, unsigned soff, u32x2 v) { __builtin_amdgcn_raw_buffer_store_b64(v, r, voff + soff, 0, 16); }
DEV bf16x8 ld16_wt(const __amdgpu_buffer_rsrc_t& r, unsigned voff, unsigned soff) {
    return __builtin_bit_cast(bf16x8, __builtin_amdgcn_raw_buffer_load_b128(r, voff, soff, 16));
}
DEV void signal_stores(LAS char* sm, int word, char* flag, unsigned tag, int lane, unsigned waves, int replicas = 1) {
    asm volatile("s_waitcnt vmcnt(0)" ::: "memory");
    unsigned old = 0;
    if (lane == 0) old = __hip_atomic_fetch_add((LAS unsigned*)(sm + word), 1u, __ATOMIC_RELAXED, __HIP_MEMORY_SCOPE_WORKGROUP);
    old = __builtin_amdgcn_readfirstlane(old);
    if (old == waves - 1u && lane < replicas) __hip_atomic_store((GAS u64*)(flag + lane * 1024), flag_of(tag), RLX_AGENT);
}
DEV float phi(float t) {
    const float e = __builtin_amdgcn_fmed3f(__builtin_amdgcn_exp2f(t * 1.44269504088896341f), 0.f, 1.f);
    return (e + 1e-6f) + __builtin_amdgcn_fmed3f(t, 0.f, __builtin_inff());
}
DEV bf16x8 pack_pair(const f32x4& lo, const f32x4& hi) {
    return as_frag((u32x4){pk2(lo[0], lo[1]), pk2(lo[2], lo[3]), pk2(hi[0], hi[1]), pk2(hi[2], hi[3])});
}

DEV void scan_step(float& u0, float& u1, float& u2, float& u3, float& u4, const float (&e)[5], const float (&a)[5],
                   const float (&b)[5]) {
    asm("v_fmac_f32 %0, %10, %5\n\tv_fmac_f32 %1, %11, %6\n\tv_fmac_f32 %2, %12, %7\n\t"
        "v_fmac_f32 %3, %13, %8\n\tv_fmac_f32 %4, %14, %9\n\t"
        "v_fmac_f32_dpp %0, %5, %15 quad_perm:[1,0,3,2] row_mask:0xf bank_mask:0xf bound_ctrl:1\n\t"
        "v_fmac_f32_dpp %1, %6, %16 quad_perm:[1,0,3,2] row_mask:0xf bank_mask:0xf bound_ctrl:1\n\t"
        "v_fmac_f32_dpp %2, %7, %17 quad_perm:[1,0,3,2] row_mask:0xf bank_mask:0xf bound_ctrl:1\n\t"
        "v_fmac_f32_dpp %3, %8, %18 quad_perm:[1,0,3,2] row_mask:0xf bank_mask:0xf bound_ctrl:1\n\t"
        "v_fmac_f32_dpp %4, %9, %19 quad_perm:[1,0,3,2] row_mask:0xf bank_mask:0xf bound_ctrl:1"
        : "+v"(u0), "+v"(u1), "+v"(u2), "+v"(u3), "+v"(u4)
        : "v"(e[0]), "v"(e[1]), "v"(e[2]), "v"(e[3]), "v"(e[4]), "v"(a[0]), "v"(a[1]), "v"(a[2]), "v"(a[3]), "v"(a[4]),
          "v"(b[0]), "v"(b[1]), "v"(b[2]), "v"(b[3]), "v"(b[4]));
}

DEV void scan_step_nd(float (&t)[5], float u0, float u1, float u2, float u3, float u4, const float (&e)[5],
                      const float (&a)[5], const float (&b)[5]) {
    asm("v_fma_f32 %0, %15, %10, %5\n\tv_fma_f32 %1, %16, %11, %6\n\tv_fma_f32 %2, %17, %12, %7\n\t"
        "v_fma_f32 %3, %18, %13, %8\n\tv_fma_f32 %4, %19, %14, %9\n\t"
        "v_fmac_f32_dpp %0, %10, %20 quad_perm:[1,0,3,2] row_mask:0xf bank_mask:0xf bound_ctrl:1\n\t"
        "v_fmac_f32_dpp %1, %11, %21 quad_perm:[1,0,3,2] row_mask:0xf bank_mask:0xf bound_ctrl:1\n\t"
        "v_fmac_f32_dpp %2, %12, %22 quad_perm:[1,0,3,2] row_mask:0xf bank_mask:0xf bound_ctrl:1\n\t"
        "v_fmac_f32_dpp %3, %13, %23 quad_perm:[1,0,3,2] row_mask:0xf bank_mask:0xf bound_ctrl:1\n\t"
        "v_fmac_f32_dpp %4, %14, %24 quad_perm:[1,0,3,2] row_mask:0xf bank_mask:0xf bound_ctrl:1"
        : "=&v"(t[0]), "=&v"(t[1]), "=&v"(t[2]), "=&v"(t[3]), "=&v"(t[4])
        : "v"(u0), "v"(u1), "v"(u2), "v"(u3), "v"(u4), "v"(e[0]), "v"(e[1]), "v"(e[2]), "v"(e[3]), "v"(e[4]),
          "v"(a[0]), "v"(a[1]), "v"(a[2]), "v"(a[3]), "v"(a[4]), "v"(b[0]), "v"(b[1]), "v"(b[2]), "v"(b[3]), "v"(b[4]));
}

struct Args {
    const char* x; const u16* lre; const u16* lim; const char* bre; const char* bim; const char* cre; const char* cim;
    const u16* logdt; const char* wqkv; const char* wo; const u16* gw; const u16* dskip; char* ws; float* out;
    const char* code;
};

DEV void channel_consts(u16 lre, u16 lim, u16 logdt, float& ar, float& ai, float& cr, float& ci) {
    const float lr = bf2f(lre), li = bf2f(lim);
    const float dt = bfr(fexp(bf2f(logdt)));
    const float zr = dt * lr, zi = dt * li, ex = fexp(zr);
    const float rv = zi * 0.15915494309189535f, rr = rv - __builtin_rintf(rv);
    ar = ex * __builtin_amdgcn_cosf(rr); ai = ex * __builtin_amdgcn_sinf(rr);
    const float nr = ar - 1.f, ni = ai, dn = lr * lr + li * li;
    cr = (nr * lr + ni * li) / dn; ci = (ni * lr - nr * li) / dn;
}
DEV float dot2(unsigned x, unsigned y, float s) {
    return __builtin_amdgcn_fdot2_f32_bf16(__builtin_bit_cast(bf16x2_t, x), __builtin_bit_cast(bf16x2_t, y), s, false);
}

constexpr int P2_RED = 0;
constexpr int P2_PW = 4, P2_PA = 5;
DEV bool flags_up(const u32x4& v, u64 fl) {
    const unsigned f0 = (unsigned)fl, f1 = (unsigned)(fl >> 32);
    return wave_all((v[0] == f0) & (v[1] == f1) & (v[2] == f0) & (v[3] == f1));
}
DEV void wait_flags8(const Args& a, int ti, int part, u64 fl) {
    GAS u64* fp = (GAS u64*)(a.ws + WS_FACT) + ti * 32 + part * 16 + (threadIdx.x & 7);
#pragma unroll 1
    for (unsigned spins = 0; spins < SPIN_CAP; ++spins) {
        if (wave_all(__hip_atomic_load(fp, RLX_AGENT) == fl)) break;
        __builtin_amdgcn_s_sleep(SLP_ACT);
    }
}
DEV unsigned p2_koff(int lw, int st) { return (st < 10 ? 10 * lw + st : 30 + 5 * lw + st) * 1024; }
DEV int p2_unit(int lw, int i) { return 5120 * (7 * lw + i); }
DEV void p2_prefetch(const Args& a, LAS char* sm, int lw, int lane, unsigned tag, int poller, bf16x8 (&fw)[P2_PW][5]) {
    const int b = blockIdx.x, xcd = b & 7, idx = b >> 3;
    const int cg = (xcd & 1) * 4 + (idx >> 3);
    if (lw == poller) {
        const __amdgpu_buffer_rsrc_t rf = rsrc(a.ws + WS_FW2T + (xcd * 2 + (b >> 7)) * 1024, 1024);
#pragma unroll 1
        for (unsigned spins = 0; spins < SPIN_CAP; ++spins) {
            if (flags_up(__builtin_amdgcn_raw_buffer_load_b128(rf, lane * 16, 0, 16), flag_of(tag))) break;
            __builtin_amdgcn_s_sleep(SLP_W2T);
        }
        word_set(sm + L_W2T, 1u);
    } else word_wait(sm + L_W2T, 1u);
    STAMP2(a.ws, lw + 4, 0);
    const unsigned wbase = 5 * cg * TS, vo = lane * 16;
    const char* src = a.ws + WS_W2T + wbase + vo;
#pragma unroll
    for (int i = 0; i < 7; ++i)
#pragma unroll
        for (int n = 0; n < 5; ++n)
            glds16_wt(src + (size_t)n * TS + p2_koff(lw, 4 + i), (unsigned)(size_t)(sm + p2_unit(lw, i) + n * 1024));
    const __amdgpu_buffer_rsrc_t rw = rsrc(a.ws + WS_W2T, 40 * TS);
#pragma unroll
    for (int st = 0; st < P2_PW; ++st)
#pragma unroll
        for (int n = 0; n < 5; ++n) fw[st][n] = ld16_wt(rw, vo, wbase + n * TS + p2_koff(lw, st));
}
DEV void p2_main(const Args& a, LAS char* sm, int lw, int lane, unsigned nonce, int poller, bf16x8 (&fw)[P2_PW][5]) {
    const int b = blockIdx.x, xcd = b & 7, idx = b >> 3;
    const int tt = (xcd >> 1) * 8 + (idx & 7), cg = (xcd & 1) * 4 + (idx >> 3);
    const int c = lane & 15, q = lane >> 4;
    const unsigned tag = mk_tag(nonce);
    const u64 fl = flag_of(tag);
    constexpr int PA = P2_PA;
    const __amdgpu_buffer_rsrc_t rw = rsrc(a.ws + WS_W2T, 40 * TS), ra = rsrc(a.ws + WS_ACT, 128 * TS);
    const unsigned wbase = 5 * cg * TS, abase = 4 * tt * TS, vo = lane * 16;
    bf16x8 fa[PA][4];
    auto ldw = [&](int st, int sl) LAM {
#pragma unroll
        for (int n = 0; n < 5; ++n) fw[sl][n] = ld16_wt(rw, vo, wbase + n * TS + p2_koff(lw, st));
    };
    auto lda = [&](int st) LAM {
#pragma unroll
        for (int t = 0; t < 4; ++t) fa[st % PA][t] = ld16_wt(ra, vo, abase + t * TS + p2_koff(lw, st));
    };
    WAIT_VM(0);
    if (lw == poller) { wait_flags8(a, tt >> 1, 0, fl); word_set(sm + L_ACTH, 1u); }
    else word_wait(sm + L_ACTH, 1u);
    STAMP2(a.ws, lw + 4, 1);
#pragma unroll
    for (int st = 0; st < 3; ++st) lda(st);
    f32x4 acc[5][4];
    const f32x4 z4 = {0.f, 0.f, 0.f, 0.f};
    auto slot = [&](int st) LAM { return st < 4 ? st : st <= 10 ? (st & 1) : st == 11 ? 2 : st == 12 ? 3 : st == 13 ? 1 : 0; };
#pragma unroll
    for (int st = 0; st < 15; ++st) {
        if (st <= 4) lda(st + 3);
        if (st == 6) word_wait(sm + L_ACTO, 1u);
        if (st >= 4 && st + 4 < 15) lda(st + 4);
        SCHED();
        if (st >= 3 && st < 10) {
#pragma unroll
            for (int n = 0; n < 5; ++n) fw[slot(st + 1)][n] = lds_frag(sm + p2_unit(lw, st - 3) + vo + n * 1024);
        }
#pragma unroll
        for (int n = 0; n < 5; ++n)
#pragma unroll
            for (int t = 0; t < 4; ++t) acc[n][t] = mfma(fw[slot(st)][n], fa[st % PA][t], st == 0 ? z4 : acc[n][t]);
        SCHED();
        if (st == 7) ldw(11, slot(11));
        if (st == 9) { wait_lgkm0(); if (lane == 0) word_add(sm + L_WR); ldw(12, slot(12)); }
        if (st == 10) ldw(13, slot(13));
        if (st == 11) ldw(14, slot(14));
    }
    STAMP2(a.ws, lw + 4, 2);
    word_wait(sm + L_WR, 4u);
    word_wait(sm + L_DONE, 4u);
    LAS float* rw_ = (LAS float*)(sm + P2_RED) + lw * (64 * 84);
#pragma unroll
    for (int t = 0; t < 4; ++t)
#pragma unroll
        for (int n = 0; n < 5; ++n) *(LAS f32x4*)(rw_ + (16 * t + c) * 84 + 16 * n + 4 * q) = acc[n][t];
    lds_bar();
    STAMP2(a.ws, lw + 4, 3);
}

DEV void p2_final(const Args& a, LAS char* sm, int cw, int lane, unsigned nonce) {
    const unsigned tag = mk_tag(nonce);
    const int b = blockIdx.x, xcd = b & 7, idx = b >> 3;
    const int tt = (xcd >> 1) * 8 + (idx & 7), cg = (xcd & 1) * 4 + (idx >> 3);
    const int tid = cw * 64 + lane;
    u32x2 xv[5], dv[5]; float gv[5];
    int tok[5], n0[5];
#pragma unroll
    for (int i = 0; i < 5; ++i) {
        const int id = tid + 256 * i, r = id / 20, c4 = id - 20 * r;
        tok[i] = 64 * tt + 16 * ((r & 15) >> 2) + 4 * (r >> 4) + (r & 3); n0[i] = 80 * cg + 4 * c4;
        xv[i] = *(const u32x2*)((const u16*)a.x + tok[i] * 640 + n0[i]);
        dv[i] = *(const u32x2*)(a.dskip + n0[i]);
    }
#pragma unroll 1
    for (unsigned spins = 0; spins < SPIN_CAP; ++spins) {
        u64 v[5];
#pragma unroll
        for (int i = 0; i < 5; ++i) v[i] = __hip_atomic_load((GAS u64*)(a.ws + WS_G) + tok[i], RLX_AGENT);
        bool ok = true;
#pragma unroll
        for (int i = 0; i < 5; ++i) { gv[i] = __uint_as_float((unsigned)v[i]); ok &= (unsigned)(v[i] >> 32) == tag; }
        if (wave_all(ok)) break;
        __builtin_amdgcn_s_sleep(2);
    }
    f32x4 sk[5];
#pragma unroll
    for (int i = 0; i < 5; ++i) {
        const unsigned p01 = pk2(bf_lo(xv[i][0]) * bf_lo(dv[i][0]), bf_hi(xv[i][0]) * bf_hi(dv[i][0]));
        const unsigned p23 = pk2(bf_lo(xv[i][1]) * bf_lo(dv[i][1]), bf_hi(xv[i][1]) * bf_hi(dv[i][1]));
        sk[i] = (f32x4){gv[i] * bf_lo(p01), gv[i] * bf_hi(p01), gv[i] * bf_lo(p23), gv[i] * bf_hi(p23)};
    }
    asm volatile("" : "+v"(sk[0]), "+v"(sk[1]), "+v"(sk[2]), "+v"(sk[3]), "+v"(sk[4]));
    if (cw == 0) {
        wait_flags8(a, tt >> 1, 1, flag_of(tag));
        word_set(sm + L_ACTO, 1u);
        if (tt == 31 && cg == 0) {
            word_wait(sm + L_ACTH, 1u);
            if (lane == 0) __hip_atomic_store((GAS unsigned*)(a.ws + WS_NONCE), nonce + 1u, RLX_AGENT);
        }
    }
    STAMP2(a.ws, cw, 0);
    bar();
    STAMP2(a.ws, cw, 2);
#pragma unroll
    for (int i = 0; i < 5; ++i) {
        const int id = tid + 256 * i, r = id / 20, c4 = id - 20 * r;
        LAS float* p = (LAS float*)(sm + P2_RED) + r * 84 + 4 * c4;
        sk[i] += (*(LAS f32x4*)p + *(LAS f32x4*)(p + 64 * 84)) + (*(LAS f32x4*)(p + 128 * 84) + *(LAS f32x4*)(p + 192 * 84));
    }
    asm volatile("" : "+v"(sk[0]), "+v"(sk[1]), "+v"(sk[2]), "+v"(sk[3]), "+v"(sk[4]));
#define OUT_AUX 16
    {
        const __amdgpu_buffer_rsrc_t ro = rsrc((const char*)a.out, 2048 * 640 * 4);
#pragma unroll
        for (int i = 0; i < 5; ++i)
            __builtin_amdgcn_raw_buffer_store_b128(__builtin_bit_cast(u32x4, sk[i]), ro, (tok[i] * 640 + n0[i]) * 4, 0, OUT_AUX);
    }
    STAMP2(a.ws, cw, 3);
}

DEV void job_issue(const Args& a, LAS char* stg, int jb, int lane) {
    if (jb < 400) {
        const int cb = jb / 40, nt = jb - 40 * cb;
        const unsigned o = (16 * nt + (lane >> 3)) * 1280 + cb * 128 + (lane & 7) * 16;
        glds16(a.cre + o, stg); glds16(a.cre + o + 8 * 1280, stg + 1024);
        glds16(a.cim + o, stg + 2048); glds16(a.cim + o + 8 * 1280, stg + 3072);
    } else if (jb < 500) {
        const int jo = jb - 400, s5 = jo / 5, n8 = jo - 5 * s5, uu = s5 / 5, s = s5 - 5 * uu;
        const int lo = 160 * uu + (s == 0 ? 0 : s == 1 ? 32 : s == 2 ? 64 : s == 3 ? 80 : 112);
        const int hi = 160 * uu + (s == 0 ? 16 : s == 1 ? 48 : s == 2 ? 144 : s == 3 ? 96 : 128);
        const unsigned o = (lane >> 4) * 1280 + n8 * 256 + (lane & 15) * 16;
#pragma unroll
        for (int k = 0; k < 4; ++k) {
            glds16(a.wo + (lo + 4 * k) * 1280 + o, stg + 1024 * k);
            glds16(a.wo + (hi + 4 * k) * 1280 + o, stg + 4096 + 1024 * k);
        }
    }
}
DEV u32x2 lds_rd8(LAS char* p) { u32x2 v; asm volatile("ds_read_b64 %0, %1" : "=v"(v) : "v"((unsigned)(size_t)p) : "memory"); return v; }
DEV f32x4 lds_rd16(LAS char* p) { f32x4 v; asm volatile("ds_read_b128 %0, %1" : "=v"(v) : "v"((unsigned)(size_t)p) : "memory"); return v; }
DEV u32x2 lds_rdtr(LAS char* p) { u32x2 v; asm volatile("ds_read_b64_tr_b16 %0, %1" : "=v"(v) : "v"((unsigned)(size_t)p) : "memory"); return v; }
DEV void job_finish(const Args& a, LAS char* stg, LAS char* tab, int jb, int lane) {
    const int c = lane & 15, q = lane >> 4;
    const __amdgpu_buffer_rsrc_t rw = rsrc(a.ws + WS_W2T, 40 * TS);
    if (jb < 400) {
        const int cb = jb / 40, nt = jb - 40 * cb;
        u32x2 re[4], im[4]; f32x4 c01[4], c23[4];
#pragma unroll
        for (int k = 0; k < 4; ++k) {
            re[k] = lds_rd8(stg + c * 128 + 32 * k + 8 * q); im[k] = lds_rd8(stg + 2048 + c * 128 + 32 * k + 8 * q);
            c01[k] = lds_rd16(tab + (16 * k + 4 * q) * 8); c23[k] = lds_rd16(tab + (16 * k + 4 * q) * 8 + 16);
        }
        asm volatile("s_waitcnt lgkmcnt(0)" : "+v"(re[0]), "+v"(re[1]), "+v"(re[2]), "+v"(re[3]), "+v"(im[0]), "+v"(im[1]), "+v"(im[2]), "+v"(im[3]),
                     "+v"(c01[0]), "+v"(c01[1]), "+v"(c01[2]), "+v"(c01[3]), "+v"(c23[0]), "+v"(c23[1]), "+v"(c23[2]), "+v"(c23[3]) :: "memory");
        u32x4 w[4];
#pragma unroll
        for (int k = 0; k < 4; ++k) {
            const float r0 = bf_lo(re[k][0]), r1 = bf_hi(re[k][0]), r2 = bf_lo(re[k][1]), r3 = bf_hi(re[k][1]);
            const float i0 = bf_lo(im[k][0]), i1 = bf_hi(im[k][0]), i2 = bf_lo(im[k][1]), i3 = bf_hi(im[k][1]);
            w[k] = (u32x4){pk2(r0 * c01[k][0] - i0 * c01[k][1], -(r0 * c01[k][1] + i0 * c01[k][0])),
                           pk2(r1 * c01[k][2] - i1 * c01[k][3], -(r1 * c01[k][3] + i1 * c01[k][2])),
                           pk2(r2 * c23[k][0] - i2 * c23[k][1], -(r2 * c23[k][1] + i2 * c23[k][0])),
                           pk2(r3 * c23[k][2] - i3 * c23[k][3], -(r3 * c23[k][3] + i3 * c23[k][2]))};
        }
#pragma unroll
        for (int k = 0; k < 4; ++k) st16_wt(rw, lane * 16, nt * TS + (4 * cb + k) * 1024, w[k]);
    } else if (jb < 500) {
        const int jo = jb - 400, s5 = jo / 5, n8 = jo - 5 * s5;
        u32x2 lo[8], hi[8];
#pragma unroll
        for (int k = 0; k < 8; ++k) {
            LAS char* b = stg + (4 * q + (c >> 2)) * 256 + 32 * k + 8 * (c & 3);
            lo[k] = lds_rdtr(b); hi[k] = lds_rdtr(b + 4096);
        }
        asm volatile("s_waitcnt lgkmcnt(0)" : "+v"(lo[0]), "+v"(lo[1]), "+v"(lo[2]), "+v"(lo[3]), "+v"(lo[4]), "+v"(lo[5]), "+v"(lo[6]), "+v"(lo[7]),
                     "+v"(hi[0]), "+v"(hi[1]), "+v"(hi[2]), "+v"(hi[3]), "+v"(hi[4]), "+v"(hi[5]), "+v"(hi[6]), "+v"(hi[7]) :: "memory");
#pragma unroll
        for (int k = 0; k < 8; ++k)
            st16_wt(rw, lane * 16, (8 * n8 + k) * TS + (40 + s5) * 1024, (u32x4){lo[k][0], lo[k][1], hi[k][0], hi[k][1]});
    }
}

#define TOUCH 1
#define TOUCH_JOB 0
#define TOUCH_S0 6
constexpr int L_JUNK = 160000;
DEV void touch(const char* p, LAS char* sm) {
    unsigned keep;
    asm volatile("s_mov_b32 %0, m0\n\ts_mov_b32 m0, %2\n\ts_nop 0\n\tglobal_load_lds_dword %1, off\n\ts_mov_b32 m0, %0"
                 : "=&s"(keep) : "v"(p), "s"((unsigned)(size_t)(sm + L_JUNK)) : "memory");
}
DEV void touch_job(const Args& a, LAS char* sm, int jb, int lane) {
    if (jb < 400) {
        const int cb = jb / 40, nt = jb - 40 * cb;
        touch((lane & 32 ? a.cim : a.cre) + (16 * nt + (lane & 15)) * 1280 + cb * 128, sm);
    } else if (jb < 500) {
        const int jo = jb - 400, s5 = jo / 5, n8 = jo - 5 * s5, uu = s5 / 5, s = s5 - 5 * uu;
        const int lo = 160 * uu + (s == 0 ? 0 : s == 1 ? 32 : s == 2 ? 64 : s == 3 ? 80 : 112);
        const int hi = 160 * uu + (s == 0 ? 16 : s == 1 ? 48 : s == 2 ? 144 : s == 3 ? 96 : 128);
        touch(a.wo + ((lane & 32 ? hi : lo) + (lane & 15)) * 1280 + n8 * 256 + (lane & 16 ? 128 : 0), sm);
    }
}
#define TOUCH_CODE 1
constexpr int CODE_LINES = 470;
DEV void touch_code(const Args& a, LAS char* sm, int lane) {
    const int l = (blockIdx.x >> 3) * 15 + lane;
    if (lane < 15 && l < CODE_LINES) touch(a.code + l * 128, sm);
}
DEV void touch_streams(const Args& a, LAS char* sm, int cw, int lane) {
    const int b = blockIdx.x, xcd = b & 7, tq = xcd >> 1, gh = xcd & 1;
#pragma unroll
    for (int r = 0; r < 2; ++r) {
        const int e = (b >> 3) * 4 + cw + 128 * r, s = TOUCH_S0 + e / 33, k = e - 33 * (e / 33);
        if (s < 10) {
            const char* p;
            if (k < 8) p = a.x + (size_t)((4 * tq + (k >> 1)) * 128 + 64 * (k & 1) + lane) * 1280 + 128 * s;
            else if (k < 18) {
                const int li = 64 * (k - 8) + lane, g = li / 160, rem = li - 160 * g;
                p = (rem < 80 ? a.bre : a.bim) + (size_t)(80 * (4 * gh + g) + (rem < 80 ? rem : rem - 80)) * 1280 + 128 * s;
            } else {
                const int li = 64 * (k - 18) + lane, kr = li / 15, rem = li - 15 * kr, sec = rem / 5;
                p = a.wqkv + (size_t)(64 * s + kr) * 3840 + sec * 1280 + 640 * gh + 128 * (rem - 5 * sec);
            }
            touch(p, sm);
        }
    }
}
DEV float gate_of(const Args& a, int tok, unsigned tag) {
    u64 v = 0;
#pragma unroll 1
    for (unsigned spins = 0; spins < SPIN_CAP; ++spins) {
        v = __hip_atomic_load((GAS u64*)(a.ws + WS_G) + tok, RLX_AGENT);
        if (wave_all((unsigned)(v >> 32) == tag)) break;
        __builtin_amdgcn_s_sleep(1);
    }
    return __uint_as_float((unsigned)v);
}
DEV int x_token(int R) { const int rho = R & 15; return (R & 64) + 16 * (rho >> 2) + 4 * ((R >> 4) & 3) + (rho & 3); }

DEV void a_loader(const Args& a, LAS char* sm, int ti, int gj, int lw, int lane) {
    STAMP(a.ws, lw + 4, 0);
    const unsigned nonce = __hip_atomic_load((GAS unsigned*)(a.ws + WS_NONCE), RLX_AGENT);
    unsigned off[9];
    {
        const int pos = lane & 7, rl = lane >> 3, im = lw & 1;
#pragma unroll
        for (int u = 0; u < 4; ++u) {
            const int R = 8 * (lw + 4 * u) + rl;
            off[u] = x_token(R) * 1280 + ((pos ^ (R & 7)) << 4);
        }
#pragma unroll
        for (int u = 4; u < 9; ++u) off[u] = (8 * ((lw + 4 * (u - 4)) >> 1) + rl) * 1280 + ((pos ^ rl ^ (im << 2)) << 4);
    }
    const char* xb = a.x + (size_t)ti * (128 * 1280);
    const char* bb = ((lw & 1) ? a.bim : a.bre) + (size_t)gj * (80 * 1280);
    auto issue_a = [&](int s) LAM {
        LAS char* dst = sm + (s % NS_A) * SLOT_A + lw * 1024;
#pragma unroll
        for (int u = 0; u < 4; ++u) glds16(xb + 128 * s + off[u], dst + 4096 * u);
        glds16(bb + 128 * s + off[4], dst + 4096 * 4);
    };
    auto issue_b = [&](int s) LAM {
        LAS char* dst = sm + (s % NS_A) * SLOT_A + lw * 1024;
#pragma unroll
        for (int u = 5; u < 9; ++u) glds16(bb + 128 * s + off[u], dst + 4096 * u);
    };
    STAMP(a.ws, lw + 4, 1);
    issue_a(0); issue_b(0); issue_a(1); issue_b(1); issue_a(2); issue_b(2);
    WAIT_VM(18);
    bar();
#pragma unroll 1
    for (int s = 0; s < 7; ++s) {
        if (s > 0) ring_wait(sm + L_FREE, s);
        issue_a(s + 3);
        WAIT_VM(14);
        ring_put(sm + L_FULL + 4 * lw, s + 2);
        issue_b(s + 3);
    }
    WAIT_VM(9);
    ring_put(sm + L_FULL + 4 * lw, 9);
    WAIT_VM(0);
    ring_put(sm + L_FULL + 4 * lw, 10);
    STAMP(a.ws, lw + 4, 2);
    const unsigned tag = mk_tag(nonce);
    const int tl = lw * 64 + lane;
    const int jb = (ti * 8 + gj) * 4 + lw;
    ring_wait(sm + L_FREE, 8);
    job_issue(a, sm + A_STG + lw * 8192, jb, lane);
    if (tl < 128) ((LAS float*)(sm + A_G))[tl] = gate_of(a, 128 * ti + tl, tag);
    WAIT_VM(0);
    job_finish(a, sm + A_STG + lw * 8192, sm + A_JTAB + lw * 512, jb, lane);
    STAMP(a.ws, lw + 4, 9);
    __builtin_amdgcn_s_setprio(2);
    lds_bar();
    STAMP(a.ws, lw + 4, 3);
    if (lw < 3) {
        const int col = tl < 160 ? tl : 159;
        LAS float* tab = (LAS float*)(sm + A_TAB);
        GAS u64* ea = (GAS u64*)(a.ws + WS_EA) + gj * 160;
        const float a16 = tab[2 * 160 + col], b16 = tab[3 * 160 + col], a128 = tab[4 * 160 + col], b128 = tab[5 * 160 + col];
        LAS float* sg = (LAS float*)(sm + A_SEG);
        float pre[8], P = 0.f;
#pragma unroll
        for (int s = 0; s < 8; ++s) { pre[s] = P; P = cmul_add(a16, b16, P, sg[s * 160 + col]); }
        if (tl < 160) __hip_atomic_store(ea + ti * 1280 + col, granule(tag, __float_as_uint(P)), RLX_AGENT);
        STAMP(a.ws, lw + 4, 4);
        signal_stores(sm, L_DONE2, a.ws + WS_FW2T + (ti * 8 + gj) * 8, tag, lane, 4u, NREP);
        float H = 0.f;
        if (ti > 0) {
            float ev[15];
#pragma unroll 1
            for (unsigned spins = 0; spins < SPIN_CAP; ++spins) {
                u64 v[15];
#pragma unroll
                for (int i = 0; i < 15; ++i) v[i] = __hip_atomic_load(ea + (i < ti ? i : 0) * 1280 + col, RLX_AGENT);
                bool ok = true;
#pragma unroll
                for (int i = 0; i < 15; ++i) { ev[i] = __uint_as_float((unsigned)v[i]); ok &= (unsigned)(v[i] >> 32) == tag; }
                if (wave_all(ok)) break;
                __builtin_amdgcn_s_sleep(SLP_LB);
            }
#pragma unroll
            for (int i = 0; i < 15; ++i) if (i < ti) H = cmul_add(a128, b128, H, ev[i]);
        }
        STAMP(a.ws, lw + 4, 6);
#pragma unroll
        for (int s = 0; s < 8; ++s) { if (tl < 160) sg[s * 160 + col] = H + pre[s]; H = cmul_add(a16, b16, H, 0.f); }
        wait_lgkm0();
        if (lane == 0) word_add(sm + L_HS);
    } else signal_stores(sm, L_DONE2, a.ws + WS_FW2T + (ti * 8 + gj) * 8, tag, lane, 4u, NREP);
    __builtin_amdgcn_s_setprio(0);
    STAMP(a.ws, lw + 4, 8);
    word_wait(sm + L_DONE, 4u);
    bf16x8 fw[P2_PW][5];
    p2_prefetch(a, sm, lw, lane, tag, 3, fw);
    p2_main(a, sm, lw, lane, nonce, 3, fw);
}

DEV void a_pass1(f32x4 (&acc)[4][5], const float (&ca)[5], const float (&cb)[5], LAS float* es) {
    float e[5];
#pragma unroll
    for (int n = 0; n < 5; ++n) e[n] = acc[0][n][0];
#pragma unroll
    for (int t = 1; t < 16; ++t) {
        const int i = t >> 2, r = t & 3;
        float y[5];
        scan_step_nd(y, acc[i][0][r], acc[i][1][r], acc[i][2][r], acc[i][3][r], acc[i][4][r], e, ca, cb);
#pragma unroll
        for (int n = 0; n < 5; ++n) e[n] = y[n];
    }
#pragma unroll
    for (int n = 0; n < 5; ++n) es[16 * n] = e[n];
}
DEV void a_pass2(f32x4 (&acc)[4][5], const float (&ca)[5], const float (&cb)[5], LAS float* hs, LAS float* gt, LAS char* ht,
                 LAS char* hr, const __amdgpu_buffer_rsrc_t& ract, unsigned vo, unsigned ob) {
    float e[5];
#pragma unroll
    for (int n = 0; n < 5; ++n) e[n] = hs[16 * n];
    asm volatile("s_nop 1" : "+v"(e[0]), "+v"(e[1]), "+v"(e[2]), "+v"(e[3]), "+v"(e[4]));
    bf16x8 hf[4][2];
    auto rd = [&](int m) LAM {
#pragma unroll
        for (int k = 0; k < 2; ++k) {
            LAS char* b = hr + 32 * k * HT_STRIDE + 32 * m;
            hf[m][k] = lds_tr_frag(b, b + 4 * HT_STRIDE);
        }
    };
    auto st = [&](int m) LAM {
#pragma unroll
        for (int k = 0; k < 2; ++k) st16_wt(ract, vo, ob + m * TS + k * 1024, __builtin_bit_cast(u32x4, hf[m][k]));
    };
#pragma unroll
    for (int m = 0; m < 4; ++m) {
        const f32x4 g = *(LAS f32x4*)(gt + 4 * m);
#pragma unroll
        for (int r = 0; r < 4; ++r) {
            float u0 = acc[m][0][r], u1 = acc[m][1][r], u2 = acc[m][2][r], u3 = acc[m][3][r], u4 = acc[m][4][r];
            scan_step(u0, u1, u2, u3, u4, e, ca, cb);
            e[0] = u0; e[1] = u1; e[2] = u2; e[3] = u3; e[4] = u4;
            acc[m][0][r] = u0 * g[r]; acc[m][1][r] = u1 * g[r]; acc[m][2][r] = u2 * g[r]; acc[m][3][r] = u3 * g[r]; acc[m][4][r] = u4 * g[r];
        }
        SCHED();
        if (m >= 2) st(m - 2);
#pragma unroll
        for (int n = 0; n < 5; ++n)
            *(LAS u32x2*)(ht + 16 * n * HT_STRIDE + 32 * m) = (u32x2){pk2(acc[m][n][0], acc[m][n][1]), pk2(acc[m][n][2], acc[m][n][3])};
        rd(m);
        SCHED();
    }
    st(2); st(3);
#pragma unroll
    for (int m = 0; m < 4; ++m) { KEEP(hf[m][0]); KEEP(hf[m][1]); }
}

DEV void a_consumer(const Args& a, LAS char* sm, int ti, int gj, int cw, int lane) {
    const int mh = cw >> 1, nh = cw & 1, c = lane & 15, q = lane >> 4, tid = cw * 64 + lane;
    STAMP(a.ws, cw, 0);
    const unsigned nonce = __hip_atomic_load((GAS unsigned*)(a.ws + WS_NONCE), RLX_AGENT);
    const int jb = (ti * 8 + gj) * 4 + cw, pj = (jb < 400 ? 64 * (jb / 40) : 0) + lane;
    const u16 jlre = a.lre[pj], jlim = a.lim[pj], jldt = a.logdt[pj];
    const int gtok = 128 * ti + 16 * gj + 8 * (cw & 1) + (lane >> 3), pch = 80 * gj + (tid < 80 ? tid : 0);
    u32x4 xv[10], gv[10];
    u16 rlre = 0, rlim = 0, rldt = 0;
    if (cw >= 2) {
        const u32x4* xr = (const u32x4*)(a.x + (size_t)gtok * 1280) + (lane & 7);
        const u32x4* gr = (const u32x4*)a.gw + (lane & 7);
#pragma unroll
        for (int i = 0; i < 10; ++i) { xv[i] = xr[8 * i]; gv[i] = gr[8 * i]; }
    } else { rlre = a.lre[pch]; rlim = a.lim[pch]; rldt = a.logdt[pch]; }
    __builtin_amdgcn_s_setprio(1);
    f32x4 acc[4][5];
#pragma unroll
    for (int n = 0; n < 5; ++n)
#pragma unroll
        for (int m = 0; m < 4; ++m) acc[m][n] = (f32x4){0.f, 0.f, 0.f, 0.f};
    int xo[2], wa[2];
#pragma unroll
    for (int kk = 0; kk < 2; ++kk) {
        xo[kk] = (64 * mh + c) * 128 + (((4 * kk + q) ^ (c & 7)) << 4);
        wa[kk] = 16384 + (80 * nh + 8 * (c & 1) + (c >> 1)) * 128 + (((4 * kk + q) ^ (c >> 1) ^ ((c & 1) << 2)) << 4);
    }
    bf16x8 xa[2][4], wb[2][5];
    auto rdp = [&](int buf, LAS char* s0, int kk) LAM {
#pragma unroll
        for (int m = 0; m < 4; ++m) xa[buf][m] = lds_frag(s0 + xo[kk] + 2048 * m);
#pragma unroll
        for (int n = 0; n < 5; ++n) wb[buf][n] = lds_frag(s0 + wa[kk] + 2048 * n);
    };
    auto mm_head = [&](int buf) LAM {
#pragma unroll
        for (int m = 0; m < 4; ++m) acc[m][0] = mfma(xa[buf][m], wb[buf][0], acc[m][0]);
    };
    auto mm_tail = [&](int buf) LAM {
#pragma unroll
        for (int n = 1; n < 5; ++n)
#pragma unroll
            for (int m = 0; m < 4; ++m) acc[m][n] = mfma(xa[buf][m], wb[buf][n], acc[m][n]);
    };
    if (tid < L_WORDS) ((LAS unsigned*)(sm + L_FULL))[tid] = tid < 4 ? 1u : 0u;
#pragma unroll
    for (int n = 0; n < 5; ++n)
        asm volatile("" : "+v"(acc[0][n]), "+v"(acc[1][n]), "+v"(acc[2][n]), "+v"(acc[3][n]));
    asm volatile("" : "+v"(xo[0]), "+v"(xo[1]), "+v"(wa[0]), "+v"(wa[1]));
    const unsigned tag = mk_tag(nonce);
    if (cw >= 2) {
        float s0 = 0.f, s1 = 0.f;
#pragma unroll
        for (int i = 0; i < 10; ++i) {
            s0 = dot2(xv[i][0], gv[i][0], s0); s1 = dot2(xv[i][1], gv[i][1], s1);
            s0 = dot2(xv[i][2], gv[i][2], s0); s1 = dot2(xv[i][3], gv[i][3], s1);
        }
        float s = s0 + s1;
        s += lane_xor(s, 1); s += lane_xor(s, 2); s += lane_xor(s, 4);
        const float lg = bfr(s), g = bfr(1.f / (1.f + fexp(-lg)));
        if ((lane & 7) == 0) __hip_atomic_store((GAS u64*)(a.ws + WS_G) + gtok, granule(tag, __float_as_uint(g)), RLX_AGENT);
    } else if (tid < 80) {
        float ar, ai, cr, ci;
        channel_consts(rlre, rlim, rldt, ar, ai, cr, ci);
        float pr = ar, pi = ai;
#pragma unroll
        for (int k = 0; k < 4; ++k) { const float t = pr * pr - pi * pi; pi = 2.f * pr * pi; pr = t; }
        const float r16 = pr, i16 = pi;
#pragma unroll
        for (int k = 0; k < 3; ++k) { const float t = pr * pr - pi * pi; pi = 2.f * pr * pi; pr = t; }
        LAS f32x2* tab = (LAS f32x2*)(sm + A_TAB);
        tab[0 * 80 + tid] = (f32x2){ar, ar};
        tab[1 * 80 + tid] = (f32x2){-ai, ai};
        tab[2 * 80 + tid] = (f32x2){r16, r16};
        tab[3 * 80 + tid] = (f32x2){-i16, i16};
        tab[4 * 80 + tid] = (f32x2){pr, pr};
        tab[5 * 80 + tid] = (f32x2){-pi, pi};
    }
    {
        float ar, ai, cr, ci;
        channel_consts(jlre, jlim, jldt, ar, ai, cr, ci);
        ((LAS f32x2*)(sm + A_JTAB + cw * 512))[lane] = (f32x2){2.f * cr, 2.f * ci};
    }
    if (tid < 160) __hip_atomic_store((GAS u64*)(a.ws + WS_EA) + (ti * 8 + gj) * 160 + tid, (u64)0, RLX_AGENT);
    if (TOUCH_CODE && cw == 0) touch_code(a, sm, lane);
    if (TOUCH) touch_streams(a, sm, cw, lane);
    if (TOUCH_JOB) touch_job(a, sm, jb, lane);
    STAMP(a.ws, cw, 1);
    lds_bar();
    STAMP(a.ws, cw, 2);
    STAMPC(a.ws, cw, 14);
    rdp(0, sm, 0);
#pragma unroll 1
    for (int s = 0; s < 10; ++s) {
        wait_lgkm0();
        SCHED();
        rdp(1, sm + (s & 3) * SLOT_A, 1);
        u32x4 fl = *(volatile LAS u32x4*)(sm + L_FULL);
        mm_head(0); mm_tail(0);
        interleave<20, 10, IL_A>();
        SCHED();
        wait_lgkm0();
        asm volatile("" ::: "memory");
        *(volatile LAS unsigned*)(sm + L_FREE + 4 * cw) = s + 1;
        const unsigned need = s < 9 ? s + 2 : 0;
        unsigned landed = __builtin_amdgcn_readfirstlane(umin4(fl));
        LAS char* s0 = sm + ((s + 1) & 3) * SLOT_A;
        asm volatile("" : "+s"(landed), "+v"(s0));
        mm_head(1);
        SCHED();
        if (__builtin_expect(landed < need, 0)) ring_spin(sm + L_FULL, need);
        asm volatile("" ::: "memory");
        SCHED();
        rdp(0, s0, 0);
        mm_tail(1);
        interleave<16, 9, IL_A - 3>();
        SCHED();
    }
    __builtin_amdgcn_s_setprio(0);
    asm volatile("s_nop 7\n\ts_nop 7" ::: "memory");
    STAMP(a.ws, cw, 3);
    STAMPC(a.ws, cw, 15);
    LAS float* tab = (LAS float*)(sm + A_TAB);
    LAS float* sg = (LAS float*)(sm + A_SEG) + (4 * mh + q) * 160 + 80 * nh + c;
    float ca[5], cb[5];
#pragma unroll
    for (int n = 0; n < 5; ++n) { const int col = 80 * nh + 16 * n + c; ca[n] = tab[col]; cb[n] = tab[160 + col]; }
    a_pass1(acc, ca, cb, sg);
    WAIT_VM(0);
    STAMP(a.ws, cw, 4);
    lds_bar();
    STAMP(a.ws, cw, 5);
    word_wait(sm + L_HS, 3u);
    STAMP(a.ws, cw, 8);
    {
        const int qq = (lane & 15) >> 2, p = lane & 3;
        const __amdgpu_buffer_rsrc_t ract = rsrc(a.ws + WS_ACT, 128 * TS);
        LAS float* gt = (LAS float*)(sm + A_G) + 64 * mh + 16 * q;
        LAS char* ht = sm + A_HT + (80 * nh + c) * HT_STRIDE + (64 * mh + 4 * q) * 2;
        LAS char* hr = sm + A_HT + (8 * q + qq) * HT_STRIDE + 128 * mh + 8 * p;
        const unsigned ob = (8 * ti + 4 * mh) * TS + 5 * gj * 1024;
        a_pass2(acc, ca, cb, sg, gt, ht, hr + 96 * nh * HT_STRIDE, ract, lane * 16, ob + 3072 * nh);
        STAMP(a.ws, cw, 9);
        wait_lgkm0();
        if (lane == 0) word_add(sm + L_HT + 4 * mh);
        word_wait(sm + L_HT + 4 * mh, 2u);
        bf16x8 hf[2];
#pragma unroll
        for (int i = 0; i < 2; ++i) {
            LAS char* b = hr + 64 * HT_STRIDE + 32 * (2 * nh + i);
            hf[i] = lds_tr_frag(b, b + 4 * HT_STRIDE);
        }
#pragma unroll
        for (int i = 0; i < 2; ++i) st16_wt(ract, lane * 16, ob + (2 * nh + i) * TS + 2048, __builtin_bit_cast(u32x4, hf[i]));
    }
    wait_lgkm0();
    STAMP(a.ws, cw, 10);
    signal_stores(sm, L_DONE, a.ws + WS_FACT + ti * 256 + gj * 8, tag, lane, 4u);
    STAMP(a.ws, cw, 11);
    p2_final(a, sm, cw, lane, nonce);
}

DEV void b_loader(const Args& a, LAS char* sm, int ti, int gj, int lw, int lane) {
    STAMP(a.ws, lw + 4, 0);
    const unsigned nonce = __hip_atomic_load((GAS unsigned*)(a.ws + WS_NONCE), RLX_AGENT);
    unsigned off[12];
    {
        const int pos = lane & 7, rl = lane >> 3;
#pragma unroll
        for (int u = 0; u < 4; ++u) {
            const int R = 8 * (lw + 4 * u) + rl;
            off[u] = x_token(R) * 1280 + ((pos ^ (R & 7)) << 4);
        }
#pragma unroll
        for (int u = 4; u < 12; ++u) {
            const int wi = lw + 4 * (u - 4), sec = wi / 10, ch = 64 * (wi - 10 * sec) + lane, rho = ch / 10, cc = ch - 10 * rho;
            off[u] = (rho ^ (((rho >> 3) & 1) << 2)) * 3840 + cc * 16 + (sec == 0 ? 1280 : sec == 1 ? 2560 : 0);
        }
    }
    const char* xb = a.x + (size_t)ti * (128 * 1280);
    const char* wb = a.wqkv + 160 * gj;
    const bool full = lw < 2;
    auto issue_a = [&](int s) LAM {
        LAS char* dst = sm + (s % NS_B) * SLOT_B + lw * 1024;
#pragma unroll
        for (int u = 0; u < 4; ++u) glds16(xb + 128 * s + off[u], dst + 4096 * u);
#pragma unroll
        for (int u = 4; u < 6; ++u) glds16(wb + (size_t)(64 * 3840) * s + off[u], dst + 4096 * u);
    };
    auto issue_b = [&](int s) LAM {
        LAS char* dst = sm + (s % NS_B) * SLOT_B + lw * 1024;
#pragma unroll
        for (int u = 6; u < 12; ++u) if (u < 11 || full) glds16(wb + (size_t)(64 * 3840) * s + off[u], dst + 4096 * u);
    };
    STAMP(a.ws, lw + 4, 1);
    issue_a(0); issue_b(0); issue_a(1); issue_b(1);
    if (full) WAIT_VM(12); else WAIT_VM(11);
    bar();
#pragma unroll 1
    for (int s = 0; s < 8; ++s) {
        if (s > 0) ring_wait(sm + L_FREE, s);
        issue_a(s + 2);
        WAIT_VM(6);
        ring_put(sm + L_FULL + 4 * lw, s + 2);
        issue_b(s + 2);
    }
    WAIT_VM(0);
    ring_put(sm + L_FULL + 4 * lw, 10);
    STAMP(a.ws, lw + 4, 2);
    __builtin_amdgcn_s_setprio(2);
    const unsigned tag = mk_tag(nonce);
    const int tl = lw * 64 + lane;
    if (tl < 128) ((LAS float*)(sm + B_G))[tl] = bfr(1.f - gate_of(a, 128 * ti + tl, tag));
    {
        int o0[2], o1[2];
#pragma unroll
        for (int it = 0; it < 2; ++it) {
            const int g = 2 * (tl + 256 * it < 420 ? tl + 256 * it : 0);
            const int h = g / 210, rem = g - 210 * h, e = rem / 10, d = 2 * (rem - 10 * e);
            const bool kv = rem < 200;
            o0[it] = kv ? B_KVT + ((20 * h + e) * 84 + 20 * h + d) * 4 : B_Z + (20 * h + 2 * (rem - 200)) * 4;
            o1[it] = o0[it] + (kv ? 80 * 84 * 4 : 320);
        }
        asm volatile("" : "+v"(o0[0]), "+v"(o0[1]), "+v"(o1[0]), "+v"(o1[1]));
        const __amdgpu_buffer_rsrc_t rk = rsrc(a.ws + WS_KV, 16 * 8 * 840 * 8);
        word_wait(sm + L_KV, 4u);
        STAMP(a.ws, lw + 4, 3);
        u32x4 pv[2];
#pragma unroll
        for (int it = 0; it < 2; ++it) {
            const f32x4 v = *(LAS f32x4*)(sm + o0[it]) + *(LAS f32x4*)(sm + o1[it]);
            pv[it] = (u32x4){pk2(v[0], v[1]), tag, pk2(v[2], v[3]), tag};
        }
        asm volatile("" : "+v"(pv[0]), "+v"(pv[1]) :: "memory");
        wait_lgkm0();
        if (lane == 0) word_add(sm + L_PUB);
        st16_wt(rk, tl * 16, (ti * 8 + gj) * 6720, pv[0]);
        if (tl + 256 < 420) st16_wt(rk, (tl + 256) * 16, (ti * 8 + gj) * 6720, pv[1]);
        asm volatile("s_nop 1" ::"v"(pv[0]), "v"(pv[1]));
    }
    STAMP(a.ws, lw + 4, 4);
    if (lw == 0) {
        const int cnt = ti < 8 ? 53 : 52, g0 = ti < 8 ? 53 * ti : 424 + 52 * (ti - 8);
        const int g = g0 + (lane < cnt ? lane : 0);
        GAS u64* src = (GAS u64*)(a.ws + WS_KV) + gj * 840 + g;
        unsigned v[16];
#pragma unroll 1
        for (unsigned spins = 0; spins < SPIN_CAP; ++spins) {
            u64 x[16];
#pragma unroll
            for (int i = 0; i < 16; ++i) x[i] = __hip_atomic_load(src + i * (8 * 840), RLX_AGENT);
            bool ok = true;
#pragma unroll
            for (int i = 0; i < 16; ++i) { v[i] = (unsigned)x[i]; ok &= (unsigned)(x[i] >> 32) == tag; }
            if (wave_all(ok)) break;
            __builtin_amdgcn_s_sleep(1);
        }
        float s0 = 0.f, s1 = 0.f;
#pragma unroll
        for (int i = 0; i < 16; ++i) { s0 += bf_lo(v[i]); s1 += bf_hi(v[i]); }
        if (lane < cnt) __hip_atomic_store((GAS u64*)(a.ws + WS_TOT) + gj * 840 + g, granule(tag, pk2(s0, s1)), RLX_AGENT);
        STAMP(a.ws, lw + 4, 5);
    }
    bf16x8 fw[P2_PW][5];
    if (lw == 3) {
        word_wait(sm + L_PUB, 4u);
        p2_prefetch(a, sm, lw, lane, tag, 2, fw);
        const __amdgpu_buffer_rsrc_t rs = rsrc(a.ws + WS_TOT, 8 * 840 * 8);
        u32x4 v[7];
#pragma unroll 1
        for (unsigned spins = 0; spins < SPIN_CAP; ++spins) {
            bool ok = true;
#pragma unroll
            for (int k = 0; k < 7; ++k) v[k] = __builtin_amdgcn_raw_buffer_load_b128(rs, gj * 6720 + (lane + 64 * k < 420 ? lane + 64 * k : 419) * 16, 0, 16);
#pragma unroll
            for (int k = 0; k < 7; ++k) ok &= (v[k][1] == tag) & (v[k][3] == tag);
            if (wave_all(ok)) break;
            __builtin_amdgcn_s_sleep(SLP_TOT);
        }
#pragma unroll
        for (int k = 0; k < 7; ++k) if (lane + 64 * k < 420) ((LAS u32x2*)(sm + B_TOT))[lane + 64 * k] = (u32x2){v[k][0], v[k][2]};
        wait_lgkm0();
        word_set(sm + L_TOT, 1u);
        STAMP(a.ws, lw + 4, 5);
    }
    __builtin_amdgcn_s_setprio(0);
    if (lw != 3) {
        word_wait(sm + L_PUB, 4u);
        p2_prefetch(a, sm, lw, lane, tag, 2, fw);
    }
    p2_main(a, sm, lw, lane, nonce, 2, fw);
}

DEV void b_consumer(const Args& a, LAS char* sm, int ti, int gj, int cw, int lane) {
    const int mh = cw >> 1, nh = cw & 1, c = lane & 15, q = lane >> 4, tid = cw * 64 + lane;
    STAMP(a.ws, cw, 0);
    const unsigned nonce = __hip_atomic_load((GAS unsigned*)(a.ws + WS_NONCE), RLX_AGENT), tag = mk_tag(nonce);
    {
        const __amdgpu_buffer_rsrc_t rk = rsrc(a.ws + WS_KV, 16 * 8 * 840 * 8);
        const u32x4 z = {0u, 0u, 0u, 0u};
        st16_wt(rk, tid * 16, (ti * 8 + gj) * 6720, z);
        if (tid + 256 < 420) st16_wt(rk, (tid + 256) * 16, (ti * 8 + gj) * 6720, z);
        const int cnt = ti < 8 ? 53 : 52, g0 = ti < 8 ? 53 * ti : 424 + 52 * (ti - 8);
        if (tid < cnt) __hip_atomic_store((GAS u64*)(a.ws + WS_TOT) + gj * 840 + g0 + tid, (u64)0, RLX_AGENT);
    }
    __builtin_amdgcn_s_setprio(1);
    f32x4 an[5][4], at[5][2];
#pragma unroll
    for (int t = 0; t < 5; ++t) {
#pragma unroll
        for (int i = 0; i < 4; ++i) an[t][i] = (f32x4){0.f, 0.f, 0.f, 0.f};
        at[t][0] = at[t][1] = (f32x4){0.f, 0.f, 0.f, 0.f};
    }
    int xlo[2], xhi[2], wr[2];
#pragma unroll
    for (int kk = 0; kk < 2; ++kk) {
        const int sw = ((4 * kk + q) ^ (c & 7)) << 4;
        xlo[kk] = (64 * mh + 32 * nh + c) * 128 + sw;
        xhi[kk] = (64 * mh + 32 * (1 - nh) + c) * 128 + sw;
        wr[kk] = 16384 + (8 * q + 4 * (kk ^ (q & 1)) + (c >> 2)) * 160 + 8 * (c & 3);
    }
    bf16x8 xa[2][4], wb[2][5], wt[2][5];
    auto rdp = [&](int buf, LAS char* s0, int kk) LAM {
        LAS char* q0 = s0 + kk * 5120 + wr[0]; LAS char* q1 = s0 + kk * 5120 + wr[1];
        LAS char* a0 = q0 + 96 * nh; LAS char* a1 = q1 + 96 * nh;
        xa[buf][0] = lds_frag(s0 + xlo[kk]);
        wb[buf][0] = lds_tr_frag(a0, a1);
        wb[buf][1] = lds_tr_frag(a0 + 32, a1 + 32);
        xa[buf][1] = lds_frag(s0 + xlo[kk] + 2048);
        wb[buf][2] = lds_tr_frag(q0 + 64 + 10240 * nh, q1 + 64 + 10240 * nh);
        wb[buf][3] = lds_tr_frag(a0 + 10240, a1 + 10240);
        xa[buf][2] = lds_frag(s0 + xhi[kk]);
        wb[buf][4] = lds_tr_frag(a0 + 10272, a1 + 10272);
        wt[buf][0] = lds_tr_frag(q0 + 20480, q1 + 20480);
        xa[buf][3] = lds_frag(s0 + xhi[kk] + 2048);
        wt[buf][1] = lds_tr_frag(q0 + 20512, q1 + 20512);
        wt[buf][2] = lds_tr_frag(q0 + 20544, q1 + 20544);
        wt[buf][3] = lds_tr_frag(q0 + 20576, q1 + 20576);
        wt[buf][4] = lds_tr_frag(q0 + 20608, q1 + 20608);
    };
    auto mm_head = [&](int buf) LAM {
#pragma unroll
        for (int i = 0; i < 4; ++i) an[0][i] = mfma(xa[buf][i], wb[buf][0], an[0][i]);
    };
    auto mm_tail = [&](int buf) LAM {
#pragma unroll
        for (int t = 1; t < 5; ++t)
#pragma unroll
            for (int i = 0; i < 4; ++i) an[t][i] = mfma(xa[buf][i], wb[buf][t], an[t][i]);
#pragma unroll
        for (int t = 0; t < 5; ++t)
#pragma unroll
            for (int i = 0; i < 2; ++i) at[t][i] = mfma(wt[buf][t], xa[buf][i], at[t][i]);
    };
    if (tid < L_WORDS) ((LAS unsigned*)(sm + L_FULL))[tid] = tid < 4 ? 1u : 0u;
#pragma unroll
    for (int t = 0; t < 5; ++t)
        asm volatile("" : "+v"(an[t][0]), "+v"(an[t][1]), "+v"(an[t][2]), "+v"(an[t][3]), "+v"(at[t][0]), "+v"(at[t][1]));
    if (TOUCH_CODE && cw == 0) touch_code(a, sm, lane);
    if (TOUCH) touch_streams(a, sm, cw, lane);
    STAMP(a.ws, cw, 1);
    lds_bar();
    STAMP(a.ws, cw, 2);
    STAMPC(a.ws, cw, 14);
    rdp(0, sm, 0);
    int slot = 0;
#pragma unroll 1
    for (int s = 0; s < 10; ++s) {
        wait_lgkm0();
        SCHED();
        rdp(1, sm + slot * SLOT_B, 1);
        u32x4 fl = *(volatile LAS u32x4*)(sm + L_FULL);
        mm_head(0); mm_tail(0);
        interleave<30, 25, IL_B>();
        SCHED();
        wait_lgkm0();
        asm volatile("" ::: "memory");
        *(volatile LAS unsigned*)(sm + L_FREE + 4 * cw) = s + 1;
        slot = slot == 2 ? 0 : slot + 1;
        const unsigned need = s < 9 ? s + 2 : 0;
        unsigned landed = __builtin_amdgcn_readfirstlane(umin4(fl));
        LAS char* s0 = sm + slot * SLOT_B;
        asm volatile("" : "+s"(landed), "+v"(s0));
        mm_head(1);
        SCHED();
        if (__builtin_expect(landed < need, 0)) ring_spin(sm + L_FULL, need);
        asm volatile("" ::: "memory");
        SCHED();
        rdp(0, s0, 0);
        mm_tail(1);
        interleave<26, 24, 24>();
        SCHED();
    }
    STAMP(a.ws, cw, 3);
    STAMPC(a.ws, cw, 15);
    bf16x8 fr[5][2];
#pragma unroll
    for (int t = 0; t < 2; ++t)
#pragma unroll
        for (int i = 0; i < 4; ++i)
#pragma unroll
            for (int r = 0; r < 4; ++r) an[t][i][r] = phi(an[t][i][r]);
    if (nh == 0) {
#pragma unroll
        for (int i = 0; i < 4; ++i)
#pragma unroll
            for (int r = 0; r < 4; ++r) an[2][i][r] = phi(an[2][i][r]);
    }
#pragma unroll
    for (int t = 0; t < 5; ++t) { fr[t][0] = pack_pair(an[t][0], an[t][1]); fr[t][1] = pack_pair(an[t][2], an[t][3]); }
    {
        LAS char* xs = sm + B_X1 + cw * 2048 + lane * 16;
        *(LAS bf16x8*)(xs + (nh << 10)) = fr[2][0];
        *(LAS bf16x8*)(xs + ((1 ^ nh) << 10)) = fr[2][1];
    }
    wait_lgkm0();
    word_set(sm + L_X1 + 4 * cw, 1u);
#pragma unroll
    for (int t = 0; t < 5; ++t) asm volatile("" : "+v"(fr[t][0]), "+v"(fr[t][1]));
    STAMP(a.ws, cw, 4);
    word_wait(sm + L_X1 + 4 * (cw ^ 1), 1u);
    ring_spin(sm + L_FREE, 10);
    asm volatile("" ::: "memory");
    {
        LAS char* xr = sm + B_X1 + (cw ^ 1) * 2048 + lane * 16;
        const bf16x8 R0 = *(LAS bf16x8*)(xr + (nh << 10)), R1 = *(LAS bf16x8*)(xr + ((1 ^ nh) << 10));
        const bf16x8 ones = as_frag((u32x4){0x3f803f80u, 0x3f803f80u, 0x3f803f80u, 0x3f803f80u});
        const f32x4 z4 = {0.f, 0.f, 0.f, 0.f};
        LAS float* kvt = (LAS float*)(sm + B_KVT) + mh * (80 * 84) + c * 84 + 4 * q;
        LAS float* zz = (LAS float*)(sm + B_Z) + mh * 80 + 4 * q;
        auto prod = [&](const bf16x8& k0, const bf16x8& k1, const bf16x8& v0, const bf16x8& v1, int ta, int tb) LAM {
            *(LAS f32x4*)(kvt + 16 * tb * 84 + 16 * ta) = mfma(k1, v1, mfma(k0, v0, z4));
        };
        auto zsum = [&](const bf16x8& k0, const bf16x8& k1, int ta) LAM {
            const f32x4 d = mfma(k1, ones, mfma(k0, ones, z4));
            if (c == 0) *(LAS f32x4*)(zz + 16 * ta) = d;
        };
        const int b0 = 3 * nh;
        prod(fr[0][0], fr[0][1], fr[3][0], fr[3][1], b0, b0);
        prod(fr[0][0], fr[0][1], fr[4][0], fr[4][1], b0, b0 + 1);
        prod(fr[1][0], fr[1][1], fr[3][0], fr[3][1], b0 + 1, b0);
        prod(fr[1][0], fr[1][1], fr[4][0], fr[4][1], b0 + 1, b0 + 1);
        zsum(fr[0][0], fr[0][1], b0);
        zsum(fr[1][0], fr[1][1], b0 + 1);
        if (nh == 0) {
            prod(fr[2][0], fr[2][1], fr[4][0], fr[4][1], 2, 1);
            prod(fr[1][0], fr[1][1], R0, R1, 1, 2);
            prod(fr[2][0], fr[2][1], R0, R1, 2, 2);
            zsum(fr[2][0], fr[2][1], 2);
        } else {
            prod(fr[0][0], fr[0][1], fr[2][0], fr[2][1], 3, 2);
            prod(R0, R1, fr[3][0], fr[3][1], 2, 3);
        }
    }
    WAIT_VM(0);
    wait_lgkm0();
    if (lane == 0) word_add(sm + L_KV);
    __builtin_amdgcn_s_setprio(0);
    STAMP(a.ws, cw, 5);
    bf16x8 P[2][3];
#pragma unroll
    for (int i = 0; i < 2; ++i) {
#pragma unroll
        for (int t = 0; t < 5; ++t)
#pragma unroll
            for (int r = 0; r < 4; ++r) at[t][i][r] = phi(at[t][i][r]);
        P[i][0] = pack_pair(at[0][i], at[1][i]);
        P[i][1] = pack_pair(at[2][i], at[3][i]);
        P[i][2] = as_frag((u32x4){pk2(at[4][i][0], at[4][i][1]), pk2(at[4][i][2], at[4][i][3]), 0u, 0u});
    }
    int adr[21];
    {
        int dh[5], db[5], eh[5], eb[5];
#pragma unroll
        for (int k = 0; k < 5; ++k) {
            const int d0 = 16 * k + 4 * q, e0 = 16 * k + c;
            dh[k] = d0 / 20; db[k] = B_TOT + dh[k] * 840 + (d0 - 20 * dh[k]) * 2;
            eh[k] = e0 / 20; eb[k] = (e0 - 20 * eh[k]) * 40;
        }
        auto ad = [&](int k, int E) LAM { return dh[k] == eh[E] ? db[k] + eb[E] : L_ZERO; };
        adr[0] = ad(0, 0); adr[1] = ad(1, 0);
        adr[2] = ad(0, 1); adr[3] = ad(1, 1); adr[4] = ad(2, 1); adr[5] = ad(3, 1);
        adr[6] = ad(1, 2); adr[7] = ad(2, 2); adr[8] = ad(3, 2);
        adr[9] = ad(2, 3); adr[10] = ad(3, 3); adr[11] = ad(4, 3);
        adr[12] = ad(3, 4); adr[13] = ad(4, 4);
#pragma unroll
        for (int k = 0; k < 5; ++k) adr[14 + k] = dh[k] == (c & 3) ? db[k] + 800 : L_ZERO;
        adr[19] = L_ZERO; adr[20] = B_G + 4 * (64 * mh + 16 * (c >> 2) + 8 * nh + (c & 3));
    }
#pragma unroll
    for (int i = 0; i < 2; ++i) asm volatile("" : "+v"(P[i][0]), "+v"(P[i][1]), "+v"(P[i][2]));
    asm volatile("" : "+v"(adr[0]), "+v"(adr[1]), "+v"(adr[2]), "+v"(adr[3]), "+v"(adr[4]), "+v"(adr[5]), "+v"(adr[6]),
                      "+v"(adr[7]), "+v"(adr[8]), "+v"(adr[9]), "+v"(adr[10]), "+v"(adr[11]), "+v"(adr[12]), "+v"(adr[13]));
    asm volatile("" : "+v"(adr[14]), "+v"(adr[15]), "+v"(adr[16]), "+v"(adr[17]), "+v"(adr[18]), "+v"(adr[20]));
    const __amdgpu_buffer_rsrc_t ract = rsrc(a.ws + WS_ACT, 128 * TS);
    u32x4 os[2][2]; u32x2 o8[2];
    STAMP(a.ws, cw, 6);
    word_wait(sm + L_TOT, 1u);
    word_wait(sm + L_PUB, 4u);
    STAMP(a.ws, cw, 7);
    {
        auto h4 = [&](int i) LAM -> u32x2 { return *(LAS u32x2*)(sm + adr[i]); };
        auto fr2 = [&](const u32x2& lo, const u32x2& hi) LAM { return as_frag((u32x4){lo[0], lo[1], hi[0], hi[1]}); };
        const u32x2 zz = {0u, 0u};
        const bf16x8 Z0 = fr2(h4(14), h4(15)), Z1 = fr2(h4(16), h4(17)), Z2 = fr2(h4(18), zz);
        const bf16x8 F0 = fr2(h4(0), h4(1)), F1 = fr2(h4(2), h4(3)), F2 = fr2(h4(4), h4(5));
        const bf16x8 F3 = fr2(zz, h4(6)), F4 = fr2(h4(7), h4(8)), F5 = fr2(h4(9), h4(10));
        const bf16x8 F6 = fr2(h4(11), zz), F7 = fr2(zz, h4(12)), F8 = fr2(h4(13), zz);
        const float ogv[2] = {*(LAS float*)(sm + adr[20]), *(LAS float*)(sm + adr[20] + 16)};
        const f32x4 z4 = {0.f, 0.f, 0.f, 0.f};
        const int uu = gj >> 1, odd = gj & 1;
#pragma unroll
        for (int i = 0; i < 2; ++i) {
            const f32x4 dn = mfma(Z2, P[i][2], mfma(Z1, P[i][1], mfma(Z0, P[i][0], z4)));
            const f32x4 n0 = mfma(F0, P[i][0], z4);
            const f32x4 n1 = mfma(F2, P[i][1], mfma(F1, P[i][0], z4));
            const f32x4 n2 = mfma(F4, P[i][1], mfma(F3, P[i][0], z4));
            const f32x4 n3 = mfma(F6, P[i][2], mfma(F5, P[i][1], z4));
            const f32x4 n4 = mfma(F8, P[i][2], mfma(F7, P[i][1], z4));
            const float og = ogv[i];
            const float r0 = og * __builtin_amdgcn_rcpf(dn[0] + 1e-6f), r1 = og * __builtin_amdgcn_rcpf(dn[1] + 1e-6f);
            const float r2 = og * __builtin_amdgcn_rcpf(dn[2] + 1e-6f), r3 = og * __builtin_amdgcn_rcpf(dn[3] + 1e-6f);
            const float s1 = q < 1 ? r0 : r1, s2 = q < 2 ? r1 : r2, s3 = q < 3 ? r2 : r3;
            os[i][0] = (u32x4){pk2(n0[0] * r0, n0[1] * r0), pk2(n0[2] * r0, n0[3] * r0), pk2(n1[0] * s1, n1[1] * s1), pk2(n1[2] * s1, n1[3] * s1)};
            os[i][1] = (u32x4){pk2(n2[0] * s2, n2[1] * s2), pk2(n2[2] * s2, n2[3] * s2), pk2(n3[0] * s3, n3[1] * s3), pk2(n3[2] * s3, n3[3] * s3)};
            o8[i] = (u32x2){pk2(n4[0] * r3, n4[1] * r3), pk2(n4[2] * r3, n4[3] * r3)};
        }
#pragma unroll
        for (int i = 0; i < 2; ++i) {
            const unsigned ob = (8 * ti + 4 * mh + 2 * nh + i) * TS + (40 + 5 * uu) * 1024;
            st16_wt(ract, lane * 16, ob + (odd ? 3072 : 0), os[i][0]);
            st16_wt(ract, lane * 16, ob + (odd ? 4096 : 1024), os[i][1]);
            st8_wt(ract, lane * 16 + 8 * odd, ob + 2048, o8[i]);
        }
    }
    KEEP(os[0][0]); KEEP(os[0][1]); KEEP(os[1][0]); KEEP(os[1][1]);
    STAMP(a.ws, cw, 8);
    signal_stores(sm, L_DONE, a.ws + WS_FACT + ti * 256 + 128 + gj * 8, tag, lane, 4u);
    STAMP(a.ws, cw, 9);
    p2_final(a, sm, cw, lane, nonce);
}

}

__global__ __launch_bounds__(512, 2) NOPK void gm_fused(const void* x, const void* lre, const void* lim, const void* bre,
                                                   const void* bim, const void* cre, const void* cim, const void* logdt,
                                                   const void* dskip, const void* wqkv, const void* wo, const void* gw,
                                                   void* ws, void* out) {
    __shared__ __attribute__((aligned(1024))) char smem[LDS_BYTES];
    LAS char* sm = (LAS char*)smem;
    u64 pc0;
    asm volatile("s_getpc_b64 %0" : "=s"(pc0));
    const char* code = (const char*)(pc0 & ~127ull);
#define MAPMODE 2
    const int b = blockIdx.x, xcd = b & 7, idx = b >> 3;
    const bool roleA = MAPMODE == 1 ? idx < 16 : MAPMODE == 2 ? idx >= 16 : xcd < 4;
    const int ti = MAPMODE != 0 ? 4 * (xcd >> 1) + (idx & 3) : 8 * ((xcd & 3) >> 1) + (idx & 7);
    const int gj = MAPMODE != 0 ? 4 * (xcd & 1) + ((idx >> 2) & 3) : 4 * (xcd & 1) + (idx >> 3);
    const int wave = __builtin_amdgcn_readfirstlane(threadIdx.x >> 6), lane = threadIdx.x & 63;
    const Args a{(const char*)x, (const u16*)lre, (const u16*)lim, (const char*)bre, (const char*)bim, (const char*)cre,
                 (const char*)cim, (const u16*)logdt, (const char*)wqkv, (const char*)wo, (const u16*)gw,
                 (const u16*)dskip, (char*)ws, (float*)out, code};
    STAMPL(ws, 0);
    if (roleA) {
        if (wave < 4) a_consumer(a, sm, ti, gj, wave, lane);
        else a_loader(a, sm, ti, gj, wave - 4, lane);
    } else {
        if (wave < 4) b_consumer(a, sm, ti, gj, wave, lane);
        else b_loader(a, sm, ti, gj, wave - 4, lane);
    }
    STAMPL(ws, 1);
}

extern "C" void kernel_launch(void* const* d_in, const int* in_sizes, int n_in, void* d_out, int out_size, void* d_ws,
                              size_t ws_size, hipStream_t stream) {
    gm_fused<<<256, 512, 0, stream>>>(d_in[0], d_in[1], d_in[2], d_in[3], d_in[4], d_in[5], d_in[6], d_in[7], d_in[8],
                                      d_in[10], d_in[11], d_in[12], d_ws, d_out);
}
```

```cpp
#include <hip/hip_runtime.h>
#include <hip/hip_bf16.h>
#include <stdint.h>

namespace {

typedef __attribute__((ext_vector_type(8))) short bf16x8;
typedef __attribute__((ext_vector_type(4))) short s16x4;
typedef __attribute__((ext_vector_type(4))) float f32x4;
typedef __attribute__((ext_vector_type(2))) float f32x2;
typedef __attribute__((ext_vector_type(2))) __bf16 bf16x2_t;
typedef __attribute__((ext_vector_type(4))) unsigned u32x4;
typedef __attribute__((ext_vector_type(2))) unsigned u32x2;
typedef unsigned long long u64;
typedef unsigned short u16;
#define LAS __attribute__((address_space(3)))
#define GAS __attribute__((address_space(1)))
#if defined(__HIP_DEVICE_COMPILE__)
#define NOPK __attribute__((target("no-packed-fp32-ops")))
#else
#define NOPK
#endif
#define DEV __device__ __forceinline__
#define LAM __attribute__((always_inline))
#define RLX_AGENT __ATOMIC_RELAXED, __HIP_MEMORY_SCOPE_AGENT

constexpr size_t WS_NONCE = 0;
constexpr size_t WS_EA    = 4096;
constexpr size_t WS_KV    = WS_EA + 16 * 8 * 160 * 8;
constexpr size_t WS_TOT   = WS_KV + 16 * 8 * 840 * 8;
constexpr size_t WS_G     = WS_TOT + 8 * 840 * 8;
constexpr size_t WS_FACT  = WS_G + 2048 * 8;
constexpr size_t WS_FW2T  = WS_FACT + 16 * 256;
constexpr int NREP = 16;
constexpr size_t WS_W2T   = (WS_FW2T + NREP * 1024 + 1023) / 1024 * 1024;
constexpr size_t TS = 61440;
constexpr size_t WS_ACT   = WS_W2T + 40 * TS;
constexpr size_t WS_PROF  = 32u << 20;

#define STAMP(ws, wave, i) do {} while (0)
#define STAMPC(ws, wave, i) do {} while (0)
#define TIMED(acc, stmt) do { stmt; } while (0)
#define STAMPV(ws, wave, i, v) do {} while (0)
#define STAMP2(ws, wave, i) do {} while (0)

#define STAMPL(ws, i) do {} while (0)

constexpr int LDS_BYTES = 163840;
constexpr int SLOT_A = 36864, NS_A = 4;
constexpr int A_STG  = 110592;
constexpr int A_TAB  = 147456;
constexpr int A_JTAB = A_TAB + 3840;
constexpr int A_G    = A_JTAB + 2048;
constexpr int A_SEG  = A_G + 512;
constexpr int A_HT = 47104, HT_STRIDE = 272;
static_assert(A_SEG + 5120 <= 159776, "lds");
constexpr int SLOT_B = 47104, NS_B = 3;
constexpr int B_KVT = 0;
constexpr int B_Z   = B_KVT + 2 * 80 * 84 * 4;
constexpr int B_X1  = 141312;
constexpr int B_G   = B_X1 + 8192;
constexpr int B_TOT = B_G + 512;
static_assert(B_TOT + 3360 <= 159776, "lds");

#define SLP_W2T 8
#define SLP_ACT 4
#define SLP_LB 2
#define SLP_TOT 2
#define IL_A 16
#define IL_B 27
constexpr unsigned SPIN_CAP = 1u << 18;

DEV unsigned pk2(float a, float b) {
    return __builtin_bit_cast(unsigned, __builtin_convertvector((f32x2){a, b}, bf16x2_t));
}
DEV float bfr(float a) { return __uint_as_float(pk2(a, 0.f) << 16); }
DEV float bf_lo(unsigned u) { return __uint_as_float(u << 16); }
DEV float bf_hi(unsigned u) { return __uint_as_float(u & 0xffff0000u); }
DEV float bf2f(u16 u) { return __uint_as_float((unsigned)u << 16); }
DEV float swp(float x) {
    return __builtin_bit_cast(float, __builtin_amdgcn_mov_dpp(__builtin_bit_cast(int, x), 0xB1, 0xF, 0xF, true));
}
DEV float cmul_add(float a, float bs, float e, float u) { return fmaf(bs, swp(e), fmaf(a, e, u)); }
DEV f32x4 mfma(bf16x8 a, bf16x8 b, f32x4 c) { return __builtin_amdgcn_mfma_f32_16x16x32_bf16(a, b, c, 0, 0, 0); }
DEV bf16x8 as_frag(u32x4 v) { return __builtin_bit_cast(bf16x8, v); }
DEV bf16x8 lds_frag(LAS char* p) { return *(LAS bf16x8*)p; }
DEV bf16x8 lds_tr_frag(LAS char* p0, LAS char* p1) {
    s16x4 lo = __builtin_amdgcn_ds_read_tr16_b64_v4i16((LAS s16x4*)p0);
    s16x4 hi = __builtin_amdgcn_ds_read_tr16_b64_v4i16((LAS s16x4*)p1);
    return __builtin_shufflevector(lo, hi, 0, 1, 2, 3, 4, 5, 6, 7);
}
DEV void glds16_wt(const char* g, unsigned l) {
    unsigned keep;
    asm volatile("s_mov_b32 %0, m0\n\ts_mov_b32 m0, %2\n\ts_nop 0\n\tglobal_load_lds_dwordx4 %1, off sc1\n\ts_mov_b32 m0, %0"
                 : "=&s"(keep) : "v"(g), "s"(l) : "memory");
}
DEV void glds16(const char* g, LAS char* l) {
    __builtin_amdgcn_global_load_lds((const GAS unsigned*)g, (LAS unsigned*)l, 16, 0, 0);
}
DEV void bar() { asm volatile("" ::: "memory"); __builtin_amdgcn_s_barrier(); asm volatile("" ::: "memory"); }
DEV void wait_lgkm0() { __builtin_amdgcn_s_waitcnt(0xC07F); }
DEV void lds_bar() { asm volatile("" ::: "memory"); wait_lgkm0(); bar(); }
constexpr int L_FULL = 159776, L_FREE = L_FULL + 16;
constexpr int L_DONE = L_FULL + 32;
constexpr int L_KV = L_FULL + 36;
constexpr int L_W2T = L_FULL + 40;
constexpr int L_ACTH = L_FULL + 44, L_ACTO = L_FULL + 76;
constexpr int L_X1 = L_FULL + 48, L_ZERO = L_FULL + 64;
constexpr int L_DONE2 = L_FULL + 72;
constexpr int L_WR = L_FULL + 84;
constexpr int L_TOT = L_FULL + 88;
constexpr int L_HS = L_FULL + 96, L_HT = L_FULL + 100;
constexpr int L_PUB = L_FULL + 108;
constexpr int L_WORDS = 28;
DEV unsigned umin4(const u32x4& f) { const unsigned a = f[0] < f[1] ? f[0] : f[1], b = f[2] < f[3] ? f[2] : f[3]; return a < b ? a : b; }
DEV void ring_put(LAS char* p, unsigned v) {
    asm volatile("ds_write_b32 %0, %1" ::"v"((unsigned)(size_t)p), "v"(v) : "memory");
}
DEV void ring_wait(LAS char* p, unsigned need) {
#pragma unroll 1
    for (unsigned spins = 0; spins < SPIN_CAP; ++spins) {
        u32x4 f;
        asm volatile("ds_read_b128 %0, %1\n\ts_waitcnt lgkmcnt(0)" : "=&v"(f) : "v"((unsigned)(size_t)p) : "memory");
        if ((unsigned)__builtin_amdgcn_readfirstlane(umin4(f)) >= need) break;
        __builtin_amdgcn_s_sleep(1);
    }
}
DEV void word_wait(LAS char* p, unsigned need) {
#pragma unroll 1
    for (unsigned spins = 0; spins < SPIN_CAP; ++spins) {
        if ((unsigned)__builtin_amdgcn_readfirstlane(*(volatile LAS unsigned*)p) >= need) break;
        __builtin_amdgcn_s_sleep(1);
    }
    asm volatile("" ::: "memory");
}
DEV void word_set(LAS char* p, unsigned v) { asm volatile("" ::: "memory"); *(volatile LAS unsigned*)p = v; }
DEV unsigned word_add(LAS char* p) {
    return __hip_atomic_fetch_add((LAS unsigned*)p, 1u, __ATOMIC_RELAXED, __HIP_MEMORY_SCOPE_WORKGROUP);
}
DEV void ring_spin(LAS char* p, unsigned need) {
#pragma unroll 1
    for (unsigned spins = 0; spins < SPIN_CAP; ++spins) {
        const u32x4 f = *(volatile LAS u32x4*)p;
        if ((unsigned)__builtin_amdgcn_readfirstlane(umin4(f)) >= need) break;
    }
}
#define WAIT_VM(n) asm volatile("s_waitcnt vmcnt(" #n ")" ::: "memory")
#define SCHED() __builtin_amdgcn_sched_barrier(0)
#define KEEP(x) asm volatile("" ::"v"(x))
template <int NM, int NR, int NS> DEV void interleave() {
#pragma unroll
    for (int i = 0; i < NS; ++i) {
        __builtin_amdgcn_sched_group_barrier(0x8, 1, 0);
        if ((i + 1) * NR / NS != i * NR / NS) __builtin_amdgcn_sched_group_barrier(0x100, 1, 0);
    }
    __builtin_amdgcn_sched_group_barrier(0x8, NM - NS, 0);
}
DEV bool wave_all(bool p) { return __builtin_amdgcn_ballot_w64(p) == __builtin_amdgcn_ballot_w64(true); }
DEV float lane_xor(float v, int m) {
    const int x = __builtin_bit_cast(int, v);
    const int y = m == 1 ? __builtin_amdgcn_mov_dpp(x, 0xB1, 0xF, 0xF, true) : m == 2 ? __builtin_amdgcn_mov_dpp(x, 0x4E, 0xF, 0xF, true)
                                                                              : __builtin_amdgcn_ds_swizzle(x, 0x101F);
    return __builtin_bit_cast(float, y);
}
DEV float fexp(float x) { return __builtin_amdgcn_exp2f(x * 1.44269504088896341f); }
DEV unsigned mk_tag(unsigned n) { return ((n + 1u) * 0x9E3779B1u) | 1u; }
DEV u64 granule(unsigned tag, unsigned v) { return ((u64)tag << 32) | v; }
DEV u64 flag_of(unsigned tag) { return granule(tag, tag * 0x85EBCA6Bu + 0x5BD1E995u); }
DEV __amdgpu_buffer_rsrc_t rsrc(const char* p, unsigned bytes) { return __builtin_amdgcn_make_buffer_rsrc((void*)p, 0, bytes, 0x00020000); }
DEV void st16_wt(const __amdgpu_buffer_rsrc_t& r, unsigned voff, unsigned soff, u32x4 v) { __builtin_amdgcn_raw_buffer_store_b128(v, r, voff + soff, 0, 16); }
DEV void st8_wt(const __amdgpu_buffer_rsrc_t& r, unsigned voff, unsigned soff, u32x2 v) { __builtin_amdgcn_raw_buffer_store_b64(v, r, voff + soff, 0, 16); }
DEV bf16x8 ld16_wt(const __amdgpu_buffer_rsrc_t& r, unsigned voff, unsigned soff) {
    return __builtin_bit_cast(bf16x8, __builtin_amdgcn_raw_buffer_load_b128(r, voff, soff, 16));
}
DEV void signal_stores(LAS char* sm, int word, char* flag, unsigned tag, int lane, unsigned waves, int replicas = 1) {
    asm volatile("s_waitcnt vmcnt(0)" ::: "memory");
    unsigned old = 0;
    if (lane == 0) old = __hip_atomic_fetch_add((LAS unsigned*)(sm + word), 1u, __ATOMIC_RELAXED, __HIP_MEMORY_SCOPE_WORKGROUP);
    old = __builtin_amdgcn_readfirstlane(old);
    if (old == waves - 1u && lane < replicas) __hip_atomic_store((GAS u64*)(flag + lane * 1024), flag_of(tag), RLX_AGENT);
}
DEV float phi(float t) {
    const float e = __builtin_amdgcn_fmed3f(__builtin_amdgcn_exp2f(t * 1.44269504088896341f), 0.f, 1.f);
    return (e + 1e-6f) + __builtin_amdgcn_fmed3f(t, 0.f, __builtin_inff());
}
DEV bf16x8 pack_pair(const f32x4& lo, const f32x4& hi) {
    return as_frag((u32x4){pk2(lo[0], lo[1]), pk2(lo[2], lo[3]), pk2(hi[0], hi[1]), pk2(hi[2], hi[3])});
}

DEV void scan_step(float& u0, float& u1, float& u2, float& u3, float& u4, const float (&e)[5], const float (&a)[5],
                   const float (&b)[5]) {
    asm("v_fmac_f32 %0, %10, %5\n\tv_fmac_f32 %1, %11, %6\n\tv_fmac_f32 %2, %12, %7\n\t"
        "v_fmac_f32 %3, %13, %8\n\tv_fmac_f32 %4, %14, %9\n\t"
        "v_fmac_f32_dpp %0, %5, %15 quad_perm:[1,0,3,2] row_mask:0xf bank_mask:0xf bound_ctrl:1\n\t"
        "v_fmac_f32_dpp %1, %6, %16 quad_perm:[1,0,3,2] row_mask:0xf bank_mask:0xf bound_ctrl:1\n\t"
        "v_fmac_f32_dpp %2, %7, %17 quad_perm:[1,0,3,2] row_mask:0xf bank_mask:0xf bound_ctrl:1\n\t"
        "v_fmac_f32_dpp %3, %8, %18 quad_perm:[1,0,3,2] row_mask:0xf bank_mask:0xf bound_ctrl:1\n\t"
        "v_fmac_f32_dpp %4, %9, %19 quad_perm:[1,0,3,2] row_mask:0xf bank_mask:0xf bound_ctrl:1"
        : "+v"(u0), "+v"(u1), "+v"(u2), "+v"(u3), "+v"(u4)
        : "v"(e[0]), "v"(e[1]), "v"(e[2]), "v"(e[3]), "v"(e[4]), "v"(a[0]), "v"(a[1]), "v"(a[2]), "v"(a[3]), "v"(a[4]),
          "v"(b[0]), "v"(b[1]), "v"(b[2]), "v"(b[3]), "v"(b[4]));
}

DEV void scan_step_nd(float (&t)[5], float u0, float u1, float u2, float u3, float u4, const float (&e)[5],
                      const float (&a)[5], const float (&b)[5]) {
    asm("v_fma_f32 %0, %15, %10, %5\n\tv_fma_f32 %1, %16, %11, %6\n\tv_fma_f32 %2, %17, %12, %7\n\t"
        "v_fma_f32 %3, %18, %13, %8\n\tv_fma_f32 %4, %19, %14, %9\n\t"
        "v_fmac_f32_dpp %0, %10, %20 quad_perm:[1,0,3,2] row_mask:0xf bank_mask:0xf bound_ctrl:1\n\t"
        "v_fmac_f32_dpp %1, %11, %21 quad_perm:[1,0,3,2] row_mask:0xf bank_mask:0xf bound_ctrl:1\n\t"
        "v_fmac_f32_dpp %2, %12, %22 quad_perm:[1,0,3,2] row_mask:0xf bank_mask:0xf bound_ctrl:1\n\t"
        "v_fmac_f32_dpp %3, %13, %23 quad_perm:[1,0,3,2] row_mask:0xf bank_mask:0xf bound_ctrl:1\n\t"
        "v_fmac_f32_dpp %4, %14, %24 quad_perm:[1,0,3,2] row_mask:0xf bank_mask:0xf bound_ctrl:1"
        : "=&v"(t[0]), "=&v"(t[1]), "=&v"(t[2]), "=&v"(t[3]), "=&v"(t[4])
        : "v"(u0), "v"(u1), "v"(u2), "v"(u3), "v"(u4), "v"(e[0]), "v"(e[1]), "v"(e[2]), "v"(e[3]), "v"(e[4]),
          "v"(a[0]), "v"(a[1]), "v"(a[2]), "v"(a[3]), "v"(a[4]), "v"(b[0]), "v"(b[1]), "v"(b[2]), "v"(b[3]), "v"(b[4]));
}

struct Args {
    const char* x; const u16* lre; const u16* lim; const char* bre; const char* bim; const char* cre; const char* cim;
    const u16* logdt; const char* wqkv; const char* wo; const u16* gw; const u16* dskip; char* ws; float* out;
    const char* code;
};

DEV void channel_consts(u16 lre, u16 lim, u16 logdt, float& ar, float& ai, float& cr, float& ci) {
    const float lr = bf2f(lre), li = bf2f(lim);
    const float dt = bfr(fexp(bf2f(logdt)));
    const float zr = dt * lr, zi = dt * li, ex = fexp(zr);
    const float rv = zi * 0.15915494309189535f, rr = rv - __builtin_rintf(rv);
    ar = ex * __builtin_amdgcn_cosf(rr); ai = ex * __builtin_amdgcn_sinf(rr);
    const float nr = ar - 1.f, ni = ai, dn = lr * lr + li * li;
    cr = (nr * lr + ni * li) / dn; ci = (ni * lr - nr * li) / dn;
}
DEV float dot2(unsigned x, unsigned y, float s) {
    return __builtin_amdgcn_fdot2_f32_bf16(__builtin_bit_cast(bf16x2_t, x), __builtin_bit_cast(bf16x2_t, y), s, false);
}

constexpr int P2_RED = 0;
constexpr int P2_PW = 4, P2_PA = 5;
DEV bool flags_up(const u32x4& v, u64 fl) {
    const unsigned f0 = (unsigned)fl, f1 = (unsigned)(fl >> 32);
    return wave_all((v[0] == f0) & (v[1] == f1) & (v[2] == f0) & (v[3] == f1));
}
DEV void wait_flags8(const Args& a, int ti, int part, u64 fl) {
    GAS u64* fp = (GAS u64*)(a.ws + WS_FACT) + ti * 32 + part * 16 + (threadIdx.x & 7);
#pragma unroll 1
    for (unsigned spins = 0; spins < SPIN_CAP; ++spins) {
        if (wave_all(__hip_atomic_load(fp, RLX_AGENT) == fl)) break;
        __builtin_amdgcn_s_sleep(SLP_ACT);
    }
}
DEV unsigned p2_koff(int lw, int st) { return (st < 10 ? 10 * lw + st : 30 + 5 * lw + st) * 1024; }
DEV int p2_unit(int lw, int i) { return 5120 * (7 * lw + i); }
DEV void p2_prefetch(const Args& a, LAS char* sm, int lw, int lane, unsigned tag, int poller, bf16x8 (&fw)[P2_PW][5]) {
    const int b = blockIdx.x, xcd = b & 7, idx = b >> 3;
    const int cg = (xcd & 1) * 4 + (idx >> 3);
    if (lw == poller) {
        const __amdgpu_buffer_rsrc_t rf = rsrc(a.ws + WS_FW2T + (xcd * 2 + (b >> 7)) * 1024, 1024);
#pragma unroll 1
        for (unsigned spins = 0; spins < SPIN_CAP; ++spins) {
            if (flags_up(__builtin_amdgcn_raw_buffer_load_b128(rf, lane * 16, 0, 16), flag_of(tag))) break;
            __builtin_amdgcn_s_sleep(SLP_W2T);
        }
        word_set(sm + L_W2T, 1u);
    } else word_wait(sm + L_W2T, 1u);
    STAMP2(a.ws, lw + 4, 0);
    const unsigned wbase = 5 * cg * TS, vo = lane * 16;
    const char* src = a.ws + WS_W2T + wbase + vo;
#pragma unroll
    for (int i = 0; i < 7; ++i)
#pragma unroll
        for (int n = 0; n < 5; ++n)
            glds16_wt(src + (size_t)n * TS + p2_koff(lw, 4 + i), (unsigned)(size_t)(sm + p2_unit(lw, i) + n * 1024));
    const __amdgpu_buffer_rsrc_t rw = rsrc(a.ws + WS_W2T, 40 * TS);
#pragma unroll
    for (int st = 0; st < P2_PW; ++st)
#pragma unroll
        for (int n = 0; n < 5; ++n) fw[st][n] = ld16_wt(rw, vo, wbase + n * TS + p2_koff(lw, st));
}
DEV void p2_main(const Args& a, LAS char* sm, int lw, int lane, unsigned nonce, int poller, bf16x8 (&fw)[P2_PW][5]) {
    const int b = blockIdx.x, xcd = b & 7, idx = b >> 3;
    const int tt = (xcd >> 1) * 8 + (idx & 7), cg = (xcd & 1) * 4 + (idx >> 3);
    const int c = lane & 15, q = lane >> 4;
    const unsigned tag = mk_tag(nonce);
    const u64 fl = flag_of(tag);
    constexpr int PA = P2_PA;
    const __amdgpu_buffer_rsrc_t rw = rsrc(a.ws + WS_W2T, 40 * TS), ra = rsrc(a.ws + WS_ACT, 128 * TS);
    const unsigned wbase = 5 * cg * TS, abase = 4 * tt * TS, vo = lane * 16;
    bf16x8 fa[PA][4];
    auto ldw = [&](int st, int sl) LAM {
#pragma unroll
        for (int n = 0; n < 5; ++n) fw[sl][n] = ld16_wt(rw, vo, wbase + n * TS + p2_koff(lw, st));
    };
    auto lda = [&](int st) LAM {
#pragma unroll
        for (int t = 0; t < 4; ++t) fa[st % PA][t] = ld16_wt(ra, vo, abase + t * TS + p2_koff(lw, st));
    };
    WAIT_VM(0);
    if (lw == poller) { wait_flags8(a, tt >> 1, 0, fl); word_set(sm + L_ACTH, 1u); }
    else word_wait(sm + L_ACTH, 1u);
    STAMP2(a.ws, lw + 4, 1);
#pragma unroll
    for (int st = 0; st < 3; ++st) lda(st);
    f32x4 acc[5][4];
    const f32x4 z4 = {0.f, 0.f, 0.f, 0.f};
    auto slot = [&](int st) LAM { return st < 4 ? st : st <= 10 ? (st & 1) : st == 11 ? 2 : st == 12 ? 3 : st == 13 ? 1 : 0; };
#pragma unroll
    for (int st = 0; st < 15; ++st) {
        if (st <= 4) lda(st + 3);
        if (st == 6) word_wait(sm + L_ACTO, 1u);
        if (st >= 4 && st + 4 < 15) lda(st + 4);
        SCHED();
        if (st >= 3 && st < 10) {
#pragma unroll
            for (int n = 0; n < 5; ++n) fw[slot(st + 1)][n] = lds_frag(sm + p2_unit(lw, st - 3) + vo + n * 1024);
        }
#pragma unroll
        for (int n = 0; n < 5; ++n)
#pragma unroll
            for (int t = 0; t < 4; ++t) acc[n][t] = mfma(fw[slot(st)][n], fa[st % PA][t], st == 0 ? z4 : acc[n][t]);
        SCHED();
        if (st == 7) ldw(11, slot(11));
        if (st == 9) { wait_lgkm0(); if (lane == 0) word_add(sm + L_WR); ldw(12, slot(12)); }
        if (st == 10) ldw(13, slot(13));
        if (st == 11) ldw(14, slot(14));
    }
    STAMP2(a.ws, lw + 4, 2);
    word_wait(sm + L_WR, 4u);
    word_wait(sm + L_DONE, 4u);
    LAS float* rw_ = (LAS float*)(sm + P2_RED) + lw * (64 * 84);
#pragma unroll
    for (int t = 0; t < 4; ++t)
#pragma unroll
        for (int n = 0; n < 5; ++n) *(LAS f32x4*)(rw_ + (16 * t + c) * 84 + 16 * n + 4 * q) = acc[n][t];
    lds_bar();
    STAMP2(a.ws, lw + 4, 3);
}

DEV void p2_final(const Args& a, LAS char* sm, int cw, int lane, unsigned nonce) {
    const unsigned tag = mk_tag(nonce);
    const int b = blockIdx.x, xcd = b & 7, idx = b >> 3;
    const int tt = (xcd >> 1) * 8 + (idx & 7), cg = (xcd & 1) * 4 + (idx >> 3);
    const int tid = cw * 64 + lane;
    u32x2 xv[5], dv[5]; float gv[5];
    int tok[5], n0[5];
#pragma unroll
    for (int i = 0; i < 5; ++i) {
        const int id = tid + 256 * i, r = id / 20, c4 = id - 20 * r;
        tok[i] = 64 * tt + 16 * ((r & 15) >> 2) + 4 * (r >> 4) + (r & 3); n0[i] = 80 * cg + 4 * c4;
        xv[i] = *(const u32x2*)((const u16*)a.x + tok[i] * 640 + n0[i]);
        dv[i] = *(const u32x2*)(a.dskip + n0[i]);
    }
#pragma unroll 1
    for (unsigned spins = 0; spins < SPIN_CAP; ++spins) {
        u64 v[5];
#pragma unroll
        for (int i = 0; i < 5; ++i) v[i] = __hip_atomic_load((GAS u64*)(a.ws + WS_G) + tok[i], RLX_AGENT);
        bool ok = true;
#pragma unroll
        for (int i = 0; i < 5; ++i) { gv[i] = __uint_as_float((unsigned)v[i]); ok &= (unsigned)(v[i] >> 32) == tag; }
        if (wave_all(ok)) break;
        __builtin_amdgcn_s_sleep(2);
    }
    f32x4 sk[5];
#pragma unroll
    for (int i = 0; i < 5; ++i) {
        const unsigned p01 = pk2(bf_lo(xv[i][0]) * bf_lo(dv[i][0]), bf_hi(xv[i][0]) * bf_hi(dv[i][0]));
        const unsigned p23 = pk2(bf_lo(xv[i][1]) * bf_lo(dv[i][1]), bf_hi(xv[i][1]) * bf_hi(dv[i][1]));
        sk[i] = (f32x4){gv[i] * bf_lo(p01), gv[i] * bf_hi(p01), gv[i] * bf_lo(p23), gv[i] * bf_hi(p23)};
    }
    asm volatile("" : "+v"(sk[0]), "+v"(sk[1]), "+v"(sk[2]), "+v"(sk[3]), "+v"(sk[4]));
    if (cw == 0) {
        wait_flags8(a, tt >> 1, 1, flag_of(tag));
        word_set(sm + L_ACTO, 1u);
        if (tt == 31 && cg == 0) {
            word_wait(sm + L_ACTH, 1u);
            if (lane == 0) __hip_atomic_store((GAS unsigned*)(a.ws + WS_NONCE), nonce + 1u, RLX_AGENT);
        }
    }
    STAMP2(a.ws, cw, 0);
    bar();
    STAMP2(a.ws, cw, 2);
#pragma unroll
    for (int i = 0; i < 5; ++i) {
        const int id = tid + 256 * i, r = id / 20, c4 = id - 20 * r;
        LAS float* p = (LAS float*)(sm + P2_RED) + r * 84 + 4 * c4;
        sk[i] += (*(LAS f32x4*)p + *(LAS f32x4*)(p + 64 * 84)) + (*(LAS f32x4*)(p + 128 * 84) + *(LAS f32x4*)(p + 192 * 84));
    }
    asm volatile("" : "+v"(sk[0]), "+v"(sk[1]), "+v"(sk[2]), "+v"(sk[3]), "+v"(sk[4]));
#define OUT_AUX 16
    {
        const __amdgpu_buffer_rsrc_t ro = rsrc((const char*)a.out, 2048 * 640 * 4);
#pragma unroll
        for (int i = 0; i < 5; ++i)
            __builtin_amdgcn_raw_buffer_store_b128(__builtin_bit_cast(u32x4, sk[i]), ro, (tok[i] * 640 + n0[i]) * 4, 0, OUT_AUX);
    }
    STAMP2(a.ws, cw, 3);
}

DEV void job_issue(const Args& a, LAS char* stg, int jb, int lane) {
    if (jb < 400) {
        const int cb = jb / 40, nt = jb - 40 * cb;
        const unsigned o = (16 * nt + (lane >> 3)) * 1280 + cb * 128 + (lane & 7) * 16;
        glds16(a.cre + o, stg); glds16(a.cre + o + 8 * 1280, stg + 1024);
        glds16(a.cim + o, stg + 2048); glds16(a.cim + o + 8 * 1280, stg + 3072);
    } else if (jb < 500) {
        const int jo = jb - 400, s5 = jo / 5, n8 = jo - 5 * s5, uu = s5 / 5, s = s5 - 5 * uu;
        const int lo = 160 * uu + (s == 0 ? 0 : s == 1 ? 32 : s == 2 ? 64 : s == 3 ? 80 : 112);
        const int hi = 160 * uu + (s == 0 ? 16 : s == 1 ? 48 : s == 2 ? 144 : s == 3 ? 96 : 128);
        const unsigned o = (lane >> 4) * 1280 + n8 * 256 + (lane & 15) * 16;
#pragma unroll
        for (int k = 0; k < 4; ++k) {
            glds16(a.wo + (lo + 4 * k) * 1280 + o, stg + 1024 * k);
            glds16(a.wo + (hi + 4 * k) * 1280 + o, stg + 4096 + 1024 * k);
        }
    }
}
DEV u32x2 lds_rd8(LAS char* p) { u32x2 v; asm volatile("ds_read_b64 %0, %1" : "=v"(v) : "v"((unsigned)(size_t)p) : "memory"); return v; }
DEV f32x4 lds_rd16(LAS char* p) { f32x4 v; asm volatile("ds_read_b128 %0, %1" : "=v"(v) : "v"((unsigned)(size_t)p) : "memory"); return v; }
DEV u32x2 lds_rdtr(LAS char* p) { u32x2 v; asm volatile("ds_read_b64_tr_b16 %0, %1" : "=v"(v) : "v"((unsigned)(size_t)p) : "memory"); return v; }
DEV void job_finish(const Args& a, LAS char* stg, LAS char* tab, int jb, int lane) {
    const int c = lane & 15, q = lane >> 4;
    const __amdgpu_buffer_rsrc_t rw = rsrc(a.ws + WS_W2T, 40 * TS);
    if (jb < 400) {
        const int cb = jb / 40, nt = jb - 40 * cb;
        u32x2 re[4], im[4]; f32x4 c01[4], c23[4];
#pragma unroll
        for (int k = 0; k < 4; ++k) {
            re[k] = lds_rd8(stg + c * 128 + 32 * k + 8 * q); im[k] = lds_rd8(stg + 2048 + c * 128 + 32 * k + 8 * q);
            c01[k] = lds_rd16(tab + (16 * k + 4 * q) * 8); c23[k] = lds_rd16(tab + (16 * k + 4 * q) * 8 + 16);
        }
        asm volatile("s_waitcnt lgkmcnt(0)" : "+v"(re[0]), "+v"(re[1]), "+v"(re[2]), "+v"(re[3]), "+v"(im[0]), "+v"(im[1]), "+v"(im[2]), "+v"(im[3]),
                     "+v"(c01[0]), "+v"(c01[1]), "+v"(c01[2]), "+v"(c01[3]), "+v"(c23[0]), "+v"(c23[1]), "+v"(c23[2]), "+v"(c23[3]) :: "memory");
        u32x4 w[4];
#pragma unroll
        for (int k = 0; k < 4; ++k) {
            const float r0 = bf_lo(re[k][0]), r1 = bf_hi(re[k][0]), r2 = bf_lo(re[k][1]), r3 = bf_hi(re[k][1]);
            const float i0 = bf_lo(im[k][0]), i1 = bf_hi(im[k][0]), i2 = bf_lo(im[k][1]), i3 = bf_hi(im[k][1]);
            w[k] = (u32x4){pk2(r0 * c01[k][0] - i0 * c01[k][1], -(r0 * c01[k][1] + i0 * c01[k][0])),
                           pk2(r1 * c01[k][2] - i1 * c01[k][3], -(r1 * c01[k][3] + i1 * c01[k][2])),
                           pk2(r2 * c23[k][0] - i2 * c23[k][1], -(r2 * c23[k][1] + i2 * c23[k][0])),
                           pk2(r3 * c23[k][2] - i3 * c23[k][3], -(r3 * c23[k][3] + i3 * c23[k][2]))};
        }
#pragma unroll
        for (int k = 0; k < 4; ++k) st16_wt(rw, lane * 16, nt * TS + (4 * cb + k) * 1024, w[k]);
    } else if (jb < 500) {
        const int jo = jb - 400, s5 = jo / 5, n8 = jo - 5 * s5;
        u32x2 lo[8], hi[8];
#pragma unroll
        for (int k = 0; k < 8; ++k) {
            LAS char* b = stg + (4 * q + (c >> 2)) * 256 + 32 * k + 8 * (c & 3);
            lo[k] = lds_rdtr(b); hi[k] = lds_rdtr(b + 4096);
        }
        asm volatile("s_waitcnt lgkmcnt(0)" : "+v"(lo[0]), "+v"(lo[1]), "+v"(lo[2]), "+v"(lo[3]), "+v"(lo[4]), "+v"(lo[5]), "+v"(lo[6]), "+v"(lo[7]),
                     "+v"(hi[0]), "+v"(hi[1]), "+v"(hi[2]), "+v"(hi[3]), "+v"(hi[4]), "+v"(hi[5]), "+v"(hi[6]), "+v"(hi[7]) :: "memory");
#pragma unroll
        for (int k = 0; k < 8; ++k)
            st16_wt(rw, lane * 16, (8 * n8 + k) * TS + (40 + s5) * 1024, (u32x4){lo[k][0], lo[k][1], hi[k][0], hi[k][1]});
    }
}

#define TOUCH 1
#define TOUCH_JOB 0
#define TOUCH_S0 6
constexpr int L_JUNK = 160000;
DEV void touch(const char* p, LAS char* sm) {
    unsigned keep;
    asm volatile("s_mov_b32 %0, m0\n\ts_mov_b32 m0, %2\n\ts_nop 0\n\tglobal_load_lds_dword %1, off\n\ts_mov_b32 m0, %0"
                 : "=&s"(keep) : "v"(p), "s"((unsigned)(size_t)(sm + L_JUNK)) : "memory");
}
DEV void touch_job(const Args& a, LAS char* sm, int jb, int lane) {
    if (jb < 400) {
        const int cb = jb / 40, nt = jb - 40 * cb;
        touch((lane & 32 ? a.cim : a.cre) + (16 * nt + (lane & 15)) * 1280 + cb * 128, sm);
    } else if (jb < 500) {
        const int jo = jb - 400, s5 = jo / 5, n8 = jo - 5 * s5, uu = s5 / 5, s = s5 - 5 * uu;
        const int lo = 160 * uu + (s == 0 ? 0 : s == 1 ? 32 : s == 2 ? 64 : s == 3 ? 80 : 112);
        const int hi = 160 * uu + (s == 0 ? 16 : s == 1 ? 48 : s == 2 ? 144 : s == 3 ? 96 : 128);
        touch(a.wo + ((lane & 32 ? hi : lo) + (lane & 15)) * 1280 + n8 * 256 + (lane & 16 ? 128 : 0), sm);
    }
}
#define TOUCH_CODE 1
constexpr int CODE_LINES = 470;
DEV void touch_code(const char* code, LAS char* sm, int lane) {
    const int l = (blockIdx.x >> 3) * 15 + lane;
    if (lane < 15 && l < CODE_LINES) touch(code + l * 128, sm);
}
#define TOUCH_TLB 0
DEV void touch_pages(const Args& a, LAS char* sm, int lane) {
    const int e = ((blockIdx.x >> 3) * 5 + lane) % 134;
    if (lane < 5) touch(e < 90 ? a.ws + (size_t)e * 131072 : e < 130 ? (const char*)a.out + (size_t)(e - 90) * 131072
                        : e == 130 ? a.cre : e == 131 ? a.cim : e == 132 ? a.wo : (const char*)a.dskip, sm);
}
DEV void touch_streams(const Args& a, LAS char* sm, int cw, int lane) {
    const int b = blockIdx.x, xcd = b & 7, tq = xcd >> 1, gh = xcd & 1;
#pragma unroll
    for (int r = 0; r < 2; ++r) {
        const int e = (b >> 3) * 4 + cw + 128 * r, s = TOUCH_S0 + e / 33, k = e - 33 * (e / 33);
        if (s < 10) {
            const char* p;
            if (k < 8) p = a.x + (size_t)((4 * tq + (k >> 1)) * 128 + 64 * (k & 1) + lane) * 1280 + 128 * s;
            else if (k < 18) {
                const int li = 64 * (k - 8) + lane, g = li / 160, rem = li - 160 * g;
                p = (rem < 80 ? a.bre : a.bim) + (size_t)(80 * (4 * gh + g) + (rem < 80 ? rem : rem - 80)) * 1280 + 128 * s;
            } else {
                const int li = 64 * (k - 18) + lane, kr = li / 15, rem = li - 15 * kr, sec = rem / 5;
                p = a.wqkv + (size_t)(64 * s + kr) * 3840 + sec * 1280 + 640 * gh + 128 * (rem - 5 * sec);
            }
            touch(p, sm);
        }
    }
}
DEV float gate_of(const Args& a, int tok, unsigned tag) {
    u64 v = 0;
#pragma unroll 1
    for (unsigned spins = 0; spins < SPIN_CAP; ++spins) {
        v = __hip_atomic_load((GAS u64*)(a.ws + WS_G) + tok, RLX_AGENT);
        if (wave_all((unsigned)(v >> 32) == tag)) break;
        __builtin_amdgcn_s_sleep(1);
    }
    return __uint_as_float((unsigned)v);
}
DEV int x_token(int R) { const int rho = R & 15; return (R & 64) + 16 * (rho >> 2) + 4 * ((R >> 4) & 3) + (rho & 3); }

DEV void a_loader(const Args& a, LAS char* sm, int ti, int gj, int lw, int lane) {
    STAMP(a.ws, lw + 4, 0);
    const unsigned nonce = __hip_atomic_load((GAS unsigned*)(a.ws + WS_NONCE), RLX_AGENT);
    unsigned off[9];
    {
        const int pos = lane & 7, rl = lane >> 3, im = lw & 1;
#pragma unroll
        for (int u = 0; u < 4; ++u) {
            const int R = 8 * (lw + 4 * u) + rl;
            off[u] = x_token(R) * 1280 + ((pos ^ (R & 7)) << 4);
        }
#pragma unroll
        for (int u = 4; u < 9; ++u) off[u] = (8 * ((lw + 4 * (u - 4)) >> 1) + rl) * 1280 + ((pos ^ rl ^ (im << 2)) << 4);
    }
    const char* xb = a.x + (size_t)ti * (128 * 1280);
    const char* bb = ((lw & 1) ? a.bim : a.bre) + (size_t)gj * (80 * 1280);
    auto issue_a = [&](int s) LAM {
        LAS char* dst = sm + (s % NS_A) * SLOT_A + lw * 1024;
#pragma unroll
        for (int u = 0; u < 4; ++u) glds16(xb + 128 * s + off[u], dst + 4096 * u);
        glds16(bb + 128 * s + off[4], dst + 4096 * 4);
    };
    auto issue_b = [&](int s) LAM {
        LAS char* dst = sm + (s % NS_A) * SLOT_A + lw * 1024;
#pragma unroll
        for (int u = 5; u < 9; ++u) glds16(bb + 128 * s + off[u], dst + 4096 * u);
    };
    STAMP(a.ws, lw + 4, 1);
    issue_a(0); issue_b(0); issue_a(1); issue_b(1); issue_a(2); issue_b(2);
    WAIT_VM(18);
    bar();
#pragma unroll 1
    for (int s = 0; s < 7; ++s) {
        if (s > 0) ring_wait(sm + L_FREE, s);
        issue_a(s + 3);
        WAIT_VM(14);
        ring_put(sm + L_FULL + 4 * lw, s + 2);
        issue_b(s + 3);
    }
    WAIT_VM(9);
    ring_put(sm + L_FULL + 4 * lw, 9);
    WAIT_VM(0);
    ring_put(sm + L_FULL + 4 * lw, 10);
    STAMP(a.ws, lw + 4, 2);
    const unsigned tag = mk_tag(nonce);
    const int tl = lw * 64 + lane;
    const int jb = (ti * 8 + gj) * 4 + lw;
    ring_wait(sm + L_FREE, 8);
    job_issue(a, sm + A_STG + lw * 8192, jb, lane);
    if (tl < 128) ((LAS float*)(sm + A_G))[tl] = gate_of(a, 128 * ti + tl, tag);
    WAIT_VM(0);
    job_finish(a, sm + A_STG + lw * 8192, sm + A_JTAB + lw * 512, jb, lane);
    STAMP(a.ws, lw + 4, 9);
    __builtin_amdgcn_s_setprio(2);
    lds_bar();
    STAMP(a.ws, lw + 4, 3);
    if (lw < 3) {
        const int col = tl < 160 ? tl : 159;
        LAS float* tab = (LAS float*)(sm + A_TAB);
        GAS u64* ea = (GAS u64*)(a.ws + WS_EA) + gj * 160;
        const float a16 = tab[2 * 160 + col], b16 = tab[3 * 160 + col], a128 = tab[4 * 160 + col], b128 = tab[5 * 160 + col];
        LAS float* sg = (LAS float*)(sm + A_SEG);
        float pre[8], P = 0.f;
#pragma unroll
        for (int s = 0; s < 8; ++s) { pre[s] = P; P = cmul_add(a16, b16, P, sg[s * 160 + col]); }
        if (tl < 160) __hip_atomic_store(ea + ti * 1280 + col, granule(tag, __float_as_uint(P)), RLX_AGENT);
        STAMP(a.ws, lw + 4, 4);
        signal_stores(sm, L_DONE2, a.ws + WS_FW2T + (ti * 8 + gj) * 8, tag, lane, 4u, NREP);
        float H = 0.f;
        if (ti > 0) {
            float ev[15];
#pragma unroll 1
            for (unsigned spins = 0; spins < SPIN_CAP; ++spins) {
                u64 v[15];
#pragma unroll
                for (int i = 0; i < 15; ++i) v[i] = __hip_atomic_load(ea + (i < ti ? i : 0) * 1280 + col, RLX_AGENT);
                bool ok = true;
#pragma unroll
                for (int i = 0; i < 15; ++i) { ev[i] = __uint_as_float((unsigned)v[i]); ok &= (unsigned)(v[i] >> 32) == tag; }
                if (wave_all(ok)) break;
                __builtin_amdgcn_s_sleep(SLP_LB);
            }
#pragma unroll
            for (int i = 0; i < 15; ++i) if (i < ti) H = cmul_add(a128, b128, H, ev[i]);
        }
        STAMP(a.ws, lw + 4, 6);
#pragma unroll
        for (int s = 0; s < 8; ++s) { if (tl < 160) sg[s * 160 + col] = H + pre[s]; H = cmul_add(a16, b16, H, 0.f); }
        wait_lgkm0();
        if (lane == 0) word_add(sm + L_HS);
    } else signal_stores(sm, L_DONE2, a.ws + WS_FW2T + (ti * 8 + gj) * 8, tag, lane, 4u, NREP);
    __builtin_amdgcn_s_setprio(0);
    STAMP(a.ws, lw + 4, 8);
    word_wait(sm + L_DONE, 4u);
    bf16x8 fw[P2_PW][5];
    p2_prefetch(a, sm, lw, lane, tag, 3, fw);
    p2_main(a, sm, lw, lane, nonce, 3, fw);
}

DEV void a_pass1(f32x4 (&acc)[4][5], const float (&ca)[5], const float (&cb)[5], LAS float* es) {
    float e[5];
#pragma unroll
    for (int n = 0; n < 5; ++n) e[n] = acc[0][n][0];
#pragma unroll
    for (int t = 1; t < 16; ++t) {
        const int i = t >> 2, r = t & 3;
        float y[5];
        scan_step_nd(y, acc[i][0][r], acc[i][1][r], acc[i][2][r], acc[i][3][r], acc[i][4][r], e, ca, cb);
#pragma unroll
        for (int n = 0; n < 5; ++n) e[n] = y[n];
    }
#pragma unroll
    for (int n = 0; n < 5; ++n) es[16 * n] = e[n];
}
DEV void a_pass2(f32x4 (&acc)[4][5], const float (&ca)[5], const float (&cb)[5], LAS float* hs, LAS float* gt, LAS char* ht,
                 LAS char* hr, const __amdgpu_buffer_rsrc_t& ract, unsigned vo, unsigned ob) {
    float e[5];
#pragma unroll
    for (int n = 0; n < 5; ++n) e[n] = hs[16 * n];
    asm volatile("s_nop 1" : "+v"(e[0]), "+v"(e[1]), "+v"(e[2]), "+v"(e[3]), "+v"(e[4]));
    bf16x8 hf[4][2];
    auto rd = [&](int m) LAM {
#pragma unroll
        for (int k = 0; k < 2; ++k) {
            LAS char* b = hr + 32 * k * HT_STRIDE + 32 * m;
            hf[m][k] = lds_tr_frag(b, b + 4 * HT_STRIDE);
        }
    };
    auto st = [&](int m) LAM {
#pragma unroll
        for (int k = 0; k < 2; ++k) st16_wt(ract, vo, ob + m * TS + k * 1024, __builtin_bit_cast(u32x4, hf[m][k]));
    };
#pragma unroll
    for (int m = 0; m < 4; ++m) {
        const f32x4 g = *(LAS f32x4*)(gt + 4 * m);
#pragma unroll
        for (int r = 0; r < 4; ++r) {
            float u0 = acc[m][0][r], u1 = acc[m][1][r], u2 = acc[m][2][r], u3 = acc[m][3][r], u4 = acc[m][4][r];
            scan_step(u0, u1, u2, u3, u4, e, ca, cb);
            e[0] = u0; e[1] = u1; e[2] = u2; e[3] = u3; e[4] = u4;
            acc[m][0][r] = u0 * g[r]; acc[m][1][r] = u1 * g[r]; acc[m][2][r] = u2 * g[r]; acc[m][3][r] = u3 * g[r]; acc[m][4][r] = u4 * g[r];
        }
        SCHED();
        if (m >= 2) st(m - 2);
#pragma unroll
        for (int n = 0; n < 5; ++n)
            *(LAS u32x2*)(ht + 16 * n * HT_STRIDE + 32 * m) = (u32x2){pk2(acc[m][n][0], acc[m][n][1]), pk2(acc[m][n][2], acc[m][n][3])};
        rd(m);
        SCHED();
    }
    st(2); st(3);
#pragma unroll
    for (int m = 0; m < 4; ++m) { KEEP(hf[m][0]); KEEP(hf[m][1]); }
}

DEV void a_consumer(const Args& a, LAS char* sm, int ti, int gj, int cw, int lane) {
    const int mh = cw >> 1, nh = cw & 1, c = lane & 15, q = lane >> 4, tid = cw * 64 + lane;
    STAMP(a.ws, cw, 0);
    const unsigned nonce = __hip_atomic_load((GAS unsigned*)(a.ws + WS_NONCE), RLX_AGENT);
    const int jb = (ti * 8 + gj) * 4 + cw, pj = (jb < 400 ? 64 * (jb / 40) : 0) + lane;
    const u16 jlre = a.lre[pj], jlim = a.lim[pj], jldt = a.logdt[pj];
    const int gtok = 128 * ti + 16 * gj + 8 * (cw & 1) + (lane >> 3), pch = 80 * gj + (tid < 80 ? tid : 0);
    u32x4 xv[10], gv[10];
    u16 rlre = 0, rlim = 0, rldt = 0;
    if (cw >= 2) {
        const u32x4* xr = (const u32x4*)(a.x + (size_t)gtok * 1280) + (lane & 7);
        const u32x4* gr = (const u32x4*)a.gw + (lane & 7);
#pragma unroll
        for (int i = 0; i < 10; ++i) { xv[i] = xr[8 * i]; gv[i] = gr[8 * i]; }
    } else { rlre = a.lre[pch]; rlim = a.lim[pch]; rldt = a.logdt[pch]; }
    __builtin_amdgcn_s_setprio(1);
    f32x4 acc[4][5];
#pragma unroll
    for (int n = 0; n < 5; ++n)
#pragma unroll
        for (int m = 0; m < 4; ++m) acc[m][n] = (f32x4){0.f, 0.f, 0.f, 0.f};
    int xo[2], wa[2];
#pragma unroll
    for (int kk = 0; kk < 2; ++kk) {
        xo[kk] = (64 * mh + c) * 128 + (((4 * kk + q) ^ (c & 7)) << 4);
        wa[kk] = 16384 + (80 * nh + 8 * (c & 1) + (c >> 1)) * 128 + (((4 * kk + q) ^ (c >> 1) ^ ((c & 1) << 2)) << 4);
    }
    bf16x8 xa[2][4], wb[2][5];
    auto rdp = [&](int buf, LAS char* s0, int kk) LAM {
#pragma unroll
        for (int m = 0; m < 4; ++m) xa[buf][m] = lds_frag(s0 + xo[kk] + 2048 * m);
#pragma unroll
        for (int n = 0; n < 5; ++n) wb[buf][n] = lds_frag(s0 + wa[kk] + 2048 * n);
    };
    auto mm_head = [&](int buf) LAM {
#pragma unroll
        for (int m = 0; m < 4; ++m) acc[m][0] = mfma(xa[buf][m], wb[buf][0], acc[m][0]);
    };
    auto mm_tail = [&](int buf) LAM {
#pragma unroll
        for (int n = 1; n < 5; ++n)
#pragma unroll
            for (int m = 0; m < 4; ++m) acc[m][n] = mfma(xa[buf][m], wb[buf][n], acc[m][n]);
    };
    if (tid < L_WORDS) ((LAS unsigned*)(sm + L_FULL))[tid] = tid < 4 ? 1u : 0u;
#pragma unroll
    for (int n = 0; n < 5; ++n)
        asm volatile("" : "+v"(acc[0][n]), "+v"(acc[1][n]), "+v"(acc[2][n]), "+v"(acc[3][n]));
    asm volatile("" : "+v"(xo[0]), "+v"(xo[1]), "+v"(wa[0]), "+v"(wa[1]));
    const unsigned tag = mk_tag(nonce);
    if (cw >= 2) {
        float s0 = 0.f, s1 = 0.f;
#pragma unroll
        for (int i = 0; i < 10; ++i) {
            s0 = dot2(xv[i][0], gv[i][0], s0); s1 = dot2(xv[i][1], gv[i][1], s1);
            s0 = dot2(xv[i][2], gv[i][2], s0); s1 = dot2(xv[i][3], gv[i][3], s1);
        }
        float s = s0 + s1;
        s += lane_xor(s, 1); s += lane_xor(s, 2); s += lane_xor(s, 4);
        const float lg = bfr(s), g = bfr(1.f / (1.f + fexp(-lg)));
        if ((lane & 7) == 0) __hip_atomic_store((GAS u64*)(a.ws + WS_G) + gtok, granule(tag, __float_as_uint(g)), RLX_AGENT);
    } else if (tid < 80) {
        float ar, ai, cr, ci;
        channel_consts(rlre, rlim, rldt, ar, ai, cr, ci);
        float pr = ar, pi = ai;
#pragma unroll
        for (int k = 0; k < 4; ++k) { const float t = pr * pr - pi * pi; pi = 2.f * pr * pi; pr = t; }
        const float r16 = pr, i16 = pi;
#pragma unroll
        for (int k = 0; k < 3; ++k) { const float t = pr * pr - pi * pi; pi = 2.f * pr * pi; pr = t; }
        LAS f32x2* tab = (LAS f32x2*)(sm + A_TAB);
        tab[0 * 80 + tid] = (f32x2){ar, ar};
        tab[1 * 80 + tid] = (f32x2){-ai, ai};
        tab[2 * 80 + tid] = (f32x2){r16, r16};
        tab[3 * 80 + tid] = (f32x2){-i16, i16};
        tab[4 * 80 + tid] = (f32x2){pr, pr};
        tab[5 * 80 + tid] = (f32x2){-pi, pi};
    }
    {
        float ar, ai, cr, ci;
        channel_consts(jlre, jlim, jldt, ar, ai, cr, ci);
        ((LAS f32x2*)(sm + A_JTAB + cw * 512))[lane] = (f32x2){2.f * cr, 2.f * ci};
    }
    if (tid < 160) __hip_atomic_store((GAS u64*)(a.ws + WS_EA) + (ti * 8 + gj) * 160 + tid, (u64)0, RLX_AGENT);
    if (TOUCH_CODE == 1 && cw == 0) touch_code(a.code, sm, lane);
    if (TOUCH_TLB && cw == 1) touch_pages(a, sm, lane);
    if (TOUCH) touch_streams(a, sm, cw, lane);
    if (TOUCH_JOB) touch_job(a, sm, jb, lane);
    STAMP(a.ws, cw, 1);
    lds_bar();
    STAMP(a.ws, cw, 2);
    STAMPC(a.ws, cw, 14);
    rdp(0, sm, 0);
#pragma unroll 1
    for (int s = 0; s < 10; ++s) {
        wait_lgkm0();
        SCHED();
        rdp(1, sm + (s & 3) * SLOT_A, 1);
        u32x4 fl = *(volatile LAS u32x4*)(sm + L_FULL);
        mm_head(0); mm_tail(0);
        interleave<20, 10, IL_A>();
        SCHED();
        wait_lgkm0();
        asm volatile("" ::: "memory");
        *(volatile LAS unsigned*)(sm + L_FREE + 4 * cw) = s + 1;
        const unsigned need = s < 9 ? s + 2 : 0;
        unsigned landed = __builtin_amdgcn_readfirstlane(umin4(fl));
        LAS char* s0 = sm + ((s + 1) & 3) * SLOT_A;
        asm volatile("" : "+s"(landed), "+v"(s0));
        mm_head(1);
        SCHED();
        if (__builtin_expect(landed < need, 0)) ring_spin(sm + L_FULL, need);
        asm volatile("" ::: "memory");
        SCHED();
        rdp(0, s0, 0);
        mm_tail(1);
        interleave<16, 9, IL_A - 3>();
        SCHED();
    }
    __builtin_amdgcn_s_setprio(0);
    asm volatile("s_nop 7\n\ts_nop 7" ::: "memory");
    STAMP(a.ws, cw, 3);
    STAMPC(a.ws, cw, 15);
    LAS float* tab = (LAS float*)(sm + A_TAB);
    LAS float* sg = (LAS float*)(sm + A_SEG) + (4 * mh + q) * 160 + 80 * nh + c;
    float ca[5], cb[5];
#pragma unroll
    for (int n = 0; n < 5; ++n) { const int col = 80 * nh + 16 * n + c; ca[n] = tab[col]; cb[n] = tab[160 + col]; }
    a_pass1(acc, ca, cb, sg);
    WAIT_VM(0);
    STAMP(a.ws, cw, 4);
    lds_bar();
    STAMP(a.ws, cw, 5);
    word_wait(sm + L_HS, 3u);
    STAMP(a.ws, cw, 8);
    {
        const int qq = (lane & 15) >> 2, p = lane & 3;
        const __amdgpu_buffer_rsrc_t ract = rsrc(a.ws + WS_ACT, 128 * TS);
        LAS float* gt = (LAS float*)(sm + A_G) + 64 * mh + 16 * q;
        LAS char* ht = sm + A_HT + (80 * nh + c) * HT_STRIDE + (64 * mh + 4 * q) * 2;
        LAS char* hr = sm + A_HT + (8 * q + qq) * HT_STRIDE + 128 * mh + 8 * p;
        const unsigned ob = (8 * ti + 4 * mh) * TS + 5 * gj * 1024;
        a_pass2(acc, ca, cb, sg, gt, ht, hr + 96 * nh * HT_STRIDE, ract, lane * 16, ob + 3072 * nh);
        STAMP(a.ws, cw, 9);
        wait_lgkm0();
        if (lane == 0) word_add(sm + L_HT + 4 * mh);
        word_wait(sm + L_HT + 4 * mh, 2u);
        bf16x8 hf[2];
#pragma unroll
        for (int i = 0; i < 2; ++i) {
            LAS char* b = hr + 64 * HT_STRIDE + 32 * (2 * nh + i);
            hf[i] = lds_tr_frag(b, b + 4 * HT_STRIDE);
        }
#pragma unroll
        for (int i = 0; i < 2; ++i) st16_wt(ract, lane * 16, ob + (2 * nh + i) * TS + 2048, __builtin_bit_cast(u32x4, hf[i]));
    }
    wait_lgkm0();
    STAMP(a.ws, cw, 10);
    signal_stores(sm, L_DONE, a.ws + WS_FACT + ti * 256 + gj * 8, tag, lane, 4u);
    STAMP(a.ws, cw, 11);
    p2_final(a, sm, cw, lane, nonce);
}

DEV void b_loader(const Args& a, LAS char* sm, int ti, int gj, int lw, int lane) {
    STAMP(a.ws, lw + 4, 0);
    const unsigned nonce = __hip_atomic_load((GAS unsigned*)(a.ws + WS_NONCE), RLX_AGENT);
    unsigned off[12];
    {
        const int pos = lane & 7, rl = lane >> 3;
#pragma unroll
        for (int u = 0; u < 4; ++u) {
            const int R = 8 * (lw + 4 * u) + rl;
            off[u] = x_token(R) * 1280 + ((pos ^ (R & 7)) << 4);
        }
#pragma unroll
        for (int u = 4; u < 12; ++u) {
            const int wi = lw + 4 * (u - 4), sec = wi / 10, ch = 64 * (wi - 10 * sec) + lane, rho = ch / 10, cc = ch - 10 * rho;
            off[u] = (rho ^ (((rho >> 3) & 1) << 2)) * 3840 + cc * 16 + (sec == 0 ? 1280 : sec == 1 ? 2560 : 0);
        }
    }
    const char* xb = a.x + (size_t)ti * (128 * 1280);
    const char* wb = a.wqkv + 160 * gj;
    const bool full = lw < 2;
    auto issue_a = [&](int s) LAM {
        LAS char* dst = sm + (s % NS_B) * SLOT_B + lw * 1024;
#pragma unroll
        for (int u = 0; u < 4; ++u) glds16(xb + 128 * s + off[u], dst + 4096 * u);
#pragma unroll
        for (int u = 4; u < 6; ++u) glds16(wb + (size_t)(64 * 3840) * s + off[u], dst + 4096 * u);
    };
    auto issue_b = [&](int s) LAM {
        LAS char* dst = sm + (s % NS_B) * SLOT_B + lw * 1024;
#pragma unroll
        for (int u = 6; u < 12; ++u) if (u < 11 || full) glds16(wb + (size_t)(64 * 3840) * s + off[u], dst + 4096 * u);
    };
    STAMP(a.ws, lw + 4, 1);
    issue_a(0); issue_b(0); issue_a(1); issue_b(1);
    if (full) WAIT_VM(12); else WAIT_VM(11);
    bar();
#pragma unroll 1
    for (int s = 0; s < 8; ++s) {
        if (s > 0) ring_wait(sm + L_FREE, s);
        issue_a(s + 2);
        WAIT_VM(6);
        ring_put(sm + L_FULL + 4 * lw, s + 2);
        issue_b(s + 2);
    }
    WAIT_VM(0);
    ring_put(sm + L_FULL + 4 * lw, 10);
    STAMP(a.ws, lw + 4, 2);
    __builtin_amdgcn_s_setprio(2);
    const unsigned tag = mk_tag(nonce);
    const int tl = lw * 64 + lane;
    if (tl < 128) ((LAS float*)(sm + B_G))[tl] = bfr(1.f - gate_of(a, 128 * ti + tl, tag));
    {
        int o0[2], o1[2];
#pragma unroll
        for (int it = 0; it < 2; ++it) {
            const int g = 2 * (tl + 256 * it < 420 ? tl + 256 * it : 0);
            const int h = g / 210, rem = g - 210 * h, e = rem / 10, d = 2 * (rem - 10 * e);
            const bool kv = rem < 200;
            o0[it] = kv ? B_KVT + ((20 * h + e) * 84 + 20 * h + d) * 4 : B_Z + (20 * h + 2 * (rem - 200)) * 4;
            o1[it] = o0[it] + (kv ? 80 * 84 * 4 : 320);
        }
        asm volatile("" : "+v"(o0[0]), "+v"(o0[1]), "+v"(o1[0]), "+v"(o1[1]));
        const __amdgpu_buffer_rsrc_t rk = rsrc(a.ws + WS_KV, 16 * 8 * 840 * 8);
        word_wait(sm + L_KV, 4u);
        STAMP(a.ws, lw + 4, 3);
        u32x4 pv[2];
#pragma unroll
        for (int it = 0; it < 2; ++it) {
            const f32x4 v = *(LAS f32x4*)(sm + o0[it]) + *(LAS f32x4*)(sm + o1[it]);
            pv[it] = (u32x4){pk2(v[0], v[1]), tag, pk2(v[2], v[3]), tag};
        }
        asm volatile("" : "+v"(pv[0]), "+v"(pv[1]) :: "memory");
        wait_lgkm0();
        if (lane == 0) word_add(sm + L_PUB);
        st16_wt(rk, tl * 16, (ti * 8 + gj) * 6720, pv[0]);
        if (tl + 256 < 420) st16_wt(rk, (tl + 256) * 16, (ti * 8 + gj) * 6720, pv[1]);
        asm volatile("s_nop 1" ::"v"(pv[0]), "v"(pv[1]));
    }
    STAMP(a.ws, lw + 4, 4);
    if (lw == 0) {
        const int cnt = ti < 8 ? 53 : 52, g0 = ti < 8 ? 53 * ti : 424 + 52 * (ti - 8);
        const int g = g0 + (lane < cnt ? lane : 0);
        GAS u64* src = (GAS u64*)(a.ws + WS_KV) + gj * 840 + g;
        unsigned v[16];
#pragma unroll 1
        for (unsigned spins = 0; spins < SPIN_CAP; ++spins) {
            u64 x[16];
#pragma unroll
            for (int i = 0; i < 16; ++i) x[i] = __hip_atomic_load(src + i * (8 * 840), RLX_AGENT);
            bool ok = true;
#pragma unroll
            for (int i = 0; i < 16; ++i) { v[i] = (unsigned)x[i]; ok &= (unsigned)(x[i] >> 32) == tag; }
            if (wave_all(ok)) break;
            __builtin_amdgcn_s_sleep(1);
        }
        float s0 = 0.f, s1 = 0.f;
#pragma unroll
        for (int i = 0; i < 16; ++i) { s0 += bf_lo(v[i]); s1 += bf_hi(v[i]); }
        if (lane < cnt) __hip_atomic_store((GAS u64*)(a.ws + WS_TOT) + gj * 840 + g, granule(tag, pk2(s0, s1)), RLX_AGENT);
        STAMP(a.ws, lw + 4, 5);
    }
    bf16x8 fw[P2_PW][5];
    if (lw == 3) {
        word_wait(sm + L_PUB, 4u);
        p2_prefetch(a, sm, lw, lane, tag, 2, fw);
        const __amdgpu_buffer_rsrc_t rs = rsrc(a.ws + WS_TOT, 8 * 840 * 8);
        u32x4 v[7];
#pragma unroll 1
        for (unsigned spins = 0; spins < SPIN_CAP; ++spins) {
            bool ok = true;
#pragma unroll
            for (int k = 0; k < 7; ++k) v[k] = __builtin_amdgcn_raw_buffer_load_b128(rs, gj * 6720 + (lane + 64 * k < 420 ? lane + 64 * k : 419) * 16, 0, 16);
#pragma unroll
            for (int k = 0; k < 7; ++k) ok &= (v[k][1] == tag) & (v[k][3] == tag);
            if (wave_all(ok)) break;
            __builtin_amdgcn_s_sleep(SLP_TOT);
        }
#pragma unroll
        for (int k = 0; k < 7; ++k) if (lane + 64 * k < 420) ((LAS u32x2*)(sm + B_TOT))[lane + 64 * k] = (u32x2){v[k][0], v[k][2]};
        wait_lgkm0();
        word_set(sm + L_TOT, 1u);
        STAMP(a.ws, lw + 4, 5);
    }
    __builtin_amdgcn_s_setprio(0);
    if (lw != 3) {
        word_wait(sm + L_PUB, 4u);
        p2_prefetch(a, sm, lw, lane, tag, 2, fw);
    }
    p2_main(a, sm, lw, lane, nonce, 2, fw);
}

DEV void b_consumer(const Args& a, LAS char* sm, int ti, int gj, int cw, int lane) {
    const int mh = cw >> 1, nh = cw & 1, c = lane & 15, q = lane >> 4, tid = cw * 64 + lane;
    STAMP(a.ws, cw, 0);
    const unsigned nonce = __hip_atomic_load((GAS unsigned*)(a.ws + WS_NONCE), RLX_AGENT), tag = mk_tag(nonce);
    {
        const __amdgpu_buffer_rsrc_t rk = rsrc(a.ws + WS_KV, 16 * 8 * 840 * 8);
        const u32x4 z = {0u, 0u, 0u, 0u};
        st16_wt(rk, tid * 16, (ti * 8 + gj) * 6720, z);
        if (tid + 256 < 420) st16_wt(rk, (tid + 256) * 16, (ti * 8 + gj) * 6720, z);
        const int cnt = ti < 8 ? 53 : 52, g0 = ti < 8 ? 53 * ti : 424 + 52 * (ti - 8);
        if (tid < cnt) __hip_atomic_store((GAS u64*)(a.ws + WS_TOT) + gj * 840 + g0 + tid, (u64)0, RLX_AGENT);
    }
    __builtin_amdgcn_s_setprio(1);
    f32x4 an[5][4], at[5][2];
#pragma unroll
    for (int t = 0; t < 5; ++t) {
#pragma unroll
        for (int i = 0; i < 4; ++i) an[t][i] = (f32x4){0.f, 0.f, 0.f, 0.f};
        at[t][0] = at[t][1] = (f32x4){0.f, 0.f, 0.f, 0.f};
    }
    int xlo[2], xhi[2], wr[2];
#pragma unroll
    for (int kk = 0; kk < 2; ++kk) {
        const int sw = ((4 * kk + q) ^ (c & 7)) << 4;
        xlo[kk] = (64 * mh + 32 * nh + c) * 128 + sw;
        xhi[kk] = (64 * mh + 32 * (1 - nh) + c) * 128 + sw;
        wr[kk] = 16384 + (8 * q + 4 * (kk ^ (q & 1)) + (c >> 2)) * 160 + 8 * (c & 3);
    }
    bf16x8 xa[2][4], wb[2][5], wt[2][5];
    auto rdp = [&](int buf, LAS char* s0, int kk) LAM {
        LAS char* q0 = s0 + kk * 5120 + wr[0]; LAS char* q1 = s0 + kk * 5120 + wr[1];
        LAS char* a0 = q0 + 96 * nh; LAS char* a1 = q1 + 96 * nh;
        xa[buf][0] = lds_frag(s0 + xlo[kk]);
        wb[buf][0] = lds_tr_frag(a0, a1);
        wb[buf][1] = lds_tr_frag(a0 + 32, a1 + 32);
        xa[buf][1] = lds_frag(s0 + xlo[kk] + 2048);
        wb[buf][2] = lds_tr_frag(q0 + 64 + 10240 * nh, q1 + 64 + 10240 * nh);
        wb[buf][3] = lds_tr_frag(a0 + 10240, a1 + 10240);
        xa[buf][2] = lds_frag(s0 + xhi[kk]);
        wb[buf][4] = lds_tr_frag(a0 + 10272, a1 + 10272);
        wt[buf][0] = lds_tr_frag(q0 + 20480, q1 + 20480);
        xa[buf][3] = lds_frag(s0 + xhi[kk] + 2048);
        wt[buf][1] = lds_tr_frag(q0 + 20512, q1 + 20512);
        wt[buf][2] = lds_tr_frag(q0 + 20544, q1 + 20544);
        wt[buf][3] = lds_tr_frag(q0 + 20576, q1 + 20576);
        wt[buf][4] = lds_tr_frag(q0 + 20608, q1 + 20608);
    };
    auto mm_head = [&](int buf) LAM {
#pragma unroll
        for (int i = 0; i < 4; ++i) an[0][i] = mfma(xa[buf][i], wb[buf][0], an[0][i]);
    };
    auto mm_tail = [&](int buf) LAM {
#pragma unroll
        for (int t = 1; t < 5; ++t)
#pragma unroll
            for (int i = 0; i < 4; ++i) an[t][i] = mfma(xa[buf][i], wb[buf][t], an[t][i]);
#pragma unroll
        for (int t = 0; t < 5; ++t)
#pragma unroll
            for (int i = 0; i < 2; ++i) at[t][i] = mfma(wt[buf][t], xa[buf][i], at[t][i]);
    };
    if (tid < L_WORDS) ((LAS unsigned*)(sm + L_FULL))[tid] = tid < 4 ? 1u : 0u;
#pragma unroll
    for (int t = 0; t < 5; ++t)
        asm volatile("" : "+v"(an[t][0]), "+v"(an[t][1]), "+v"(an[t][2]), "+v"(an[t][3]), "+v"(at[t][0]), "+v"(at[t][1]));
    if (TOUCH_CODE == 1 && cw == 0) touch_code(a.code, sm, lane);
    if (TOUCH_TLB && cw == 1) touch_pages(a, sm, lane);
    if (TOUCH) touch_streams(a, sm, cw, lane);
    STAMP(a.ws, cw, 1);
    lds_bar();
    STAMP(a.ws, cw, 2);
    STAMPC(a.ws, cw, 14);
    rdp(0, sm, 0);
    int slot = 0;
#pragma unroll 1
    for (int s = 0; s < 10; ++s) {
        wait_lgkm0();
        SCHED();
        rdp(1, sm + slot * SLOT_B, 1);
        u32x4 fl = *(volatile LAS u32x4*)(sm + L_FULL);
        mm_head(0); mm_tail(0);
        interleave<30, 25, IL_B>();
        SCHED();
        wait_lgkm0();
        asm volatile("" ::: "memory");
        *(volatile LAS unsigned*)(sm + L_FREE + 4 * cw) = s + 1;
        slot = slot == 2 ? 0 : slot + 1;
        const unsigned need = s < 9 ? s + 2 : 0;
        unsigned landed = __builtin_amdgcn_readfirstlane(umin4(fl));
        LAS char* s0 = sm + slot * SLOT_B;
        asm volatile("" : "+s"(landed), "+v"(s0));
        mm_head(1);
        SCHED();
        if (__builtin_expect(landed < need, 0)) ring_spin(sm + L_FULL, need);
        asm volatile("" ::: "memory");
        SCHED();
        rdp(0, s0, 0);
        mm_tail(1);
        interleave<26, 24, 24>();
        SCHED();
    }
    STAMP(a.ws, cw, 3);
    STAMPC(a.ws, cw, 15);
    bf16x8 fr[5][2];
#pragma unroll
    for (int t = 0; t < 2; ++t)
#pragma unroll
        for (int i = 0; i < 4; ++i)
#pragma unroll
            for (int r = 0; r < 4; ++r) an[t][i][r] = phi(an[t][i][r]);
    if (nh == 0) {
#pragma unroll
        for (int i = 0; i < 4; ++i)
#pragma unroll
            for (int r = 0; r < 4; ++r) an[2][i][r] = phi(an[2][i][r]);
    }
#pragma unroll
    for (int t = 0; t < 5; ++t) { fr[t][0] = pack_pair(an[t][0], an[t][1]); fr[t][1] = pack_pair(an[t][2], an[t][3]); }
    {
        LAS char* xs = sm + B_X1 + cw * 2048 + lane * 16;
        *(LAS bf16x8*)(xs + (nh << 10)) = fr[2][0];
        *(LAS bf16x8*)(xs + ((1 ^ nh) << 10)) = fr[2][1];
    }
    wait_lgkm0();
    word_set(sm + L_X1 + 4 * cw, 1u);
#pragma unroll
    for (int t = 0; t < 5; ++t) asm volatile("" : "+v"(fr[t][0]), "+v"(fr[t][1]));
    STAMP(a.ws, cw, 4);
    word_wait(sm + L_X1 + 4 * (cw ^ 1), 1u);
    ring_spin(sm + L_FREE, 10);
    asm volatile("" ::: "memory");
    {
        LAS char* xr = sm + B_X1 + (cw ^ 1) * 2048 + lane * 16;
        const bf16x8 R0 = *(LAS bf16x8*)(xr + (nh << 10)), R1 = *(LAS bf16x8*)(xr + ((1 ^ nh) << 10));
        const bf16x8 ones = as_frag((u32x4){0x3f803f80u, 0x3f803f80u, 0x3f803f80u, 0x3f803f80u});
        const f32x4 z4 = {0.f, 0.f, 0.f, 0.f};
        LAS float* kvt = (LAS float*)(sm + B_KVT) + mh * (80 * 84) + c * 84 + 4 * q;
        LAS float* zz = (LAS float*)(sm + B_Z) + mh * 80 + 4 * q;
        auto prod = [&](const bf16x8& k0, const bf16x8& k1, const bf16x8& v0, const bf16x8& v1, int ta, int tb) LAM {
            *(LAS f32x4*)(kvt + 16 * tb * 84 + 16 * ta) = mfma(k1, v1, mfma(k0, v0, z4));
        };
        auto zsum = [&](const bf16x8& k0, const bf16x8& k1, int ta) LAM {
            const f32x4 d = mfma(k1, ones, mfma(k0, ones, z4));
            if (c == 0) *(LAS f32x4*)(zz + 16 * ta) = d;
        };
        const int b0 = 3 * nh;
        prod(fr[0][0], fr[0][1], fr[3][0], fr[3][1], b0, b0);
        prod(fr[0][0], fr[0][1], fr[4][0], fr[4][1], b0, b0 + 1);
        prod(fr[1][0], fr[1][1], fr[3][0], fr[3][1], b0 + 1, b0);
        prod(fr[1][0], fr[1][1], fr[4][0], fr[4][1], b0 + 1, b0 + 1);
        zsum(fr[0][0], fr[0][1], b0);
        zsum(fr[1][0], fr[1][1], b0 + 1);
        if (nh == 0) {
            prod(fr[2][0], fr[2][1], fr[4][0], fr[4][1], 2, 1);
            prod(fr[1][0], fr[1][1], R0, R1, 1, 2);
            prod(fr[2][0], fr[2][1], R0, R1, 2, 2);
            zsum(fr[2][0], fr[2][1], 2);
        } else {
            prod(fr[0][0], fr[0][1], fr[2][0], fr[2][1], 3, 2);
            prod(R0, R1, fr[3][0], fr[3][1], 2, 3);
        }
    }
    WAIT_VM(0);
    wait_lgkm0();
    if (lane == 0) word_add(sm + L_KV);
    __builtin_amdgcn_s_setprio(0);
    STAMP(a.ws, cw, 5);
    bf16x8 P[2][3];
#pragma unroll
    for (int i = 0; i < 2; ++i) {
#pragma unroll
        for (int t = 0; t < 5; ++t)
#pragma unroll
            for (int r = 0; r < 4; ++r) at[t][i][r] = phi(at[t][i][r]);
        P[i][0] = pack_pair(at[0][i], at[1][i]);
        P[i][1] = pack_pair(at[2][i], at[3][i]);
        P[i][2] = as_frag((u32x4){pk2(at[4][i][0], at[4][i][1]), pk2(at[4][i][2], at[4][i][3]), 0u, 0u});
    }
    int adr[21];
    {
        int dh[5], db[5], eh[5], eb[5];
#pragma unroll
        for (int k = 0; k < 5; ++k) {
            const int d0 = 16 * k + 4 * q, e0 = 16 * k + c;
            dh[k] = d0 / 20; db[k] = B_TOT + dh[k] * 840 + (d0 - 20 * dh[k]) * 2;
            eh[k] = e0 / 20; eb[k] = (e0 - 20 * eh[k]) * 40;
        }
        auto ad = [&](int k, int E) LAM { return dh[k] == eh[E] ? db[k] + eb[E] : L_ZERO; };
        adr[0] = ad(0, 0); adr[1] = ad(1, 0);
        adr[2] = ad(0, 1); adr[3] = ad(1, 1); adr[4] = ad(2, 1); adr[5] = ad(3, 1);
        adr[6] = ad(1, 2); adr[7] = ad(2, 2); adr[8] = ad(3, 2);
        adr[9] = ad(2, 3); adr[10] = ad(3, 3); adr[11] = ad(4, 3);
        adr[12] = ad(3, 4); adr[13] = ad(4, 4);
#pragma unroll
        for (int k = 0; k < 5; ++k) adr[14 + k] = dh[k] == (c & 3) ? db[k] + 800 : L_ZERO;
        adr[19] = L_ZERO; adr[20] = B_G + 4 * (64 * mh + 16 * (c >> 2) + 8 * nh + (c & 3));
    }
#pragma unroll
    for (int i = 0; i < 2; ++i) asm volatile("" : "+v"(P[i][0]), "+v"(P[i][1]), "+v"(P[i][2]));
    asm volatile("" : "+v"(adr[0]), "+v"(adr[1]), "+v"(adr[2]), "+v"(adr[3]), "+v"(adr[4]), "+v"(adr[5]), "+v"(adr[6]),
                      "+v"(adr[7]), "+v"(adr[8]), "+v"(adr[9]), "+v"(adr[10]), "+v"(adr[11]), "+v"(adr[12]), "+v"(adr[13]));
    asm volatile("" : "+v"(adr[14]), "+v"(adr[15]), "+v"(adr[16]), "+v"(adr[17]), "+v"(adr[18]), "+v"(adr[20]));
    const __amdgpu_buffer_rsrc_t ract = rsrc(a.ws + WS_ACT, 128 * TS);
    u32x4 os[2][2]; u32x2 o8[2];
    STAMP(a.ws, cw, 6);
    word_wait(sm + L_TOT, 1u);
    word_wait(sm + L_PUB, 4u);
    STAMP(a.ws, cw, 7);
    {
        auto h4 = [&](int i) LAM -> u32x2 { return *(LAS u32x2*)(sm + adr[i]); };
        auto fr2 = [&](const u32x2& lo, const u32x2& hi) LAM { return as_frag((u32x4){lo[0], lo[1], hi[0], hi[1]}); };
        const u32x2 zz = {0u, 0u};
        const bf16x8 Z0 = fr2(h4(14), h4(15)), Z1 = fr2(h4(16), h4(17)), Z2 = fr2(h4(18), zz);
        const bf16x8 F0 = fr2(h4(0), h4(1)), F1 = fr2(h4(2), h4(3)), F2 = fr2(h4(4), h4(5));
        const bf16x8 F3 = fr2(zz, h4(6)), F4 = fr2(h4(7), h4(8)), F5 = fr2(h4(9), h4(10));
        const bf16x8 F6 = fr2(h4(11), zz), F7 = fr2(zz, h4(12)), F8 = fr2(h4(13), zz);
        const float ogv[2] = {*(LAS float*)(sm + adr[20]), *(LAS float*)(sm + adr[20] + 16)};
        const f32x4 z4 = {0.f, 0.f, 0.f, 0.f};
        const int uu = gj >> 1, odd = gj & 1;
#pragma unroll
        for (int i = 0; i < 2; ++i) {
            const f32x4 dn = mfma(Z2, P[i][2], mfma(Z1, P[i][1], mfma(Z0, P[i][0], z4)));
            const f32x4 n0 = mfma(F0, P[i][0], z4);
            const f32x4 n1 = mfma(F2, P[i][1], mfma(F1, P[i][0], z4));
            const f32x4 n2 = mfma(F4, P[i][1], mfma(F3, P[i][0], z4));
            const f32x4 n3 = mfma(F6, P[i][2], mfma(F5, P[i][1], z4));
            const f32x4 n4 = mfma(F8, P[i][2], mfma(F7, P[i][1], z4));
            const float og = ogv[i];
            const float r0 = og * __builtin_amdgcn_rcpf(dn[0] + 1e-6f), r1 = og * __builtin_amdgcn_rcpf(dn[1] + 1e-6f);
            const float r2 = og * __builtin_amdgcn_rcpf(dn[2] + 1e-6f), r3 = og * __builtin_amdgcn_rcpf(dn[3] + 1e-6f);
            const float s1 = q < 1 ? r0 : r1, s2 = q < 2 ? r1 : r2, s3 = q < 3 ? r2 : r3;
            os[i][0] = (u32x4){pk2(n0[0] * r0, n0[1] * r0), pk2(n0[2] * r0, n0[3] * r0), pk2(n1[0] * s1, n1[1] * s1), pk2(n1[2] * s1, n1[3] * s1)};
            os[i][1] = (u32x4){pk2(n2[0] * s2, n2[1] * s2), pk2(n2[2] * s2, n2[3] * s2), pk2(n3[0] * s3, n3[1] * s3), pk2(n3[2] * s3, n3[3] * s3)};
            o8[i] = (u32x2){pk2(n4[0] * r3, n4[1] * r3), pk2(n4[2] * r3, n4[3] * r3)};
        }
#pragma unroll
        for (int i = 0; i < 2; ++i) {
            const unsigned ob = (8 * ti + 4 * mh + 2 * nh + i) * TS + (40 + 5 * uu) * 1024;
            st16_wt(ract, lane * 16, ob + (odd ? 3072 : 0), os[i][0]);
            st16_wt(ract, lane * 16, ob + (odd ? 4096 : 1024), os[i][1]);
            st8_wt(ract, lane * 16 + 8 * odd, ob + 2048, o8[i]);
        }
    }
    KEEP(os[0][0]); KEEP(os[0][1]); KEEP(os[1][0]); KEEP(os[1][1]);
    STAMP(a.ws, cw, 8);
    signal_stores(sm, L_DONE, a.ws + WS_FACT + ti * 256 + 128 + gj * 8, tag, lane, 4u);
    STAMP(a.ws, cw, 9);
    p2_final(a, sm, cw, lane, nonce);
}

}

__global__ __launch_bounds__(512, 2) NOPK void gm_fused(const void* x, const void* lre, const void* lim, const void* bre,
                                                   const void* bim, const void* cre, const void* cim, const void* logdt,
                                                   const void* dskip, const void* wqkv, const void* wo, const void* gw,
                                                   void* ws, void* out) {
    __shared__ __attribute__((aligned(1024))) char smem[LDS_BYTES];
    LAS char* sm = (LAS char*)smem;
    u64 pc0;
    asm volatile("s_getpc_b64 %0" : "=s"(pc0));
    const char* code = (const char*)(pc0 & ~127ull);
    if (TOUCH_CODE == 2 && (threadIdx.x >> 6) == 0) touch_code(code, sm, threadIdx.x & 63);
#define MAPMODE 2
    const int b = blockIdx.x, xcd = b & 7, idx = b >> 3;
    const bool roleA = MAPMODE == 1 ? idx < 16 : MAPMODE == 2 ? idx >= 16 : xcd < 4;
    const int ti = MAPMODE != 0 ? 4 * (xcd >> 1) + (idx & 3) : 8 * ((xcd & 3) >> 1) + (idx & 7);
    const int gj = MAPMODE != 0 ? 4 * (xcd & 1) + ((idx >> 2) & 3) : 4 * (xcd & 1) + (idx >> 3);
    const int wave = __builtin_amdgcn_readfirstlane(threadIdx.x >> 6), lane = threadIdx.x & 63;
    const Args a{(const char*)x, (const u16*)lre, (const u16*)lim, (const char*)bre, (const char*)bim, (const char*)cre,
                 (const char*)cim, (const u16*)logdt, (const char*)wqkv, (const char*)wo, (const u16*)gw,
                 (const u16*)dskip, (char*)ws, (float*)out, code};
    STAMPL(ws, 0);
    if (roleA) {
        if (wave < 4) a_consumer(a, sm, ti, gj, wave, lane);
        else a_loader(a, sm, ti, gj, wave - 4, lane);
    } else {
        if (wave < 4) b_consumer(a, sm, ti, gj, wave, lane);
        else b_loader(a, sm, ti, gj, wave - 4, lane);
    }
    STAMPL(ws, 1);
}

extern "C" void kernel_launch(void* const* d_in, const int* in_sizes, int n_in, void* d_out, int out_size, void* d_ws,
                              size_t ws_size, hipStream_t stream) {
    gm_fused<<<256, 512, 0, stream>>>(d_in[0], d_in[1], d_in[2], d_in[3], d_in[4], d_in[5], d_in[6], d_in[7], d_in[8],
                                      d_in[10], d_in[11], d_in[12], d_ws, d_out);
}
```

```cpp
#include <hip/hip_runtime.h>
#include <hip/hip_bf16.h>
#include <stdint.h>

namespace {

typedef __attribute__((ext_vector_type(8))) short bf16x8;
typedef __attribute__((ext_vector_type(4))) short s16x4;
typedef __attribute__((ext_vector_type(4))) float f32x4;
typedef __attribute__((ext_vector_type(2))) float f32x2;
typedef __attribute__((ext_vector_type(2))) __bf16 bf16x2_t;
typedef __attribute__((ext_vector_type(4))) unsigned u32x4;
typedef __attribute__((ext_vector_type(2))) unsigned u32x2;
typedef unsigned long long u64;
typedef unsigned short u16;
#define LAS __attribute__((address_space(3)))
#define GAS __attribute__((address_space(1)))
#if defined(__HIP_DEVICE_COMPILE__)
#define NOPK __attribute__((target("no-packed-fp32-ops")))
#else
#define NOPK
#endif
#define DEV __device__ __forceinline__
#define LAM __attribute__((always_inline))
#define RLX_AGENT __ATOMIC_RELAXED, __HIP_MEMORY_SCOPE_AGENT

constexpr size_t WS_NONCE = 0;
constexpr size_t WS_EA    = 4096;
constexpr size_t WS_KV    = WS_EA + 16 * 8 * 160 * 8;
constexpr size_t WS_TOT   = WS_KV + 16 * 8 * 840 * 8;
constexpr size_t WS_G     = WS_TOT + 8 * 840 * 8;
constexpr size_t WS_FACT  = WS_G + 2048 * 8;
constexpr size_t WS_FW2T  = WS_FACT + 16 * 256;
constexpr int NREP = 16;
constexpr size_t WS_W2T   = (WS_FW2T + NREP * 1024 + 1023) / 1024 * 1024;
constexpr size_t TS = 61440;
constexpr size_t WS_ACT   = WS_W2T + 40 * TS;
constexpr size_t WS_PROF  = 32u << 20;

#define STAMP(ws, wave, i) do {} while (0)
#define STAMPC(ws, wave, i) do {} while (0)
#define TIMED(acc, stmt) do { stmt; } while (0)
#define STAMPV(ws, wave, i, v) do {} while (0)
#define STAMP2(ws, wave, i) do {} while (0)

#define STAMPL(ws, i) do {} while (0)

constexpr int LDS_BYTES = 163840;
constexpr int SLOT_A = 36864, NS_A = 4;
constexpr int A_STG  = 110592;
constexpr int A_TAB  = 147456;
constexpr int A_JTAB = A_TAB + 3840;
constexpr int A_G    = A_JTAB + 2048;
constexpr int A_SEG  = A_G + 512;
constexpr int A_HT = 47104, HT_STRIDE = 272;
static_assert(A_SEG + 5120 <= 159776, "lds");
constexpr int SLOT_B = 47104, NS_B = 3;
constexpr int B_KVT = 0;
constexpr int B_Z   = B_KVT + 2 * 80 * 84 * 4;
constexpr int B_X1  = 141312;
constexpr int B_G   = B_X1 + 8192;
constexpr int B_TOT = B_G + 512;
static_assert(B_TOT + 3360 <= 159776, "lds");

#define SLP_W2T 8
#define SLP_ACT 4
#define SLP_LB 2
#define SLP_TOT 2
#define TOPWAIT 1
#define IL_A2 13
#define IL_B2 24
#define IL_A 16
#define IL_B 27
constexpr unsigned SPIN_CAP = 1u << 18;

DEV unsigned pk2(float a, float b) {
    return __builtin_bit_cast(unsigned, __builtin_convertvector((f32x2){a, b}, bf16x2_t));
}
DEV float bfr(float a) { return __uint_as_float(pk2(a, 0.f) << 16); }
DEV float bf_lo(unsigned u) { return __uint_as_float(u << 16); }
DEV float bf_hi(unsigned u) { return __uint_as_float(u & 0xffff0000u); }
DEV float bf2f(u16 u) { return __uint_as_float((unsigned)u << 16); }
DEV float swp(float x) {
    return __builtin_bit_cast(float, __builtin_amdgcn_mov_dpp(__builtin_bit_cast(int, x), 0xB1, 0xF, 0xF, true));
}
DEV float cmul_add(float a, float bs, float e, float u) { return fmaf(bs, swp(e), fmaf(a, e, u)); }
DEV f32x4 mfma(bf16x8 a, bf16x8 b, f32x4 c) { return __builtin_amdgcn_mfma_f32_16x16x32_bf16(a, b, c, 0, 0, 0); }
DEV bf16x8 as_frag(u32x4 v) { return __builtin_bit_cast(bf16x8, v); }
DEV bf16x8 lds_frag(LAS char* p) { return *(LAS bf16x8*)p; }
DEV bf16x8 lds_tr_frag(LAS char* p0, LAS char* p1) {
    s16x4 lo = __builtin_amdgcn_ds_read_tr16_b64_v4i16((LAS s16x4*)p0);
    s16x4 hi = __builtin_amdgcn_ds_read_tr16_b64_v4i16((LAS s16x4*)p1);
    return __builtin_shufflevector(lo, hi, 0, 1, 2, 3, 4, 5, 6, 7);
}
DEV void glds16_wt(const char* g, unsigned l) {
    unsigned keep;
    asm volatile("s_mov_b32 %0, m0\n\ts_mov_b32 m0, %2\n\ts_nop 0\n\tglobal_load_lds_dwordx4 %1, off sc1\n\ts_mov_b32 m0, %0"
                 : "=&s"(keep) : "v"(g), "s"(l) : "memory");
}
DEV void glds16(const char* g, LAS char* l) {
    __builtin_amdgcn_global_load_lds((const GAS unsigned*)g, (LAS unsigned*)l, 16, 0, 0);
}
DEV void bar() { asm volatile("" ::: "memory"); __builtin_amdgcn_s_barrier(); asm volatile("" ::: "memory"); }
DEV void wait_lgkm0() { __builtin_amdgcn_s_waitcnt(0xC07F); }
DEV void lds_bar() { asm volatile("" ::: "memory"); wait_lgkm0(); bar(); }
constexpr int L_FULL = 159776, L_FREE = L_FULL + 16;
constexpr int L_DONE = L_FULL + 32;
constexpr int L_KV = L_FULL + 36;
constexpr int L_W2T = L_FULL + 40;
constexpr int L_ACTH = L_FULL + 44, L_ACTO = L_FULL + 76;
constexpr int L_X1 = L_FULL + 48, L_ZERO = L_FULL + 64;
constexpr int L_DONE2 = L_FULL + 72;
constexpr int L_WR = L_FULL + 84;
constexpr int L_TOT = L_FULL + 88;
constexpr int L_HS = L_FULL + 96, L_HT = L_FULL + 100;
constexpr int L_PUB = L_FULL + 108;
constexpr int L_WORDS = 28;
DEV unsigned umin4(const u32x4& f) { const unsigned a = f[0] < f[1] ? f[0] : f[1], b = f[2] < f[3] ? f[2] : f[3]; return a < b ? a : b; }
DEV void ring_put(LAS char* p, unsigned v) {
    asm volatile("ds_write_b32 %0, %1" ::"v"((unsigned)(size_t)p), "v"(v) : "memory");
}
DEV void ring_wait(LAS char* p, unsigned need) {
#pragma unroll 1
    for (unsigned spins = 0; spins < SPIN_CAP; ++spins) {
        u32x4 f;
        asm volatile("ds_read_b128 %0, %1\n\ts_waitcnt lgkmcnt(0)" : "=&v"(f) : "v"((unsigned)(size_t)p) : "memory");
        if ((unsigned)__builtin_amdgcn_readfirstlane(umin4(f)) >= need) break;
        __builtin_amdgcn_s_sleep(1);
    }
}
DEV void word_wait(LAS char* p, unsigned need) {
#pragma unroll 1
    for (unsigned spins = 0; spins < SPIN_CAP; ++spins) {
        if ((unsigned)__builtin_amdgcn_readfirstlane(*(volatile LAS unsigned*)p) >= need) break;
        __builtin_amdgcn_s_sleep(1);
    }
    asm volatile("" ::: "memory");
}
DEV void word_set(LAS char* p, unsigned v) { asm volatile("" ::: "memory"); *(volatile LAS unsigned*)p = v; }
DEV unsigned word_add(LAS char* p) {
    return __hip_atomic_fetch_add((LAS unsigned*)p, 1u, __ATOMIC_RELAXED, __HIP_MEMORY_SCOPE_WORKGROUP);
}
DEV void ring_spin(LAS char* p, unsigned need) {
#pragma unroll 1
    for (unsigned spins = 0; spins < SPIN_CAP; ++spins) {
        const u32x4 f = *(volatile LAS u32x4*)p;
        if ((unsigned)__builtin_amdgcn_readfirstlane(umin4(f)) >= need) break;
    }
}
#define WAIT_VM(n) asm volatile("s_waitcnt vmcnt(" #n ")" ::: "memory")
#define SCHED() __builtin_amdgcn_sched_barrier(0)
#define KEEP(x) asm volatile("" ::"v"(x))
template <int NM, int NR, int NS> DEV void interleave() {
#pragma unroll
    for (int i = 0; i < NS; ++i) {
        __builtin_amdgcn_sched_group_barrier(0x8, 1, 0);
        if ((i + 1) * NR / NS != i * NR / NS) __builtin_amdgcn_sched_group_barrier(0x100, 1, 0);
    }
    __builtin_amdgcn_sched_group_barrier(0x8, NM - NS, 0);
}
DEV bool wave_all(bool p) { return __builtin_amdgcn_ballot_w64(p) == __builtin_amdgcn_ballot_w64(true); }
DEV float lane_xor(float v, int m) {
    const int x = __builtin_bit_cast(int, v);
    const int y = m == 1 ? __builtin_amdgcn_mov_dpp(x, 0xB1, 0xF, 0xF, true) : m == 2 ? __builtin_amdgcn_mov_dpp(x, 0x4E, 0xF, 0xF, true)
                                                                              : __builtin_amdgcn_ds_swizzle(x, 0x101F);
    return __builtin_bit_cast(float, y);
}
DEV float fexp(float x) { return __builtin_amdgcn_exp2f(x * 1.44269504088896341f); }
DEV unsigned mk_tag(unsigned n) { return ((n + 1u) * 0x9E3779B1u) | 1u; }
DEV u64 granule(unsigned tag, unsigned v) { return ((u64)tag << 32) | v; }
DEV u64 flag_of(unsigned tag) { return granule(tag, tag * 0x85EBCA6Bu + 0x5BD1E995u); }
DEV __amdgpu_buffer_rsrc_t rsrc(const char* p, unsigned bytes) { return __builtin_amdgcn_make_buffer_rsrc((void*)p, 0, bytes, 0x00020000); }
DEV void st16_wt(const __amdgpu_buffer_rsrc_t& r, unsigned voff, unsigned soff, u32x4 v) { __builtin_amdgcn_raw_buffer_store_b128(v, r, voff + soff, 0, 16); }
DEV void st8_wt(const __amdgpu_buffer_rsrc_t& r, unsigned voff, unsigned soff, u32x2 v) { __builtin_amdgcn_raw_buffer_store_b64(v, r, voff + soff, 0, 16); }
DEV bf16x8 ld16_wt(const __amdgpu_buffer_rsrc_t& r, unsigned voff, unsigned soff) {
    return __builtin_bit_cast(bf16x8, __builtin_amdgcn_raw_buffer_load_b128(r, voff, soff, 16));
}
DEV void signal_stores(LAS char* sm, int word, char* flag, unsigned tag, int lane, unsigned waves, int replicas = 1) {
    asm volatile("s_waitcnt vmcnt(0)" ::: "memory");
    unsigned old = 0;
    if (lane == 0) old = __hip_atomic_fetch_add((LAS unsigned*)(sm + word), 1u, __ATOMIC_RELAXED, __HIP_MEMORY_SCOPE_WORKGROUP);
    old = __builtin_amdgcn_readfirstlane(old);
    if (old == waves - 1u && lane < replicas) __hip_atomic_store((GAS u64*)(flag + lane * 1024), flag_of(tag), RLX_AGENT);
}
DEV float phi(float t) {
    const float e = __builtin_amdgcn_fmed3f(__builtin_amdgcn_exp2f(t * 1.44269504088896341f), 0.f, 1.f);
    return (e + 1e-6f) + __builtin_amdgcn_fmed3f(t, 0.f, __builtin_inff());
}
DEV bf16x8 pack_pair(const f32x4& lo, const f32x4& hi) {
    return as_frag((u32x4){pk2(lo[0], lo[1]), pk2(lo[2], lo[3]), pk2(hi[0], hi[1]), pk2(hi[2], hi[3])});
}

DEV void scan_step(float& u0, float& u1, float& u2, float& u3, float& u4, const float (&e)[5], const float (&a)[5],
                   const float (&b)[5]) {
    asm("v_fmac_f32 %0, %10, %5\n\tv_fmac_f32 %1, %11, %6\n\tv_fmac_f32 %2, %12, %7\n\t"
        "v_fmac_f32 %3, %13, %8\n\tv_fmac_f32 %4, %14, %9\n\t"
        "v_fmac_f32_dpp %0, %5, %15 quad_perm:[1,0,3,2] row_mask:0xf bank_mask:0xf bound_ctrl:1\n\t"
        "v_fmac_f32_dpp %1, %6, %16 quad_perm:[1,0,3,2] row_mask:0xf bank_mask:0xf bound_ctrl:1\n\t"
        "v_fmac_f32_dpp %2, %7, %17 quad_perm:[1,0,3,2] row_mask:0xf bank_mask:0xf bound_ctrl:1\n\t"
        "v_fmac_f32_dpp %3, %8, %18 quad_perm:[1,0,3,2] row_mask:0xf bank_mask:0xf bound_ctrl:1\n\t"
        "v_fmac_f32_dpp %4, %9, %19 quad_perm:[1,0,3,2] row_mask:0xf bank_mask:0xf bound_ctrl:1"
        : "+v"(u0), "+v"(u1), "+v"(u2), "+v"(u3), "+v"(u4)
        : "v"(e[0]), "v"(e[1]), "v"(e[2]), "v"(e[3]), "v"(e[4]), "v"(a[0]), "v"(a[1]), "v"(a[2]), "v"(a[3]), "v"(a[4]),
          "v"(b[0]), "v"(b[1]), "v"(b[2]), "v"(b[3]), "v"(b[4]));
}

DEV void scan_step_nd(float (&t)[5], float u0, float u1, float u2, float u3, float u4, const float (&e)[5],
                      const float (&a)[5], const float (&b)[5]) {
    asm("v_fma_f32 %0, %15, %10, %5\n\tv_fma_f32 %1, %16, %11, %6\n\tv_fma_f32 %2, %17, %12, %7\n\t"
        "v_fma_f32 %3, %18, %13, %8\n\tv_fma_f32 %4, %19, %14, %9\n\t"
        "v_fmac_f32_dpp %0, %10, %20 quad_perm:[1,0,3,2] row_mask:0xf bank_mask:0xf bound_ctrl:1\n\t"
        "v_fmac_f32_dpp %1, %11, %21 quad_perm:[1,0,3,2] row_mask:0xf bank_mask:0xf bound_ctrl:1\n\t"
        "v_fmac_f32_dpp %2, %12, %22 quad_perm:[1,0,3,2] row_mask:0xf bank_mask:0xf bound_ctrl:1\n\t"
        "v_fmac_f32_dpp %3, %13, %23 quad_perm:[1,0,3,2] row_mask:0xf bank_mask:0xf bound_ctrl:1\n\t"
        "v_fmac_f32_dpp %4, %14, %24 quad_perm:[1,0,3,2] row_mask:0xf bank_mask:0xf bound_ctrl:1"
        : "=&v"(t[0]), "=&v"(t[1]), "=&v"(t[2]), "=&v"(t[3]), "=&v"(t[4])
        : "v"(u0), "v"(u1), "v"(u2), "v"(u3), "v"(u4), "v"(e[0]), "v"(e[1]), "v"(e[2]), "v"(e[3]), "v"(e[4]),
          "v"(a[0]), "v"(a[1]), "v"(a[2]), "v"(a[3]), "v"(a[4]), "v"(b[0]), "v"(b[1]), "v"(b[2]), "v"(b[3]), "v"(b[4]));
}

struct Args {
    const char* x; const u16* lre; const u16* lim; const char* bre; const char* bim; const char* cre; const char* cim;
    const u16* logdt; const char* wqkv; const char* wo; const u16* gw; const u16* dskip; char* ws; float* out;
};

DEV void channel_consts(u16 lre, u16 lim, u16 logdt, float& ar, float& ai, float& cr, float& ci) {
    const float lr = bf2f(lre), li = bf2f(lim);
    const float dt = bfr(fexp(bf2f(logdt)));
    const float zr = dt * lr, zi = dt * li, ex = fexp(zr);
    const float rv = zi * 0.15915494309189535f, rr = rv - __builtin_rintf(rv);
    ar = ex * __builtin_amdgcn_cosf(rr); ai = ex * __builtin_amdgcn_sinf(rr);
    const float nr = ar - 1.f, ni = ai, dn = lr * lr + li * li;
    cr = (nr * lr + ni * li) / dn; ci = (ni * lr - nr * li) / dn;
}
DEV float dot2(unsigned x, unsigned y, float s) {
    return __builtin_amdgcn_fdot2_f32_bf16(__builtin_bit_cast(bf16x2_t, x), __builtin_bit_cast(bf16x2_t, y), s, false);
}

constexpr int P2_RED = 0;
constexpr int P2_SK = 86016;
#define OUT_AUX 16
DEV int p2_tok(int tt, int id) { const int r = id / 20; return 64 * tt + 16 * ((r & 15) >> 2) + 4 * (r >> 4) + (r & 3); }
DEV int p2_col(int cg, int id) { return 80 * cg + 4 * (id - 20 * (id / 20)); }
template <int N> DEV void p2_finish(const Args& a, LAS char* sm, int tt, int cg, int id0, f32x4 (&v)[N]) {
#pragma unroll
    for (int i = 0; i < N; ++i) {
        const int id = id0 + 256 * i, r = id / 20, c4 = id - 20 * r;
        LAS float* p = (LAS float*)(sm + P2_RED) + r * 84 + 4 * c4;
        v[i] += (*(LAS f32x4*)p + *(LAS f32x4*)(p + 64 * 84)) + (*(LAS f32x4*)(p + 128 * 84) + *(LAS f32x4*)(p + 192 * 84));
    }
#pragma unroll
    for (int i = 0; i < N; ++i) asm volatile("" : "+v"(v[i]));
    const __amdgpu_buffer_rsrc_t ro = rsrc((const char*)a.out, 2048 * 640 * 4);
#pragma unroll
    for (int i = 0; i < N; ++i) {
        const int id = id0 + 256 * i;
        __builtin_amdgcn_raw_buffer_store_b128(__builtin_bit_cast(u32x4, v[i]), ro, (p2_tok(tt, id) * 640 + p2_col(cg, id)) * 4, 0, OUT_AUX);
    }
}
constexpr int P2_PW = 4, P2_PA = 5;
DEV bool flags_up(const u32x4& v, u64 fl) {
    const unsigned f0 = (unsigned)fl, f1 = (unsigned)(fl >> 32);
    return wave_all((v[0] == f0) & (v[1] == f1) & (v[2] == f0) & (v[3] == f1));
}
DEV void wait_flags8(const Args& a, int ti, int part, u64 fl) {
    GAS u64* fp = (GAS u64*)(a.ws + WS_FACT) + ti * 32 + part * 16 + (threadIdx.x & 7);
#pragma unroll 1
    for (unsigned spins = 0; spins < SPIN_CAP; ++spins) {
        if (wave_all(__hip_atomic_load(fp, RLX_AGENT) == fl)) break;
        __builtin_amdgcn_s_sleep(SLP_ACT);
    }
}
DEV unsigned p2_koff(int lw, int st) { return (st < 10 ? 10 * lw + st : 30 + 5 * lw + st) * 1024; }
DEV int p2_unit(int lw, int i) { return 5120 * (7 * lw + i); }
DEV void p2_prefetch(const Args& a, LAS char* sm, int lw, int lane, unsigned tag, int poller, bf16x8 (&fw)[P2_PW][5]) {
    const int b = blockIdx.x, xcd = b & 7, idx = b >> 3;
    const int cg = (xcd & 1) * 4 + (idx >> 3);
    if (lw == poller) {
        const __amdgpu_buffer_rsrc_t rf = rsrc(a.ws + WS_FW2T + (xcd * 2 + (b >> 7)) * 1024, 1024);
#pragma unroll 1
        for (unsigned spins = 0; spins < SPIN_CAP; ++spins) {
            if (flags_up(__builtin_amdgcn_raw_buffer_load_b128(rf, lane * 16, 0, 16), flag_of(tag))) break;
            __builtin_amdgcn_s_sleep(SLP_W2T);
        }
        word_set(sm + L_W2T, 1u);
    } else word_wait(sm + L_W2T, 1u);
    STAMP2(a.ws, lw + 4, 0);
    const unsigned wbase = 5 * cg * TS, vo = lane * 16;
    const char* src = a.ws + WS_W2T + wbase + vo;
#pragma unroll
    for (int i = 0; i < 7; ++i)
#pragma unroll
        for (int n = 0; n < 5; ++n)
            glds16_wt(src + (size_t)n * TS + p2_koff(lw, 4 + i), (unsigned)(size_t)(sm + p2_unit(lw, i) + n * 1024));
    const __amdgpu_buffer_rsrc_t rw = rsrc(a.ws + WS_W2T, 40 * TS);
#pragma unroll
    for (int st = 0; st < P2_PW; ++st)
#pragma unroll
        for (int n = 0; n < 5; ++n) fw[st][n] = ld16_wt(rw, vo, wbase + n * TS + p2_koff(lw, st));
}
DEV void p2_main(const Args& a, LAS char* sm, int lw, int lane, unsigned nonce, int poller, bf16x8 (&fw)[P2_PW][5]) {
    const int b = blockIdx.x, xcd = b & 7, idx = b >> 3;
    const int tt = (xcd >> 1) * 8 + (idx & 7), cg = (xcd & 1) * 4 + (idx >> 3);
    const int c = lane & 15, q = lane >> 4;
    const unsigned tag = mk_tag(nonce);
    const u64 fl = flag_of(tag);
    constexpr int PA = P2_PA;
    const __amdgpu_buffer_rsrc_t rw = rsrc(a.ws + WS_W2T, 40 * TS), ra = rsrc(a.ws + WS_ACT, 128 * TS);
    const unsigned wbase = 5 * cg * TS, abase = 4 * tt * TS, vo = lane * 16;
    bf16x8 fa[PA][4];
    auto ldw = [&](int st, int sl) LAM {
#pragma unroll
        for (int n = 0; n < 5; ++n) fw[sl][n] = ld16_wt(rw, vo, wbase + n * TS + p2_koff(lw, st));
    };
    auto lda = [&](int st) LAM {
#pragma unroll
        for (int t = 0; t < 4; ++t) fa[st % PA][t] = ld16_wt(ra, vo, abase + t * TS + p2_koff(lw, st));
    };
    WAIT_VM(0);
    if (lw == poller) { wait_flags8(a, tt >> 1, 0, fl); word_set(sm + L_ACTH, 1u); }
    else word_wait(sm + L_ACTH, 1u);
    STAMP2(a.ws, lw + 4, 1);
#pragma unroll
    for (int st = 0; st < 3; ++st) lda(st);
    f32x4 acc[5][4];
    const f32x4 z4 = {0.f, 0.f, 0.f, 0.f};
    auto slot = [&](int st) LAM { return st < 4 ? st : st <= 10 ? (st & 1) : st == 11 ? 2 : st == 12 ? 3 : st == 13 ? 1 : 0; };
#pragma unroll
    for (int st = 0; st < 15; ++st) {
        if (st <= 4) lda(st + 3);
        if (st == 6) word_wait(sm + L_ACTO, 1u);
        if (st >= 4 && st + 4 < 15) lda(st + 4);
        SCHED();
        if (st >= 3 && st < 10) {
#pragma unroll
            for (int n = 0; n < 5; ++n) fw[slot(st + 1)][n] = lds_frag(sm + p2_unit(lw, st - 3) + vo + n * 1024);
        }
#pragma unroll
        for (int n = 0; n < 5; ++n)
#pragma unroll
            for (int t = 0; t < 4; ++t) acc[n][t] = mfma(fw[slot(st)][n], fa[st % PA][t], st == 0 ? z4 : acc[n][t]);
        SCHED();
        if (st == 7) ldw(11, slot(11));
        if (st == 9) { wait_lgkm0(); if (lane == 0) word_add(sm + L_WR); ldw(12, slot(12)); }
        if (st == 10) ldw(13, slot(13));
        if (st == 11) ldw(14, slot(14));
    }
    STAMP2(a.ws, lw + 4, 2);
    word_wait(sm + L_WR, 4u);
    word_wait(sm + L_DONE, 4u);
    LAS float* rw_ = (LAS float*)(sm + P2_RED) + lw * (64 * 84);
#pragma unroll
    for (int t = 0; t < 4; ++t)
#pragma unroll
        for (int n = 0; n < 5; ++n) *(LAS f32x4*)(rw_ + (16 * t + c) * 84 + 16 * n + 4 * q) = acc[n][t];
    lds_bar();
    STAMP2(a.ws, lw + 4, 3);
    f32x4 sk[2];
#pragma unroll
    for (int i = 0; i < 2; ++i) sk[i] = *(LAS f32x4*)(sm + P2_SK + (256 * i + 64 * lw + lane) * 16);
    p2_finish<2>(a, sm, tt, cg, 768 + 64 * lw + lane, sk);
    STAMP2(a.ws, lw + 4, 6);
}

DEV void p2_final(const Args& a, LAS char* sm, int cw, int lane, unsigned nonce) {
    const unsigned tag = mk_tag(nonce);
    const int b = blockIdx.x, xcd = b & 7, idx = b >> 3;
    const int tt = (xcd >> 1) * 8 + (idx & 7), cg = (xcd & 1) * 4 + (idx >> 3);
    const int tid = cw * 64 + lane;
    u32x2 xv[5], dv[5]; float gv[5];
    int tok[5], n0[5];
#pragma unroll
    for (int i = 0; i < 5; ++i) {
        tok[i] = p2_tok(tt, tid + 256 * i); n0[i] = p2_col(cg, tid + 256 * i);
        xv[i] = *(const u32x2*)((const u16*)a.x + tok[i] * 640 + n0[i]);
        dv[i] = *(const u32x2*)(a.dskip + n0[i]);
    }
#pragma unroll 1
    for (unsigned spins = 0; spins < SPIN_CAP; ++spins) {
        u64 v[5];
#pragma unroll
        for (int i = 0; i < 5; ++i) v[i] = __hip_atomic_load((GAS u64*)(a.ws + WS_G) + tok[i], RLX_AGENT);
        bool ok = true;
#pragma unroll
        for (int i = 0; i < 5; ++i) { gv[i] = __uint_as_float((unsigned)v[i]); ok &= (unsigned)(v[i] >> 32) == tag; }
        if (wave_all(ok)) break;
        __builtin_amdgcn_s_sleep(2);
    }
    f32x4 sk[5];
#pragma unroll
    for (int i = 0; i < 5; ++i) {
        const unsigned p01 = pk2(bf_lo(xv[i][0]) * bf_lo(dv[i][0]), bf_hi(xv[i][0]) * bf_hi(dv[i][0]));
        const unsigned p23 = pk2(bf_lo(xv[i][1]) * bf_lo(dv[i][1]), bf_hi(xv[i][1]) * bf_hi(dv[i][1]));
        sk[i] = (f32x4){gv[i] * bf_lo(p01), gv[i] * bf_hi(p01), gv[i] * bf_lo(p23), gv[i] * bf_hi(p23)};
    }
    asm volatile("" : "+v"(sk[0]), "+v"(sk[1]), "+v"(sk[2]), "+v"(sk[3]), "+v"(sk[4]));
    if (cw == 0) {
        wait_flags8(a, tt >> 1, 1, flag_of(tag));
        word_set(sm + L_ACTO, 1u);
        if (tt == 31 && cg == 0) {
            word_wait(sm + L_ACTH, 1u);
            if (lane == 0) __hip_atomic_store((GAS unsigned*)(a.ws + WS_NONCE), nonce + 1u, RLX_AGENT);
        }
    }
    word_wait(sm + L_WR, 4u);
    word_wait(sm + L_DONE, 4u);
#pragma unroll
    for (int i = 0; i < 2; ++i) *(LAS f32x4*)(sm + P2_SK + (256 * i + tid) * 16) = sk[3 + i];
    STAMP2(a.ws, cw, 0);
    lds_bar();
    STAMP2(a.ws, cw, 2);
    f32x4 v[3] = {sk[0], sk[1], sk[2]};
    p2_finish<3>(a, sm, tt, cg, tid, v);
    STAMP2(a.ws, cw, 3);
}

DEV void job_issue(const Args& a, LAS char* stg, int jb, int lane) {
    if (jb < 400) {
        const int cb = jb / 40, nt = jb - 40 * cb;
        const unsigned o = (16 * nt + (lane >> 3)) * 1280 + cb * 128 + (lane & 7) * 16;
        glds16(a.cre + o, stg); glds16(a.cre + o + 8 * 1280, stg + 1024);
        glds16(a.cim + o, stg + 2048); glds16(a.cim + o + 8 * 1280, stg + 3072);
    } else if (jb < 500) {
        const int jo = jb - 400, s5 = jo / 5, n8 = jo - 5 * s5, uu = s5 / 5, s = s5 - 5 * uu;
        const int lo = 160 * uu + (s == 0 ? 0 : s == 1 ? 32 : s == 2 ? 64 : s == 3 ? 80 : 112);
        const int hi = 160 * uu + (s == 0 ? 16 : s == 1 ? 48 : s == 2 ? 144 : s == 3 ? 96 : 128);
        const unsigned o = (lane >> 4) * 1280 + n8 * 256 + (lane & 15) * 16;
#pragma unroll
        for (int k = 0; k < 4; ++k) {
            glds16(a.wo + (lo + 4 * k) * 1280 + o, stg + 1024 * k);
            glds16(a.wo + (hi + 4 * k) * 1280 + o, stg + 4096 + 1024 * k);
        }
    }
}
DEV u32x2 lds_rd8(LAS char* p) { u32x2 v; asm volatile("ds_read_b64 %0, %1" : "=v"(v) : "v"((unsigned)(size_t)p) : "memory"); return v; }
DEV f32x4 lds_rd16(LAS char* p) { f32x4 v; asm volatile("ds_read_b128 %0, %1" : "=v"(v) : "v"((unsigned)(size_t)p) : "memory"); return v; }
DEV u32x2 lds_rdtr(LAS char* p) { u32x2 v; asm volatile("ds_read_b64_tr_b16 %0, %1" : "=v"(v) : "v"((unsigned)(size_t)p) : "memory"); return v; }
DEV void job_finish(const Args& a, LAS char* stg, LAS char* tab, int jb, int lane) {
    const int c = lane & 15, q = lane >> 4;
    const __amdgpu_buffer_rsrc_t rw = rsrc(a.ws + WS_W2T, 40 * TS);
    if (jb < 400) {
        const int cb = jb / 40, nt = jb - 40 * cb;
        u32x2 re[4], im[4]; f32x4 c01[4], c23[4];
#pragma unroll
        for (int k = 0; k < 4; ++k) {
            re[k] = lds_rd8(stg + c * 128 + 32 * k + 8 * q); im[k] = lds_rd8(stg + 2048 + c * 128 + 32 * k + 8 * q);
            c01[k] = lds_rd16(tab + (16 * k + 4 * q) * 8); c23[k] = lds_rd16(tab + (16 * k + 4 * q) * 8 + 16);
        }
        asm volatile("s_waitcnt lgkmcnt(0)" : "+v"(re[0]), "+v"(re[1]), "+v"(re[2]), "+v"(re[3]), "+v"(im[0]), "+v"(im[1]), "+v"(im[2]), "+v"(im[3]),
                     "+v"(c01[0]), "+v"(c01[1]), "+v"(c01[2]), "+v"(c01[3]), "+v"(c23[0]), "+v"(c23[1]), "+v"(c23[2]), "+v"(c23[3]) :: "memory");
        u32x4 w[4];
#pragma unroll
        for (int k = 0; k < 4; ++k) {
            const float r0 = bf_lo(re[k][0]), r1 = bf_hi(re[k][0]), r2 = bf_lo(re[k][1]), r3 = bf_hi(re[k][1]);
            const float i0 = bf_lo(im[k][0]), i1 = bf_hi(im[k][0]), i2 = bf_lo(im[k][1]), i3 = bf_hi(im[k][1]);
            w[k] = (u32x4){pk2(r0 * c01[k][0] - i0 * c01[k][1], -(r0 * c01[k][1] + i0 * c01[k][0])),
                           pk2(r1 * c01[k][2] - i1 * c01[k][3], -(r1 * c01[k][3] + i1 * c01[k][2])),
                           pk2(r2 * c23[k][0] - i2 * c23[k][1], -(r2 * c23[k][1] + i2 * c23[k][0])),
                           pk2(r3 * c23[k][2] - i3 * c23[k][3], -(r3 * c23[k][3] + i3 * c23[k][2]))};
        }
#pragma unroll
        for (int k = 0; k < 4; ++k) st16_wt(rw, lane * 16, nt * TS + (4 * cb + k) * 1024, w[k]);
    } else if (jb < 500) {
        const int jo = jb - 400, s5 = jo / 5, n8 = jo - 5 * s5;
        u32x2 lo[8], hi[8];
#pragma unroll
        for (int k = 0; k < 8; ++k) {
            LAS char* b = stg + (4 * q + (c >> 2)) * 256 + 32 * k + 8 * (c & 3);
            lo[k] = lds_rdtr(b); hi[k] = lds_rdtr(b + 4096);
        }
        asm volatile("s_waitcnt lgkmcnt(0)" : "+v"(lo[0]), "+v"(lo[1]), "+v"(lo[2]), "+v"(lo[3]), "+v"(lo[4]), "+v"(lo[5]), "+v"(lo[6]), "+v"(lo[7]),
                     "+v"(hi[0]), "+v"(hi[1]), "+v"(hi[2]), "+v"(hi[3]), "+v"(hi[4]), "+v"(hi[5]), "+v"(hi[6]), "+v"(hi[7]) :: "memory");
#pragma unroll
        for (int k = 0; k < 8; ++k)
            st16_wt(rw, lane * 16, (8 * n8 + k) * TS + (40 + s5) * 1024, (u32x4){lo[k][0], lo[k][1], hi[k][0], hi[k][1]});
    }
}

#define TOUCH 1
#define TOUCH_S0 6
constexpr int L_JUNK = 160000;
DEV void touch(const char* p, LAS char* sm) {
    unsigned keep;
    asm volatile("s_mov_b32 %0, m0\n\ts_mov_b32 m0, %2\n\ts_nop 0\n\tglobal_load_lds_dword %1, off\n\ts_mov_b32 m0, %0"
                 : "=&s"(keep) : "v"(p), "s"((unsigned)(size_t)(sm + L_JUNK)) : "memory");
}
#define TOUCH_CODE 1
constexpr int CODE_LINES = 470;
DEV void touch_code(const char* code, LAS char* sm, int lane) {
    const int l = (blockIdx.x >> 3) * 15 + lane;
    if (lane < 15 && l < CODE_LINES) touch(code + l * 128, sm);
}
DEV void touch_streams(const Args& a, LAS char* sm, int cw, int lane) {
    const int b = blockIdx.x, xcd = b & 7, tq = xcd >> 1, gh = xcd & 1;
#pragma unroll
    for (int r = 0; r < 2; ++r) {
        const int e = (b >> 3) * 4 + cw + 128 * r, s = TOUCH_S0 + e / 33, k = e - 33 * (e / 33);
        if (s < 10) {
            const char* p;
            if (k < 8) p = a.x + (size_t)((4 * tq + (k >> 1)) * 128 + 64 * (k & 1) + lane) * 1280 + 128 * s;
            else if (k < 18) {
                const int li = 64 * (k - 8) + lane, g = li / 160, rem = li - 160 * g;
                p = (rem < 80 ? a.bre : a.bim) + (size_t)(80 * (4 * gh + g) + (rem < 80 ? rem : rem - 80)) * 1280 + 128 * s;
            } else {
                const int li = 64 * (k - 18) + lane, kr = li / 15, rem = li - 15 * kr, sec = rem / 5;
                p = a.wqkv + (size_t)(64 * s + kr) * 3840 + sec * 1280 + 640 * gh + 128 * (rem - 5 * sec);
            }
            touch(p, sm);
        }
    }
}
DEV float gate_of(const Args& a, int tok, unsigned tag) {
    u64 v = 0;
#pragma unroll 1
    for (unsigned spins = 0; spins < SPIN_CAP; ++spins) {
        v = __hip_atomic_load((GAS u64*)(a.ws + WS_G) + tok, RLX_AGENT);
        if (wave_all((unsigned)(v >> 32) == tag)) break;
        __builtin_amdgcn_s_sleep(1);
    }
    return __uint_as_float((unsigned)v);
}
DEV int x_token(int R) { const int rho = R & 15; return (R & 64) + 16 * (rho >> 2) + 4 * ((R >> 4) & 3) + (rho & 3); }

DEV void a_loader(const Args& a, LAS char* sm, int ti, int gj, int lw, int lane) {
    STAMP(a.ws, lw + 4, 0);
    const unsigned nonce = __hip_atomic_load((GAS unsigned*)(a.ws + WS_NONCE), RLX_AGENT);
    unsigned off[9];
    {
        const int pos = lane & 7, rl = lane >> 3, im = lw & 1;
#pragma unroll
        for (int u = 0; u < 4; ++u) {
            const int R = 8 * (lw + 4 * u) + rl;
            off[u] = x_token(R) * 1280 + ((pos ^ (R & 7)) << 4);
        }
#pragma unroll
        for (int u = 4; u < 9; ++u) off[u] = (8 * ((lw + 4 * (u - 4)) >> 1) + rl) * 1280 + ((pos ^ rl ^ (im << 2)) << 4);
    }
    const char* xb = a.x + (size_t)ti * (128 * 1280);
    const char* bb = ((lw & 1) ? a.bim : a.bre) + (size_t)gj * (80 * 1280);
    auto issue_a = [&](int s) LAM {
        LAS char* dst = sm + (s % NS_A) * SLOT_A + lw * 1024;
#pragma unroll
        for (int u = 0; u < 4; ++u) glds16(xb + 128 * s + off[u], dst + 4096 * u);
        glds16(bb + 128 * s + off[4], dst + 4096 * 4);
    };
    auto issue_b = [&](int s) LAM {
        LAS char* dst = sm + (s % NS_A) * SLOT_A + lw * 1024;
#pragma unroll
        for (int u = 5; u < 9; ++u) glds16(bb + 128 * s + off[u], dst + 4096 * u);
    };
    STAMP(a.ws, lw + 4, 1);
    issue_a(0); issue_b(0); issue_a(1); issue_b(1); issue_a(2); issue_b(2);
    WAIT_VM(18);
    bar();
#pragma unroll 1
    for (int s = 0; s < 7; ++s) {
        if (s > 0) ring_wait(sm + L_FREE, s);
        issue_a(s + 3);
        WAIT_VM(14);
        ring_put(sm + L_FULL + 4 * lw, s + 2);
        issue_b(s + 3);
    }
    WAIT_VM(9);
    ring_put(sm + L_FULL + 4 * lw, 9);
    WAIT_VM(0);
    ring_put(sm + L_FULL + 4 * lw, 10);
    STAMP(a.ws, lw + 4, 2);
    const unsigned tag = mk_tag(nonce);
    const int tl = lw * 64 + lane;
    const int jb = (ti * 8 + gj) * 4 + lw;
    ring_wait(sm + L_FREE, 8);
    job_issue(a, sm + A_STG + lw * 8192, jb, lane);
    if (tl < 128) ((LAS float*)(sm + A_G))[tl] = gate_of(a, 128 * ti + tl, tag);
    WAIT_VM(0);
    job_finish(a, sm + A_STG + lw * 8192, sm + A_JTAB + lw * 512, jb, lane);
    STAMP(a.ws, lw + 4, 9);
    __builtin_amdgcn_s_setprio(2);
    lds_bar();
    STAMP(a.ws, lw + 4, 3);
    if (lw < 3) {
        const int col = tl < 160 ? tl : 159;
        LAS float* tab = (LAS float*)(sm + A_TAB);
        GAS u64* ea = (GAS u64*)(a.ws + WS_EA) + gj * 160;
        const float a16 = tab[2 * 160 + col], b16 = tab[3 * 160 + col], a128 = tab[4 * 160 + col], b128 = tab[5 * 160 + col];
        LAS float* sg = (LAS float*)(sm + A_SEG);
        float pre[8], P = 0.f;
#pragma unroll
        for (int s = 0; s < 8; ++s) { pre[s] = P; P = cmul_add(a16, b16, P, sg[s * 160 + col]); }
        if (tl < 160) __hip_atomic_store(ea + ti * 1280 + col, granule(tag, __float_as_uint(P)), RLX_AGENT);
        STAMP(a.ws, lw + 4, 4);
        signal_stores(sm, L_DONE2, a.ws + WS_FW2T + (ti * 8 + gj) * 8, tag, lane, 4u, NREP);
        float H = 0.f;
        if (ti > 0) {
            float ev[15];
#pragma unroll 1
            for (unsigned spins = 0; spins < SPIN_CAP; ++spins) {
                u64 v[15];
#pragma unroll
                for (int i = 0; i < 15; ++i) v[i] = __hip_atomic_load(ea + (i < ti ? i : 0) * 1280 + col, RLX_AGENT);
                bool ok = true;
#pragma unroll
                for (int i = 0; i < 15; ++i) { ev[i] = __uint_as_float((unsigned)v[i]); ok &= (unsigned)(v[i] >> 32) == tag; }
                if (wave_all(ok)) break;
                __builtin_amdgcn_s_sleep(SLP_LB);
            }
#pragma unroll
            for (int i = 0; i < 15; ++i) if (i < ti) H = cmul_add(a128, b128, H, ev[i]);
        }
        STAMP(a.ws, lw + 4, 6);
#pragma unroll
        for (int s = 0; s < 8; ++s) { if (tl < 160) sg[s * 160 + col] = H + pre[s]; H = cmul_add(a16, b16, H, 0.f); }
        wait_lgkm0();
        if (lane == 0) word_add(sm + L_HS);
    } else signal_stores(sm, L_DONE2, a.ws + WS_FW2T + (ti * 8 + gj) * 8, tag, lane, 4u, NREP);
    __builtin_amdgcn_s_setprio(0);
    STAMP(a.ws, lw + 4, 8);
    word_wait(sm + L_DONE, 4u);
    bf16x8 fw[P2_PW][5];
    p2_prefetch(a, sm, lw, lane, tag, 3, fw);
    p2_main(a, sm, lw, lane, nonce, 3, fw);
}

DEV void a_pass1(f32x4 (&acc)[4][5], const float (&ca)[5], const float (&cb)[5], LAS float* es) {
    float e[5];
#pragma unroll
    for (int n = 0; n < 5; ++n) e[n] = acc[0][n][0];
#pragma unroll
    for (int t = 1; t < 16; ++t) {
        const int i = t >> 2, r = t & 3;
        float y[5];
        scan_step_nd(y, acc[i][0][r], acc[i][1][r], acc[i][2][r], acc[i][3][r], acc[i][4][r], e, ca, cb);
#pragma unroll
        for (int n = 0; n < 5; ++n) e[n] = y[n];
    }
#pragma unroll
    for (int n = 0; n < 5; ++n) es[16 * n] = e[n];
}
DEV void a_pass2(f32x4 (&acc)[4][5], const float (&ca)[5], const float (&cb)[5], LAS float* hs, LAS float* gt, LAS char* ht,
                 LAS char* hr, const __amdgpu_buffer_rsrc_t& ract, unsigned vo, unsigned ob) {
    float e[5];
#pragma unroll
    for (int n = 0; n < 5; ++n) e[n] = hs[16 * n];
    asm volatile("s_nop 1" : "+v"(e[0]), "+v"(e[1]), "+v"(e[2]), "+v"(e[3]), "+v"(e[4]));
    bf16x8 hf[4][2];
    auto rd = [&](int m) LAM {
#pragma unroll
        for (int k = 0; k < 2; ++k) {
            LAS char* b = hr + 32 * k * HT_STRIDE + 32 * m;
            hf[m][k] = lds_tr_frag(b, b + 4 * HT_STRIDE);
        }
    };
    auto st = [&](int m) LAM {
#pragma unroll
        for (int k = 0; k < 2; ++k) st16_wt(ract, vo, ob + m * TS + k * 1024, __builtin_bit_cast(u32x4, hf[m][k]));
    };
#pragma unroll
    for (int m = 0; m < 4; ++m) {
        const f32x4 g = *(LAS f32x4*)(gt + 4 * m);
#pragma unroll
        for (int r = 0; r < 4; ++r) {
            float u0 = acc[m][0][r], u1 = acc[m][1][r], u2 = acc[m][2][r], u3 = acc[m][3][r], u4 = acc[m][4][r];
            scan_step(u0, u1, u2, u3, u4, e, ca, cb);
            e[0] = u0; e[1] = u1; e[2] = u2; e[3] = u3; e[4] = u4;
            acc[m][0][r] = u0 * g[r]; acc[m][1][r] = u1 * g[r]; acc[m][2][r] = u2 * g[r]; acc[m][3][r] = u3 * g[r]; acc[m][4][r] = u4 * g[r];
        }
        SCHED();
        if (m >= 2) st(m - 2);
#pragma unroll
        for (int n = 0; n < 5; ++n)
            *(LAS u32x2*)(ht + 16 * n * HT_STRIDE + 32 * m) = (u32x2){pk2(acc[m][n][0], acc[m][n][1]), pk2(acc[m][n][2], acc[m][n][3])};
        rd(m);
        SCHED();
    }
    st(2); st(3);
#pragma unroll
    for (int m = 0; m < 4; ++m) { KEEP(hf[m][0]); KEEP(hf[m][1]); }
}

DEV void a_consumer(const Args& a, LAS char* sm, int ti, int gj, int cw, int lane) {
    const int mh = cw >> 1, nh = cw & 1, c = lane & 15, q = lane >> 4, tid = cw * 64 + lane;
    STAMP(a.ws, cw, 0);
    const unsigned nonce = __hip_atomic_load((GAS unsigned*)(a.ws + WS_NONCE), RLX_AGENT);
    const int jb = (ti * 8 + gj) * 4 + cw, pj = (jb < 400 ? 64 * (jb / 40) : 0) + lane;
    const u16 jlre = a.lre[pj], jlim = a.lim[pj], jldt = a.logdt[pj];
    const int gtok = 128 * ti + 16 * gj + 8 * (cw & 1) + (lane >> 3), pch = 80 * gj + (tid < 80 ? tid : 0);
    u32x4 xv[10], gv[10];
    u16 rlre = 0, rlim = 0, rldt = 0;
    if (cw >= 2) {
        const u32x4* xr = (const u32x4*)(a.x + (size_t)gtok * 1280) + (lane & 7);
        const u32x4* gr = (const u32x4*)a.gw + (lane & 7);
#pragma unroll
        for (int i = 0; i < 10; ++i) { xv[i] = xr[8 * i]; gv[i] = gr[8 * i]; }
    } else { rlre = a.lre[pch]; rlim = a.lim[pch]; rldt = a.logdt[pch]; }
    __builtin_amdgcn_s_setprio(1);
    f32x4 acc[4][5];
#pragma unroll
    for (int n = 0; n < 5; ++n)
#pragma unroll
        for (int m = 0; m < 4; ++m) acc[m][n] = (f32x4){0.f, 0.f, 0.f, 0.f};
    int xo[2], wa[2];
#pragma unroll
    for (int kk = 0; kk < 2; ++kk) {
        xo[kk] = (64 * mh + c) * 128 + (((4 * kk + q) ^ (c & 7)) << 4);
        wa[kk] = 16384 + (80 * nh + 8 * (c & 1) + (c >> 1)) * 128 + (((4 * kk + q) ^ (c >> 1) ^ ((c & 1) << 2)) << 4);
    }
    bf16x8 xa[2][4], wb[2][5];
    auto rdp = [&](int buf, LAS char* s0, int kk) LAM {
#pragma unroll
        for (int m = 0; m < 4; ++m) xa[buf][m] = lds_frag(s0 + xo[kk] + 2048 * m);
#pragma unroll
        for (int n = 0; n < 5; ++n) wb[buf][n] = lds_frag(s0 + wa[kk] + 2048 * n);
    };
    auto mm_head = [&](int buf) LAM {
#pragma unroll
        for (int m = 0; m < 4; ++m) acc[m][0] = mfma(xa[buf][m], wb[buf][0], acc[m][0]);
    };
    auto mm_tail = [&](int buf) LAM {
#pragma unroll
        for (int n = 1; n < 5; ++n)
#pragma unroll
            for (int m = 0; m < 4; ++m) acc[m][n] = mfma(xa[buf][m], wb[buf][n], acc[m][n]);
    };
    if (tid < L_WORDS) ((LAS unsigned*)(sm + L_FULL))[tid] = tid < 4 ? 1u : 0u;
#pragma unroll
    for (int n = 0; n < 5; ++n)
        asm volatile("" : "+v"(acc[0][n]), "+v"(acc[1][n]), "+v"(acc[2][n]), "+v"(acc[3][n]));
    asm volatile("" : "+v"(xo[0]), "+v"(xo[1]), "+v"(wa[0]), "+v"(wa[1]));
    const unsigned tag = mk_tag(nonce);
    if (cw >= 2) {
        float s0 = 0.f, s1 = 0.f;
#pragma unroll
        for (int i = 0; i < 10; ++i) {
            s0 = dot2(xv[i][0], gv[i][0], s0); s1 = dot2(xv[i][1], gv[i][1], s1);
            s0 = dot2(xv[i][2], gv[i][2], s0); s1 = dot2(xv[i][3], gv[i][3], s1);
        }
        float s = s0 + s1;
        s += lane_xor(s, 1); s += lane_xor(s, 2); s += lane_xor(s, 4);
        const float lg = bfr(s), g = bfr(1.f / (1.f + fexp(-lg)));
        if ((lane & 7) == 0) __hip_atomic_store((GAS u64*)(a.ws + WS_G) + gtok, granule(tag, __float_as_uint(g)), RLX_AGENT);
    } else if (tid < 80) {
        float ar, ai, cr, ci;
        channel_consts(rlre, rlim, rldt, ar, ai, cr, ci);
        float pr = ar, pi = ai;
#pragma unroll
        for (int k = 0; k < 4; ++k) { const float t = pr * pr - pi * pi; pi = 2.f * pr * pi; pr = t; }
        const float r16 = pr, i16 = pi;
#pragma unroll
        for (int k = 0; k < 3; ++k) { const float t = pr * pr - pi * pi; pi = 2.f * pr * pi; pr = t; }
        LAS f32x2* tab = (LAS f32x2*)(sm + A_TAB);
        tab[0 * 80 + tid] = (f32x2){ar, ar};
        tab[1 * 80 + tid] = (f32x2){-ai, ai};
        tab[2 * 80 + tid] = (f32x2){r16, r16};
        tab[3 * 80 + tid] = (f32x2){-i16, i16};
        tab[4 * 80 + tid] = (f32x2){pr, pr};
        tab[5 * 80 + tid] = (f32x2){-pi, pi};
    }
    {
        float ar, ai, cr, ci;
        channel_consts(jlre, jlim, jldt, ar, ai, cr, ci);
        ((LAS f32x2*)(sm + A_JTAB + cw * 512))[lane] = (f32x2){2.f * cr, 2.f * ci};
    }
    if (tid < 160) __hip_atomic_store((GAS u64*)(a.ws + WS_EA) + (ti * 8 + gj) * 160 + tid, (u64)0, RLX_AGENT);
    if (TOUCH) touch_streams(a, sm, cw, lane);
    STAMP(a.ws, cw, 1);
    lds_bar();
    STAMP(a.ws, cw, 2);
    STAMPC(a.ws, cw, 14);
    rdp(0, sm, 0);
#pragma unroll 1
    for (int s = 0; s < 10; ++s) {
        if (TOPWAIT) wait_lgkm0();
        SCHED();
        rdp(1, sm + (s & 3) * SLOT_A, 1);
        u32x4 fl = *(volatile LAS u32x4*)(sm + L_FULL);
        mm_head(0); mm_tail(0);
        interleave<20, 10, IL_A>();
        SCHED();
        wait_lgkm0();
        asm volatile("" ::: "memory");
        *(volatile LAS unsigned*)(sm + L_FREE + 4 * cw) = s + 1;
        const unsigned need = s < 9 ? s + 2 : 0;
        unsigned landed = __builtin_amdgcn_readfirstlane(umin4(fl));
        LAS char* s0 = sm + ((s + 1) & 3) * SLOT_A;
        asm volatile("" : "+s"(landed), "+v"(s0));
        mm_head(1);
        SCHED();
        if (__builtin_expect(landed < need, 0)) ring_spin(sm + L_FULL, need);
        asm volatile("" ::: "memory");
        SCHED();
        rdp(0, s0, 0);
        mm_tail(1);
        interleave<16, 9, IL_A2>();
        SCHED();
    }
    __builtin_amdgcn_s_setprio(0);
    asm volatile("s_nop 7\n\ts_nop 7" ::: "memory");
    STAMP(a.ws, cw, 3);
    STAMPC(a.ws, cw, 15);
    LAS float* tab = (LAS float*)(sm + A_TAB);
    LAS float* sg = (LAS float*)(sm + A_SEG) + (4 * mh + q) * 160 + 80 * nh + c;
    float ca[5], cb[5];
#pragma unroll
    for (int n = 0; n < 5; ++n) { const int col = 80 * nh + 16 * n + c; ca[n] = tab[col]; cb[n] = tab[160 + col]; }
    a_pass1(acc, ca, cb, sg);
    WAIT_VM(0);
    STAMP(a.ws, cw, 4);
    lds_bar();
    STAMP(a.ws, cw, 5);
    word_wait(sm + L_HS, 3u);
    STAMP(a.ws, cw, 8);
    {
        const int qq = (lane & 15) >> 2, p = lane & 3;
        const __amdgpu_buffer_rsrc_t ract = rsrc(a.ws + WS_ACT, 128 * TS);
        LAS float* gt = (LAS float*)(sm + A_G) + 64 * mh + 16 * q;
        LAS char* ht = sm + A_HT + (80 * nh + c) * HT_STRIDE + (64 * mh + 4 * q) * 2;
        LAS char* hr = sm + A_HT + (8 * q + qq) * HT_STRIDE + 128 * mh + 8 * p;
        const unsigned ob = (8 * ti + 4 * mh) * TS + 5 * gj * 1024;
        a_pass2(acc, ca, cb, sg, gt, ht, hr + 96 * nh * HT_STRIDE, ract, lane * 16, ob + 3072 * nh);
        STAMP(a.ws, cw, 9);
        wait_lgkm0();
        if (lane == 0) word_add(sm + L_HT + 4 * mh);
        word_wait(sm + L_HT + 4 * mh, 2u);
        bf16x8 hf[2];
#pragma unroll
        for (int i = 0; i < 2; ++i) {
            LAS char* b = hr + 64 * HT_STRIDE + 32 * (2 * nh + i);
            hf[i] = lds_tr_frag(b, b + 4 * HT_STRIDE);
        }
#pragma unroll
        for (int i = 0; i < 2; ++i) st16_wt(ract, lane * 16, ob + (2 * nh + i) * TS + 2048, __builtin_bit_cast(u32x4, hf[i]));
    }
    wait_lgkm0();
    STAMP(a.ws, cw, 10);
    signal_stores(sm, L_DONE, a.ws + WS_FACT + ti * 256 + gj * 8, tag, lane, 4u);
    STAMP(a.ws, cw, 11);
    p2_final(a, sm, cw, lane, nonce);
}

DEV void b_loader(const Args& a, LAS char* sm, int ti, int gj, int lw, int lane) {
    STAMP(a.ws, lw + 4, 0);
    const unsigned nonce = __hip_atomic_load((GAS unsigned*)(a.ws + WS_NONCE), RLX_AGENT);
    unsigned off[12];
    {
        const int pos = lane & 7, rl = lane >> 3;
#pragma unroll
        for (int u = 0; u < 4; ++u) {
            const int R = 8 * (lw + 4 * u) + rl;
            off[u] = x_token(R) * 1280 + ((pos ^ (R & 7)) << 4);
        }
#pragma unroll
        for (int u = 4; u < 12; ++u) {
            const int wi = lw + 4 * (u - 4), sec = wi / 10, ch = 64 * (wi - 10 * sec) + lane, rho = ch / 10, cc = ch - 10 * rho;
            off[u] = (rho ^ (((rho >> 3) & 1) << 2)) * 3840 + cc * 16 + (sec == 0 ? 1280 : sec == 1 ? 2560 : 0);
        }
    }
    const char* xb = a.x + (size_t)ti * (128 * 1280);
    const char* wb = a.wqkv + 160 * gj;
    const bool full = lw < 2;
    auto issue_a = [&](int s) LAM {
        LAS char* dst = sm + (s % NS_B) * SLOT_B + lw * 1024;
#pragma unroll
        for (int u = 0; u < 4; ++u) glds16(xb + 128 * s + off[u], dst + 4096 * u);
#pragma unroll
        for (int u = 4; u < 6; ++u) glds16(wb + (size_t)(64 * 3840) * s + off[u], dst + 4096 * u);
    };
    auto issue_b = [&](int s) LAM {
        LAS char* dst = sm + (s % NS_B) * SLOT_B + lw * 1024;
#pragma unroll
        for (int u = 6; u < 12; ++u) if (u < 11 || full) glds16(wb + (size_t)(64 * 3840) * s + off[u], dst + 4096 * u);
    };
    STAMP(a.ws, lw + 4, 1);
    issue_a(0); issue_b(0); issue_a(1); issue_b(1);
    if (full) WAIT_VM(12); else WAIT_VM(11);
    bar();
#pragma unroll 1
    for (int s = 0; s < 8; ++s) {
        if (s > 0) ring_wait(sm + L_FREE, s);
        issue_a(s + 2);
        WAIT_VM(6);
        ring_put(sm + L_FULL + 4 * lw, s + 2);
        issue_b(s + 2);
    }
    WAIT_VM(0);
    ring_put(sm + L_FULL + 4 * lw, 10);
    STAMP(a.ws, lw + 4, 2);
    __builtin_amdgcn_s_setprio(2);
    const unsigned tag = mk_tag(nonce);
    const int tl = lw * 64 + lane;
    if (tl < 128) ((LAS float*)(sm + B_G))[tl] = bfr(1.f - gate_of(a, 128 * ti + tl, tag));
    {
        int o0[2], o1[2];
#pragma unroll
        for (int it = 0; it < 2; ++it) {
            const int g = 2 * (tl + 256 * it < 420 ? tl + 256 * it : 0);
            const int h = g / 210, rem = g - 210 * h, e = rem / 10, d = 2 * (rem - 10 * e);
            const bool kv = rem < 200;
            o0[it] = kv ? B_KVT + ((20 * h + e) * 84 + 20 * h + d) * 4 : B_Z + (20 * h + 2 * (rem - 200)) * 4;
            o1[it] = o0[it] + (kv ? 80 * 84 * 4 : 320);
        }
        asm volatile("" : "+v"(o0[0]), "+v"(o0[1]), "+v"(o1[0]), "+v"(o1[1]));
        const __amdgpu_buffer_rsrc_t rk = rsrc(a.ws + WS_KV, 16 * 8 * 840 * 8);
        word_wait(sm + L_KV, 4u);
        STAMP(a.ws, lw + 4, 3);
        u32x4 pv[2];
#pragma unroll
        for (int it = 0; it < 2; ++it) {
            const f32x4 v = *(LAS f32x4*)(sm + o0[it]) + *(LAS f32x4*)(sm + o1[it]);
            pv[it] = (u32x4){pk2(v[0], v[1]), tag, pk2(v[2], v[3]), tag};
        }
        asm volatile("" : "+v"(pv[0]), "+v"(pv[1]) :: "memory");
        wait_lgkm0();
        if (lane == 0) word_add(sm + L_PUB);
        st16_wt(rk, tl * 16, (ti * 8 + gj) * 6720, pv[0]);
        if (tl + 256 < 420) st16_wt(rk, (tl + 256) * 16, (ti * 8 + gj) * 6720, pv[1]);
        asm volatile("s_nop 1" ::"v"(pv[0]), "v"(pv[1]));
    }
    STAMP(a.ws, lw + 4, 4);
    if (lw == 0) {
        const int cnt = ti < 8 ? 53 : 52, g0 = ti < 8 ? 53 * ti : 424 + 52 * (ti - 8);
        const int g = g0 + (lane < cnt ? lane : 0);
        GAS u64* src = (GAS u64*)(a.ws + WS_KV) + gj * 840 + g;
        unsigned v[16];
#pragma unroll 1
        for (unsigned spins = 0; spins < SPIN_CAP; ++spins) {
            u64 x[16];
#pragma unroll
            for (int i = 0; i < 16; ++i) x[i] = __hip_atomic_load(src + i * (8 * 840), RLX_AGENT);
            bool ok = true;
#pragma unroll
            for (int i = 0; i < 16; ++i) { v[i] = (unsigned)x[i]; ok &= (unsigned)(x[i] >> 32) == tag; }
            if (wave_all(ok)) break;
            __builtin_amdgcn_s_sleep(1);
        }
        float s0 = 0.f, s1 = 0.f;
#pragma unroll
        for (int i = 0; i < 16; ++i) { s0 += bf_lo(v[i]); s1 += bf_hi(v[i]); }
        if (lane < cnt) __hip_atomic_store((GAS u64*)(a.ws + WS_TOT) + gj * 840 + g, granule(tag, pk2(s0, s1)), RLX_AGENT);
        STAMP(a.ws, lw + 4, 5);
    }
    bf16x8 fw[P2_PW][5];
    if (lw == 3) {
        word_wait(sm + L_PUB, 4u);
        p2_prefetch(a, sm, lw, lane, tag, 2, fw);
        const __amdgpu_buffer_rsrc_t rs = rsrc(a.ws + WS_TOT, 8 * 840 * 8);
        u32x4 v[7];
#pragma unroll 1
        for (unsigned spins = 0; spins < SPIN_CAP; ++spins) {
            bool ok = true;
#pragma unroll
            for (int k = 0; k < 7; ++k) v[k] = __builtin_amdgcn_raw_buffer_load_b128(rs, gj * 6720 + (lane + 64 * k < 420 ? lane + 64 * k : 419) * 16, 0, 16);
#pragma unroll
            for (int k = 0; k < 7; ++k) ok &= (v[k][1] == tag) & (v[k][3] == tag);
            if (wave_all(ok)) break;
            __builtin_amdgcn_s_sleep(SLP_TOT);
        }
#pragma unroll
        for (int k = 0; k < 7; ++k) if (lane + 64 * k < 420) ((LAS u32x2*)(sm + B_TOT))[lane + 64 * k] = (u32x2){v[k][0], v[k][2]};
        wait_lgkm0();
        word_set(sm + L_TOT, 1u);
        STAMP(a.ws, lw + 4, 5);
    }
    __builtin_amdgcn_s_setprio(0);
    if (lw != 3) {
        word_wait(sm + L_PUB, 4u);
        p2_prefetch(a, sm, lw, lane, tag, 2, fw);
    }
    p2_main(a, sm, lw, lane, nonce, 2, fw);
}

DEV void b_consumer(const Args& a, LAS char* sm, int ti, int gj, int cw, int lane) {
    const int mh = cw >> 1, nh = cw & 1, c = lane & 15, q = lane >> 4, tid = cw * 64 + lane;
    STAMP(a.ws, cw, 0);
    const unsigned nonce = __hip_atomic_load((GAS unsigned*)(a.ws + WS_NONCE), RLX_AGENT), tag = mk_tag(nonce);
    {
        const __amdgpu_buffer_rsrc_t rk = rsrc(a.ws + WS_KV, 16 * 8 * 840 * 8);
        const u32x4 z = {0u, 0u, 0u, 0u};
        st16_wt(rk, tid * 16, (ti * 8 + gj) * 6720, z);
        if (tid + 256 < 420) st16_wt(rk, (tid + 256) * 16, (ti * 8 + gj) * 6720, z);
        const int cnt = ti < 8 ? 53 : 52, g0 = ti < 8 ? 53 * ti : 424 + 52 * (ti - 8);
        if (tid < cnt) __hip_atomic_store((GAS u64*)(a.ws + WS_TOT) + gj * 840 + g0 + tid, (u64)0, RLX_AGENT);
    }
    __builtin_amdgcn_s_setprio(1);
    f32x4 an[5][4], at[5][2];
#pragma unroll
    for (int t = 0; t < 5; ++t) {
#pragma unroll
        for (int i = 0; i < 4; ++i) an[t][i] = (f32x4){0.f, 0.f, 0.f, 0.f};
        at[t][0] = at[t][1] = (f32x4){0.f, 0.f, 0.f, 0.f};
    }
    int xlo[2], xhi[2], wr[2];
#pragma unroll
    for (int kk = 0; kk < 2; ++kk) {
        const int sw = ((4 * kk + q) ^ (c & 7)) << 4;
        xlo[kk] = (64 * mh + 32 * nh + c) * 128 + sw;
        xhi[kk] = (64 * mh + 32 * (1 - nh) + c) * 128 + sw;
        wr[kk] = 16384 + (8 * q + 4 * (kk ^ (q & 1)) + (c >> 2)) * 160 + 8 * (c & 3);
    }
    bf16x8 xa[2][4], wb[2][5], wt[2][5];
    auto rdp = [&](int buf, LAS char* s0, int kk) LAM {
        LAS char* q0 = s0 + kk * 5120 + wr[0]; LAS char* q1 = s0 + kk * 5120 + wr[1];
        LAS char* a0 = q0 + 96 * nh; LAS char* a1 = q1 + 96 * nh;
        xa[buf][0] = lds_frag(s0 + xlo[kk]);
        wb[buf][0] = lds_tr_frag(a0, a1);
        wb[buf][1] = lds_tr_frag(a0 + 32, a1 + 32);
        xa[buf][1] = lds_frag(s0 + xlo[kk] + 2048);
        wb[buf][2] = lds_tr_frag(q0 + 64 + 10240 * nh, q1 + 64 + 10240 * nh);
        wb[buf][3] = lds_tr_frag(a0 + 10240, a1 + 10240);
        xa[buf][2] = lds_frag(s0 + xhi[kk]);
        wb[buf][4] = lds_tr_frag(a0 + 10272, a1 + 10272);
        wt[buf][0] = lds_tr_frag(q0 + 20480, q1 + 20480);
        xa[buf][3] = lds_frag(s0 + xhi[kk] + 2048);
        wt[buf][1] = lds_tr_frag(q0 + 20512, q1 + 20512);
        wt[buf][2] = lds_tr_frag(q0 + 20544, q1 + 20544);
        wt[buf][3] = lds_tr_frag(q0 + 20576, q1 + 20576);
        wt[buf][4] = lds_tr_frag(q0 + 20608, q1 + 20608);
    };
    auto mm_head = [&](int buf) LAM {
#pragma unroll
        for (int i = 0; i < 4; ++i) an[0][i] = mfma(xa[buf][i], wb[buf][0], an[0][i]);
    };
    auto mm_tail = [&](int buf) LAM {
#pragma unroll
        for (int t = 1; t < 5; ++t)
#pragma unroll
            for (int i = 0; i < 4; ++i) an[t][i] = mfma(xa[buf][i], wb[buf][t], an[t][i]);
#pragma unroll
        for (int t = 0; t < 5; ++t)
#pragma unroll
            for (int i = 0; i < 2; ++i) at[t][i] = mfma(wt[buf][t], xa[buf][i], at[t][i]);
    };
    if (tid < L_WORDS) ((LAS unsigned*)(sm + L_FULL))[tid] = tid < 4 ? 1u : 0u;
#pragma unroll
    for (int t = 0; t < 5; ++t)
        asm volatile("" : "+v"(an[t][0]), "+v"(an[t][1]), "+v"(an[t][2]), "+v"(an[t][3]), "+v"(at[t][0]), "+v"(at[t][1]));
    if (TOUCH) touch_streams(a, sm, cw, lane);
    STAMP(a.ws, cw, 1);
    lds_bar();
    STAMP(a.ws, cw, 2);
    STAMPC(a.ws, cw, 14);
    rdp(0, sm, 0);
    int slot = 0;
#pragma unroll 1
    for (int s = 0; s < 10; ++s) {
        if (TOPWAIT) wait_lgkm0();
        SCHED();
        rdp(1, sm + slot * SLOT_B, 1);
        u32x4 fl = *(volatile LAS u32x4*)(sm + L_FULL);
        mm_head(0); mm_tail(0);
        interleave<30, 25, IL_B>();
        SCHED();
        wait_lgkm0();
        asm volatile("" ::: "memory");
        *(volatile LAS unsigned*)(sm + L_FREE + 4 * cw) = s + 1;
        slot = slot == 2 ? 0 : slot + 1;
        const unsigned need = s < 9 ? s + 2 : 0;
        unsigned landed = __builtin_amdgcn_readfirstlane(umin4(fl));
        LAS char* s0 = sm + slot * SLOT_B;
        asm volatile("" : "+s"(landed), "+v"(s0));
        mm_head(1);
        SCHED();
        if (__builtin_expect(landed < need, 0)) ring_spin(sm + L_FULL, need);
        asm volatile("" ::: "memory");
        SCHED();
        rdp(0, s0, 0);
        mm_tail(1);
        interleave<26, 24, IL_B2>();
        SCHED();
    }
    STAMP(a.ws, cw, 3);
    STAMPC(a.ws, cw, 15);
    bf16x8 fr[5][2];
#pragma unroll
    for (int t = 0; t < 2; ++t)
#pragma unroll
        for (int i = 0; i < 4; ++i)
#pragma unroll
            for (int r = 0; r < 4; ++r) an[t][i][r] = phi(an[t][i][r]);
    if (nh == 0) {
#pragma unroll
        for (int i = 0; i < 4; ++i)
#pragma unroll
            for (int r = 0; r < 4; ++r) an[2][i][r] = phi(an[2][i][r]);
    }
#pragma unroll
    for (int t = 0; t < 5; ++t) { fr[t][0] = pack_pair(an[t][0], an[t][1]); fr[t][1] = pack_pair(an[t][2], an[t][3]); }
    {
        LAS char* xs = sm + B_X1 + cw * 2048 + lane * 16;
        *(LAS bf16x8*)(xs + (nh << 10)) = fr[2][0];
        *(LAS bf16x8*)(xs + ((1 ^ nh) << 10)) = fr[2][1];
    }
    wait_lgkm0();
    word_set(sm + L_X1 + 4 * cw, 1u);
#pragma unroll
    for (int t = 0; t < 5; ++t) asm volatile("" : "+v"(fr[t][0]), "+v"(fr[t][1]));
    STAMP(a.ws, cw, 4);
    word_wait(sm + L_X1 + 4 * (cw ^ 1), 1u);
    ring_spin(sm + L_FREE, 10);
    asm volatile("" ::: "memory");
    {
        LAS char* xr = sm + B_X1 + (cw ^ 1) * 2048 + lane * 16;
        const bf16x8 R0 = *(LAS bf16x8*)(xr + (nh << 10)), R1 = *(LAS bf16x8*)(xr + ((1 ^ nh) << 10));
        const bf16x8 ones = as_frag((u32x4){0x3f803f80u, 0x3f803f80u, 0x3f803f80u, 0x3f803f80u});
        const f32x4 z4 = {0.f, 0.f, 0.f, 0.f};
        LAS float* kvt = (LAS float*)(sm + B_KVT) + mh * (80 * 84) + c * 84 + 4 * q;
        LAS float* zz = (LAS float*)(sm + B_Z) + mh * 80 + 4 * q;
        auto prod = [&](const bf16x8& k0, const bf16x8& k1, const bf16x8& v0, const bf16x8& v1, int ta, int tb) LAM {
            *(LAS f32x4*)(kvt + 16 * tb * 84 + 16 * ta) = mfma(k1, v1, mfma(k0, v0, z4));
        };
        auto zsum = [&](const bf16x8& k0, const bf16x8& k1, int ta) LAM {
            const f32x4 d = mfma(k1, ones, mfma(k0, ones, z4));
            if (c == 0) *(LAS f32x4*)(zz + 16 * ta) = d;
        };
        const int b0 = 3 * nh;
        prod(fr[0][0], fr[0][1], fr[3][0], fr[3][1], b0, b0);
        prod(fr[0][0], fr[0][1], fr[4][0], fr[4][1], b0, b0 + 1);
        prod(fr[1][0], fr[1][1], fr[3][0], fr[3][1], b0 + 1, b0);
        prod(fr[1][0], fr[1][1], fr[4][0], fr[4][1], b0 + 1, b0 + 1);
        zsum(fr[0][0], fr[0][1], b0);
        zsum(fr[1][0], fr[1][1], b0 + 1);
        if (nh == 0) {
            prod(fr[2][0], fr[2][1], fr[4][0], fr[4][1], 2, 1);
            prod(fr[1][0], fr[1][1], R0, R1, 1, 2);
            prod(fr[2][0], fr[2][1], R0, R1, 2, 2);
            zsum(fr[2][0], fr[2][1], 2);
        } else {
            prod(fr[0][0], fr[0][1], fr[2][0], fr[2][1], 3, 2);
            prod(R0, R1, fr[3][0], fr[3][1], 2, 3);
        }
    }
    WAIT_VM(0);
    wait_lgkm0();
    if (lane == 0) word_add(sm + L_KV);
    __builtin_amdgcn_s_setprio(0);
    STAMP(a.ws, cw, 5);
    bf16x8 P[2][3];
#pragma unroll
    for (int i = 0; i < 2; ++i) {
#pragma unroll
        for (int t = 0; t < 5; ++t)
#pragma unroll
            for (int r = 0; r < 4; ++r) at[t][i][r] = phi(at[t][i][r]);
        P[i][0] = pack_pair(at[0][i], at[1][i]);
        P[i][1] = pack_pair(at[2][i], at[3][i]);
        P[i][2] = as_frag((u32x4){pk2(at[4][i][0], at[4][i][1]), pk2(at[4][i][2], at[4][i][3]), 0u, 0u});
    }
    int adr[21];
    {
        int dh[5], db[5], eh[5], eb[5];
#pragma unroll
        for (int k = 0; k < 5; ++k) {
            const int d0 = 16 * k + 4 * q, e0 = 16 * k + c;
            dh[k] = d0 / 20; db[k] = B_TOT + dh[k] * 840 + (d0 - 20 * dh[k]) * 2;
            eh[k] = e0 / 20; eb[k] = (e0 - 20 * eh[k]) * 40;
        }
        auto ad = [&](int k, int E) LAM { return dh[k] == eh[E] ? db[k] + eb[E] : L_ZERO; };
        adr[0] = ad(0, 0); adr[1] = ad(1, 0);
        adr[2] = ad(0, 1); adr[3] = ad(1, 1); adr[4] = ad(2, 1); adr[5] = ad(3, 1);
        adr[6] = ad(1, 2); adr[7] = ad(2, 2); adr[8] = ad(3, 2);
        adr[9] = ad(2, 3); adr[10] = ad(3, 3); adr[11] = ad(4, 3);
        adr[12] = ad(3, 4); adr[13] = ad(4, 4);
#pragma unroll
        for (int k = 0; k < 5; ++k) adr[14 + k] = dh[k] == (c & 3) ? db[k] + 800 : L_ZERO;
        adr[19] = L_ZERO; adr[20] = B_G + 4 * (64 * mh + 16 * (c >> 2) + 8 * nh + (c & 3));
    }
#pragma unroll
    for (int i = 0; i < 2; ++i) asm volatile("" : "+v"(P[i][0]), "+v"(P[i][1]), "+v"(P[i][2]));
    asm volatile("" : "+v"(adr[0]), "+v"(adr[1]), "+v"(adr[2]), "+v"(adr[3]), "+v"(adr[4]), "+v"(adr[5]), "+v"(adr[6]),
                      "+v"(adr[7]), "+v"(adr[8]), "+v"(adr[9]), "+v"(adr[10]), "+v"(adr[11]), "+v"(adr[12]), "+v"(adr[13]));
    asm volatile("" : "+v"(adr[14]), "+v"(adr[15]), "+v"(adr[16]), "+v"(adr[17]), "+v"(adr[18]), "+v"(adr[20]));
    const __amdgpu_buffer_rsrc_t ract = rsrc(a.ws + WS_ACT, 128 * TS);
    u32x4 os[2][2]; u32x2 o8[2];
    STAMP(a.ws, cw, 6);
    word_wait(sm + L_TOT, 1u);
    word_wait(sm + L_PUB, 4u);
    STAMP(a.ws, cw, 7);
    {
        auto h4 = [&](int i) LAM -> u32x2 { return *(LAS u32x2*)(sm + adr[i]); };
        auto fr2 = [&](const u32x2& lo, const u32x2& hi) LAM { return as_frag((u32x4){lo[0], lo[1], hi[0], hi[1]}); };
        const u32x2 zz = {0u, 0u};
        const bf16x8 Z0 = fr2(h4(14), h4(15)), Z1 = fr2(h4(16), h4(17)), Z2 = fr2(h4(18), zz);
        const bf16x8 F0 = fr2(h4(0), h4(1)), F1 = fr2(h4(2), h4(3)), F2 = fr2(h4(4), h4(5));
        const bf16x8 F3 = fr2(zz, h4(6)), F4 = fr2(h4(7), h4(8)), F5 = fr2(h4(9), h4(10));
        const bf16x8 F6 = fr2(h4(11), zz), F7 = fr2(zz, h4(12)), F8 = fr2(h4(13), zz);
        const float ogv[2] = {*(LAS float*)(sm + adr[20]), *(LAS float*)(sm + adr[20] + 16)};
        const f32x4 z4 = {0.f, 0.f, 0.f, 0.f};
        const int uu = gj >> 1, odd = gj & 1;
#pragma unroll
        for (int i = 0; i < 2; ++i) {
            const f32x4 dn = mfma(Z2, P[i][2], mfma(Z1, P[i][1], mfma(Z0, P[i][0], z4)));
            const f32x4 n0 = mfma(F0, P[i][0], z4);
            const f32x4 n1 = mfma(F2, P[i][1], mfma(F1, P[i][0], z4));
            const f32x4 n2 = mfma(F4, P[i][1], mfma(F3, P[i][0], z4));
            const f32x4 n3 = mfma(F6, P[i][2], mfma(F5, P[i][1], z4));
            const f32x4 n4 = mfma(F8, P[i][2], mfma(F7, P[i][1], z4));
            const float og = ogv[i];
            const float r0 = og * __builtin_amdgcn_rcpf(dn[0] + 1e-6f), r1 = og * __builtin_amdgcn_rcpf(dn[1] + 1e-6f);
            const float r2 = og * __builtin_amdgcn_rcpf(dn[2] + 1e-6f), r3 = og * __builtin_amdgcn_rcpf(dn[3] + 1e-6f);
            const float s1 = q < 1 ? r0 : r1, s2 = q < 2 ? r1 : r2, s3 = q < 3 ? r2 : r3;
            os[i][0] = (u32x4){pk2(n0[0] * r0, n0[1] * r0), pk2(n0[2] * r0, n0[3] * r0), pk2(n1[0] * s1, n1[1] * s1), pk2(n1[2] * s1, n1[3] * s1)};
            os[i][1] = (u32x4){pk2(n2[0] * s2, n2[1] * s2), pk2(n2[2] * s2, n2[3] * s2), pk2(n3[0] * s3, n3[1] * s3), pk2(n3[2] * s3, n3[3] * s3)};
            o8[i] = (u32x2){pk2(n4[0] * r3, n4[1] * r3), pk2(n4[2] * r3, n4[3] * r3)};
        }
#pragma unroll
        for (int i = 0; i < 2; ++i) {
            const unsigned ob = (8 * ti + 4 * mh + 2 * nh + i) * TS + (40 + 5 * uu) * 1024;
            st16_wt(ract, lane * 16, ob + (odd ? 3072 : 0), os[i][0]);
            st16_wt(ract, lane * 16, ob + (odd ? 4096 : 1024), os[i][1]);
            st8_wt(ract, lane * 16 + 8 * odd, ob + 2048, o8[i]);
        }
    }
    KEEP(os[0][0]); KEEP(os[0][1]); KEEP(os[1][0]); KEEP(os[1][1]);
    STAMP(a.ws, cw, 8);
    signal_stores(sm, L_DONE, a.ws + WS_FACT + ti * 256 + 128 + gj * 8, tag, lane, 4u);
    STAMP(a.ws, cw, 9);
    p2_final(a, sm, cw, lane, nonce);
}

}

__global__ __launch_bounds__(512, 2) NOPK void gm_fused(const void* x, const void* lre, const void* lim, const void* bre,
                                                   const void* bim, const void* cre, const void* cim, const void* logdt,
                                                   const void* dskip, const void* wqkv, const void* wo, const void* gw,
                                                   void* ws, void* out) {
    __shared__ __attribute__((aligned(1024))) char smem[LDS_BYTES];
    LAS char* sm = (LAS char*)smem;
    u64 pc0;
    asm volatile("s_getpc_b64 %0" : "=s"(pc0));
    const char* code = (const char*)(pc0 & ~127ull);
    if (TOUCH_CODE && (threadIdx.x >> 6) == 0) touch_code(code, sm, threadIdx.x & 63);
#define MAPMODE 2
    const int b = blockIdx.x, xcd = b & 7, idx = b >> 3;
    const bool roleA = MAPMODE == 1 ? idx < 16 : MAPMODE == 2 ? idx >= 16 : xcd < 4;
    const int ti = MAPMODE != 0 ? 4 * (xcd >> 1) + (idx & 3) : 8 * ((xcd & 3) >> 1) + (idx & 7);
    const int gj = MAPMODE != 0 ? 4 * (xcd & 1) + ((idx >> 2) & 3) : 4 * (xcd & 1) + (idx >> 3);
    const int wave = __builtin_amdgcn_readfirstlane(threadIdx.x >> 6), lane = threadIdx.x & 63;
    const Args a{(const char*)x, (const u16*)lre, (const u16*)lim, (const char*)bre, (const char*)bim, (const char*)cre,
                 (const char*)cim, (const u16*)logdt, (const char*)wqkv, (const char*)wo, (const u16*)gw,
                 (const u16*)dskip, (char*)ws, (float*)out};
    STAMPL(ws, 0);
    if (roleA) {
        if (wave < 4) a_consumer(a, sm, ti, gj, wave, lane);
        else a_loader(a, sm, ti, gj, wave - 4, lane);
    } else {
        if (wave < 4) b_consumer(a, sm, ti, gj, wave, lane);
        else b_loader(a, sm, ti, gj, wave - 4, lane);
    }
    STAMPL(ws, 1);
}

extern "C" void kernel_launch(void* const* d_in, const int* in_sizes, int n_in, void* d_out, int out_size, void* d_ws,
                              size_t ws_size, hipStream_t stream) {
    gm_fused<<<256, 512, 0, stream>>>(d_in[0], d_in[1], d_in[2], d_in[3], d_in[4], d_in[5], d_in[6], d_in[7], d_in[8],
                                      d_in[10], d_in[11], d_in[12], d_ws, d_out);
}
```
